```python
import torch

def _softmax(x, axis=-1):
    return torch.softmax(x, dim=axis)
def _gelu(x):
    return torch.nn.functional.gelu(x, approximate="tanh")
def _silu(x):
    return torch.nn.functional.silu(x)
def _sigmoid(x):
    return torch.sigmoid(x)
def _layernorm(x, axis=-1, eps=1e-5):
    m = x.mean(dim=axis, keepdim=True)
    v = x.var(dim=axis, keepdim=True, unbiased=False)
    return (x - m) / torch.sqrt(v + eps)
def _scatter_add(tgt, idx, src, axis=0):
    return tgt.clone().index_add_(axis, idx, src)
def _segment_sum(src, seg, n):
    out = torch.zeros((n,) + src.shape[1:], dtype=src.dtype)
    return out.index_add_(0, seg, src)
def _pad(x, pw):
    flat = tuple(v for pair in reversed(pw) for v in pair)
    return torch.nn.functional.pad(x, flat)

kernel_name = 'recurrent_scan__sliding_block_cached__glu_gated_bilinear__prompt'

def setup_inputs():

    _w = (lambda a: (lambda t: t.to(torch.bfloat16) if t.is_floating_point() else t)(torch.as_tensor(a)))
    def _A_setup():
        B, Sq, D = 1, 2048, 1024
        Hq, Hkv, Sk, Dh = 32, 4, 2048, 128
        assert Hq % Hkv == 0, (Hq, Hkv)
        return {
            "x": _w((lambda *s: torch.randn(*s, dtype=torch.bfloat16) * 0.05)(B, Sq, D)),
            "k_cache": _w((lambda *s: torch.randn(*s, dtype=torch.bfloat16) * 1.6)(B, Hkv, Sk, Dh)),
            "v_cache": _w((lambda *s: torch.randn(*s, dtype=torch.bfloat16) * 1.6)(B, Hkv, Sk, Dh)),
            "wq": _w((lambda *s: torch.randn(*s, dtype=torch.bfloat16) * 1.6)(D, Hq * Dh) * (D ** -0.5)),
            "wo": _w((lambda *s: torch.randn(*s, dtype=torch.bfloat16) * 1.6)(Hq * Dh, D) * ((Hq * Dh) ** -0.5)),
        }
    _A_in = _A_setup()

    def _B_setup():
        B, S, D, F = 1, 2048, 1024, 3584
        return {
            "x": _w((lambda *s: torch.randn(*s, dtype=torch.bfloat16) * 0.05)(B, S, D)),
            "w_gate": _w((lambda *s: torch.randn(*s, dtype=torch.bfloat16) * 0.2)(D, F) * (D ** -0.5)),
            "w_up": _w((lambda *s: torch.randn(*s, dtype=torch.bfloat16) * 0.2)(D, F) * (D ** -0.5)),
            "w_down": _w((lambda *s: torch.randn(*s, dtype=torch.bfloat16) * 0.2)(F, D) * (F ** -0.5)),
        }
    _B_in = _B_setup()

    return {
        "A_x": _A_in["x"],
        "A_k_cache": _A_in["k_cache"],
        "A_v_cache": _A_in["v_cache"],
        "A_wq": _A_in["wq"],
        "A_wo": _A_in["wo"],
        "B_x": _B_in["x"],
        "B_w_gate": _B_in["w_gate"],
        "B_w_up": _B_in["w_up"],
        "B_w_down": _B_in["w_down"],
    }

def reference(A_x, A_k_cache, A_v_cache, A_wq, A_wo, B_x, B_w_gate, B_w_up, B_w_down):
    def _A_ref(x, k_cache, v_cache, wq, wo):
        B, Sq, D = x.shape
        Hq, Dh = 32, 128
        Hkv, Sk = k_cache.shape[1], k_cache.shape[2]
        W = min(4096, max(256, Sk // 4))
        rep = Hq // Hkv
        q = (lambda x, *d: x.permute(*d))((x @ wq).reshape(B, Sq, Hq, Dh), 0, 2, 1, 3)
        k = (lambda x, n, axis: torch.repeat_interleave(x, n, dim=axis))(k_cache[:, :, Sk - W:, :], rep, axis=1)
        v = (lambda x, n, axis: torch.repeat_interleave(x, n, dim=axis))(v_cache[:, :, Sk - W:, :], rep, axis=1)
        scores = torch.einsum("bhqd,bhkd->bhqk", q, k) * (Dh ** -0.5)
        p = _softmax(scores, axis=-1)
        o = torch.einsum("bhqk,bhkd->bhqd", p, v)
        return (lambda x, *d: x.permute(*d))(o, 0, 2, 1, 3).reshape(B, Sq, Hq * Dh) @ wo

    def _B_ref(x, w_gate, w_up, w_down):
        g = torch.einsum("bsd,df->bsf", x, w_gate)
        u = torch.einsum("bsd,df->bsf", x, w_up)
        h = g * u
        return torch.einsum("bsf,fd->bsd", h, w_down)

    x = A_x
    xn = _layernorm(x, axis=-1)
    s = _A_ref(xn, A_k_cache, A_v_cache, A_wq, A_wo)
    g = _silu(_B_ref(xn, B_w_gate, B_w_up, B_w_down))
    return x + s * g
```

```cpp
#include <hip/hip_runtime.h>
#include <stdint.h>

typedef unsigned short u16;
typedef unsigned int u32;
constexpr u32 WS_XN = 0, WS_OF = 8u << 20, WS_HF = 25u << 20, WS_ST = 64u << 20, WS_SKEW = 4352;
constexpr int LDS_BYTES = 160 * 1024;

struct Args { const u16 *x, *kc, *vc, *wq, *wo, *wg, *wu, *wd; char* ws; u16* out; uint64_t ord[16]; };

__global__ __launch_bounds__(256, 1) void fused_block(Args args_) {
  asm volatile(
    "s_load_dwordx16 s[4:19], s[0:1], 0x0\n\t"
    "s_getpc_b64 s[28:29]\n\t"
    ".Lfb_pc:\n\t"
    "s_lshr_b32 s26, s2, 3\n\t"
    "s_lshl_b32 s26, s26, 7\n\t"
    "v_lshrrev_b32 v5, 4, v0\n\t"
    "v_lshl_add_u32 v5, v5, 12, s26\n\t"
    "v_min_u32 v5, .Lfb_end-.Lfb_pc, v5\n\t"
    "global_load_dword v226, v5, s[28:29]\n\t"
    "v_bfe_u32 v4, v0, 1, 3\n\t"
    "v_lshlrev_b32 v2, 11, v4\n\t"
    "v_and_b32 v4, 1, v0\n\t"
    "v_lshl_or_b32 v2, v4, 4, v2\n\t"
    "v_lshrrev_b32 v4, 4, v0\n\t"
    "v_lshl_or_b32 v2, v4, 6, v2\n\t"
    "s_and_b32 s60, s2, 7\n\t"
    "s_lshl_b32 s60, s60, 2\n\t"
    "s_bfe_u32 s77, s2, 131075\n\t"
    "s_add_u32 s60, s60, s77\n\t"
    "s_and_b32 s31, s2, 224\n\t"
    "s_add_u32 s31, s31, s60\n\t"
    "s_lshl_b32 s78, s31, 14\n\t"
    "s_waitcnt lgkmcnt(0)\n\t"
    "s_add_u32 s24, s4, s78\n\t"
    "s_addc_u32 s25, s5, 0\n\t"
    "global_load_dwordx4 v[200:203], v2, s[24:25]\n\t"
    "global_load_dwordx4 v[204:207], v2, s[24:25] offset:32\n\t"
    "global_load_dwordx4 v[208:211], v2, s[24:25] offset:1024\n\t"
    "global_load_dwordx4 v[212:215], v2, s[24:25] offset:1056\n\t"
    "s_load_dwordx2 s[88:89], s[0:1], 0x40\n\t"
    "s_lshr_b32 s61, s2, 5\n\t"
    "s_lshl_b32 s26, s61, 3\n\t"
    "s_add_u32 s26, s26, 80\n\t"
    "s_load_dwordx2 s[58:59], s[0:1], s26\n\t"
    "v_readfirstlane_b32 s3, v0\n\t"
    "v_and_b32 v1, 63, v0\n\t"
    "s_lshr_b32 s26, s31, 6\n\t"
    "s_lshl_b32 s26, s26, 19\n\t"
    "s_bfe_u32 s27, s31, 262146\n\t"
    "s_lshl_b32 s27, s27, 13\n\t"
    "s_add_u32 s26, s26, s27\n\t"
    "s_add_u32 s26, s26, 393216\n\t"
    "s_lshr_b32 s3, s3, 6\n\t"
    "s_and_b32 s27, s31, 1\n\t"
    "s_bitcmp1_b32 s31, 1\n\t"
    "s_cselect_b64 s[6:7], s[8:9], s[6:7]\n\t"
    "s_cselect_b32 s77, 7, 12\n\t"
    "s_cselect_b64 vcc, -1, 0\n\t"
    "s_lshl_b32 s27, s27, s77\n\t"
    "s_add_u32 s26, s26, s27\n\t"
    "v_lshlrev_b32 v3, 4, v0\n\t"
    "v_lshrrev_b32 v4, 3, v0\n\t"
    "v_and_b32 v5, 7, v0\n\t"
    "v_lshlrev_b32 v5, 4, v5\n\t"
    "v_lshl_add_u32 v4, v4, 8, v5\n\t"
    "v_cndmask_b32 v3, v3, v4, vcc\n\t"
    "v_mov_b32 v4, 50331648\n\t"
    "s_lshl_b32 s27, s3, 10\n\t"
    "s_add_u32 m0, s27, 148480\n\t"
    "s_add_u32 s6, s6, s26\n\t"
    "s_addc_u32 s7, s7, 0\n\t"
    "global_load_lds_dwordx4 v3, s[6:7]\n\t"
    "s_mov_b32 s90, 134217728\n\t"
    "s_mov_b32 s91, 131072\n\t"
    "v_and_b32 v8, 7, v1\n\t"
    "v_bfe_u32 v9, v1, 3, 2\n\t"
    "s_lshl_b32 s26, s61, 2\n\t"
    "s_add_u32 s26, s26, s3\n\t"
    "s_lshl_b32 s26, s26, 2\n\t"
    "s_and_b32 s27, s26, 128\n\t"
    "s_mul_i32 s27, s27, 3\n\t"
    "s_add_u32 s26, s26, s27\n\t"
    "v_add_u32 v9, s26, v9\n\t"
    "v_mov_b32 v6, s16\n\t"
    "v_mov_b32 v7, s17\n\t"
    "v_mov_b32 v5, s14\n\t"
    "v_mov_b32 v10, s15\n\t"
    "v_cmp_gt_u32 vcc, 5, v8\n\t"
    "v_subrev_u32 v11, 5, v8\n\t"
    "v_subrev_u32 v12, 2, v8\n\t"
    "v_cndmask_b32 v6, v6, v5, vcc\n\t"
    "v_cndmask_b32 v7, v7, v10, vcc\n\t"
    "v_cndmask_b32 v11, v11, v12, vcc\n\t"
    "v_mul_u32_u24 v11, 110, v11\n\t"
    "v_and_b32 v11, -4, v11\n\t"
    "s_mul_i32 s26, s60, 224\n\t"
    "v_mul_u32_u24 v12, 7168, v9\n\t"
    "v_add3_u32 v11, v11, s26, v12\n\t"
    "s_lshl_b32 s26, s60, 8\n\t"
    "v_lshlrev_b32 v12, 13, v9\n\t"
    "v_lshl_add_u32 v12, v8, 7, v12\n\t"
    "v_add_u32 v12, s26, v12\n\t"
    "v_mov_b32 v5, s10\n\t"
    "v_mov_b32 v10, s11\n\t"
    "v_cmp_gt_u32 vcc, 2, v8\n\t"
    "s_nop 1\n\t"
    "v_cndmask_b32 v6, v6, v5, vcc\n\t"
    "v_cndmask_b32 v7, v7, v10, vcc\n\t"
    "v_cndmask_b32 v11, v11, v12, vcc\n\t"
    "v_mov_b32 v5, 3670016\n\t"
    "v_mov_b32 v10, 4194304\n\t"
    "v_cndmask_b32 v12, v5, v10, vcc\n\t"
    "v_add_co_u32 v6, vcc, v6, v11\n\t"
    "s_nop 1\n\t"
    "v_addc_co_u32 v7, vcc, 0, v7, vcc\n\t"
    "s_waitcnt lgkmcnt(0)\n\t"
    "global_load_dword v224, v4, s[88:89] sc1\n\t"
    "global_load_dword v227, v[6:7], off\n\t"
    "s_lshl_b32 s73, s3, 11\n\t"
    "s_mov_b32 s74, s73\n\t"
    "v_lshlrev_b32 v248, 4, v1\n\t"
    "s_mov_b64 s[32:33], s[10:11]\n\t"
    "s_mov_b32 s34, 8388608\n\t"
    "s_mov_b32 s35, 131072\n\t"
    "s_mov_b32 s62, 262144\n\t"
    "s_mov_b64 s[36:37], s[10:11]\n\t"
    "s_mov_b32 s38, 8388608\n\t"
    "s_mov_b32 s39, 131072\n\t"
    "s_mov_b32 s63, 262144\n\t"
    "s_mov_b64 s[40:41], s[14:15]\n\t"
    "s_mov_b32 s42, 7340032\n\t"
    "s_mov_b32 s43, 131072\n\t"
    "s_mov_b32 s64, 229376\n\t"
    "s_mov_b64 s[44:45], s[14:15]\n\t"
    "s_mov_b32 s46, 7340032\n\t"
    "s_mov_b32 s47, 131072\n\t"
    "s_mov_b32 s65, 229376\n\t"
    "s_mov_b64 s[48:49], s[16:17]\n\t"
    "s_mov_b32 s50, 7340032\n\t"
    "s_mov_b32 s51, 131072\n\t"
    "s_mov_b32 s66, 229376\n\t"
    "s_mov_b64 s[52:53], s[16:17]\n\t"
    "s_mov_b32 s54, 7340032\n\t"
    "s_mov_b32 s55, 131072\n\t"
    "s_mov_b32 s67, 229376\n\t"
    "s_lshl_b32 s77, s3, 16\n\t"
    "s_lshl_b32 s78, s60, 8\n\t"
    "s_add_u32 s77, s77, s78\n\t"
    "v_lshrrev_b32 v8, 4, v1\n\t"
    "v_bfe_u32 v9, v1, 1, 3\n\t"
    "v_xor_b32 v9, v8, v9\n\t"
    "v_and_b32 v5, 16, v248\n\t"
    "v_lshl_add_u32 v5, v9, 5, v5\n\t"
    "v_lshl_add_u32 v5, v8, 13, v5\n\t"
    "v_add_u32 v242, s77, v5\n\t"
    "v_xor_b32 v5, 128, v242\n\t"
    "v_add_u32 v243, 32768, v5\n\t"
    "s_and_b32 s25, s60, 1\n\t"
    "s_lshl_b32 s25, s25, 1\n\t"
    "s_mul_i32 s27, s60, 224\n\t"
    "s_lshl_b32 s77, s25, 4\n\t"
    "s_add_u32 s27, s27, s77\n\t"
    "s_sub_u32 s25, 14, s25\n\t"
    "s_lshl_b32 s77, s3, 7\n\t"
    "s_mov_b32 s78, 6944\n\t"
    "v_mov_b32 v9, 4294967072\n\t"
    "v_add_u32 v5, s77, v1\n\t"
    "v_mul_u32_u24 v6, 4682, v5\n\t"
    "v_lshrrev_b32 v6, 16, v6\n\t"
    "v_mul_u32_u24 v7, 14, v6\n\t"
    "v_sub_u32 v7, v5, v7\n\t"
    "v_cmp_le_u32 vcc, s25, v7\n\t"
    "v_lshl_add_u32 v8, v5, 4, s27\n\t"
    "v_mad_u32_u24 v8, v6, s78, v8\n\t"
    "v_cndmask_b32 v7, 0, v9, vcc\n\t"
    "v_add_u32 v244, v8, v7\n\t"
    "v_add_u32 v246, v8, v7\n\t"
    "v_add_u32 v5, 64, v5\n\t"
    "v_mul_u32_u24 v6, 4682, v5\n\t"
    "v_lshrrev_b32 v6, 16, v6\n\t"
    "v_mul_u32_u24 v7, 14, v6\n\t"
    "v_sub_u32 v7, v5, v7\n\t"
    "v_cmp_le_u32 vcc, s25, v7\n\t"
    "v_lshl_add_u32 v8, v5, 4, s27\n\t"
    "v_mad_u32_u24 v8, v6, s78, v8\n\t"
    "v_cndmask_b32 v7, 0, v9, vcc\n\t"
    "v_add_u32 v245, v8, v7\n\t"
    "v_add_u32 v247, v8, v7\n\t"
    "s_cmp_eq_u32 s3, 3\n\t"
    "s_cselect_b32 s46, 0, s46\n\t"
    "s_cselect_b32 s65, 0, s65\n\t"
    "s_cselect_b32 s54, 0, s54\n\t"
    "s_cselect_b32 s67, 0, s67\n\t"
    "s_and_b32 s57, s58, 15\n\t"
    "s_lshr_b64 s[58:59], s[58:59], 4\n\t"
    "s_lshl_b32 s72, s57, 13\n\t"
    "s_lshl_b32 s57, s57, 1\n\t"
    "s_add_u32 m0, s74, 0\n\t"
    "s_mul_i32 s56, s62, s57\n\t"
    "buffer_load_dwordx4 v242, s[32:35], s56 offen sc1 lds\n\t"
    "s_add_u32 m0, s74, 1024\n\t"
    "s_mul_i32 s56, s63, s57\n\t"
    "buffer_load_dwordx4 v243, s[36:39], s56 offen sc1 lds\n\t"
    "s_add_u32 m0, s74, 8192\n\t"
    "s_mul_i32 s56, s64, s57\n\t"
    "buffer_load_dwordx4 v244, s[40:43], s56 offen sc1 lds\n\t"
    "s_load_dwordx2 s[8:9], s[0:1], 0x48\n\t"
    "s_lshr_b32 s77, s2, 3\n\t"
    "s_add_u32 s77, s77, s61\n\t"
    "s_and_b32 s77, s77, 3\n\t"
    "s_lshl_b32 s77, s77, 4\n\t"
    "s_add_u32 s77, s77, 144\n\t"
    "s_add_u32 s78, s77, 8\n\t"
    "s_load_dwordx2 s[14:15], s[0:1], s77\n\t"
    "s_load_dwordx2 s[16:17], s[0:1], s78\n\t"
    "v_bfe_u32 v4, v0, 1, 3\n\t"
    "s_lshr_b32 s77, s31, 3\n\t"
    "s_mul_i32 s77, s77, 135424\n\t"
    "s_bfe_u32 s78, s31, 131073\n\t"
    "s_lshl_b32 s78, s78, 10\n\t"
    "s_add_u32 s77, s77, s78\n\t"
    "s_and_b32 s78, s31, 1\n\t"
    "s_lshl_b32 s78, s78, 7\n\t"
    "s_add_u32 s77, s77, s78\n\t"
    "v_lshl_add_u32 v217, v4, 4, s77\n\t"
    "v_lshrrev_b32 v2, 4, v0\n\t"
    "v_lshl_add_u32 v217, v2, 12, v217\n\t"
    "v_and_b32 v2, 1, v0\n\t"
    "v_lshl_add_u32 v217, v2, 9, v217\n\t"
    "s_lshl_b32 s77, s31, 12\n\t"
    "s_add_u32 s77, s77, 5242880\n\t"
    "v_lshl_add_u32 v218, v0, 4, s77\n\t"
    "s_lshl_b32 s77, s3, 2\n\t"
    "s_add_u32 s77, s77, 147456\n\t"
    "v_lshl_add_u32 v225, v4, 5, s77\n\t"
    "s_bitcmp1_b32 s31, 1\n\t"
    "s_cselect_b64 vcc, -1, 0\n\t"
    "s_cselect_b32 s77, 5, 6\n\t"
    "s_lshl_b32 s77, s3, s77\n\t"
    "s_add_u32 s77, s77, 148480\n\t"
    "v_and_b32 v2, 15, v1\n\t"
    "v_lshrrev_b32 v3, 4, v1\n\t"
    "v_lshlrev_b32 v3, 3, v3\n\t"
    "v_lshl_add_u32 v216, v2, 8, v3\n\t"
    "v_lshrrev_b32 v2, 2, v1\n\t"
    "v_and_b32 v3, 3, v1\n\t"
    "v_lshlrev_b32 v3, 3, v3\n\t"
    "v_lshl_add_u32 v4, v2, 7, v3\n\t"
    "v_cndmask_b32 v216, v216, v4, vcc\n\t"
    "v_add_u32 v216, s77, v216\n\t"
    "s_mov_b32 s24, 0xffff0000\n\t"
    "s_waitcnt vmcnt(6)\n\t"
    ".p2align 3\n\t"
    "v_lshlrev_b32 v112, 16, v200\n\t"
    "v_and_b32 v113, s24, v200\n\t"
    "v_lshlrev_b32 v114, 16, v201\n\t"
    "v_and_b32 v115, s24, v201\n\t"
    "v_lshlrev_b32 v116, 16, v202\n\t"
    "v_and_b32 v117, s24, v202\n\t"
    "v_lshlrev_b32 v118, 16, v203\n\t"
    "v_and_b32 v119, s24, v203\n\t"
    "v_lshlrev_b32 v120, 16, v204\n\t"
    "v_and_b32 v121, s24, v204\n\t"
    "v_lshlrev_b32 v122, 16, v205\n\t"
    "v_and_b32 v123, s24, v205\n\t"
    "v_lshlrev_b32 v124, 16, v206\n\t"
    "v_and_b32 v125, s24, v206\n\t"
    "v_lshlrev_b32 v126, 16, v207\n\t"
    "v_and_b32 v127, s24, v207\n\t"
    "v_lshlrev_b32 v128, 16, v208\n\t"
    "v_and_b32 v129, s24, v208\n\t"
    "v_lshlrev_b32 v130, 16, v209\n\t"
    "v_and_b32 v131, s24, v209\n\t"
    "v_lshlrev_b32 v132, 16, v210\n\t"
    "v_and_b32 v133, s24, v210\n\t"
    "v_lshlrev_b32 v134, 16, v211\n\t"
    "v_and_b32 v135, s24, v211\n\t"
    "v_lshlrev_b32 v136, 16, v212\n\t"
    "v_and_b32 v137, s24, v212\n\t"
    "v_lshlrev_b32 v138, 16, v213\n\t"
    "v_and_b32 v139, s24, v213\n\t"
    "v_lshlrev_b32 v140, 16, v214\n\t"
    "v_and_b32 v141, s24, v214\n\t"
    "v_lshlrev_b32 v142, 16, v215\n\t"
    "v_and_b32 v143, s24, v215\n\t"
    "v_pk_add_f32 v[144:145], v[112:113], v[114:115]\n\t"
    "v_pk_add_f32 v[146:147], v[116:117], v[118:119]\n\t"
    "v_pk_add_f32 v[148:149], v[120:121], v[122:123]\n\t"
    "v_pk_add_f32 v[150:151], v[124:125], v[126:127]\n\t"
    "v_pk_add_f32 v[152:153], v[128:129], v[130:131]\n\t"
    "v_pk_add_f32 v[154:155], v[132:133], v[134:135]\n\t"
    "v_pk_add_f32 v[156:157], v[136:137], v[138:139]\n\t"
    "v_pk_add_f32 v[158:159], v[140:141], v[142:143]\n\t"
    "v_pk_add_f32 v[144:145], v[144:145], v[152:153]\n\t"
    "v_pk_add_f32 v[146:147], v[146:147], v[154:155]\n\t"
    "v_pk_add_f32 v[148:149], v[148:149], v[156:157]\n\t"
    "v_pk_add_f32 v[150:151], v[150:151], v[158:159]\n\t"
    "v_pk_add_f32 v[144:145], v[144:145], v[148:149]\n\t"
    "v_pk_add_f32 v[146:147], v[146:147], v[150:151]\n\t"
    "v_pk_add_f32 v[144:145], v[144:145], v[146:147]\n\t"
    "v_add_f32 v180, v144, v145\n\t"
    "s_nop 1\n\t"
    "v_add_f32_dpp v180, v180, v180 quad_perm:[1,0,3,2] row_mask:0xf bank_mask:0xf\n\t"
    "v_mov_b32 v182, v180\n\t"
    "s_nop 1\n\t"
    "v_permlane16_swap_b32 v180, v182\n\t"
    "v_add_f32 v180, v180, v182\n\t"
    "v_mov_b32 v182, v180\n\t"
    "s_nop 1\n\t"
    "v_permlane32_swap_b32 v180, v182\n\t"
    "v_add_f32 v180, v180, v182\n\t"
    "v_mul_f32 v184, 0xbb800000, v180\n\t"
    "v_mul_f32 v185, 0xbb800000, v180\n\t"
    "ds_write_b32 v225, v180 offset:0\n\t"
    "v_pk_add_f32 v[112:113], v[112:113], v[184:185]\n\t"
    "v_pk_add_f32 v[114:115], v[114:115], v[184:185]\n\t"
    "v_pk_add_f32 v[116:117], v[116:117], v[184:185]\n\t"
    "v_pk_add_f32 v[118:119], v[118:119], v[184:185]\n\t"
    "v_pk_add_f32 v[120:121], v[120:121], v[184:185]\n\t"
    "v_pk_add_f32 v[122:123], v[122:123], v[184:185]\n\t"
    "v_pk_add_f32 v[124:125], v[124:125], v[184:185]\n\t"
    "v_pk_add_f32 v[126:127], v[126:127], v[184:185]\n\t"
    "v_pk_add_f32 v[128:129], v[128:129], v[184:185]\n\t"
    "v_pk_add_f32 v[130:131], v[130:131], v[184:185]\n\t"
    "v_pk_add_f32 v[132:133], v[132:133], v[184:185]\n\t"
    "v_pk_add_f32 v[134:135], v[134:135], v[184:185]\n\t"
    "v_pk_add_f32 v[136:137], v[136:137], v[184:185]\n\t"
    "v_pk_add_f32 v[138:139], v[138:139], v[184:185]\n\t"
    "v_pk_add_f32 v[140:141], v[140:141], v[184:185]\n\t"
    "v_pk_add_f32 v[142:143], v[142:143], v[184:185]\n\t"
    "v_pk_mul_f32 v[144:145], v[112:113], v[112:113]\n\t"
    "v_pk_mul_f32 v[146:147], v[114:115], v[114:115]\n\t"
    "v_pk_mul_f32 v[148:149], v[116:117], v[116:117]\n\t"
    "v_pk_mul_f32 v[150:151], v[118:119], v[118:119]\n\t"
    "v_pk_fma_f32 v[144:145], v[120:121], v[120:121], v[144:145]\n\t"
    "v_pk_fma_f32 v[146:147], v[122:123], v[122:123], v[146:147]\n\t"
    "v_pk_fma_f32 v[148:149], v[124:125], v[124:125], v[148:149]\n\t"
    "v_pk_fma_f32 v[150:151], v[126:127], v[126:127], v[150:151]\n\t"
    "v_pk_fma_f32 v[144:145], v[128:129], v[128:129], v[144:145]\n\t"
    "v_pk_fma_f32 v[146:147], v[130:131], v[130:131], v[146:147]\n\t"
    "v_pk_fma_f32 v[148:149], v[132:133], v[132:133], v[148:149]\n\t"
    "v_pk_fma_f32 v[150:151], v[134:135], v[134:135], v[150:151]\n\t"
    "v_pk_fma_f32 v[144:145], v[136:137], v[136:137], v[144:145]\n\t"
    "v_pk_fma_f32 v[146:147], v[138:139], v[138:139], v[146:147]\n\t"
    "v_pk_fma_f32 v[148:149], v[140:141], v[140:141], v[148:149]\n\t"
    "v_pk_fma_f32 v[150:151], v[142:143], v[142:143], v[150:151]\n\t"
    "v_pk_add_f32 v[144:145], v[144:145], v[148:149]\n\t"
    "v_pk_add_f32 v[146:147], v[146:147], v[150:151]\n\t"
    "v_pk_add_f32 v[144:145], v[144:145], v[146:147]\n\t"
    "v_add_f32 v181, v144, v145\n\t"
    "s_nop 1\n\t"
    "v_add_f32_dpp v181, v181, v181 quad_perm:[1,0,3,2] row_mask:0xf bank_mask:0xf\n\t"
    "v_mov_b32 v182, v181\n\t"
    "s_nop 1\n\t"
    "v_permlane16_swap_b32 v181, v182\n\t"
    "v_add_f32 v181, v181, v182\n\t"
    "v_mov_b32 v182, v181\n\t"
    "s_nop 1\n\t"
    "v_permlane32_swap_b32 v181, v182\n\t"
    "v_add_f32 v181, v181, v182\n\t"
    "ds_write_b32 v225, v181 offset:16\n\t"
    "v_and_b32 v225, 0xffffffe0, v225\n\t"
    "s_waitcnt lgkmcnt(0)\n\t"
    "s_barrier\n\t"
    "ds_read_b128 v[188:191], v225 offset:0\n\t"
    "ds_read_b128 v[192:195], v225 offset:16\n\t"
    "s_waitcnt lgkmcnt(0)\n\t"
    "v_add_f32 v144, v188, v189\n\t"
    "v_add_f32 v145, v190, v191\n\t"
    "v_add_f32 v182, v144, v145\n\t"
    "v_mul_f32 v182, 0x3a800000, v182\n\t"
    "v_add_f32 v144, v192, v193\n\t"
    "v_add_f32 v145, v194, v195\n\t"
    "v_add_f32_e64 v183, v144, v145\n\t"
    "v_mul_f32 v183, 0x3a800000, v183\n\t"
    "v_mul_f32_e64 v147, -0.5, v182\n\t"
    "v_fmamk_f32 v146, v188, 0x3b000000, v147\n\t"
    "v_fmac_f32 v183, v146, v146\n\t"
    "s_nop 0\n\t"
    "v_fmamk_f32 v146, v189, 0x3b000000, v147\n\t"
    "v_fmac_f32 v183, v146, v146\n\t"
    "s_nop 0\n\t"
    "v_fmamk_f32 v146, v190, 0x3b000000, v147\n\t"
    "v_fmac_f32 v183, v146, v146\n\t"
    "s_nop 0\n\t"
    "v_fmamk_f32 v146, v191, 0x3b000000, v147\n\t"
    "v_fmac_f32 v183, v146, v146\n\t"
    "s_nop 0\n\t"
    "v_cvt_pk_bf16_f32 v182, v182, v182\n\t"
    "v_and_b32_e64 v182, s24, v182\n\t"
    "v_cvt_pk_bf16_f32 v183, v183, v183\n\t"
    "v_and_b32_e64 v183, s24, v183\n\t"
    "v_add_f32 v183, 0x3727c5ac, v183\n\t"
    "v_cvt_pk_bf16_f32 v183, v183, v183\n\t"
    "v_and_b32 v183, s24, v183\n\t"
    "v_sqrt_f32 v183, v183\n\t"
    "s_nop 0\n\t"
    "s_nop 0\n\t"
    "v_cvt_pk_bf16_f32 v183, v183, v183\n\t"
    "v_and_b32 v183, s24, v183\n\t"
    "v_rcp_f32 v196, v183\n\t"
    "v_add_f32 v144, v184, v182\n\t"
    "v_mov_b32 v197, v196\n\t"
    "v_mul_f32 v198, -v144, v196\n\t"
    "v_mov_b32_e64 v199, v198\n\t"
    "v_pk_fma_f32 v[112:113], v[112:113], v[196:197], v[198:199]\n\t"
    "v_pk_fma_f32 v[114:115], v[114:115], v[196:197], v[198:199]\n\t"
    "v_pk_fma_f32 v[120:121], v[120:121], v[196:197], v[198:199]\n\t"
    "v_pk_fma_f32 v[122:123], v[122:123], v[196:197], v[198:199]\n\t"
    "v_cvt_pk_bf16_f32 v160, v112, v113\n\t"
    "v_cvt_pk_bf16_f32 v161, v114, v115\n\t"
    "v_cvt_pk_bf16_f32 v162, v120, v121\n\t"
    "v_cvt_pk_bf16_f32 v163, v122, v123\n\t"
    "v_pk_fma_f32 v[116:117], v[116:117], v[196:197], v[198:199]\n\t"
    "v_pk_fma_f32 v[118:119], v[118:119], v[196:197], v[198:199]\n\t"
    "v_pk_fma_f32 v[124:125], v[124:125], v[196:197], v[198:199]\n\t"
    "v_pk_fma_f32 v[126:127], v[126:127], v[196:197], v[198:199]\n\t"
    "v_cvt_pk_bf16_f32 v164, v116, v117\n\t"
    "v_cvt_pk_bf16_f32 v165, v118, v119\n\t"
    "v_cvt_pk_bf16_f32 v166, v124, v125\n\t"
    "v_cvt_pk_bf16_f32 v167, v126, v127\n\t"
    "v_pk_fma_f32 v[128:129], v[128:129], v[196:197], v[198:199]\n\t"
    "v_pk_fma_f32 v[130:131], v[130:131], v[196:197], v[198:199]\n\t"
    "v_pk_fma_f32 v[136:137], v[136:137], v[196:197], v[198:199]\n\t"
    "v_pk_fma_f32 v[138:139], v[138:139], v[196:197], v[198:199]\n\t"
    "v_cvt_pk_bf16_f32 v168, v128, v129\n\t"
    "v_cvt_pk_bf16_f32 v169, v130, v131\n\t"
    "v_cvt_pk_bf16_f32 v170, v136, v137\n\t"
    "v_cvt_pk_bf16_f32 v171, v138, v139\n\t"
    "v_pk_fma_f32 v[132:133], v[132:133], v[196:197], v[198:199]\n\t"
    "v_pk_fma_f32 v[134:135], v[134:135], v[196:197], v[198:199]\n\t"
    "v_pk_fma_f32 v[140:141], v[140:141], v[196:197], v[198:199]\n\t"
    "v_pk_fma_f32 v[142:143], v[142:143], v[196:197], v[198:199]\n\t"
    "v_cvt_pk_bf16_f32 v172, v132, v133\n\t"
    "v_cvt_pk_bf16_f32 v173, v134, v135\n\t"
    "v_cvt_pk_bf16_f32 v174, v140, v141\n\t"
    "v_cvt_pk_bf16_f32 v175, v142, v143\n\t"
    "s_mov_b32 s27, 65536\n\t"
    "s_cmp_lg_u32 s3, 0\n\t"
    "s_cbranch_scc1 28f\n\t"
    "buffer_store_dwordx4 v[160:163], v217, s[88:91], 0 offen sc1\n\t"
    "buffer_store_dwordx4 v[164:167], v217, s[88:91], 0 offen offset:256 sc1\n\t"
    "buffer_store_dwordx4 v[168:171], v217, s[88:91], s27 offen sc1\n\t"
    "buffer_store_dwordx4 v[172:175], v217, s[88:91], s27 offen offset:256 sc1\n\t"
    "28:\n\t"
    "v_mov_b32 v220, 0\n\t"
    "v_mov_b32 v221, 0\n\t"
    "v_mov_b32 v222, 0\n\t"
    "v_mov_b32 v223, 0\n\t"
    "s_lshr_b32 s77, s31, 3\n\t"
    "s_lshl_b32 s77, s77, 7\n\t"
    "s_and_b32 s78, s31, 7\n\t"
    "s_lshl_b32 s78, s78, 2\n\t"
    "v_mfma_f32_32x32x16_bf16 a[0:15], v[220:223], v[220:223], 0\n\t"
    "s_add_u32 s77, s77, s78\n\t"
    "s_add_u32 s21, s77, 50397184\n\t"
    "s_lshl_b32 s24, s61, 2\n\t"
    "s_add_u32 s24, s24, s3\n\t"
    "v_mfma_f32_32x32x16_bf16 a[16:31], v[220:223], v[220:223], 0\n\t"
    "s_mul_i32 s77, s24, 135424\n\t"
    "s_add_u32 s68, s88, s77\n\t"
    "s_addc_u32 s69, s89, 0\n\t"
    "s_mov_b32 s70, 131072\n\t"
    "v_mfma_f32_32x32x16_bf16 a[32:47], v[220:223], v[220:223], 0\n\t"
    "s_mov_b32 s71, 131072\n\t"
    "s_mov_b32 s75, 0\n\t"
    "s_mov_b32 s76, 8\n\t"
    "s_mov_b32 s87, 0\n\t"
    "v_mfma_f32_32x32x16_bf16 a[48:63], v[220:223], v[220:223], 0\n\t"
    "v_and_b32 v2, 7, v1\n\t"
    "s_and_b32 s78, s60, 7\n\t"
    "s_lshl_b32 s78, s78, 12\n\t"
    "s_add_u32 s78, s78, 50397184\n\t"
    "v_mfma_f32_32x32x16_bf16 a[64:79], v[220:223], v[220:223], 0\n\t"
    "s_lshl_b32 s77, s24, 7\n\t"
    "s_add_u32 s77, s77, s78\n\t"
    "v_lshl_add_u32 v219, v2, 2, s77\n\t"
    "s_lshr_b32 s77, s60, 3\n\t"
    "v_mfma_f32_32x32x16_bf16 a[80:95], v[220:223], v[220:223], 0\n\t"
    "s_lshl_b32 s77, s77, 10\n\t"
    "s_add_u32 s77, s77, s78\n\t"
    "v_lshl_add_u32 v231, v2, 2, s77\n\t"
    "v_lshrrev_b32 v2, 3, v1\n\t"
    "v_mfma_f32_32x32x16_bf16 a[96:111], v[220:223], v[220:223], 0\n\t"
    "v_lshl_add_u32 v231, v2, 7, v231\n\t"
    "v_add_u32 v249, 4096, v248\n\t"
    "v_mov_b32 v252, 0x3e0293ee\n\t"
    "v_mov_b32 v253, 0x3e0293ee\n\t"
    "v_mfma_f32_32x32x16_bf16 a[112:127], v[220:223], v[220:223], 0\n\t"
    "v_lshrrev_b32 v2, 2, v1\n\t"
    "v_and_b32 v3, 3, v1\n\t"
    "v_lshlrev_b32 v3, 3, v3\n\t"
    "s_mov_b32 s77, 224\n\t"
    "v_mfma_f32_32x32x16_bf16 a[128:143], v[220:223], v[220:223], 0\n\t"
    "v_mad_u32_u24 v241, v2, s77, v3\n\t"
    "v_lshrrev_b32 v254, 6, v248\n\t"
    "v_bfe_u32 v255, v248, 4, 2\n\t"
    "v_lshlrev_b32 v255, 3, v255\n\t"
    "v_mfma_f32_32x32x16_bf16 a[144:159], v[220:223], v[220:223], 0\n\t"
    "v_lshl_add_u32 v255, v254, 8, v255\n\t"
    "v_and_b32 v254, 7, v254\n\t"
    "v_lshl_add_u32 v232, v254, 5, v255\n\t"
    "v_xor_b32 v233, 32, v232\n\t"
    "v_mfma_f32_32x32x16_bf16 a[160:175], v[220:223], v[220:223], 0\n\t"
    "v_xor_b32 v234, 64, v232\n\t"
    "v_xor_b32 v235, 96, v232\n\t"
    "v_xor_b32 v236, 128, v232\n\t"
    "v_xor_b32 v237, 160, v232\n\t"
    "v_mfma_f32_32x32x16_bf16 a[176:191], v[220:223], v[220:223], 0\n\t"
    "v_xor_b32 v238, 192, v232\n\t"
    "v_xor_b32 v239, 224, v232\n\t"
    "s_lshr_b32 s77, s60, 3\n\t"
    "s_lshl_b32 s77, s77, 18\n\t"
    "v_mfma_f32_32x32x16_bf16 a[192:207], v[220:223], v[220:223], 0\n\t"
    "s_add_u32 s77, s77, 5242880\n\t"
    "s_add_u32 s80, s88, s77\n\t"
    "s_addc_u32 s81, s89, 0\n\t"
    "s_mov_b32 s82, 262144\n\t"
    "v_mfma_f32_32x32x16_bf16 a[208:223], v[220:223], v[220:223], 0\n\t"
    "s_mov_b32 s83, 131072\n\t"
    "s_lshl_b32 s85, s3, 12\n\t"
    "s_add_u32 s85, s85, 16384\n\t"
    "s_mul_i32 s77, s24, 528640\n\t"
    "v_mfma_f32_32x32x16_bf16 a[224:239], v[220:223], v[220:223], 0\n\t"
    "s_lshl_b32 s78, s60, 14\n\t"
    "s_add_u32 s77, s77, s78\n\t"
    "s_add_u32 s92, s77, 8388608\n\t"
    "s_add_u32 s93, s77, 8392704\n\t"
    "v_mfma_f32_32x32x16_bf16 v[16:31], v[220:223], v[220:223], 0\n\t"
    "s_add_u32 s94, s77, 8396800\n\t"
    "s_add_u32 s95, s77, 8400896\n\t"
    "s_mul_i32 s77, s60, 7\n\t"
    "s_add_u32 s77, s77, 1\n\t"
    "v_mfma_f32_32x32x16_bf16 v[32:47], v[220:223], v[220:223], 0\n\t"
    "s_lshr_b32 s77, s77, 1\n\t"
    "s_lshl_b32 s77, s77, 12\n\t"
    "s_mul_i32 s78, s24, 463104\n\t"
    "s_add_u32 s77, s77, s78\n\t"
    "v_mfma_f32_32x32x16_bf16 v[48:63], v[220:223], v[220:223], 0\n\t"
    "s_add_u32 s96, s77, 26214400\n\t"
    "s_add_u32 s97, s77, 26218496\n\t"
    "s_add_u32 s98, s77, 26222592\n\t"
    "s_add_u32 s99, s77, 26226688\n\t"
    "v_mfma_f32_32x32x16_bf16 v[64:79], v[220:223], v[220:223], 0\n\t"
    "s_add_u32 s77, s77, 26210312\n\t"
    "s_bitcmp1_b32 s60, 0\n\t"
    "s_cselect_b32 s99, s77, s99\n\t"
    "s_lshl_b32 s77, s61, 5\n\t"
    "v_mfma_f32_32x32x16_bf16 v[80:95], v[220:223], v[220:223], 0\n\t"
    "s_add_u32 s77, s77, s60\n\t"
    "s_lshl_b32 s77, s77, 2\n\t"
    "s_add_u32 s22, s77, 50593792\n\t"
    "v_mfma_f32_32x32x16_bf16 v[96:111], v[220:223], v[220:223], 0\n\t"
    "s_waitcnt vmcnt(0)\n\t"
    "v_readfirstlane_b32 s20, v224\n\t"
    "s_add_u32 s26, s21, 65536\n\t"
    "s_mov_b32 s27, 65536\n\t"
    "s_nop 0\n\t"
    "s_add_u32 s20, s20, 1\n\t"
    "s_cmp_lg_u32 s3, 0\n\t"
    "s_cbranch_scc1 10f\n\t"
    "v_lshrrev_b32 v254, 4, v248\n\t"
    "v_min_u32 v254, 7, v254\n\t"
    "v_lshlrev_b32 v254, 12, v254\n\t"
    "v_mov_b32 v255, s20\n\t"
    "buffer_store_dword v255, v254, s[88:91], s26 offen sc1\n\t"
    "10:\n\t"
    "v_mfma_f32_32x32x16_bf16 v[0:15], v[220:223], v[220:223], 0\n\t"
    "s_add_u32 m0, s74, 9216\n\t"
    "s_mul_i32 s56, s65, s57\n\t"
    "buffer_load_dwordx4 v245, s[44:47], s56 offen sc1 lds\n\t"
    "s_add_u32 m0, s74, 16384\n\t"
    "s_mul_i32 s56, s66, s57\n\t"
    "buffer_load_dwordx4 v246, s[48:51], s56 offen sc1 lds\n\t"
    "s_add_u32 m0, s74, 17408\n\t"
    "s_mul_i32 s56, s67, s57\n\t"
    "buffer_load_dwordx4 v247, s[52:55], s56 offen sc1 lds\n\t"
    "s_add_u32 s57, s57, 1\n\t"
    "s_add_u32 m0, s74, 24576\n\t"
    "s_mul_i32 s56, s62, s57\n\t"
    "buffer_load_dwordx4 v242, s[32:35], s56 offen sc1 lds\n\t"
    "s_add_u32 m0, s74, 25600\n\t"
    "s_mul_i32 s56, s63, s57\n\t"
    "buffer_load_dwordx4 v243, s[36:39], s56 offen sc1 lds\n\t"
    "s_add_u32 m0, s74, 32768\n\t"
    "s_mul_i32 s56, s64, s57\n\t"
    "buffer_load_dwordx4 v244, s[40:43], s56 offen sc1 lds\n\t"
    "s_add_u32 m0, s74, 33792\n\t"
    "s_mul_i32 s56, s65, s57\n\t"
    "buffer_load_dwordx4 v245, s[44:47], s56 offen sc1 lds\n\t"
    "s_add_u32 m0, s74, 40960\n\t"
    "s_mul_i32 s56, s66, s57\n\t"
    "buffer_load_dwordx4 v246, s[48:51], s56 offen sc1 lds\n\t"
    "s_add_u32 m0, s74, 41984\n\t"
    "s_mul_i32 s56, s67, s57\n\t"
    "buffer_load_dwordx4 v247, s[52:55], s56 offen sc1 lds\n\t"
    "s_mov_b32 s25, s72\n\t"
    "s_and_b32 s57, s58, 15\n\t"
    "s_lshr_b64 s[58:59], s[58:59], 4\n\t"
    "s_lshl_b32 s72, s57, 13\n\t"
    "s_lshl_b32 s57, s57, 1\n\t"
    "s_add_u32 s74, s73, 98304\n\t"
    "s_mov_b32 s24, 65536\n\t"
    "s_add_u32 m0, s73, 49152\n\t"
    "s_mul_i32 s56, s62, s57\n\t"
    "buffer_load_dwordx4 v242, s[32:35], s56 offen sc1 lds\n\t"
    "s_add_u32 m0, s73, 50176\n\t"
    "s_mul_i32 s56, s63, s57\n\t"
    "buffer_load_dwordx4 v243, s[36:39], s56 offen sc1 lds\n\t"
    "s_add_u32 m0, s73, 57344\n\t"
    "s_mul_i32 s56, s64, s57\n\t"
    "buffer_load_dwordx4 v244, s[40:43], s56 offen sc1 lds\n\t"
    "s_add_u32 m0, s73, 58368\n\t"
    "s_mul_i32 s56, s65, s57\n\t"
    "buffer_load_dwordx4 v245, s[44:47], s56 offen sc1 lds\n\t"
    "s_add_u32 m0, s73, 65536\n\t"
    "s_mul_i32 s56, s66, s57\n\t"
    "buffer_load_dwordx4 v246, s[48:51], s56 offen sc1 lds\n\t"
    "s_add_u32 m0, s73, 66560\n\t"
    "s_mul_i32 s56, s67, s57\n\t"
    "buffer_load_dwordx4 v247, s[52:55], s56 offen sc1 lds\n\t"
    "s_mov_b32 s77, 0\n\t"
    "11:\n\t"
    "buffer_load_dword v180, v219, s[88:91], s24 offen sc1\n\t"
    "s_waitcnt vmcnt(0)\n\t"
    "v_cmp_ne_u32 vcc, s20, v180\n\t"
    "s_add_u32 s77, s77, 1\n\t"
    "s_nop 2\n\t"
    "s_cbranch_vccz 12f\n\t"
    "s_sleep 1\n\t"
    "s_cmp_lt_u32 s77, 131072\n\t"
    "s_cbranch_scc1 11b\n\t"
    "12:\n\t"
    "buffer_load_dwordx4 v[112:115], v248, s[68:71], s25 offen sc1\n\t"
    "buffer_load_dwordx4 v[128:131], v249, s[68:71], s25 offen sc1\n\t"
    "buffer_load_dwordx4 v[116:119], v248, s[68:71], s25 offen offset:1024 sc1\n\t"
    "buffer_load_dwordx4 v[132:135], v249, s[68:71], s25 offen offset:1024 sc1\n\t"
    "buffer_load_dwordx4 v[120:123], v248, s[68:71], s25 offen offset:2048 sc1\n\t"
    "buffer_load_dwordx4 v[136:139], v249, s[68:71], s25 offen offset:2048 sc1\n\t"
    "buffer_load_dwordx4 v[124:127], v248, s[68:71], s25 offen offset:3072 sc1\n\t"
    "buffer_load_dwordx4 v[140:143], v249, s[68:71], s25 offen offset:3072 sc1\n\t"
    "s_mov_b32 s27, 65536\n\t"
    "s_cmp_eq_u32 s3, 0\n\t"
    "s_cbranch_scc1 29f\n\t"
    "buffer_store_dwordx4 v[160:163], v217, s[88:91], 0 offen sc1\n\t"
    "buffer_store_dwordx4 v[164:167], v217, s[88:91], 0 offen offset:256 sc1\n\t"
    "buffer_store_dwordx4 v[168:171], v217, s[88:91], s27 offen sc1\n\t"
    "buffer_store_dwordx4 v[172:175], v217, s[88:91], s27 offen offset:256 sc1\n\t"
    "29:\n\t"
    "s_barrier\n\t"
    "s_bitcmp1_b32 s31, 1\n\t"
    "s_cbranch_scc1 20f\n\t"
    "ds_read_b64 v[212:213], v216 offset:0\n\t"
    "ds_read_b64 v[214:215], v216 offset:32\n\t"
    "s_waitcnt lgkmcnt(0)\n\t"
    "v_lshlrev_b32 v200, 16, v212\n\t"
    "v_and_b32 v201, 0xffff0000, v212\n\t"
    "v_lshlrev_b32 v202, 16, v213\n\t"
    "v_and_b32 v203, 0xffff0000, v213\n\t"
    "v_lshlrev_b32 v204, 16, v214\n\t"
    "v_and_b32 v205, 0xffff0000, v214\n\t"
    "v_lshlrev_b32 v206, 16, v215\n\t"
    "v_and_b32 v207, 0xffff0000, v215\n\t"
    "v_pk_mul_f32 v[200:201], v[200:201], v[252:253]\n\t"
    "v_pk_mul_f32 v[202:203], v[202:203], v[252:253]\n\t"
    "v_pk_mul_f32 v[204:205], v[204:205], v[252:253]\n\t"
    "v_pk_mul_f32 v[206:207], v[206:207], v[252:253]\n\t"
    "v_cvt_pk_bf16_f32 v212, v200, v201\n\t"
    "v_cvt_pk_bf16_f32 v213, v202, v203\n\t"
    "v_cvt_pk_bf16_f32 v214, v204, v205\n\t"
    "v_cvt_pk_bf16_f32 v215, v206, v207\n\t"
    "s_branch 21f\n\t"
    "20:\n\t"
    "ds_read_b64_tr_b16 v[212:213], v216 offset:0\n\t"
    "ds_read_b64_tr_b16 v[214:215], v216 offset:2048\n\t"
    "21:\n\t"
    "ds_read_b64_tr_b16 v[176:177], v232 offset:0\n\t"
    "ds_read_b64_tr_b16 v[178:179], v232 offset:4096\n\t"
    "ds_read_b64_tr_b16 v[180:181], v232 offset:24576\n\t"
    "ds_read_b64_tr_b16 v[182:183], v232 offset:28672\n\t"
    "ds_read_b64_tr_b16 v[184:185], v233 offset:0\n\t"
    "ds_read_b64_tr_b16 v[186:187], v233 offset:4096\n\t"
    "ds_read_b64_tr_b16 v[188:189], v233 offset:24576\n\t"
    "ds_read_b64_tr_b16 v[190:191], v233 offset:28672\n\t"
    "s_mov_b64 vcc, -1\n\t"
    "s_waitcnt lgkmcnt(8)\n\t"
    "buffer_store_dwordx4 v[212:215], v218, s[88:91], 0 offen sc1\n\t"
    "s_bitcmp1_b32 s2, 0\n\t"
    "s_cselect_b64 s[24:25], s[18:19], s[12:13]\n\t"
    "s_mov_b32 s26, 65536\n\t"
    "s_cselect_b32 s26, 57344, s26\n\t"
    "s_mov_b32 s27, 131072\n\t"
    "s_lshr_b32 s77, s2, 1\n\t"
    "s_mul_i32 s77, s77, s26\n\t"
    "s_add_u32 s24, s24, s77\n\t"
    "s_addc_u32 s25, s25, 0\n\t"
    "s_mov_b32 s10, 0\n\t"
    "s_mul_i32 s77, s3, 2816\n\t"
    "v_min_u32 v226, 336, v248\n\t"
    "v_lshl_add_u32 v226, v226, 3, s77\n\t"
    "s_waitcnt vmcnt(5) lgkmcnt(4)\n\t"
    "s_cmp_lg_u32 s3, 0\n\t"
    "s_cbranch_scc1 27f\n\t"
    "s_waitcnt vmcnt(1)\n\t"
    "27:\n\t"
    "s_branch 1f\n\t"
    ".Lfb_f3:\n\t"
    "s_add_u32 s57, s57, 1\n\t"
    "s_add_u32 m0, s73, 73728\n\t"
    "s_mul_i32 s56, s62, s57\n\t"
    "buffer_load_dwordx4 v242, s[32:35], s56 offen sc1 lds\n\t"
    "s_add_u32 m0, s73, 74752\n\t"
    "s_mul_i32 s56, s63, s57\n\t"
    "buffer_load_dwordx4 v243, s[36:39], s56 offen sc1 lds\n\t"
    "s_branch .Lfb_r3\n\t"
    ".Lfb_f4:\n\t"
    "s_add_u32 m0, s73, 81920\n\t"
    "s_mul_i32 s56, s64, s57\n\t"
    "buffer_load_dwordx4 v244, s[40:43], s56 offen sc1 lds\n\t"
    "s_add_u32 m0, s73, 82944\n\t"
    "s_mul_i32 s56, s65, s57\n\t"
    "buffer_load_dwordx4 v245, s[44:47], s56 offen sc1 lds\n\t"
    "s_branch .Lfb_r4\n\t"
    ".Lfb_f5:\n\t"
    "s_add_u32 m0, s73, 90112\n\t"
    "s_mul_i32 s56, s66, s57\n\t"
    "buffer_load_dwordx4 v246, s[48:51], s56 offen sc1 lds\n\t"
    "s_add_u32 m0, s73, 91136\n\t"
    "s_mul_i32 s56, s67, s57\n\t"
    "buffer_load_dwordx4 v247, s[52:55], s56 offen sc1 lds\n\t"
    "s_mov_b64 vcc, 0\n\t"
    "s_branch .Lfb_r5\n\t"
    ".Lfb_f21:\n\t"
    "s_cmp_eq_u32 s76, 7\n\t"
    "s_cbranch_scc1 25f\n\t"
    "s_cmp_lg_u32 s3, 0\n\t"
    "s_cbranch_scc1 .Lfb_r21\n\t"
    "v_lshrrev_b32 v254, 4, v248\n\t"
    "v_min_u32 v254, 7, v254\n\t"
    "v_lshlrev_b32 v254, 12, v254\n\t"
    "v_mov_b32 v255, s20\n\t"
    "buffer_store_dword v255, v254, s[88:91], s21 offen sc1\n\t"
    "s_branch .Lfb_r21\n\t"
    "25:\n\t"
    "v_cmp_ne_u32 vcc, s20, v228\n\t"
    "s_nop 3\n\t"
    "s_cbranch_vccz 27f\n\t"
    "s_mov_b32 s77, 0\n\t"
    "26:\n\t"
    "buffer_load_dword v228, v219, s[88:91], 0 offen sc1\n\t"
    "s_waitcnt vmcnt(0)\n\t"
    "v_cmp_ne_u32 vcc, s20, v228\n\t"
    "s_add_u32 s77, s77, 1\n\t"
    "s_nop 2\n\t"
    "s_cbranch_vccz 27f\n\t"
    "s_sleep 1\n\t"
    "s_cmp_lt_u32 s77, 131072\n\t"
    "s_cbranch_scc1 26b\n\t"
    "27:\n\t"
    "s_mov_b64 vcc, 0\n\t"
    "s_branch .Lfb_r21\n\t"
    ".p2align 3\n\t"
    "1:\n\t"
    "v_mfma_f32_16x16x32_bf16 a[0:3], v[176:179], v[112:115], a[0:3]\n\t"
    "ds_read_b64_tr_b16 v[192:193], v234 offset:0\n\t"
    "v_mfma_f32_16x16x32_bf16 a[0:3], v[180:183], v[128:131], a[0:3]\n\t"
    "ds_read_b64_tr_b16 v[194:195], v234 offset:4096\n\t"
    "v_mfma_f32_16x16x32_bf16 a[4:7], v[180:183], v[132:135], a[4:7]\n\t"
    "ds_read_b64_tr_b16 v[196:197], v234 offset:24576\n\t"
    "v_mfma_f32_16x16x32_bf16 a[4:7], v[176:179], v[116:119], a[4:7]\n\t"
    "ds_read_b64_tr_b16 v[198:199], v234 offset:28672\n\t"
    "v_mfma_f32_16x16x32_bf16 a[8:11], v[176:179], v[120:123], a[8:11]\n\t"
    "v_mfma_f32_16x16x32_bf16 a[8:11], v[180:183], v[136:139], a[8:11]\n\t"
    "buffer_load_dwordx4 v[144:147], v248, s[68:71], s72 offen sc1\n\t"
    "v_mfma_f32_16x16x32_bf16 a[12:15], v[180:183], v[140:143], a[12:15]\n\t"
    "v_mfma_f32_16x16x32_bf16 a[12:15], v[176:179], v[124:127], a[12:15]\n\t"
    "s_waitcnt lgkmcnt(4)\n\t"
    "v_mfma_f32_16x16x32_bf16 a[28:31], v[184:187], v[124:127], a[28:31]\n\t"
    "ds_read_b64_tr_b16 v[200:201], v235 offset:0\n\t"
    "v_mfma_f32_16x16x32_bf16 a[28:31], v[188:191], v[140:143], a[28:31]\n\t"
    "ds_read_b64_tr_b16 v[202:203], v235 offset:4096\n\t"
    "v_mfma_f32_16x16x32_bf16 a[24:27], v[188:191], v[136:139], a[24:27]\n\t"
    "ds_read_b64_tr_b16 v[204:205], v235 offset:24576\n\t"
    "v_mfma_f32_16x16x32_bf16 a[24:27], v[184:187], v[120:123], a[24:27]\n\t"
    "ds_read_b64_tr_b16 v[206:207], v235 offset:28672\n\t"
    "v_mfma_f32_16x16x32_bf16 a[20:23], v[184:187], v[116:119], a[20:23]\n\t"
    "v_mfma_f32_16x16x32_bf16 a[20:23], v[188:191], v[132:135], a[20:23]\n\t"
    "buffer_load_dwordx4 v[160:163], v249, s[68:71], s72 offen sc1\n\t"
    "v_mfma_f32_16x16x32_bf16 a[16:19], v[188:191], v[128:131], a[16:19]\n\t"
    "v_mfma_f32_16x16x32_bf16 a[16:19], v[184:187], v[112:115], a[16:19]\n\t"
    "s_waitcnt lgkmcnt(4)\n\t"
    "v_mfma_f32_16x16x32_bf16 a[32:35], v[192:195], v[112:115], a[32:35]\n\t"
    "ds_read_b64_tr_b16 v[176:177], v236 offset:0\n\t"
    "v_mfma_f32_16x16x32_bf16 a[32:35], v[196:199], v[128:131], a[32:35]\n\t"
    "ds_read_b64_tr_b16 v[178:179], v236 offset:4096\n\t"
    "v_mfma_f32_16x16x32_bf16 a[36:39], v[196:199], v[132:135], a[36:39]\n\t"
    "ds_read_b64_tr_b16 v[180:181], v236 offset:24576\n\t"
    "v_mfma_f32_16x16x32_bf16 a[36:39], v[192:195], v[116:119], a[36:39]\n\t"
    "ds_read_b64_tr_b16 v[182:183], v236 offset:28672\n\t"
    "v_mfma_f32_16x16x32_bf16 a[40:43], v[192:195], v[120:123], a[40:43]\n\t"
    "v_mfma_f32_16x16x32_bf16 a[40:43], v[196:199], v[136:139], a[40:43]\n\t"
    "buffer_load_dwordx4 v[148:151], v248, s[68:71], s72 offen offset:1024 sc1\n\t"
    "v_mfma_f32_16x16x32_bf16 a[44:47], v[196:199], v[140:143], a[44:47]\n\t"
    "v_mfma_f32_16x16x32_bf16 a[44:47], v[192:195], v[124:127], a[44:47]\n\t"
    "s_waitcnt lgkmcnt(4)\n\t"
    "v_mfma_f32_16x16x32_bf16 a[60:63], v[200:203], v[124:127], a[60:63]\n\t"
    "ds_read_b64_tr_b16 v[184:185], v237 offset:0\n\t"
    "v_mfma_f32_16x16x32_bf16 a[60:63], v[204:207], v[140:143], a[60:63]\n\t"
    "ds_read_b64_tr_b16 v[186:187], v237 offset:4096\n\t"
    "v_mfma_f32_16x16x32_bf16 a[56:59], v[204:207], v[136:139], a[56:59]\n\t"
    "ds_read_b64_tr_b16 v[188:189], v237 offset:24576\n\t"
    "v_mfma_f32_16x16x32_bf16 a[56:59], v[200:203], v[120:123], a[56:59]\n\t"
    "ds_read_b64_tr_b16 v[190:191], v237 offset:28672\n\t"
    "v_mfma_f32_16x16x32_bf16 a[52:55], v[200:203], v[116:119], a[52:55]\n\t"
    "v_mfma_f32_16x16x32_bf16 a[52:55], v[204:207], v[132:135], a[52:55]\n\t"
    "buffer_load_dwordx4 v[164:167], v249, s[68:71], s72 offen offset:1024 sc1\n\t"
    "v_mfma_f32_16x16x32_bf16 a[48:51], v[204:207], v[128:131], a[48:51]\n\t"
    "s_cbranch_vccnz .Lfb_f3\n\t"
    ".Lfb_r3:\n\t"
    "v_mfma_f32_16x16x32_bf16 a[48:51], v[200:203], v[112:115], a[48:51]\n\t"
    "s_waitcnt lgkmcnt(4)\n\t"
    "v_mfma_f32_16x16x32_bf16 a[64:67], v[176:179], v[112:115], a[64:67]\n\t"
    "ds_read_b64_tr_b16 v[192:193], v238 offset:0\n\t"
    "v_mfma_f32_16x16x32_bf16 a[64:67], v[180:183], v[128:131], a[64:67]\n\t"
    "ds_read_b64_tr_b16 v[194:195], v238 offset:4096\n\t"
    "v_mfma_f32_16x16x32_bf16 a[68:71], v[180:183], v[132:135], a[68:71]\n\t"
    "ds_read_b64_tr_b16 v[196:197], v238 offset:24576\n\t"
    "v_mfma_f32_16x16x32_bf16 a[68:71], v[176:179], v[116:119], a[68:71]\n\t"
    "ds_read_b64_tr_b16 v[198:199], v238 offset:28672\n\t"
    "v_mfma_f32_16x16x32_bf16 a[72:75], v[176:179], v[120:123], a[72:75]\n\t"
    "v_mfma_f32_16x16x32_bf16 a[72:75], v[180:183], v[136:139], a[72:75]\n\t"
    "buffer_load_dwordx4 v[152:155], v248, s[68:71], s72 offen offset:2048 sc1\n\t"
    "v_mfma_f32_16x16x32_bf16 a[76:79], v[180:183], v[140:143], a[76:79]\n\t"
    "s_cbranch_vccnz .Lfb_f4\n\t"
    ".Lfb_r4:\n\t"
    "v_mfma_f32_16x16x32_bf16 a[76:79], v[176:179], v[124:127], a[76:79]\n\t"
    "s_waitcnt lgkmcnt(4)\n\t"
    "v_mfma_f32_16x16x32_bf16 a[92:95], v[184:187], v[124:127], a[92:95]\n\t"
    "ds_read_b64_tr_b16 v[200:201], v239 offset:0\n\t"
    "v_mfma_f32_16x16x32_bf16 a[92:95], v[188:191], v[140:143], a[92:95]\n\t"
    "ds_read_b64_tr_b16 v[202:203], v239 offset:4096\n\t"
    "v_mfma_f32_16x16x32_bf16 a[88:91], v[188:191], v[136:139], a[88:91]\n\t"
    "ds_read_b64_tr_b16 v[204:205], v239 offset:24576\n\t"
    "v_mfma_f32_16x16x32_bf16 a[88:91], v[184:187], v[120:123], a[88:91]\n\t"
    "ds_read_b64_tr_b16 v[206:207], v239 offset:28672\n\t"
    "v_mfma_f32_16x16x32_bf16 a[84:87], v[184:187], v[116:119], a[84:87]\n\t"
    "v_mfma_f32_16x16x32_bf16 a[84:87], v[188:191], v[132:135], a[84:87]\n\t"
    "buffer_load_dwordx4 v[168:171], v249, s[68:71], s72 offen offset:2048 sc1\n\t"
    "v_mfma_f32_16x16x32_bf16 a[80:83], v[188:191], v[128:131], a[80:83]\n\t"
    "s_cbranch_vccnz .Lfb_f5\n\t"
    ".Lfb_r5:\n\t"
    "v_mfma_f32_16x16x32_bf16 a[80:83], v[184:187], v[112:115], a[80:83]\n\t"
    "s_waitcnt lgkmcnt(4)\n\t"
    "v_mfma_f32_16x16x32_bf16 a[96:99], v[192:195], v[112:115], a[96:99]\n\t"
    "ds_read_b64_tr_b16 v[176:177], v241 offset:8192\n\t"
    "v_mfma_f32_16x16x32_bf16 a[96:99], v[196:199], v[128:131], a[96:99]\n\t"
    "ds_read_b64_tr_b16 v[178:179], v241 offset:11776\n\t"
    "v_mfma_f32_16x16x32_bf16 a[100:103], v[196:199], v[132:135], a[100:103]\n\t"
    "ds_read_b64_tr_b16 v[180:181], v241 offset:32768\n\t"
    "v_mfma_f32_16x16x32_bf16 a[100:103], v[192:195], v[116:119], a[100:103]\n\t"
    "ds_read_b64_tr_b16 v[182:183], v241 offset:36352\n\t"
    "v_mfma_f32_16x16x32_bf16 a[104:107], v[192:195], v[120:123], a[104:107]\n\t"
    "s_add_u32 s77, s75, 49152\n\t"
    "s_cmp_lg_u32 s77, 147456\n\t"
    "s_cselect_b32 s77, s77, 0\n\t"
    "v_mfma_f32_16x16x32_bf16 a[104:107], v[196:199], v[136:139], a[104:107]\n\t"
    "buffer_load_dwordx4 v[156:159], v248, s[68:71], s72 offen offset:3072 sc1\n\t"
    "v_mfma_f32_16x16x32_bf16 a[108:111], v[196:199], v[140:143], a[108:111]\n\t"
    "s_sub_u32 s78, s77, s75\n\t"
    "v_mfma_f32_16x16x32_bf16 a[108:111], v[192:195], v[124:127], a[108:111]\n\t"
    "s_waitcnt lgkmcnt(4)\n\t"
    "v_mfma_f32_16x16x32_bf16 a[124:127], v[200:203], v[124:127], a[124:127]\n\t"
    "ds_read_b64_tr_b16 v[184:185], v241 offset:8224\n\t"
    "v_mfma_f32_16x16x32_bf16 a[124:127], v[204:207], v[140:143], a[124:127]\n\t"
    "ds_read_b64_tr_b16 v[186:187], v241 offset:11808\n\t"
    "v_mfma_f32_16x16x32_bf16 a[120:123], v[204:207], v[136:139], a[120:123]\n\t"
    "ds_read_b64_tr_b16 v[188:189], v241 offset:32800\n\t"
    "v_mfma_f32_16x16x32_bf16 a[120:123], v[200:203], v[120:123], a[120:123]\n\t"
    "ds_read_b64_tr_b16 v[190:191], v241 offset:36384\n\t"
    "v_mfma_f32_16x16x32_bf16 a[116:119], v[200:203], v[116:119], a[116:119]\n\t"
    "v_mfma_f32_16x16x32_bf16 a[116:119], v[204:207], v[132:135], a[116:119]\n\t"
    "buffer_load_dwordx4 v[172:175], v249, s[68:71], s72 offen offset:3072 sc1\n\t"
    "v_mfma_f32_16x16x32_bf16 a[112:115], v[204:207], v[128:131], a[112:115]\n\t"
    "s_and_b32 s57, s58, 15\n\t"
    "s_lshr_b64 s[58:59], s[58:59], 4\n\t"
    "s_lshl_b32 s72, s57, 13\n\t"
    "v_mfma_f32_16x16x32_bf16 a[112:115], v[200:203], v[112:115], a[112:115]\n\t"
    "s_lshl_b32 s57, s57, 1\n\t"
    "s_waitcnt lgkmcnt(4)\n\t"
    "v_mfma_f32_16x16x32_bf16 v[0:3], v[176:179], v[112:115], v[0:3]\n\t"
    "ds_read_b64_tr_b16 v[192:193], v241 offset:8256\n\t"
    "v_mfma_f32_16x16x32_bf16 v[0:3], v[180:183], v[128:131], v[0:3]\n\t"
    "ds_read_b64_tr_b16 v[194:195], v241 offset:11840\n\t"
    "v_mfma_f32_16x16x32_bf16 v[4:7], v[180:183], v[132:135], v[4:7]\n\t"
    "ds_read_b64_tr_b16 v[196:197], v241 offset:32832\n\t"
    "v_mfma_f32_16x16x32_bf16 v[4:7], v[176:179], v[116:119], v[4:7]\n\t"
    "ds_read_b64_tr_b16 v[198:199], v241 offset:36416\n\t"
    "v_mfma_f32_16x16x32_bf16 v[8:11], v[176:179], v[120:123], v[8:11]\n\t"
    "s_add_u32 m0, s74, 0\n\t"
    "s_mul_i32 s56, s62, s57\n\t"
    "v_mfma_f32_16x16x32_bf16 v[8:11], v[180:183], v[136:139], v[8:11]\n\t"
    "buffer_load_dwordx4 v242, s[32:35], s56 offen sc1 lds\n\t"
    "v_mfma_f32_16x16x32_bf16 v[12:15], v[180:183], v[140:143], v[12:15]\n\t"
    "v_add_u32 v232, s78, v232\n\t"
    "v_add_u32 v233, s78, v233\n\t"
    "v_mfma_f32_16x16x32_bf16 v[12:15], v[176:179], v[124:127], v[12:15]\n\t"
    "s_waitcnt lgkmcnt(4)\n\t"
    "v_mfma_f32_16x16x32_bf16 v[28:31], v[184:187], v[124:127], v[28:31]\n\t"
    "ds_read_b64_tr_b16 v[200:201], v241 offset:8288\n\t"
    "v_mfma_f32_16x16x32_bf16 v[28:31], v[188:191], v[140:143], v[28:31]\n\t"
    "ds_read_b64_tr_b16 v[202:203], v241 offset:11872\n\t"
    "v_mfma_f32_16x16x32_bf16 v[24:27], v[188:191], v[136:139], v[24:27]\n\t"
    "ds_read_b64_tr_b16 v[204:205], v241 offset:32864\n\t"
    "v_mfma_f32_16x16x32_bf16 v[24:27], v[184:187], v[120:123], v[24:27]\n\t"
    "ds_read_b64_tr_b16 v[206:207], v241 offset:36448\n\t"
    "v_mfma_f32_16x16x32_bf16 v[20:23], v[184:187], v[116:119], v[20:23]\n\t"
    "s_add_u32 m0, s74, 1024\n\t"
    "s_mul_i32 s56, s63, s57\n\t"
    "v_mfma_f32_16x16x32_bf16 v[20:23], v[188:191], v[132:135], v[20:23]\n\t"
    "buffer_load_dwordx4 v243, s[36:39], s56 offen sc1 lds\n\t"
    "v_mfma_f32_16x16x32_bf16 v[16:19], v[188:191], v[128:131], v[16:19]\n\t"
    "v_add_u32 v234, s78, v234\n\t"
    "v_add_u32 v235, s78, v235\n\t"
    "v_mfma_f32_16x16x32_bf16 v[16:19], v[184:187], v[112:115], v[16:19]\n\t"
    "s_waitcnt lgkmcnt(4)\n\t"
    "v_mfma_f32_16x16x32_bf16 v[32:35], v[192:195], v[112:115], v[32:35]\n\t"
    "ds_read_b64_tr_b16 v[176:177], v241 offset:8320\n\t"
    "v_mfma_f32_16x16x32_bf16 v[32:35], v[196:199], v[128:131], v[32:35]\n\t"
    "ds_read_b64_tr_b16 v[178:179], v241 offset:11904\n\t"
    "v_mfma_f32_16x16x32_bf16 v[36:39], v[196:199], v[132:135], v[36:39]\n\t"
    "ds_read_b64_tr_b16 v[180:181], v241 offset:32896\n\t"
    "v_mfma_f32_16x16x32_bf16 v[36:39], v[192:195], v[116:119], v[36:39]\n\t"
    "ds_read_b64_tr_b16 v[182:183], v241 offset:36480\n\t"
    "v_mfma_f32_16x16x32_bf16 v[40:43], v[192:195], v[120:123], v[40:43]\n\t"
    "s_add_u32 m0, s74, 8192\n\t"
    "s_mul_i32 s56, s64, s57\n\t"
    "v_mfma_f32_16x16x32_bf16 v[40:43], v[196:199], v[136:139], v[40:43]\n\t"
    "buffer_load_dwordx4 v244, s[40:43], s56 offen sc1 lds\n\t"
    "v_mfma_f32_16x16x32_bf16 v[44:47], v[196:199], v[140:143], v[44:47]\n\t"
    "v_add_u32 v236, s78, v236\n\t"
    "v_add_u32 v237, s78, v237\n\t"
    "v_mfma_f32_16x16x32_bf16 v[44:47], v[192:195], v[124:127], v[44:47]\n\t"
    "s_waitcnt lgkmcnt(4)\n\t"
    "v_mfma_f32_16x16x32_bf16 v[60:63], v[200:203], v[124:127], v[60:63]\n\t"
    "ds_read_b64_tr_b16 v[184:185], v241 offset:8352\n\t"
    "v_mfma_f32_16x16x32_bf16 v[60:63], v[204:207], v[140:143], v[60:63]\n\t"
    "ds_read_b64_tr_b16 v[186:187], v241 offset:11936\n\t"
    "v_mfma_f32_16x16x32_bf16 v[56:59], v[204:207], v[136:139], v[56:59]\n\t"
    "ds_read_b64_tr_b16 v[188:189], v241 offset:32928\n\t"
    "v_mfma_f32_16x16x32_bf16 v[56:59], v[200:203], v[120:123], v[56:59]\n\t"
    "ds_read_b64_tr_b16 v[190:191], v241 offset:36512\n\t"
    "v_mfma_f32_16x16x32_bf16 v[52:55], v[200:203], v[116:119], v[52:55]\n\t"
    "s_add_u32 m0, s74, 9216\n\t"
    "s_mul_i32 s56, s65, s57\n\t"
    "v_mfma_f32_16x16x32_bf16 v[52:55], v[204:207], v[132:135], v[52:55]\n\t"
    "buffer_load_dwordx4 v245, s[44:47], s56 offen sc1 lds\n\t"
    "v_mfma_f32_16x16x32_bf16 v[48:51], v[204:207], v[128:131], v[48:51]\n\t"
    "v_add_u32 v238, s78, v238\n\t"
    "v_add_u32 v239, s78, v239\n\t"
    "v_mfma_f32_16x16x32_bf16 v[48:51], v[200:203], v[112:115], v[48:51]\n\t"
    "s_waitcnt lgkmcnt(4)\n\t"
    "v_mfma_f32_16x16x32_bf16 a[144:147], v[176:179], v[112:115], a[144:147]\n\t"
    "ds_read_b64_tr_b16 v[192:193], v241 offset:8384\n\t"
    "v_mfma_f32_16x16x32_bf16 a[144:147], v[180:183], v[128:131], a[144:147]\n\t"
    "ds_read_b64_tr_b16 v[194:195], v241 offset:11968\n\t"
    "v_mfma_f32_16x16x32_bf16 a[148:151], v[180:183], v[132:135], a[148:151]\n\t"
    "ds_read_b64_tr_b16 v[196:197], v241 offset:32960\n\t"
    "v_mfma_f32_16x16x32_bf16 a[148:151], v[176:179], v[116:119], a[148:151]\n\t"
    "ds_read_b64_tr_b16 v[198:199], v241 offset:36544\n\t"
    "v_mfma_f32_16x16x32_bf16 a[152:155], v[176:179], v[120:123], a[152:155]\n\t"
    "s_add_u32 m0, s74, 16384\n\t"
    "s_mul_i32 s56, s66, s57\n\t"
    "v_mfma_f32_16x16x32_bf16 a[152:155], v[180:183], v[136:139], a[152:155]\n\t"
    "buffer_load_dwordx4 v246, s[48:51], s56 offen sc1 lds\n\t"
    "v_mfma_f32_16x16x32_bf16 a[156:159], v[180:183], v[140:143], a[156:159]\n\t"
    "v_mfma_f32_16x16x32_bf16 a[156:159], v[176:179], v[124:127], a[156:159]\n\t"
    "s_waitcnt lgkmcnt(4)\n\t"
    "v_mfma_f32_16x16x32_bf16 a[172:175], v[184:187], v[124:127], a[172:175]\n\t"
    "ds_read_b64_tr_b16 v[200:201], v241 offset:16384\n\t"
    "v_mfma_f32_16x16x32_bf16 a[172:175], v[188:191], v[140:143], a[172:175]\n\t"
    "ds_read_b64_tr_b16 v[202:203], v241 offset:19968\n\t"
    "v_mfma_f32_16x16x32_bf16 a[168:171], v[188:191], v[136:139], a[168:171]\n\t"
    "ds_read_b64_tr_b16 v[204:205], v241 offset:40960\n\t"
    "v_mfma_f32_16x16x32_bf16 a[168:171], v[184:187], v[120:123], a[168:171]\n\t"
    "ds_read_b64_tr_b16 v[206:207], v241 offset:44544\n\t"
    "v_mfma_f32_16x16x32_bf16 a[164:167], v[184:187], v[116:119], a[164:167]\n\t"
    "s_add_u32 m0, s74, 17408\n\t"
    "s_mul_i32 s56, s67, s57\n\t"
    "v_mfma_f32_16x16x32_bf16 a[164:167], v[188:191], v[132:135], a[164:167]\n\t"
    "buffer_load_dwordx4 v247, s[52:55], s56 offen sc1 lds\n\t"
    "v_mfma_f32_16x16x32_bf16 a[160:163], v[188:191], v[128:131], a[160:163]\n\t"
    "s_add_u32 s57, s57, 1\n\t"
    "v_mfma_f32_16x16x32_bf16 a[160:163], v[184:187], v[112:115], a[160:163]\n\t"
    "s_waitcnt lgkmcnt(4)\n\t"
    "v_mfma_f32_16x16x32_bf16 a[176:179], v[192:195], v[112:115], a[176:179]\n\t"
    "ds_read_b64_tr_b16 v[176:177], v241 offset:16416\n\t"
    "v_mfma_f32_16x16x32_bf16 a[176:179], v[196:199], v[128:131], a[176:179]\n\t"
    "ds_read_b64_tr_b16 v[178:179], v241 offset:20000\n\t"
    "v_mfma_f32_16x16x32_bf16 a[180:183], v[196:199], v[132:135], a[180:183]\n\t"
    "ds_read_b64_tr_b16 v[180:181], v241 offset:40992\n\t"
    "v_mfma_f32_16x16x32_bf16 a[180:183], v[192:195], v[116:119], a[180:183]\n\t"
    "ds_read_b64_tr_b16 v[182:183], v241 offset:44576\n\t"
    "v_mfma_f32_16x16x32_bf16 a[184:187], v[192:195], v[120:123], a[184:187]\n\t"
    "s_add_u32 m0, s74, 24576\n\t"
    "s_mul_i32 s56, s62, s57\n\t"
    "v_mfma_f32_16x16x32_bf16 a[184:187], v[196:199], v[136:139], a[184:187]\n\t"
    "buffer_load_dwordx4 v242, s[32:35], s56 offen sc1 lds\n\t"
    "v_mfma_f32_16x16x32_bf16 a[188:191], v[196:199], v[140:143], a[188:191]\n\t"
    "v_mfma_f32_16x16x32_bf16 a[188:191], v[192:195], v[124:127], a[188:191]\n\t"
    "s_waitcnt lgkmcnt(4)\n\t"
    "v_mfma_f32_16x16x32_bf16 v[76:79], v[200:203], v[124:127], v[76:79]\n\t"
    "ds_read_b64_tr_b16 v[184:185], v241 offset:16448\n\t"
    "v_mfma_f32_16x16x32_bf16 v[76:79], v[204:207], v[140:143], v[76:79]\n\t"
    "ds_read_b64_tr_b16 v[186:187], v241 offset:20032\n\t"
    "v_mfma_f32_16x16x32_bf16 v[72:75], v[204:207], v[136:139], v[72:75]\n\t"
    "ds_read_b64_tr_b16 v[188:189], v241 offset:41024\n\t"
    "v_mfma_f32_16x16x32_bf16 v[72:75], v[200:203], v[120:123], v[72:75]\n\t"
    "ds_read_b64_tr_b16 v[190:191], v241 offset:44608\n\t"
    "v_mfma_f32_16x16x32_bf16 v[68:71], v[200:203], v[116:119], v[68:71]\n\t"
    "s_add_u32 m0, s74, 25600\n\t"
    "s_mul_i32 s56, s63, s57\n\t"
    "v_mfma_f32_16x16x32_bf16 v[68:71], v[204:207], v[132:135], v[68:71]\n\t"
    "buffer_load_dwordx4 v243, s[36:39], s56 offen sc1 lds\n\t"
    "v_mfma_f32_16x16x32_bf16 v[64:67], v[204:207], v[128:131], v[64:67]\n\t"
    "v_mfma_f32_16x16x32_bf16 v[64:67], v[200:203], v[112:115], v[64:67]\n\t"
    "s_waitcnt lgkmcnt(4)\n\t"
    "v_mfma_f32_16x16x32_bf16 v[80:83], v[176:179], v[112:115], v[80:83]\n\t"
    "ds_read_b64_tr_b16 v[192:193], v241 offset:16480\n\t"
    "v_mfma_f32_16x16x32_bf16 v[80:83], v[180:183], v[128:131], v[80:83]\n\t"
    "ds_read_b64_tr_b16 v[194:195], v241 offset:20064\n\t"
    "v_mfma_f32_16x16x32_bf16 v[84:87], v[180:183], v[132:135], v[84:87]\n\t"
    "ds_read_b64_tr_b16 v[196:197], v241 offset:41056\n\t"
    "v_mfma_f32_16x16x32_bf16 v[84:87], v[176:179], v[116:119], v[84:87]\n\t"
    "ds_read_b64_tr_b16 v[198:199], v241 offset:44640\n\t"
    "v_mfma_f32_16x16x32_bf16 v[88:91], v[176:179], v[120:123], v[88:91]\n\t"
    "s_add_u32 m0, s74, 32768\n\t"
    "s_mul_i32 s56, s64, s57\n\t"
    "v_mfma_f32_16x16x32_bf16 v[88:91], v[180:183], v[136:139], v[88:91]\n\t"
    "buffer_load_dwordx4 v244, s[40:43], s56 offen sc1 lds\n\t"
    "v_mfma_f32_16x16x32_bf16 v[92:95], v[180:183], v[140:143], v[92:95]\n\t"
    "v_mfma_f32_16x16x32_bf16 v[92:95], v[176:179], v[124:127], v[92:95]\n\t"
    "s_waitcnt lgkmcnt(4)\n\t"
    "v_mfma_f32_16x16x32_bf16 v[108:111], v[184:187], v[124:127], v[108:111]\n\t"
    "ds_read_b64_tr_b16 v[200:201], v241 offset:16512\n\t"
    "v_mfma_f32_16x16x32_bf16 v[108:111], v[188:191], v[140:143], v[108:111]\n\t"
    "ds_read_b64_tr_b16 v[202:203], v241 offset:20096\n\t"
    "v_mfma_f32_16x16x32_bf16 v[104:107], v[188:191], v[136:139], v[104:107]\n\t"
    "ds_read_b64_tr_b16 v[204:205], v241 offset:41088\n\t"
    "v_mfma_f32_16x16x32_bf16 v[104:107], v[184:187], v[120:123], v[104:107]\n\t"
    "ds_read_b64_tr_b16 v[206:207], v241 offset:44672\n\t"
    "v_mfma_f32_16x16x32_bf16 v[100:103], v[184:187], v[116:119], v[100:103]\n\t"
    "s_add_u32 m0, s74, 33792\n\t"
    "s_mul_i32 s56, s65, s57\n\t"
    "v_mfma_f32_16x16x32_bf16 v[100:103], v[188:191], v[132:135], v[100:103]\n\t"
    "buffer_load_dwordx4 v245, s[44:47], s56 offen sc1 lds\n\t"
    "v_mfma_f32_16x16x32_bf16 v[96:99], v[188:191], v[128:131], v[96:99]\n\t"
    "v_mfma_f32_16x16x32_bf16 v[96:99], v[184:187], v[112:115], v[96:99]\n\t"
    "s_waitcnt lgkmcnt(4)\n\t"
    "v_mfma_f32_16x16x32_bf16 a[128:131], v[192:195], v[112:115], a[128:131]\n\t"
    "ds_read_b64_tr_b16 v[176:177], v241 offset:16544\n\t"
    "v_mfma_f32_16x16x32_bf16 a[128:131], v[196:199], v[128:131], a[128:131]\n\t"
    "ds_read_b64_tr_b16 v[178:179], v241 offset:20128\n\t"
    "v_mfma_f32_16x16x32_bf16 a[132:135], v[196:199], v[132:135], a[132:135]\n\t"
    "ds_read_b64_tr_b16 v[180:181], v241 offset:41120\n\t"
    "v_mfma_f32_16x16x32_bf16 a[132:135], v[192:195], v[116:119], a[132:135]\n\t"
    "ds_read_b64_tr_b16 v[182:183], v241 offset:44704\n\t"
    "v_mfma_f32_16x16x32_bf16 a[136:139], v[192:195], v[120:123], a[136:139]\n\t"
    "s_add_u32 m0, s74, 40960\n\t"
    "s_mul_i32 s56, s66, s57\n\t"
    "v_mfma_f32_16x16x32_bf16 a[136:139], v[196:199], v[136:139], a[136:139]\n\t"
    "buffer_load_dwordx4 v246, s[48:51], s56 offen sc1 lds\n\t"
    "v_mfma_f32_16x16x32_bf16 a[140:143], v[196:199], v[140:143], a[140:143]\n\t"
    "v_mfma_f32_16x16x32_bf16 a[140:143], v[192:195], v[124:127], a[140:143]\n\t"
    "s_waitcnt lgkmcnt(4)\n\t"
    "v_mfma_f32_16x16x32_bf16 a[204:207], v[200:203], v[124:127], a[204:207]\n\t"
    "ds_read_b64_tr_b16 v[184:185], v241 offset:16576\n\t"
    "v_mfma_f32_16x16x32_bf16 a[204:207], v[204:207], v[140:143], a[204:207]\n\t"
    "ds_read_b64_tr_b16 v[186:187], v241 offset:20160\n\t"
    "v_mfma_f32_16x16x32_bf16 a[200:203], v[204:207], v[136:139], a[200:203]\n\t"
    "ds_read_b64_tr_b16 v[188:189], v241 offset:41152\n\t"
    "v_mfma_f32_16x16x32_bf16 a[200:203], v[200:203], v[120:123], a[200:203]\n\t"
    "ds_read_b64_tr_b16 v[190:191], v241 offset:44736\n\t"
    "v_mfma_f32_16x16x32_bf16 a[196:199], v[200:203], v[116:119], a[196:199]\n\t"
    "s_add_u32 m0, s74, 41984\n\t"
    "s_mul_i32 s56, s67, s57\n\t"
    "v_mfma_f32_16x16x32_bf16 a[196:199], v[204:207], v[132:135], a[196:199]\n\t"
    "buffer_load_dwordx4 v247, s[52:55], s56 offen sc1 lds\n\t"
    "v_mfma_f32_16x16x32_bf16 a[192:195], v[204:207], v[128:131], a[192:195]\n\t"
    "v_add_u32 v241, s78, v241\n\t"
    "v_mfma_f32_16x16x32_bf16 a[192:195], v[200:203], v[112:115], a[192:195]\n\t"
    "s_waitcnt lgkmcnt(4)\n\t"
    "v_mfma_f32_16x16x32_bf16 a[208:211], v[176:179], v[112:115], a[208:211]\n\t"
    "s_add_u32 s74, s73, s75\n\t"
    "s_mov_b32 s75, s77\n\t"
    "v_mfma_f32_16x16x32_bf16 a[208:211], v[180:183], v[128:131], a[208:211]\n\t"
    "v_mfma_f32_16x16x32_bf16 a[212:215], v[180:183], v[132:135], a[212:215]\n\t"
    "s_waitcnt vmcnt(12) lgkmcnt(0)\n\t"
    "s_barrier\n\t"
    "v_mfma_f32_16x16x32_bf16 a[212:215], v[176:179], v[116:119], a[212:215]\n\t"
    "ds_read_b64_tr_b16 v[192:193], v232 offset:0\n\t"
    "v_mfma_f32_16x16x32_bf16 a[216:219], v[176:179], v[120:123], a[216:219]\n\t"
    "ds_read_b64_tr_b16 v[194:195], v232 offset:4096\n\t"
    "v_mfma_f32_16x16x32_bf16 a[216:219], v[180:183], v[136:139], a[216:219]\n\t"
    "ds_read_b64_tr_b16 v[196:197], v232 offset:24576\n\t"
    "v_mfma_f32_16x16x32_bf16 a[220:223], v[180:183], v[140:143], a[220:223]\n\t"
    "ds_read_b64_tr_b16 v[198:199], v232 offset:28672\n\t"
    "v_mfma_f32_16x16x32_bf16 a[220:223], v[176:179], v[124:127], a[220:223]\n\t"
    "v_mfma_f32_16x16x32_bf16 a[236:239], v[184:187], v[124:127], a[236:239]\n\t"
    "ds_read_b64_tr_b16 v[200:201], v233 offset:0\n\t"
    "v_mfma_f32_16x16x32_bf16 a[236:239], v[188:191], v[140:143], a[236:239]\n\t"
    "ds_read_b64_tr_b16 v[202:203], v233 offset:4096\n\t"
    "v_mfma_f32_16x16x32_bf16 a[232:235], v[188:191], v[136:139], a[232:235]\n\t"
    "ds_read_b64_tr_b16 v[204:205], v233 offset:24576\n\t"
    "v_mfma_f32_16x16x32_bf16 a[232:235], v[184:187], v[120:123], a[232:235]\n\t"
    "ds_read_b64_tr_b16 v[206:207], v233 offset:28672\n\t"
    "v_mfma_f32_16x16x32_bf16 a[228:231], v[184:187], v[116:119], a[228:231]\n\t"
    "v_mfma_f32_16x16x32_bf16 a[228:231], v[188:191], v[132:135], a[228:231]\n\t"
    "s_cmp_ge_u32 s76, 7\n\t"
    "s_cbranch_scc1 .Lfb_f21\n\t"
    ".Lfb_r21:\n\t"
    "v_mfma_f32_16x16x32_bf16 a[224:227], v[188:191], v[128:131], a[224:227]\n\t"
    "v_mfma_f32_16x16x32_bf16 a[224:227], v[184:187], v[112:115], a[224:227]\n\t"
    "s_waitcnt lgkmcnt(4)\n\t"
    "v_mfma_f32_16x16x32_bf16 a[0:3], v[192:195], v[144:147], a[0:3]\n\t"
    "ds_read_b64_tr_b16 v[176:177], v234 offset:0\n\t"
    "v_mfma_f32_16x16x32_bf16 a[0:3], v[196:199], v[160:163], a[0:3]\n\t"
    "ds_read_b64_tr_b16 v[178:179], v234 offset:4096\n\t"
    "v_mfma_f32_16x16x32_bf16 a[4:7], v[196:199], v[164:167], a[4:7]\n\t"
    "ds_read_b64_tr_b16 v[180:181], v234 offset:24576\n\t"
    "v_mfma_f32_16x16x32_bf16 a[4:7], v[192:195], v[148:151], a[4:7]\n\t"
    "ds_read_b64_tr_b16 v[182:183], v234 offset:28672\n\t"
    "v_mfma_f32_16x16x32_bf16 a[8:11], v[192:195], v[152:155], a[8:11]\n\t"
    "v_mfma_f32_16x16x32_bf16 a[8:11], v[196:199], v[168:171], a[8:11]\n\t"
    "buffer_load_dwordx4 v[112:115], v248, s[68:71], s72 offen sc1\n\t"
    "v_mfma_f32_16x16x32_bf16 a[12:15], v[196:199], v[172:175], a[12:15]\n\t"
    "v_mfma_f32_16x16x32_bf16 a[12:15], v[192:195], v[156:159], a[12:15]\n\t"
    "s_waitcnt lgkmcnt(4)\n\t"
    "v_mfma_f32_16x16x32_bf16 a[28:31], v[200:203], v[156:159], a[28:31]\n\t"
    "ds_read_b64_tr_b16 v[184:185], v235 offset:0\n\t"
    "v_mfma_f32_16x16x32_bf16 a[28:31], v[204:207], v[172:175], a[28:31]\n\t"
    "ds_read_b64_tr_b16 v[186:187], v235 offset:4096\n\t"
    "v_mfma_f32_16x16x32_bf16 a[24:27], v[204:207], v[168:171], a[24:27]\n\t"
    "ds_read_b64_tr_b16 v[188:189], v235 offset:24576\n\t"
    "v_mfma_f32_16x16x32_bf16 a[24:27], v[200:203], v[152:155], a[24:27]\n\t"
    "ds_read_b64_tr_b16 v[190:191], v235 offset:28672\n\t"
    "v_mfma_f32_16x16x32_bf16 a[20:23], v[200:203], v[148:151], a[20:23]\n\t"
    "v_mfma_f32_16x16x32_bf16 a[20:23], v[204:207], v[164:167], a[20:23]\n\t"
    "buffer_load_dwordx4 v[128:131], v249, s[68:71], s72 offen sc1\n\t"
    "v_mfma_f32_16x16x32_bf16 a[16:19], v[204:207], v[160:163], a[16:19]\n\t"
    "v_mfma_f32_16x16x32_bf16 a[16:19], v[200:203], v[144:147], a[16:19]\n\t"
    "s_waitcnt lgkmcnt(4)\n\t"
    "v_mfma_f32_16x16x32_bf16 a[32:35], v[176:179], v[144:147], a[32:35]\n\t"
    "ds_read_b64_tr_b16 v[192:193], v236 offset:0\n\t"
    "v_mfma_f32_16x16x32_bf16 a[32:35], v[180:183], v[160:163], a[32:35]\n\t"
    "ds_read_b64_tr_b16 v[194:195], v236 offset:4096\n\t"
    "v_mfma_f32_16x16x32_bf16 a[36:39], v[180:183], v[164:167], a[36:39]\n\t"
    "ds_read_b64_tr_b16 v[196:197], v236 offset:24576\n\t"
    "v_mfma_f32_16x16x32_bf16 a[36:39], v[176:179], v[148:151], a[36:39]\n\t"
    "ds_read_b64_tr_b16 v[198:199], v236 offset:28672\n\t"
    "v_mfma_f32_16x16x32_bf16 a[40:43], v[176:179], v[152:155], a[40:43]\n\t"
    "v_mfma_f32_16x16x32_bf16 a[40:43], v[180:183], v[168:171], a[40:43]\n\t"
    "buffer_load_dwordx4 v[116:119], v248, s[68:71], s72 offen offset:1024 sc1\n\t"
    "v_mfma_f32_16x16x32_bf16 a[44:47], v[180:183], v[172:175], a[44:47]\n\t"
    "v_mfma_f32_16x16x32_bf16 a[44:47], v[176:179], v[156:159], a[44:47]\n\t"
    "s_waitcnt lgkmcnt(4)\n\t"
    "v_mfma_f32_16x16x32_bf16 a[60:63], v[184:187], v[156:159], a[60:63]\n\t"
    "ds_read_b64_tr_b16 v[200:201], v237 offset:0\n\t"
    "v_mfma_f32_16x16x32_bf16 a[60:63], v[188:191], v[172:175], a[60:63]\n\t"
    "ds_read_b64_tr_b16 v[202:203], v237 offset:4096\n\t"
    "v_mfma_f32_16x16x32_bf16 a[56:59], v[188:191], v[168:171], a[56:59]\n\t"
    "ds_read_b64_tr_b16 v[204:205], v237 offset:24576\n\t"
    "v_mfma_f32_16x16x32_bf16 a[56:59], v[184:187], v[152:155], a[56:59]\n\t"
    "ds_read_b64_tr_b16 v[206:207], v237 offset:28672\n\t"
    "v_mfma_f32_16x16x32_bf16 a[52:55], v[184:187], v[148:151], a[52:55]\n\t"
    "v_mfma_f32_16x16x32_bf16 a[52:55], v[188:191], v[164:167], a[52:55]\n\t"
    "buffer_load_dwordx4 v[132:135], v249, s[68:71], s72 offen offset:1024 sc1\n\t"
    "v_mfma_f32_16x16x32_bf16 a[48:51], v[188:191], v[160:163], a[48:51]\n\t"
    "v_mfma_f32_16x16x32_bf16 a[48:51], v[184:187], v[144:147], a[48:51]\n\t"
    "s_waitcnt lgkmcnt(4)\n\t"
    "v_mfma_f32_16x16x32_bf16 a[64:67], v[192:195], v[144:147], a[64:67]\n\t"
    "ds_read_b64_tr_b16 v[176:177], v238 offset:0\n\t"
    "v_mfma_f32_16x16x32_bf16 a[64:67], v[196:199], v[160:163], a[64:67]\n\t"
    "ds_read_b64_tr_b16 v[178:179], v238 offset:4096\n\t"
    "v_mfma_f32_16x16x32_bf16 a[68:71], v[196:199], v[164:167], a[68:71]\n\t"
    "ds_read_b64_tr_b16 v[180:181], v238 offset:24576\n\t"
    "v_mfma_f32_16x16x32_bf16 a[68:71], v[192:195], v[148:151], a[68:71]\n\t"
    "ds_read_b64_tr_b16 v[182:183], v238 offset:28672\n\t"
    "v_mfma_f32_16x16x32_bf16 a[72:75], v[192:195], v[152:155], a[72:75]\n\t"
    "v_mfma_f32_16x16x32_bf16 a[72:75], v[196:199], v[168:171], a[72:75]\n\t"
    "buffer_load_dwordx4 v[120:123], v248, s[68:71], s72 offen offset:2048 sc1\n\t"
    "v_mfma_f32_16x16x32_bf16 a[76:79], v[196:199], v[172:175], a[76:79]\n\t"
    "v_mfma_f32_16x16x32_bf16 a[76:79], v[192:195], v[156:159], a[76:79]\n\t"
    "s_waitcnt lgkmcnt(4)\n\t"
    "v_mfma_f32_16x16x32_bf16 a[92:95], v[200:203], v[156:159], a[92:95]\n\t"
    "ds_read_b64_tr_b16 v[184:185], v239 offset:0\n\t"
    "v_mfma_f32_16x16x32_bf16 a[92:95], v[204:207], v[172:175], a[92:95]\n\t"
    "ds_read_b64_tr_b16 v[186:187], v239 offset:4096\n\t"
    "v_mfma_f32_16x16x32_bf16 a[88:91], v[204:207], v[168:171], a[88:91]\n\t"
    "ds_read_b64_tr_b16 v[188:189], v239 offset:24576\n\t"
    "v_mfma_f32_16x16x32_bf16 a[88:91], v[200:203], v[152:155], a[88:91]\n\t"
    "ds_read_b64_tr_b16 v[190:191], v239 offset:28672\n\t"
    "v_mfma_f32_16x16x32_bf16 a[84:87], v[200:203], v[148:151], a[84:87]\n\t"
    "v_mfma_f32_16x16x32_bf16 a[84:87], v[204:207], v[164:167], a[84:87]\n\t"
    "buffer_load_dwordx4 v[136:139], v249, s[68:71], s72 offen offset:2048 sc1\n\t"
    "v_mfma_f32_16x16x32_bf16 a[80:83], v[204:207], v[160:163], a[80:83]\n\t"
    "v_mfma_f32_16x16x32_bf16 a[80:83], v[200:203], v[144:147], a[80:83]\n\t"
    "s_waitcnt lgkmcnt(4)\n\t"
    "v_mfma_f32_16x16x32_bf16 a[96:99], v[176:179], v[144:147], a[96:99]\n\t"
    "ds_read_b64_tr_b16 v[192:193], v241 offset:8192\n\t"
    "v_mfma_f32_16x16x32_bf16 a[96:99], v[180:183], v[160:163], a[96:99]\n\t"
    "ds_read_b64_tr_b16 v[194:195], v241 offset:11776\n\t"
    "v_mfma_f32_16x16x32_bf16 a[100:103], v[180:183], v[164:167], a[100:103]\n\t"
    "ds_read_b64_tr_b16 v[196:197], v241 offset:32768\n\t"
    "v_mfma_f32_16x16x32_bf16 a[100:103], v[176:179], v[148:151], a[100:103]\n\t"
    "ds_read_b64_tr_b16 v[198:199], v241 offset:36352\n\t"
    "v_mfma_f32_16x16x32_bf16 a[104:107], v[176:179], v[152:155], a[104:107]\n\t"
    "s_add_u32 s77, s75, 49152\n\t"
    "s_cmp_lg_u32 s77, 147456\n\t"
    "s_cselect_b32 s77, s77, 0\n\t"
    "v_mfma_f32_16x16x32_bf16 a[104:107], v[180:183], v[168:171], a[104:107]\n\t"
    "buffer_load_dwordx4 v[124:127], v248, s[68:71], s72 offen offset:3072 sc1\n\t"
    "v_mfma_f32_16x16x32_bf16 a[108:111], v[180:183], v[172:175], a[108:111]\n\t"
    "s_sub_u32 s78, s77, s75\n\t"
    "v_mfma_f32_16x16x32_bf16 a[108:111], v[176:179], v[156:159], a[108:111]\n\t"
    "s_waitcnt lgkmcnt(4)\n\t"
    "v_mfma_f32_16x16x32_bf16 a[124:127], v[184:187], v[156:159], a[124:127]\n\t"
    "ds_read_b64_tr_b16 v[200:201], v241 offset:8224\n\t"
    "v_mfma_f32_16x16x32_bf16 a[124:127], v[188:191], v[172:175], a[124:127]\n\t"
    "ds_read_b64_tr_b16 v[202:203], v241 offset:11808\n\t"
    "v_mfma_f32_16x16x32_bf16 a[120:123], v[188:191], v[168:171], a[120:123]\n\t"
    "ds_read_b64_tr_b16 v[204:205], v241 offset:32800\n\t"
    "v_mfma_f32_16x16x32_bf16 a[120:123], v[184:187], v[152:155], a[120:123]\n\t"
    "ds_read_b64_tr_b16 v[206:207], v241 offset:36384\n\t"
    "v_mfma_f32_16x16x32_bf16 a[116:119], v[184:187], v[148:151], a[116:119]\n\t"
    "v_mfma_f32_16x16x32_bf16 a[116:119], v[188:191], v[164:167], a[116:119]\n\t"
    "buffer_load_dwordx4 v[140:143], v249, s[68:71], s72 offen offset:3072 sc1\n\t"
    "v_mfma_f32_16x16x32_bf16 a[112:115], v[188:191], v[160:163], a[112:115]\n\t"
    "s_and_b32 s57, s58, 15\n\t"
    "s_lshr_b64 s[58:59], s[58:59], 4\n\t"
    "s_lshl_b32 s72, s57, 13\n\t"
    "v_mfma_f32_16x16x32_bf16 a[112:115], v[184:187], v[144:147], a[112:115]\n\t"
    "s_lshl_b32 s57, s57, 1\n\t"
    "s_waitcnt lgkmcnt(4)\n\t"
    "v_mfma_f32_16x16x32_bf16 v[0:3], v[192:195], v[144:147], v[0:3]\n\t"
    "ds_read_b64_tr_b16 v[176:177], v241 offset:8256\n\t"
    "v_mfma_f32_16x16x32_bf16 v[0:3], v[196:199], v[160:163], v[0:3]\n\t"
    "ds_read_b64_tr_b16 v[178:179], v241 offset:11840\n\t"
    "v_mfma_f32_16x16x32_bf16 v[4:7], v[196:199], v[164:167], v[4:7]\n\t"
    "ds_read_b64_tr_b16 v[180:181], v241 offset:32832\n\t"
    "v_mfma_f32_16x16x32_bf16 v[4:7], v[192:195], v[148:151], v[4:7]\n\t"
    "ds_read_b64_tr_b16 v[182:183], v241 offset:36416\n\t"
    "v_mfma_f32_16x16x32_bf16 v[8:11], v[192:195], v[152:155], v[8:11]\n\t"
    "s_add_u32 m0, s74, 0\n\t"
    "s_mul_i32 s56, s62, s57\n\t"
    "v_mfma_f32_16x16x32_bf16 v[8:11], v[196:199], v[168:171], v[8:11]\n\t"
    "buffer_load_dwordx4 v242, s[32:35], s56 offen sc1 lds\n\t"
    "v_mfma_f32_16x16x32_bf16 v[12:15], v[196:199], v[172:175], v[12:15]\n\t"
    "v_add_u32 v232, s78, v232\n\t"
    "v_add_u32 v233, s78, v233\n\t"
    "v_mfma_f32_16x16x32_bf16 v[12:15], v[192:195], v[156:159], v[12:15]\n\t"
    "s_waitcnt lgkmcnt(4)\n\t"
    "v_mfma_f32_16x16x32_bf16 v[28:31], v[200:203], v[156:159], v[28:31]\n\t"
    "ds_read_b64_tr_b16 v[184:185], v241 offset:8288\n\t"
    "v_mfma_f32_16x16x32_bf16 v[28:31], v[204:207], v[172:175], v[28:31]\n\t"
    "ds_read_b64_tr_b16 v[186:187], v241 offset:11872\n\t"
    "v_mfma_f32_16x16x32_bf16 v[24:27], v[204:207], v[168:171], v[24:27]\n\t"
    "ds_read_b64_tr_b16 v[188:189], v241 offset:32864\n\t"
    "v_mfma_f32_16x16x32_bf16 v[24:27], v[200:203], v[152:155], v[24:27]\n\t"
    "ds_read_b64_tr_b16 v[190:191], v241 offset:36448\n\t"
    "v_mfma_f32_16x16x32_bf16 v[20:23], v[200:203], v[148:151], v[20:23]\n\t"
    "s_add_u32 m0, s74, 1024\n\t"
    "s_mul_i32 s56, s63, s57\n\t"
    "v_mfma_f32_16x16x32_bf16 v[20:23], v[204:207], v[164:167], v[20:23]\n\t"
    "buffer_load_dwordx4 v243, s[36:39], s56 offen sc1 lds\n\t"
    "v_mfma_f32_16x16x32_bf16 v[16:19], v[204:207], v[160:163], v[16:19]\n\t"
    "v_add_u32 v234, s78, v234\n\t"
    "v_add_u32 v235, s78, v235\n\t"
    "v_mfma_f32_16x16x32_bf16 v[16:19], v[200:203], v[144:147], v[16:19]\n\t"
    "s_waitcnt lgkmcnt(4)\n\t"
    "v_mfma_f32_16x16x32_bf16 v[32:35], v[176:179], v[144:147], v[32:35]\n\t"
    "ds_read_b64_tr_b16 v[192:193], v241 offset:8320\n\t"
    "v_mfma_f32_16x16x32_bf16 v[32:35], v[180:183], v[160:163], v[32:35]\n\t"
    "ds_read_b64_tr_b16 v[194:195], v241 offset:11904\n\t"
    "v_mfma_f32_16x16x32_bf16 v[36:39], v[180:183], v[164:167], v[36:39]\n\t"
    "ds_read_b64_tr_b16 v[196:197], v241 offset:32896\n\t"
    "v_mfma_f32_16x16x32_bf16 v[36:39], v[176:179], v[148:151], v[36:39]\n\t"
    "ds_read_b64_tr_b16 v[198:199], v241 offset:36480\n\t"
    "v_mfma_f32_16x16x32_bf16 v[40:43], v[176:179], v[152:155], v[40:43]\n\t"
    "s_add_u32 m0, s74, 8192\n\t"
    "s_mul_i32 s56, s64, s57\n\t"
    "v_mfma_f32_16x16x32_bf16 v[40:43], v[180:183], v[168:171], v[40:43]\n\t"
    "buffer_load_dwordx4 v244, s[40:43], s56 offen sc1 lds\n\t"
    "v_mfma_f32_16x16x32_bf16 v[44:47], v[180:183], v[172:175], v[44:47]\n\t"
    "v_add_u32 v236, s78, v236\n\t"
    "v_add_u32 v237, s78, v237\n\t"
    "v_mfma_f32_16x16x32_bf16 v[44:47], v[176:179], v[156:159], v[44:47]\n\t"
    "s_waitcnt lgkmcnt(4)\n\t"
    "v_mfma_f32_16x16x32_bf16 v[60:63], v[184:187], v[156:159], v[60:63]\n\t"
    "ds_read_b64_tr_b16 v[200:201], v241 offset:8352\n\t"
    "v_mfma_f32_16x16x32_bf16 v[60:63], v[188:191], v[172:175], v[60:63]\n\t"
    "ds_read_b64_tr_b16 v[202:203], v241 offset:11936\n\t"
    "v_mfma_f32_16x16x32_bf16 v[56:59], v[188:191], v[168:171], v[56:59]\n\t"
    "ds_read_b64_tr_b16 v[204:205], v241 offset:32928\n\t"
    "v_mfma_f32_16x16x32_bf16 v[56:59], v[184:187], v[152:155], v[56:59]\n\t"
    "ds_read_b64_tr_b16 v[206:207], v241 offset:36512\n\t"
    "v_mfma_f32_16x16x32_bf16 v[52:55], v[184:187], v[148:151], v[52:55]\n\t"
    "s_add_u32 m0, s74, 9216\n\t"
    "s_mul_i32 s56, s65, s57\n\t"
    "v_mfma_f32_16x16x32_bf16 v[52:55], v[188:191], v[164:167], v[52:55]\n\t"
    "buffer_load_dwordx4 v245, s[44:47], s56 offen sc1 lds\n\t"
    "v_mfma_f32_16x16x32_bf16 v[48:51], v[188:191], v[160:163], v[48:51]\n\t"
    "v_add_u32 v238, s78, v238\n\t"
    "v_add_u32 v239, s78, v239\n\t"
    "v_mfma_f32_16x16x32_bf16 v[48:51], v[184:187], v[144:147], v[48:51]\n\t"
    "s_waitcnt lgkmcnt(4)\n\t"
    "v_mfma_f32_16x16x32_bf16 a[144:147], v[192:195], v[144:147], a[144:147]\n\t"
    "ds_read_b64_tr_b16 v[176:177], v241 offset:8384\n\t"
    "v_mfma_f32_16x16x32_bf16 a[144:147], v[196:199], v[160:163], a[144:147]\n\t"
    "ds_read_b64_tr_b16 v[178:179], v241 offset:11968\n\t"
    "v_mfma_f32_16x16x32_bf16 a[148:151], v[196:199], v[164:167], a[148:151]\n\t"
    "ds_read_b64_tr_b16 v[180:181], v241 offset:32960\n\t"
    "v_mfma_f32_16x16x32_bf16 a[148:151], v[192:195], v[148:151], a[148:151]\n\t"
    "ds_read_b64_tr_b16 v[182:183], v241 offset:36544\n\t"
    "v_mfma_f32_16x16x32_bf16 a[152:155], v[192:195], v[152:155], a[152:155]\n\t"
    "s_add_u32 m0, s74, 16384\n\t"
    "s_mul_i32 s56, s66, s57\n\t"
    "v_mfma_f32_16x16x32_bf16 a[152:155], v[196:199], v[168:171], a[152:155]\n\t"
    "buffer_load_dwordx4 v246, s[48:51], s56 offen sc1 lds\n\t"
    "v_mfma_f32_16x16x32_bf16 a[156:159], v[196:199], v[172:175], a[156:159]\n\t"
    "v_mfma_f32_16x16x32_bf16 a[156:159], v[192:195], v[156:159], a[156:159]\n\t"
    "s_waitcnt lgkmcnt(4)\n\t"
    "v_mfma_f32_16x16x32_bf16 a[172:175], v[200:203], v[156:159], a[172:175]\n\t"
    "ds_read_b64_tr_b16 v[184:185], v241 offset:16384\n\t"
    "v_mfma_f32_16x16x32_bf16 a[172:175], v[204:207], v[172:175], a[172:175]\n\t"
    "ds_read_b64_tr_b16 v[186:187], v241 offset:19968\n\t"
    "v_mfma_f32_16x16x32_bf16 a[168:171], v[204:207], v[168:171], a[168:171]\n\t"
    "ds_read_b64_tr_b16 v[188:189], v241 offset:40960\n\t"
    "v_mfma_f32_16x16x32_bf16 a[168:171], v[200:203], v[152:155], a[168:171]\n\t"
    "ds_read_b64_tr_b16 v[190:191], v241 offset:44544\n\t"
    "v_mfma_f32_16x16x32_bf16 a[164:167], v[200:203], v[148:151], a[164:167]\n\t"
    "s_add_u32 m0, s74, 17408\n\t"
    "s_mul_i32 s56, s67, s57\n\t"
    "v_mfma_f32_16x16x32_bf16 a[164:167], v[204:207], v[164:167], a[164:167]\n\t"
    "buffer_load_dwordx4 v247, s[52:55], s56 offen sc1 lds\n\t"
    "v_mfma_f32_16x16x32_bf16 a[160:163], v[204:207], v[160:163], a[160:163]\n\t"
    "s_add_u32 s57, s57, 1\n\t"
    "v_mfma_f32_16x16x32_bf16 a[160:163], v[200:203], v[144:147], a[160:163]\n\t"
    "s_waitcnt lgkmcnt(4)\n\t"
    "v_mfma_f32_16x16x32_bf16 a[176:179], v[176:179], v[144:147], a[176:179]\n\t"
    "ds_read_b64_tr_b16 v[192:193], v241 offset:16416\n\t"
    "v_mfma_f32_16x16x32_bf16 a[176:179], v[180:183], v[160:163], a[176:179]\n\t"
    "ds_read_b64_tr_b16 v[194:195], v241 offset:20000\n\t"
    "v_mfma_f32_16x16x32_bf16 a[180:183], v[180:183], v[164:167], a[180:183]\n\t"
    "ds_read_b64_tr_b16 v[196:197], v241 offset:40992\n\t"
    "v_mfma_f32_16x16x32_bf16 a[180:183], v[176:179], v[148:151], a[180:183]\n\t"
    "ds_read_b64_tr_b16 v[198:199], v241 offset:44576\n\t"
    "v_mfma_f32_16x16x32_bf16 a[184:187], v[176:179], v[152:155], a[184:187]\n\t"
    "s_add_u32 m0, s74, 24576\n\t"
    "s_mul_i32 s56, s62, s57\n\t"
    "v_mfma_f32_16x16x32_bf16 a[184:187], v[180:183], v[168:171], a[184:187]\n\t"
    "buffer_load_dwordx4 v242, s[32:35], s56 offen sc1 lds\n\t"
    "v_mfma_f32_16x16x32_bf16 a[188:191], v[180:183], v[172:175], a[188:191]\n\t"
    "v_mfma_f32_16x16x32_bf16 a[188:191], v[176:179], v[156:159], a[188:191]\n\t"
    "s_waitcnt lgkmcnt(4)\n\t"
    "v_mfma_f32_16x16x32_bf16 v[76:79], v[184:187], v[156:159], v[76:79]\n\t"
    "ds_read_b64_tr_b16 v[200:201], v241 offset:16448\n\t"
    "v_mfma_f32_16x16x32_bf16 v[76:79], v[188:191], v[172:175], v[76:79]\n\t"
    "ds_read_b64_tr_b16 v[202:203], v241 offset:20032\n\t"
    "v_mfma_f32_16x16x32_bf16 v[72:75], v[188:191], v[168:171], v[72:75]\n\t"
    "ds_read_b64_tr_b16 v[204:205], v241 offset:41024\n\t"
    "v_mfma_f32_16x16x32_bf16 v[72:75], v[184:187], v[152:155], v[72:75]\n\t"
    "ds_read_b64_tr_b16 v[206:207], v241 offset:44608\n\t"
    "v_mfma_f32_16x16x32_bf16 v[68:71], v[184:187], v[148:151], v[68:71]\n\t"
    "s_add_u32 m0, s74, 25600\n\t"
    "s_mul_i32 s56, s63, s57\n\t"
    "v_mfma_f32_16x16x32_bf16 v[68:71], v[188:191], v[164:167], v[68:71]\n\t"
    "buffer_load_dwordx4 v243, s[36:39], s56 offen sc1 lds\n\t"
    "v_mfma_f32_16x16x32_bf16 v[64:67], v[188:191], v[160:163], v[64:67]\n\t"
    "v_mfma_f32_16x16x32_bf16 v[64:67], v[184:187], v[144:147], v[64:67]\n\t"
    "s_waitcnt lgkmcnt(4)\n\t"
    "v_mfma_f32_16x16x32_bf16 v[80:83], v[192:195], v[144:147], v[80:83]\n\t"
    "ds_read_b64_tr_b16 v[176:177], v241 offset:16480\n\t"
    "v_mfma_f32_16x16x32_bf16 v[80:83], v[196:199], v[160:163], v[80:83]\n\t"
    "ds_read_b64_tr_b16 v[178:179], v241 offset:20064\n\t"
    "v_mfma_f32_16x16x32_bf16 v[84:87], v[196:199], v[164:167], v[84:87]\n\t"
    "ds_read_b64_tr_b16 v[180:181], v241 offset:41056\n\t"
    "v_mfma_f32_16x16x32_bf16 v[84:87], v[192:195], v[148:151], v[84:87]\n\t"
    "ds_read_b64_tr_b16 v[182:183], v241 offset:44640\n\t"
    "v_mfma_f32_16x16x32_bf16 v[88:91], v[192:195], v[152:155], v[88:91]\n\t"
    "s_add_u32 m0, s74, 32768\n\t"
    "s_mul_i32 s56, s64, s57\n\t"
    "v_mfma_f32_16x16x32_bf16 v[88:91], v[196:199], v[168:171], v[88:91]\n\t"
    "buffer_load_dwordx4 v244, s[40:43], s56 offen sc1 lds\n\t"
    "v_mfma_f32_16x16x32_bf16 v[92:95], v[196:199], v[172:175], v[92:95]\n\t"
    "v_mfma_f32_16x16x32_bf16 v[92:95], v[192:195], v[156:159], v[92:95]\n\t"
    "s_waitcnt lgkmcnt(4)\n\t"
    "v_mfma_f32_16x16x32_bf16 v[108:111], v[200:203], v[156:159], v[108:111]\n\t"
    "ds_read_b64_tr_b16 v[184:185], v241 offset:16512\n\t"
    "v_mfma_f32_16x16x32_bf16 v[108:111], v[204:207], v[172:175], v[108:111]\n\t"
    "ds_read_b64_tr_b16 v[186:187], v241 offset:20096\n\t"
    "v_mfma_f32_16x16x32_bf16 v[104:107], v[204:207], v[168:171], v[104:107]\n\t"
    "ds_read_b64_tr_b16 v[188:189], v241 offset:41088\n\t"
    "v_mfma_f32_16x16x32_bf16 v[104:107], v[200:203], v[152:155], v[104:107]\n\t"
    "ds_read_b64_tr_b16 v[190:191], v241 offset:44672\n\t"
    "v_mfma_f32_16x16x32_bf16 v[100:103], v[200:203], v[148:151], v[100:103]\n\t"
    "s_add_u32 m0, s74, 33792\n\t"
    "s_mul_i32 s56, s65, s57\n\t"
    "v_mfma_f32_16x16x32_bf16 v[100:103], v[204:207], v[164:167], v[100:103]\n\t"
    "buffer_load_dwordx4 v245, s[44:47], s56 offen sc1 lds\n\t"
    "v_mfma_f32_16x16x32_bf16 v[96:99], v[204:207], v[160:163], v[96:99]\n\t"
    "v_mfma_f32_16x16x32_bf16 v[96:99], v[200:203], v[144:147], v[96:99]\n\t"
    "s_waitcnt lgkmcnt(4)\n\t"
    "v_mfma_f32_16x16x32_bf16 a[128:131], v[176:179], v[144:147], a[128:131]\n\t"
    "ds_read_b64_tr_b16 v[192:193], v241 offset:16544\n\t"
    "v_mfma_f32_16x16x32_bf16 a[128:131], v[180:183], v[160:163], a[128:131]\n\t"
    "ds_read_b64_tr_b16 v[194:195], v241 offset:20128\n\t"
    "v_mfma_f32_16x16x32_bf16 a[132:135], v[180:183], v[164:167], a[132:135]\n\t"
    "ds_read_b64_tr_b16 v[196:197], v241 offset:41120\n\t"
    "v_mfma_f32_16x16x32_bf16 a[132:135], v[176:179], v[148:151], a[132:135]\n\t"
    "ds_read_b64_tr_b16 v[198:199], v241 offset:44704\n\t"
    "v_mfma_f32_16x16x32_bf16 a[136:139], v[176:179], v[152:155], a[136:139]\n\t"
    "s_add_u32 m0, s74, 40960\n\t"
    "s_mul_i32 s56, s66, s57\n\t"
    "v_mfma_f32_16x16x32_bf16 a[136:139], v[180:183], v[168:171], a[136:139]\n\t"
    "buffer_load_dwordx4 v246, s[48:51], s56 offen sc1 lds\n\t"
    "v_mfma_f32_16x16x32_bf16 a[140:143], v[180:183], v[172:175], a[140:143]\n\t"
    "v_mfma_f32_16x16x32_bf16 a[140:143], v[176:179], v[156:159], a[140:143]\n\t"
    "s_waitcnt lgkmcnt(4)\n\t"
    "v_mfma_f32_16x16x32_bf16 a[204:207], v[184:187], v[156:159], a[204:207]\n\t"
    "ds_read_b64_tr_b16 v[200:201], v241 offset:16576\n\t"
    "v_mfma_f32_16x16x32_bf16 a[204:207], v[188:191], v[172:175], a[204:207]\n\t"
    "ds_read_b64_tr_b16 v[202:203], v241 offset:20160\n\t"
    "v_mfma_f32_16x16x32_bf16 a[200:203], v[188:191], v[168:171], a[200:203]\n\t"
    "ds_read_b64_tr_b16 v[204:205], v241 offset:41152\n\t"
    "v_mfma_f32_16x16x32_bf16 a[200:203], v[184:187], v[152:155], a[200:203]\n\t"
    "ds_read_b64_tr_b16 v[206:207], v241 offset:44736\n\t"
    "v_mfma_f32_16x16x32_bf16 a[196:199], v[184:187], v[148:151], a[196:199]\n\t"
    "s_add_u32 m0, s74, 41984\n\t"
    "s_mul_i32 s56, s67, s57\n\t"
    "v_mfma_f32_16x16x32_bf16 a[196:199], v[188:191], v[164:167], a[196:199]\n\t"
    "buffer_load_dwordx4 v247, s[52:55], s56 offen sc1 lds\n\t"
    "v_mfma_f32_16x16x32_bf16 a[192:195], v[188:191], v[160:163], a[192:195]\n\t"
    "v_add_u32 v241, s78, v241\n\t"
    "v_mfma_f32_16x16x32_bf16 a[192:195], v[184:187], v[144:147], a[192:195]\n\t"
    "s_waitcnt lgkmcnt(4)\n\t"
    "v_mfma_f32_16x16x32_bf16 a[208:211], v[192:195], v[144:147], a[208:211]\n\t"
    "s_add_u32 s74, s73, s75\n\t"
    "s_mov_b32 s75, s77\n\t"
    "v_mfma_f32_16x16x32_bf16 a[208:211], v[196:199], v[160:163], a[208:211]\n\t"
    "v_mfma_f32_16x16x32_bf16 a[212:215], v[196:199], v[164:167], a[212:215]\n\t"
    "s_waitcnt vmcnt(12) lgkmcnt(0)\n\t"
    "s_barrier\n\t"
    "v_mfma_f32_16x16x32_bf16 a[212:215], v[192:195], v[148:151], a[212:215]\n\t"
    "ds_read_b64_tr_b16 v[176:177], v232 offset:0\n\t"
    "v_mfma_f32_16x16x32_bf16 a[216:219], v[192:195], v[152:155], a[216:219]\n\t"
    "ds_read_b64_tr_b16 v[178:179], v232 offset:4096\n\t"
    "v_mfma_f32_16x16x32_bf16 a[216:219], v[196:199], v[168:171], a[216:219]\n\t"
    "ds_read_b64_tr_b16 v[180:181], v232 offset:24576\n\t"
    "v_mfma_f32_16x16x32_bf16 a[220:223], v[196:199], v[172:175], a[220:223]\n\t"
    "ds_read_b64_tr_b16 v[182:183], v232 offset:28672\n\t"
    "v_mfma_f32_16x16x32_bf16 a[220:223], v[192:195], v[156:159], a[220:223]\n\t"
    "v_mfma_f32_16x16x32_bf16 a[236:239], v[200:203], v[156:159], a[236:239]\n\t"
    "ds_read_b64_tr_b16 v[184:185], v233 offset:0\n\t"
    "v_mfma_f32_16x16x32_bf16 a[236:239], v[204:207], v[172:175], a[236:239]\n\t"
    "ds_read_b64_tr_b16 v[186:187], v233 offset:4096\n\t"
    "v_mfma_f32_16x16x32_bf16 a[232:235], v[204:207], v[168:171], a[232:235]\n\t"
    "ds_read_b64_tr_b16 v[188:189], v233 offset:24576\n\t"
    "v_mfma_f32_16x16x32_bf16 a[232:235], v[200:203], v[152:155], a[232:235]\n\t"
    "ds_read_b64_tr_b16 v[190:191], v233 offset:28672\n\t"
    "v_mfma_f32_16x16x32_bf16 a[228:231], v[200:203], v[148:151], a[228:231]\n\t"
    "v_mfma_f32_16x16x32_bf16 a[228:231], v[204:207], v[164:167], a[228:231]\n\t"
    "v_mfma_f32_16x16x32_bf16 a[224:227], v[204:207], v[160:163], a[224:227]\n\t"
    "v_mfma_f32_16x16x32_bf16 a[224:227], v[200:203], v[144:147], a[224:227]\n\t"
    "s_waitcnt lgkmcnt(4)\n\t"
    "s_sub_u32 s76, s76, 1\n\t"
    "s_bitcmp1_b32 120, s76\n\t"
    "s_cbranch_scc1 1b\n\t"
    "s_cmp_lg_u32 s76, 7\n\t"
    "s_cbranch_scc1 37f\n\t"
    "buffer_load_dword v228, v219, s[88:91], 0 offen sc1\n\t"
    "s_branch 1b\n\t"
    "37:\n\t"
    "s_cmp_lg_u32 s76, 2\n\t"
    "s_cbranch_scc1 32f\n\t"
    "buffer_load_dword v230, v231, s[88:91], 0 offen sc1\n\t"
    "s_getpc_b64 s[28:29]\n\t"
    ".Lfb_ct0:\n\t"
    "s_lshr_b32 s77, s2, 3\n\t"
    "s_lshl_b32 s77, s77, 7\n\t"
    "s_lshl_b32 s78, s3, 12\n\t"
    "s_add_u32 s77, s77, s78\n\t"
    "s_add_u32 s77, s77, .Lfb_mid0-.Lfb_ct0+0\n\t"
    "s_min_u32 s77, s77, .Lfb_end-.Lfb_ct0\n\t"
    "v_mov_b32 v229, s77\n\t"
    "global_load_dword v229, v229, s[28:29]\n\t"
    "s_branch 1b\n\t"
    "32:\n\t"
    ".Lfb_mid0:\n\t"
    "v_mfma_f32_16x16x32_bf16 a[0:3], v[176:179], v[112:115], a[0:3]\n\t"
    "ds_read_b64_tr_b16 v[192:193], v234 offset:0\n\t"
    "v_cmp_ne_u32 vcc, s20, v230\n\t"
    "v_mfma_f32_16x16x32_bf16 a[0:3], v[180:183], v[128:131], a[0:3]\n\t"
    "ds_read_b64_tr_b16 v[194:195], v234 offset:4096\n\t"
    "v_add_u32 v250, 65536, v248\n\t"
    "v_mfma_f32_16x16x32_bf16 a[4:7], v[180:183], v[132:135], a[4:7]\n\t"
    "ds_read_b64_tr_b16 v[196:197], v234 offset:24576\n\t"
    "s_cbranch_vccnz .Lfb_wk\n\t"
    ".Lfb_wk_r:\n\t"
    "v_mfma_f32_16x16x32_bf16 a[4:7], v[176:179], v[116:119], a[4:7]\n\t"
    "ds_read_b64_tr_b16 v[198:199], v234 offset:28672\n\t"
    "s_sub_u32 s84, s85, 16384\n\t"
    "v_mfma_f32_16x16x32_bf16 a[8:11], v[176:179], v[120:123], a[8:11]\n\t"
    "buffer_load_dwordx4 v[144:147], v248, s[68:71], s72 offen sc1\n\t"
    "v_mfma_f32_16x16x32_bf16 a[8:11], v[180:183], v[136:139], a[8:11]\n\t"
    "v_add_u32 v251, 131072, v248\n\t"
    "v_mfma_f32_16x16x32_bf16 a[12:15], v[180:183], v[140:143], a[12:15]\n\t"
    "buffer_load_dwordx4 v[160:163], v249, s[68:71], s72 offen sc1\n\t"
    "v_mfma_f32_16x16x32_bf16 a[12:15], v[176:179], v[124:127], a[12:15]\n\t"
    "s_waitcnt lgkmcnt(4)\n\t"
    "v_mfma_f32_16x16x32_bf16 a[28:31], v[184:187], v[124:127], a[28:31]\n\t"
    "ds_read_b64_tr_b16 v[200:201], v235 offset:0\n\t"
    "v_mfma_f32_16x16x32_bf16 a[28:31], v[188:191], v[140:143], a[28:31]\n\t"
    "ds_read_b64_tr_b16 v[202:203], v235 offset:4096\n\t"
    "v_mfma_f32_16x16x32_bf16 a[24:27], v[188:191], v[136:139], a[24:27]\n\t"
    "ds_read_b64_tr_b16 v[204:205], v235 offset:24576\n\t"
    "v_mfma_f32_16x16x32_bf16 a[24:27], v[184:187], v[120:123], a[24:27]\n\t"
    "ds_read_b64_tr_b16 v[206:207], v235 offset:28672\n\t"
    "v_mfma_f32_16x16x32_bf16 a[20:23], v[184:187], v[116:119], a[20:23]\n\t"
    "buffer_load_dwordx4 v[148:151], v248, s[68:71], s72 offen offset:1024 sc1\n\t"
    "v_mfma_f32_16x16x32_bf16 a[20:23], v[188:191], v[132:135], a[20:23]\n\t"
    "v_mfma_f32_16x16x32_bf16 a[16:19], v[188:191], v[128:131], a[16:19]\n\t"
    "buffer_load_dwordx4 v[164:167], v249, s[68:71], s72 offen offset:1024 sc1\n\t"
    "v_mfma_f32_16x16x32_bf16 a[16:19], v[184:187], v[112:115], a[16:19]\n\t"
    "s_waitcnt lgkmcnt(4)\n\t"
    "v_mfma_f32_16x16x32_bf16 a[32:35], v[192:195], v[112:115], a[32:35]\n\t"
    "ds_read_b64_tr_b16 v[176:177], v236 offset:0\n\t"
    "v_mfma_f32_16x16x32_bf16 a[32:35], v[196:199], v[128:131], a[32:35]\n\t"
    "ds_read_b64_tr_b16 v[178:179], v236 offset:4096\n\t"
    "v_mfma_f32_16x16x32_bf16 a[36:39], v[196:199], v[132:135], a[36:39]\n\t"
    "ds_read_b64_tr_b16 v[180:181], v236 offset:24576\n\t"
    "v_mfma_f32_16x16x32_bf16 a[36:39], v[192:195], v[116:119], a[36:39]\n\t"
    "ds_read_b64_tr_b16 v[182:183], v236 offset:28672\n\t"
    "v_mfma_f32_16x16x32_bf16 a[40:43], v[192:195], v[120:123], a[40:43]\n\t"
    "buffer_load_dwordx4 v[152:155], v248, s[68:71], s72 offen offset:2048 sc1\n\t"
    "v_mfma_f32_16x16x32_bf16 a[40:43], v[196:199], v[136:139], a[40:43]\n\t"
    "v_mfma_f32_16x16x32_bf16 a[44:47], v[196:199], v[140:143], a[44:47]\n\t"
    "buffer_load_dwordx4 v[168:171], v249, s[68:71], s72 offen offset:2048 sc1\n\t"
    "v_mfma_f32_16x16x32_bf16 a[44:47], v[192:195], v[124:127], a[44:47]\n\t"
    "s_waitcnt lgkmcnt(4)\n\t"
    "v_mfma_f32_16x16x32_bf16 a[60:63], v[200:203], v[124:127], a[60:63]\n\t"
    "ds_read_b64_tr_b16 v[184:185], v237 offset:0\n\t"
    "v_mfma_f32_16x16x32_bf16 a[60:63], v[204:207], v[140:143], a[60:63]\n\t"
    "ds_read_b64_tr_b16 v[186:187], v237 offset:4096\n\t"
    "v_mfma_f32_16x16x32_bf16 a[56:59], v[204:207], v[136:139], a[56:59]\n\t"
    "ds_read_b64_tr_b16 v[188:189], v237 offset:24576\n\t"
    "v_mfma_f32_16x16x32_bf16 a[56:59], v[200:203], v[120:123], a[56:59]\n\t"
    "ds_read_b64_tr_b16 v[190:191], v237 offset:28672\n\t"
    "v_mfma_f32_16x16x32_bf16 a[52:55], v[200:203], v[116:119], a[52:55]\n\t"
    "buffer_load_dwordx4 v[156:159], v248, s[68:71], s72 offen offset:3072 sc1\n\t"
    "v_mfma_f32_16x16x32_bf16 a[52:55], v[204:207], v[132:135], a[52:55]\n\t"
    "v_mfma_f32_16x16x32_bf16 a[48:51], v[204:207], v[128:131], a[48:51]\n\t"
    "buffer_load_dwordx4 v[172:175], v249, s[68:71], s72 offen offset:3072 sc1\n\t"
    "v_mfma_f32_16x16x32_bf16 a[48:51], v[200:203], v[112:115], a[48:51]\n\t"
    "s_waitcnt lgkmcnt(4)\n\t"
    "v_mfma_f32_16x16x32_bf16 a[64:67], v[176:179], v[112:115], a[64:67]\n\t"
    "ds_read_b64_tr_b16 v[192:193], v238 offset:0\n\t"
    "s_add_u32 m0, s85, 32768\n\t"
    "v_mfma_f32_16x16x32_bf16 a[64:67], v[180:183], v[128:131], a[64:67]\n\t"
    "ds_read_b64_tr_b16 v[194:195], v238 offset:4096\n\t"
    "v_mfma_f32_16x16x32_bf16 a[68:71], v[180:183], v[132:135], a[68:71]\n\t"
    "ds_read_b64_tr_b16 v[196:197], v238 offset:24576\n\t"
    "v_mfma_f32_16x16x32_bf16 a[68:71], v[176:179], v[116:119], a[68:71]\n\t"
    "ds_read_b64_tr_b16 v[198:199], v238 offset:28672\n\t"
    "v_mfma_f32_16x16x32_bf16 a[72:75], v[176:179], v[120:123], a[72:75]\n\t"
    "buffer_load_dwordx4 v248, s[80:83], s84 offen sc1 lds\n\t"
    "v_mfma_f32_16x16x32_bf16 a[72:75], v[180:183], v[136:139], a[72:75]\n\t"
    "v_mfma_f32_16x16x32_bf16 a[76:79], v[180:183], v[140:143], a[76:79]\n\t"
    "v_mfma_f32_16x16x32_bf16 a[76:79], v[176:179], v[124:127], a[76:79]\n\t"
    "s_waitcnt lgkmcnt(4)\n\t"
    "v_mfma_f32_16x16x32_bf16 a[92:95], v[184:187], v[124:127], a[92:95]\n\t"
    "ds_read_b64_tr_b16 v[200:201], v239 offset:0\n\t"
    "v_mfma_f32_16x16x32_bf16 a[92:95], v[188:191], v[140:143], a[92:95]\n\t"
    "ds_read_b64_tr_b16 v[202:203], v239 offset:4096\n\t"
    "v_mfma_f32_16x16x32_bf16 a[88:91], v[188:191], v[136:139], a[88:91]\n\t"
    "ds_read_b64_tr_b16 v[204:205], v239 offset:24576\n\t"
    "v_mfma_f32_16x16x32_bf16 a[88:91], v[184:187], v[120:123], a[88:91]\n\t"
    "ds_read_b64_tr_b16 v[206:207], v239 offset:28672\n\t"
    "v_mfma_f32_16x16x32_bf16 a[84:87], v[184:187], v[116:119], a[84:87]\n\t"
    "buffer_load_dwordx4 v248, s[80:83], s84 offen offset:1024 sc1 lds\n\t"
    "v_mfma_f32_16x16x32_bf16 a[84:87], v[188:191], v[132:135], a[84:87]\n\t"
    "v_mfma_f32_16x16x32_bf16 a[80:83], v[188:191], v[128:131], a[80:83]\n\t"
    "v_mfma_f32_16x16x32_bf16 a[80:83], v[184:187], v[112:115], a[80:83]\n\t"
    "s_waitcnt lgkmcnt(4)\n\t"
    "v_mfma_f32_16x16x32_bf16 a[96:99], v[192:195], v[112:115], a[96:99]\n\t"
    "ds_read_b64_tr_b16 v[176:177], v241 offset:8192\n\t"
    "v_mfma_f32_16x16x32_bf16 a[96:99], v[196:199], v[128:131], a[96:99]\n\t"
    "ds_read_b64_tr_b16 v[178:179], v241 offset:11776\n\t"
    "v_mfma_f32_16x16x32_bf16 a[100:103], v[196:199], v[132:135], a[100:103]\n\t"
    "ds_read_b64_tr_b16 v[180:181], v241 offset:32768\n\t"
    "v_mfma_f32_16x16x32_bf16 a[100:103], v[192:195], v[116:119], a[100:103]\n\t"
    "ds_read_b64_tr_b16 v[182:183], v241 offset:36352\n\t"
    "v_mfma_f32_16x16x32_bf16 a[104:107], v[192:195], v[120:123], a[104:107]\n\t"
    "v_add_u32 v232, 4294868992, v232\n\t"
    "v_add_u32 v233, 4294868992, v233\n\t"
    "v_mfma_f32_16x16x32_bf16 a[104:107], v[196:199], v[136:139], a[104:107]\n\t"
    "v_add_u32 v234, 4294868992, v234\n\t"
    "v_add_u32 v235, 4294868992, v235\n\t"
    "v_mfma_f32_16x16x32_bf16 a[108:111], v[196:199], v[140:143], a[108:111]\n\t"
    "buffer_load_dwordx4 v248, s[80:83], s84 offen offset:2048 sc1 lds\n\t"
    "v_mfma_f32_16x16x32_bf16 a[108:111], v[192:195], v[124:127], a[108:111]\n\t"
    "s_waitcnt lgkmcnt(4)\n\t"
    "v_mfma_f32_16x16x32_bf16 a[124:127], v[200:203], v[124:127], a[124:127]\n\t"
    "ds_read_b64_tr_b16 v[184:185], v241 offset:8224\n\t"
    "v_mfma_f32_16x16x32_bf16 a[124:127], v[204:207], v[140:143], a[124:127]\n\t"
    "ds_read_b64_tr_b16 v[186:187], v241 offset:11808\n\t"
    "v_mfma_f32_16x16x32_bf16 a[120:123], v[204:207], v[136:139], a[120:123]\n\t"
    "ds_read_b64_tr_b16 v[188:189], v241 offset:32800\n\t"
    "v_mfma_f32_16x16x32_bf16 a[120:123], v[200:203], v[120:123], a[120:123]\n\t"
    "ds_read_b64_tr_b16 v[190:191], v241 offset:36384\n\t"
    "v_mfma_f32_16x16x32_bf16 a[116:119], v[200:203], v[116:119], a[116:119]\n\t"
    "v_add_u32 v236, 4294868992, v236\n\t"
    "v_add_u32 v237, 4294868992, v237\n\t"
    "v_mfma_f32_16x16x32_bf16 a[116:119], v[204:207], v[132:135], a[116:119]\n\t"
    "v_add_u32 v238, 4294868992, v238\n\t"
    "v_add_u32 v239, 4294868992, v239\n\t"
    "v_mfma_f32_16x16x32_bf16 a[112:115], v[204:207], v[128:131], a[112:115]\n\t"
    "buffer_load_dwordx4 v248, s[80:83], s84 offen offset:3072 sc1 lds\n\t"
    "v_mfma_f32_16x16x32_bf16 a[112:115], v[200:203], v[112:115], a[112:115]\n\t"
    "s_waitcnt lgkmcnt(4)\n\t"
    "s_add_u32 s84, s84, 16384\n\t"
    "s_add_u32 m0, s85, 49152\n\t"
    "v_mfma_f32_16x16x32_bf16 v[0:3], v[176:179], v[112:115], v[0:3]\n\t"
    "ds_read_b64_tr_b16 v[192:193], v241 offset:8256\n\t"
    "v_mfma_f32_16x16x32_bf16 v[0:3], v[180:183], v[128:131], v[0:3]\n\t"
    "ds_read_b64_tr_b16 v[194:195], v241 offset:11840\n\t"
    "v_mfma_f32_16x16x32_bf16 v[4:7], v[180:183], v[132:135], v[4:7]\n\t"
    "ds_read_b64_tr_b16 v[196:197], v241 offset:32832\n\t"
    "v_mfma_f32_16x16x32_bf16 v[4:7], v[176:179], v[116:119], v[4:7]\n\t"
    "ds_read_b64_tr_b16 v[198:199], v241 offset:36416\n\t"
    "v_mfma_f32_16x16x32_bf16 v[8:11], v[176:179], v[120:123], v[8:11]\n\t"
    "buffer_load_dwordx4 v248, s[80:83], s84 offen sc1 lds\n\t"
    "v_mfma_f32_16x16x32_bf16 v[8:11], v[180:183], v[136:139], v[8:11]\n\t"
    "v_mfma_f32_16x16x32_bf16 v[12:15], v[180:183], v[140:143], v[12:15]\n\t"
    "s_waitcnt vmcnt(13)\n\t"
    "s_barrier\n\t"
    "v_mfma_f32_16x16x32_bf16 v[12:15], v[176:179], v[124:127], v[12:15]\n\t"
    "s_waitcnt lgkmcnt(4)\n\t"
    "v_mfma_f32_16x16x32_bf16 v[28:31], v[184:187], v[124:127], v[28:31]\n\t"
    "ds_read_b64_tr_b16 v[200:201], v232 offset:0\n\t"
    "v_mfma_f32_16x16x32_bf16 v[28:31], v[188:191], v[140:143], v[28:31]\n\t"
    "ds_read_b64_tr_b16 v[202:203], v232 offset:4096\n\t"
    "v_mfma_f32_16x16x32_bf16 v[24:27], v[188:191], v[136:139], v[24:27]\n\t"
    "ds_read_b64_tr_b16 v[204:205], v232 offset:24576\n\t"
    "v_mfma_f32_16x16x32_bf16 v[24:27], v[184:187], v[120:123], v[24:27]\n\t"
    "ds_read_b64_tr_b16 v[206:207], v232 offset:28672\n\t"
    "v_mfma_f32_16x16x32_bf16 v[20:23], v[184:187], v[116:119], v[20:23]\n\t"
    "buffer_load_dwordx4 v248, s[80:83], s84 offen offset:1024 sc1 lds\n\t"
    "v_mfma_f32_16x16x32_bf16 v[20:23], v[188:191], v[132:135], v[20:23]\n\t"
    "v_mfma_f32_16x16x32_bf16 v[16:19], v[188:191], v[128:131], v[16:19]\n\t"
    "v_mfma_f32_16x16x32_bf16 v[16:19], v[184:187], v[112:115], v[16:19]\n\t"
    "s_waitcnt lgkmcnt(4)\n\t"
    "v_mfma_f32_16x16x32_bf16 v[32:35], v[192:195], v[112:115], v[32:35]\n\t"
    "ds_read_b64_tr_b16 v[176:177], v233 offset:0\n\t"
    "v_mfma_f32_16x16x32_bf16 v[32:35], v[196:199], v[128:131], v[32:35]\n\t"
    "ds_read_b64_tr_b16 v[178:179], v233 offset:4096\n\t"
    "v_mfma_f32_16x16x32_bf16 v[36:39], v[196:199], v[132:135], v[36:39]\n\t"
    "ds_read_b64_tr_b16 v[180:181], v233 offset:24576\n\t"
    "v_mfma_f32_16x16x32_bf16 v[36:39], v[192:195], v[116:119], v[36:39]\n\t"
    "ds_read_b64_tr_b16 v[182:183], v233 offset:28672\n\t"
    "v_mfma_f32_16x16x32_bf16 v[40:43], v[192:195], v[120:123], v[40:43]\n\t"
    "buffer_load_dwordx4 v248, s[80:83], s84 offen offset:2048 sc1 lds\n\t"
    "v_mfma_f32_16x16x32_bf16 v[40:43], v[196:199], v[136:139], v[40:43]\n\t"
    "v_mfma_f32_16x16x32_bf16 v[44:47], v[196:199], v[140:143], v[44:47]\n\t"
    "v_mfma_f32_16x16x32_bf16 v[44:47], v[192:195], v[124:127], v[44:47]\n\t"
    "s_waitcnt vmcnt(7) lgkmcnt(4)\n\t"
    "v_mfma_f32_16x16x32_bf16 a[12:15], v[200:203], v[156:159], a[12:15]\n\t"
    "ds_read_b64_tr_b16 v[184:185], v234 offset:0\n\t"
    "s_mul_i32 s10, s3, 2816\n\t"
    "v_mfma_f32_16x16x32_bf16 a[12:15], v[204:207], v[172:175], a[12:15]\n\t"
    "ds_read_b64_tr_b16 v[186:187], v234 offset:4096\n\t"
    "s_add_u32 s10, s10, 0\n\t"
    "v_mfma_f32_16x16x32_bf16 a[8:11], v[204:207], v[168:171], a[8:11]\n\t"
    "ds_read_b64_tr_b16 v[188:189], v234 offset:24576\n\t"
    "s_min_u32 s10, s10, s26\n\t"
    "v_mfma_f32_16x16x32_bf16 a[8:11], v[200:203], v[152:155], a[8:11]\n\t"
    "ds_read_b64_tr_b16 v[190:191], v234 offset:28672\n\t"
    "v_min_u32 v226, 336, v248\n\t"
    "v_mfma_f32_16x16x32_bf16 a[4:7], v[200:203], v[148:151], a[4:7]\n\t"
    "buffer_load_dwordx4 v248, s[80:83], s84 offen offset:3072 sc1 lds\n\t"
    "v_mfma_f32_16x16x32_bf16 a[4:7], v[204:207], v[164:167], a[4:7]\n\t"
    "s_add_u32 s84, s84, 16384\n\t"
    "s_add_u32 m0, s85, 65536\n\t"
    "v_lshlrev_b32 v226, 3, v226\n\t"
    "v_mfma_f32_16x16x32_bf16 a[0:3], v[204:207], v[160:163], a[0:3]\n\t"
    "buffer_load_dwordx4 v248, s[80:83], s84 offen sc1 lds\n\t"
    "v_mfma_f32_16x16x32_bf16 a[0:3], v[200:203], v[144:147], a[0:3]\n\t"
    "s_waitcnt lgkmcnt(4)\n\t"
    "buffer_load_dword a244, v226, s[24:27], s10 offen\n\t"
    "v_mfma_f32_16x16x32_bf16 a[16:19], v[176:179], v[144:147], a[16:19]\n\t"
    "ds_read_b64_tr_b16 v[192:193], v235 offset:0\n\t"
    "v_mfma_f32_16x16x32_bf16 a[16:19], v[180:183], v[160:163], a[16:19]\n\t"
    "ds_read_b64_tr_b16 v[194:195], v235 offset:4096\n\t"
    "v_mfma_f32_16x16x32_bf16 a[20:23], v[180:183], v[164:167], a[20:23]\n\t"
    "ds_read_b64_tr_b16 v[196:197], v235 offset:24576\n\t"
    "v_mfma_f32_16x16x32_bf16 a[20:23], v[176:179], v[148:151], a[20:23]\n\t"
    "ds_read_b64_tr_b16 v[198:199], v235 offset:28672\n\t"
    "v_mfma_f32_16x16x32_bf16 a[24:27], v[176:179], v[152:155], a[24:27]\n\t"
    "buffer_load_dwordx4 v248, s[80:83], s84 offen offset:1024 sc1 lds\n\t"
    "v_mfma_f32_16x16x32_bf16 a[24:27], v[180:183], v[168:171], a[24:27]\n\t"
    "v_mfma_f32_16x16x32_bf16 a[28:31], v[180:183], v[172:175], a[28:31]\n\t"
    "v_mfma_f32_16x16x32_bf16 a[28:31], v[176:179], v[156:159], a[28:31]\n\t"
    "s_waitcnt lgkmcnt(4)\n\t"
    "v_mfma_f32_16x16x32_bf16 a[44:47], v[184:187], v[156:159], a[44:47]\n\t"
    "ds_read_b64_tr_b16 v[200:201], v236 offset:0\n\t"
    "v_mfma_f32_16x16x32_bf16 a[44:47], v[188:191], v[172:175], a[44:47]\n\t"
    "ds_read_b64_tr_b16 v[202:203], v236 offset:4096\n\t"
    "v_mfma_f32_16x16x32_bf16 a[40:43], v[188:191], v[168:171], a[40:43]\n\t"
    "ds_read_b64_tr_b16 v[204:205], v236 offset:24576\n\t"
    "v_mfma_f32_16x16x32_bf16 a[40:43], v[184:187], v[152:155], a[40:43]\n\t"
    "ds_read_b64_tr_b16 v[206:207], v236 offset:28672\n\t"
    "v_mfma_f32_16x16x32_bf16 a[36:39], v[184:187], v[148:151], a[36:39]\n\t"
    "buffer_load_dwordx4 v248, s[80:83], s84 offen offset:2048 sc1 lds\n\t"
    "v_mfma_f32_16x16x32_bf16 a[36:39], v[188:191], v[164:167], a[36:39]\n\t"
    "v_accvgpr_read_b32 v208, a0\n\t"
    "v_accvgpr_read_b32 v209, a1\n\t"
    "v_mfma_f32_16x16x32_bf16 a[32:35], v[188:191], v[160:163], a[32:35]\n\t"
    "v_accvgpr_read_b32 v210, a2\n\t"
    "v_accvgpr_read_b32 v211, a3\n\t"
    "v_mfma_f32_16x16x32_bf16 a[32:35], v[184:187], v[144:147], a[32:35]\n\t"
    "s_waitcnt lgkmcnt(4)\n\t"
    "v_accvgpr_read_b32 v212, a16\n\t"
    "v_accvgpr_read_b32 v213, a17\n\t"
    "v_mfma_f32_16x16x32_bf16 a[48:51], v[192:195], v[144:147], a[48:51]\n\t"
    "ds_read_b64_tr_b16 v[176:177], v237 offset:0\n\t"
    "v_accvgpr_read_b32 v214, a18\n\t"
    "v_mfma_f32_16x16x32_bf16 a[48:51], v[196:199], v[160:163], a[48:51]\n\t"
    "ds_read_b64_tr_b16 v[178:179], v237 offset:4096\n\t"
    "v_accvgpr_read_b32 v215, a19\n\t"
    "v_mfma_f32_16x16x32_bf16 a[52:55], v[196:199], v[164:167], a[52:55]\n\t"
    "ds_read_b64_tr_b16 v[180:181], v237 offset:24576\n\t"
    "v_cvt_pk_bf16_f32 v208, v208, v209\n\t"
    "v_mfma_f32_16x16x32_bf16 a[52:55], v[192:195], v[148:151], a[52:55]\n\t"
    "ds_read_b64_tr_b16 v[182:183], v237 offset:28672\n\t"
    "v_cvt_pk_bf16_f32 v209, v210, v211\n\t"
    "v_mfma_f32_16x16x32_bf16 a[56:59], v[192:195], v[152:155], a[56:59]\n\t"
    "buffer_load_dwordx4 v248, s[80:83], s84 offen offset:3072 sc1 lds\n\t"
    "v_mfma_f32_16x16x32_bf16 a[56:59], v[196:199], v[168:171], a[56:59]\n\t"
    "s_add_u32 s84, s84, 16384\n\t"
    "v_cvt_pk_bf16_f32 v210, v212, v213\n\t"
    "v_cvt_pk_bf16_f32 v211, v214, v215\n\t"
    "v_mfma_f32_16x16x32_bf16 a[60:63], v[196:199], v[172:175], a[60:63]\n\t"
    "v_accvgpr_write_b32 a0, v208\n\t"
    "v_accvgpr_write_b32 a1, v209\n\t"
    "v_mfma_f32_16x16x32_bf16 a[60:63], v[192:195], v[156:159], a[60:63]\n\t"
    "s_waitcnt lgkmcnt(4)\n\t"
    "v_accvgpr_write_b32 a2, v210\n\t"
    "v_accvgpr_write_b32 a3, v211\n\t"
    "v_mfma_f32_16x16x32_bf16 a[76:79], v[200:203], v[156:159], a[76:79]\n\t"
    "ds_read_b64_tr_b16 v[184:185], v238 offset:0\n\t"
    "v_mfma_f32_16x16x32_bf16 a[76:79], v[204:207], v[172:175], a[76:79]\n\t"
    "ds_read_b64_tr_b16 v[186:187], v238 offset:4096\n\t"
    "v_mfma_f32_16x16x32_bf16 a[72:75], v[204:207], v[168:171], a[72:75]\n\t"
    "ds_read_b64_tr_b16 v[188:189], v238 offset:24576\n\t"
    "v_mfma_f32_16x16x32_bf16 a[72:75], v[200:203], v[152:155], a[72:75]\n\t"
    "ds_read_b64_tr_b16 v[190:191], v238 offset:28672\n\t"
    "v_mfma_f32_16x16x32_bf16 a[68:71], v[200:203], v[148:151], a[68:71]\n\t"
    "v_mfma_f32_16x16x32_bf16 a[68:71], v[204:207], v[164:167], a[68:71]\n\t"
    "v_accvgpr_read_b32 v216, a32\n\t"
    "v_accvgpr_read_b32 v217, a33\n\t"
    "v_mfma_f32_16x16x32_bf16 a[64:67], v[204:207], v[160:163], a[64:67]\n\t"
    "v_accvgpr_read_b32 v218, a34\n\t"
    "v_accvgpr_read_b32 v219, a35\n\t"
    "v_mfma_f32_16x16x32_bf16 a[64:67], v[200:203], v[144:147], a[64:67]\n\t"
    "s_waitcnt lgkmcnt(4)\n\t"
    "v_accvgpr_read_b32 v220, a48\n\t"
    "v_accvgpr_read_b32 v221, a49\n\t"
    "v_mfma_f32_16x16x32_bf16 a[80:83], v[176:179], v[144:147], a[80:83]\n\t"
    "ds_read_b64_tr_b16 v[192:193], v239 offset:0\n\t"
    "v_accvgpr_read_b32 v222, a50\n\t"
    "v_mfma_f32_16x16x32_bf16 a[80:83], v[180:183], v[160:163], a[80:83]\n\t"
    "ds_read_b64_tr_b16 v[194:195], v239 offset:4096\n\t"
    "v_accvgpr_read_b32 v223, a51\n\t"
    "v_mfma_f32_16x16x32_bf16 a[84:87], v[180:183], v[164:167], a[84:87]\n\t"
    "ds_read_b64_tr_b16 v[196:197], v239 offset:24576\n\t"
    "v_cvt_pk_bf16_f32 v216, v216, v217\n\t"
    "v_mfma_f32_16x16x32_bf16 a[84:87], v[176:179], v[148:151], a[84:87]\n\t"
    "ds_read_b64_tr_b16 v[198:199], v239 offset:28672\n\t"
    "v_cvt_pk_bf16_f32 v217, v218, v219\n\t"
    "v_mfma_f32_16x16x32_bf16 a[88:91], v[176:179], v[152:155], a[88:91]\n\t"
    "v_cvt_pk_bf16_f32 v218, v220, v221\n\t"
    "v_cvt_pk_bf16_f32 v219, v222, v223\n\t"
    "v_mfma_f32_16x16x32_bf16 a[88:91], v[180:183], v[168:171], a[88:91]\n\t"
    "v_accvgpr_write_b32 a32, v216\n\t"
    "v_accvgpr_write_b32 a33, v217\n\t"
    "v_mfma_f32_16x16x32_bf16 a[92:95], v[180:183], v[172:175], a[92:95]\n\t"
    "v_accvgpr_write_b32 a34, v218\n\t"
    "v_accvgpr_write_b32 a35, v219\n\t"
    "v_mfma_f32_16x16x32_bf16 a[92:95], v[176:179], v[156:159], a[92:95]\n\t"
    "s_waitcnt lgkmcnt(4)\n\t"
    "v_mfma_f32_16x16x32_bf16 a[108:111], v[184:187], v[156:159], a[108:111]\n\t"
    "ds_read_b64_tr_b16 v[200:201], v241 offset:8288\n\t"
    "v_mfma_f32_16x16x32_bf16 a[108:111], v[188:191], v[172:175], a[108:111]\n\t"
    "ds_read_b64_tr_b16 v[202:203], v241 offset:11872\n\t"
    "v_mfma_f32_16x16x32_bf16 a[104:107], v[188:191], v[168:171], a[104:107]\n\t"
    "ds_read_b64_tr_b16 v[204:205], v241 offset:32864\n\t"
    "v_mfma_f32_16x16x32_bf16 a[104:107], v[184:187], v[152:155], a[104:107]\n\t"
    "ds_read_b64_tr_b16 v[206:207], v241 offset:36448\n\t"
    "v_mfma_f32_16x16x32_bf16 a[100:103], v[184:187], v[148:151], a[100:103]\n\t"
    "v_mfma_f32_16x16x32_bf16 a[100:103], v[188:191], v[164:167], a[100:103]\n\t"
    "v_accvgpr_read_b32 v208, a64\n\t"
    "v_accvgpr_read_b32 v209, a65\n\t"
    "v_mfma_f32_16x16x32_bf16 a[96:99], v[188:191], v[160:163], a[96:99]\n\t"
    "v_accvgpr_read_b32 v210, a66\n\t"
    "v_accvgpr_read_b32 v211, a67\n\t"
    "v_mfma_f32_16x16x32_bf16 a[96:99], v[184:187], v[144:147], a[96:99]\n\t"
    "s_waitcnt lgkmcnt(4)\n\t"
    "v_accvgpr_read_b32 v212, a80\n\t"
    "v_accvgpr_read_b32 v213, a81\n\t"
    "v_mfma_f32_16x16x32_bf16 a[112:115], v[192:195], v[144:147], a[112:115]\n\t"
    "ds_read_b64_tr_b16 v[176:177], v241 offset:8320\n\t"
    "v_accvgpr_read_b32 v214, a82\n\t"
    "v_mfma_f32_16x16x32_bf16 a[112:115], v[196:199], v[160:163], a[112:115]\n\t"
    "ds_read_b64_tr_b16 v[178:179], v241 offset:11904\n\t"
    "v_accvgpr_read_b32 v215, a83\n\t"
    "v_mfma_f32_16x16x32_bf16 a[116:119], v[196:199], v[164:167], a[116:119]\n\t"
    "ds_read_b64_tr_b16 v[180:181], v241 offset:32896\n\t"
    "v_cvt_pk_bf16_f32 v208, v208, v209\n\t"
    "v_mfma_f32_16x16x32_bf16 a[116:119], v[192:195], v[148:151], a[116:119]\n\t"
    "ds_read_b64_tr_b16 v[182:183], v241 offset:36480\n\t"
    "v_cvt_pk_bf16_f32 v209, v210, v211\n\t"
    "v_mfma_f32_16x16x32_bf16 a[120:123], v[192:195], v[152:155], a[120:123]\n\t"
    "v_cvt_pk_bf16_f32 v210, v212, v213\n\t"
    "v_cvt_pk_bf16_f32 v211, v214, v215\n\t"
    "v_mfma_f32_16x16x32_bf16 a[120:123], v[196:199], v[168:171], a[120:123]\n\t"
    "v_accvgpr_write_b32 a64, v208\n\t"
    "v_accvgpr_write_b32 a65, v209\n\t"
    "v_mfma_f32_16x16x32_bf16 a[124:127], v[196:199], v[172:175], a[124:127]\n\t"
    "v_accvgpr_write_b32 a66, v210\n\t"
    "v_accvgpr_write_b32 a67, v211\n\t"
    "v_mfma_f32_16x16x32_bf16 a[124:127], v[192:195], v[156:159], a[124:127]\n\t"
    "s_waitcnt lgkmcnt(4)\n\t"
    "v_mfma_f32_16x16x32_bf16 v[60:63], v[200:203], v[124:127], v[60:63]\n\t"
    "ds_read_b64_tr_b16 v[184:185], v241 offset:8352\n\t"
    "v_mfma_f32_16x16x32_bf16 v[60:63], v[204:207], v[140:143], v[60:63]\n\t"
    "ds_read_b64_tr_b16 v[186:187], v241 offset:11936\n\t"
    "v_mfma_f32_16x16x32_bf16 v[56:59], v[204:207], v[136:139], v[56:59]\n\t"
    "ds_read_b64_tr_b16 v[188:189], v241 offset:32928\n\t"
    "v_mfma_f32_16x16x32_bf16 v[56:59], v[200:203], v[120:123], v[56:59]\n\t"
    "ds_read_b64_tr_b16 v[190:191], v241 offset:36512\n\t"
    "v_mfma_f32_16x16x32_bf16 v[52:55], v[200:203], v[116:119], v[52:55]\n\t"
    "v_mfma_f32_16x16x32_bf16 v[52:55], v[204:207], v[132:135], v[52:55]\n\t"
    "v_accvgpr_read_b32 v216, a96\n\t"
    "v_accvgpr_read_b32 v217, a97\n\t"
    "v_mfma_f32_16x16x32_bf16 v[48:51], v[204:207], v[128:131], v[48:51]\n\t"
    "v_accvgpr_read_b32 v218, a98\n\t"
    "v_accvgpr_read_b32 v219, a99\n\t"
    "v_mfma_f32_16x16x32_bf16 v[48:51], v[200:203], v[112:115], v[48:51]\n\t"
    "s_waitcnt lgkmcnt(4)\n\t"
    "v_accvgpr_read_b32 v220, a112\n\t"
    "v_accvgpr_read_b32 v221, a113\n\t"
    "v_mfma_f32_16x16x32_bf16 a[144:147], v[176:179], v[112:115], a[144:147]\n\t"
    "ds_read_b64_tr_b16 v[192:193], v241 offset:8384\n\t"
    "v_accvgpr_read_b32 v222, a114\n\t"
    "v_mfma_f32_16x16x32_bf16 a[144:147], v[180:183], v[128:131], a[144:147]\n\t"
    "ds_read_b64_tr_b16 v[194:195], v241 offset:11968\n\t"
    "v_accvgpr_read_b32 v223, a115\n\t"
    "v_mfma_f32_16x16x32_bf16 a[148:151], v[180:183], v[132:135], a[148:151]\n\t"
    "ds_read_b64_tr_b16 v[196:197], v241 offset:32960\n\t"
    "v_cvt_pk_bf16_f32 v216, v216, v217\n\t"
    "v_mfma_f32_16x16x32_bf16 a[148:151], v[176:179], v[116:119], a[148:151]\n\t"
    "ds_read_b64_tr_b16 v[198:199], v241 offset:36544\n\t"
    "v_cvt_pk_bf16_f32 v217, v218, v219\n\t"
    "v_mfma_f32_16x16x32_bf16 a[152:155], v[176:179], v[120:123], a[152:155]\n\t"
    "v_cvt_pk_bf16_f32 v218, v220, v221\n\t"
    "v_cvt_pk_bf16_f32 v219, v222, v223\n\t"
    "v_mfma_f32_16x16x32_bf16 a[152:155], v[180:183], v[136:139], a[152:155]\n\t"
    "v_accvgpr_write_b32 a96, v216\n\t"
    "v_accvgpr_write_b32 a97, v217\n\t"
    "v_mfma_f32_16x16x32_bf16 a[156:159], v[180:183], v[140:143], a[156:159]\n\t"
    "v_accvgpr_write_b32 a98, v218\n\t"
    "v_accvgpr_write_b32 a99, v219\n\t"
    "v_mfma_f32_16x16x32_bf16 a[156:159], v[176:179], v[124:127], a[156:159]\n\t"
    "s_waitcnt lgkmcnt(4)\n\t"
    "v_accvgpr_read_b32 v208, a4\n\t"
    "v_accvgpr_read_b32 v209, a5\n\t"
    "v_mfma_f32_16x16x32_bf16 a[172:175], v[184:187], v[124:127], a[172:175]\n\t"
    "ds_read_b64_tr_b16 v[200:201], v241 offset:16384\n\t"
    "v_accvgpr_read_b32 v210, a6\n\t"
    "v_mfma_f32_16x16x32_bf16 a[172:175], v[188:191], v[140:143], a[172:175]\n\t"
    "ds_read_b64_tr_b16 v[202:203], v241 offset:19968\n\t"
    "v_accvgpr_read_b32 v211, a7\n\t"
    "v_mfma_f32_16x16x32_bf16 a[168:171], v[188:191], v[136:139], a[168:171]\n\t"
    "ds_read_b64_tr_b16 v[204:205], v241 offset:40960\n\t"
    "v_accvgpr_read_b32 v212, a20\n\t"
    "v_mfma_f32_16x16x32_bf16 a[168:171], v[184:187], v[120:123], a[168:171]\n\t"
    "ds_read_b64_tr_b16 v[206:207], v241 offset:44544\n\t"
    "v_accvgpr_read_b32 v213, a21\n\t"
    "v_mfma_f32_16x16x32_bf16 a[164:167], v[184:187], v[116:119], a[164:167]\n\t"
    "v_accvgpr_read_b32 v214, a22\n\t"
    "v_accvgpr_read_b32 v215, a23\n\t"
    "v_mfma_f32_16x16x32_bf16 a[164:167], v[188:191], v[132:135], a[164:167]\n\t"
    "v_cvt_pk_bf16_f32 v208, v208, v209\n\t"
    "v_cvt_pk_bf16_f32 v209, v210, v211\n\t"
    "v_mfma_f32_16x16x32_bf16 a[160:163], v[188:191], v[128:131], a[160:163]\n\t"
    "v_cvt_pk_bf16_f32 v210, v212, v213\n\t"
    "v_cvt_pk_bf16_f32 v211, v214, v215\n\t"
    "v_mfma_f32_16x16x32_bf16 a[160:163], v[184:187], v[112:115], a[160:163]\n\t"
    "s_waitcnt lgkmcnt(4)\n\t"
    "v_accvgpr_write_b32 a4, v208\n\t"
    "v_accvgpr_write_b32 a5, v209\n\t"
    "v_mfma_f32_16x16x32_bf16 a[176:179], v[192:195], v[112:115], a[176:179]\n\t"
    "ds_read_b64_tr_b16 v[176:177], v241 offset:16416\n\t"
    "v_accvgpr_write_b32 a6, v210\n\t"
    "v_mfma_f32_16x16x32_bf16 a[176:179], v[196:199], v[128:131], a[176:179]\n\t"
    "ds_read_b64_tr_b16 v[178:179], v241 offset:20000\n\t"
    "v_accvgpr_write_b32 a7, v211\n\t"
    "v_mfma_f32_16x16x32_bf16 a[180:183], v[196:199], v[132:135], a[180:183]\n\t"
    "ds_read_b64_tr_b16 v[180:181], v241 offset:40992\n\t"
    "v_accvgpr_read_b32 v216, a36\n\t"
    "v_mfma_f32_16x16x32_bf16 a[180:183], v[192:195], v[116:119], a[180:183]\n\t"
    "ds_read_b64_tr_b16 v[182:183], v241 offset:44576\n\t"
    "v_accvgpr_read_b32 v217, a37\n\t"
    "v_mfma_f32_16x16x32_bf16 a[184:187], v[192:195], v[120:123], a[184:187]\n\t"
    "v_accvgpr_read_b32 v218, a38\n\t"
    "v_accvgpr_read_b32 v219, a39\n\t"
    "v_mfma_f32_16x16x32_bf16 a[184:187], v[196:199], v[136:139], a[184:187]\n\t"
    "v_accvgpr_read_b32 v220, a52\n\t"
    "v_accvgpr_read_b32 v221, a53\n\t"
    "v_mfma_f32_16x16x32_bf16 a[188:191], v[196:199], v[140:143], a[188:191]\n\t"
    "v_accvgpr_read_b32 v222, a54\n\t"
    "v_accvgpr_read_b32 v223, a55\n\t"
    "v_mfma_f32_16x16x32_bf16 a[188:191], v[192:195], v[124:127], a[188:191]\n\t"
    "s_waitcnt lgkmcnt(4)\n\t"
    "v_cvt_pk_bf16_f32 v216, v216, v217\n\t"
    "v_cvt_pk_bf16_f32 v217, v218, v219\n\t"
    "v_mfma_f32_16x16x32_bf16 v[76:79], v[200:203], v[124:127], v[76:79]\n\t"
    "ds_read_b64_tr_b16 v[184:185], v241 offset:16448\n\t"
    "v_cvt_pk_bf16_f32 v218, v220, v221\n\t"
    "v_mfma_f32_16x16x32_bf16 v[76:79], v[204:207], v[140:143], v[76:79]\n\t"
    "ds_read_b64_tr_b16 v[186:187], v241 offset:20032\n\t"
    "v_cvt_pk_bf16_f32 v219, v222, v223\n\t"
    "v_mfma_f32_16x16x32_bf16 v[72:75], v[204:207], v[136:139], v[72:75]\n\t"
    "ds_read_b64_tr_b16 v[188:189], v241 offset:41024\n\t"
    "v_accvgpr_write_b32 a36, v216\n\t"
    "v_mfma_f32_16x16x32_bf16 v[72:75], v[200:203], v[120:123], v[72:75]\n\t"
    "ds_read_b64_tr_b16 v[190:191], v241 offset:44608\n\t"
    "v_accvgpr_write_b32 a37, v217\n\t"
    "v_mfma_f32_16x16x32_bf16 v[68:71], v[200:203], v[116:119], v[68:71]\n\t"
    "v_accvgpr_write_b32 a38, v218\n\t"
    "v_accvgpr_write_b32 a39, v219\n\t"
    "v_mfma_f32_16x16x32_bf16 v[68:71], v[204:207], v[132:135], v[68:71]\n\t"
    "v_accvgpr_read_b32 v208, a68\n\t"
    "v_accvgpr_read_b32 v209, a69\n\t"
    "v_mfma_f32_16x16x32_bf16 v[64:67], v[204:207], v[128:131], v[64:67]\n\t"
    "v_accvgpr_read_b32 v210, a70\n\t"
    "v_accvgpr_read_b32 v211, a71\n\t"
    "v_mfma_f32_16x16x32_bf16 v[64:67], v[200:203], v[112:115], v[64:67]\n\t"
    "s_waitcnt lgkmcnt(4)\n\t"
    "v_accvgpr_read_b32 v212, a84\n\t"
    "v_accvgpr_read_b32 v213, a85\n\t"
    "v_mfma_f32_16x16x32_bf16 v[80:83], v[176:179], v[112:115], v[80:83]\n\t"
    "ds_read_b64_tr_b16 v[192:193], v241 offset:16480\n\t"
    "v_accvgpr_read_b32 v214, a86\n\t"
    "v_mfma_f32_16x16x32_bf16 v[80:83], v[180:183], v[128:131], v[80:83]\n\t"
    "ds_read_b64_tr_b16 v[194:195], v241 offset:20064\n\t"
    "v_accvgpr_read_b32 v215, a87\n\t"
    "v_mfma_f32_16x16x32_bf16 v[84:87], v[180:183], v[132:135], v[84:87]\n\t"
    "ds_read_b64_tr_b16 v[196:197], v241 offset:41056\n\t"
    "v_cvt_pk_bf16_f32 v208, v208, v209\n\t"
    "v_mfma_f32_16x16x32_bf16 v[84:87], v[176:179], v[116:119], v[84:87]\n\t"
    "ds_read_b64_tr_b16 v[198:199], v241 offset:44640\n\t"
    "v_cvt_pk_bf16_f32 v209, v210, v211\n\t"
    "v_mfma_f32_16x16x32_bf16 v[88:91], v[176:179], v[120:123], v[88:91]\n\t"
    "v_cvt_pk_bf16_f32 v210, v212, v213\n\t"
    "v_cvt_pk_bf16_f32 v211, v214, v215\n\t"
    "v_mfma_f32_16x16x32_bf16 v[88:91], v[180:183], v[136:139], v[88:91]\n\t"
    "v_accvgpr_write_b32 a68, v208\n\t"
    "v_accvgpr_write_b32 a69, v209\n\t"
    "v_mfma_f32_16x16x32_bf16 v[92:95], v[180:183], v[140:143], v[92:95]\n\t"
    "v_accvgpr_write_b32 a70, v210\n\t"
    "v_accvgpr_write_b32 a71, v211\n\t"
    "v_mfma_f32_16x16x32_bf16 v[92:95], v[176:179], v[124:127], v[92:95]\n\t"
    "s_waitcnt lgkmcnt(4)\n\t"
    "v_accvgpr_read_b32 v216, a100\n\t"
    "v_accvgpr_read_b32 v217, a101\n\t"
    "v_mfma_f32_16x16x32_bf16 v[108:111], v[184:187], v[124:127], v[108:111]\n\t"
    "ds_read_b64_tr_b16 v[200:201], v241 offset:16512\n\t"
    "v_accvgpr_read_b32 v218, a102\n\t"
    "v_mfma_f32_16x16x32_bf16 v[108:111], v[188:191], v[140:143], v[108:111]\n\t"
    "ds_read_b64_tr_b16 v[202:203], v241 offset:20096\n\t"
    "v_accvgpr_read_b32 v219, a103\n\t"
    "v_mfma_f32_16x16x32_bf16 v[104:107], v[188:191], v[136:139], v[104:107]\n\t"
    "ds_read_b64_tr_b16 v[204:205], v241 offset:41088\n\t"
    "v_accvgpr_read_b32 v220, a116\n\t"
    "v_mfma_f32_16x16x32_bf16 v[104:107], v[184:187], v[120:123], v[104:107]\n\t"
    "ds_read_b64_tr_b16 v[206:207], v241 offset:44672\n\t"
    "v_accvgpr_read_b32 v221, a117\n\t"
    "v_mfma_f32_16x16x32_bf16 v[100:103], v[184:187], v[116:119], v[100:103]\n\t"
    "v_accvgpr_read_b32 v222, a118\n\t"
    "v_accvgpr_read_b32 v223, a119\n\t"
    "v_mfma_f32_16x16x32_bf16 v[100:103], v[188:191], v[132:135], v[100:103]\n\t"
    "v_cvt_pk_bf16_f32 v216, v216, v217\n\t"
    "v_cvt_pk_bf16_f32 v217, v218, v219\n\t"
    "v_mfma_f32_16x16x32_bf16 v[96:99], v[188:191], v[128:131], v[96:99]\n\t"
    "v_cvt_pk_bf16_f32 v218, v220, v221\n\t"
    "v_cvt_pk_bf16_f32 v219, v222, v223\n\t"
    "v_mfma_f32_16x16x32_bf16 v[96:99], v[184:187], v[112:115], v[96:99]\n\t"
    "s_waitcnt lgkmcnt(4)\n\t"
    "v_accvgpr_write_b32 a100, v216\n\t"
    "v_accvgpr_write_b32 a101, v217\n\t"
    "v_mfma_f32_16x16x32_bf16 a[128:131], v[192:195], v[112:115], a[128:131]\n\t"
    "ds_read_b64_tr_b16 v[176:177], v241 offset:16544\n\t"
    "v_accvgpr_write_b32 a102, v218\n\t"
    "v_mfma_f32_16x16x32_bf16 a[128:131], v[196:199], v[128:131], a[128:131]\n\t"
    "ds_read_b64_tr_b16 v[178:179], v241 offset:20128\n\t"
    "v_accvgpr_write_b32 a103, v219\n\t"
    "v_mfma_f32_16x16x32_bf16 a[132:135], v[196:199], v[132:135], a[132:135]\n\t"
    "ds_read_b64_tr_b16 v[180:181], v241 offset:41120\n\t"
    "v_accvgpr_read_b32 v208, a8\n\t"
    "v_mfma_f32_16x16x32_bf16 a[132:135], v[192:195], v[116:119], a[132:135]\n\t"
    "ds_read_b64_tr_b16 v[182:183], v241 offset:44704\n\t"
    "v_accvgpr_read_b32 v209, a9\n\t"
    "v_mfma_f32_16x16x32_bf16 a[136:139], v[192:195], v[120:123], a[136:139]\n\t"
    "v_accvgpr_read_b32 v210, a10\n\t"
    "v_accvgpr_read_b32 v211, a11\n\t"
    "v_mfma_f32_16x16x32_bf16 a[136:139], v[196:199], v[136:139], a[136:139]\n\t"
    "v_accvgpr_read_b32 v212, a24\n\t"
    "v_accvgpr_read_b32 v213, a25\n\t"
    "v_mfma_f32_16x16x32_bf16 a[140:143], v[196:199], v[140:143], a[140:143]\n\t"
    "v_accvgpr_read_b32 v214, a26\n\t"
    "v_accvgpr_read_b32 v215, a27\n\t"
    "v_mfma_f32_16x16x32_bf16 a[140:143], v[192:195], v[124:127], a[140:143]\n\t"
    "s_waitcnt lgkmcnt(4)\n\t"
    "v_cvt_pk_bf16_f32 v208, v208, v209\n\t"
    "v_cvt_pk_bf16_f32 v209, v210, v211\n\t"
    "v_mfma_f32_16x16x32_bf16 a[204:207], v[200:203], v[124:127], a[204:207]\n\t"
    "ds_read_b64_tr_b16 v[184:185], v241 offset:16576\n\t"
    "v_cvt_pk_bf16_f32 v210, v212, v213\n\t"
    "v_mfma_f32_16x16x32_bf16 a[204:207], v[204:207], v[140:143], a[204:207]\n\t"
    "ds_read_b64_tr_b16 v[186:187], v241 offset:20160\n\t"
    "v_cvt_pk_bf16_f32 v211, v214, v215\n\t"
    "v_mfma_f32_16x16x32_bf16 a[200:203], v[204:207], v[136:139], a[200:203]\n\t"
    "ds_read_b64_tr_b16 v[188:189], v241 offset:41152\n\t"
    "v_accvgpr_write_b32 a8, v208\n\t"
    "v_mfma_f32_16x16x32_bf16 a[200:203], v[200:203], v[120:123], a[200:203]\n\t"
    "ds_read_b64_tr_b16 v[190:191], v241 offset:44736\n\t"
    "v_accvgpr_write_b32 a9, v209\n\t"
    "v_mfma_f32_16x16x32_bf16 a[196:199], v[200:203], v[116:119], a[196:199]\n\t"
    "v_add_u32 v241, 4294868992, v241\n\t"
    "v_accvgpr_write_b32 a10, v210\n\t"
    "v_mfma_f32_16x16x32_bf16 a[196:199], v[204:207], v[132:135], a[196:199]\n\t"
    "v_accvgpr_write_b32 a11, v211\n\t"
    "v_accvgpr_read_b32 v216, a40\n\t"
    "v_mfma_f32_16x16x32_bf16 a[192:195], v[204:207], v[128:131], a[192:195]\n\t"
    "v_accvgpr_read_b32 v217, a41\n\t"
    "v_accvgpr_read_b32 v218, a42\n\t"
    "v_mfma_f32_16x16x32_bf16 a[192:195], v[200:203], v[112:115], a[192:195]\n\t"
    "s_waitcnt lgkmcnt(4)\n\t"
    "v_accvgpr_read_b32 v219, a43\n\t"
    "v_accvgpr_read_b32 v220, a56\n\t"
    "v_mfma_f32_16x16x32_bf16 a[208:211], v[176:179], v[112:115], a[208:211]\n\t"
    "ds_read_b64_tr_b16 v[192:193], v241 offset:8192\n\t"
    "v_accvgpr_read_b32 v221, a57\n\t"
    "v_mfma_f32_16x16x32_bf16 a[208:211], v[180:183], v[128:131], a[208:211]\n\t"
    "ds_read_b64_tr_b16 v[194:195], v241 offset:11776\n\t"
    "v_accvgpr_read_b32 v222, a58\n\t"
    "v_mfma_f32_16x16x32_bf16 a[212:215], v[180:183], v[132:135], a[212:215]\n\t"
    "ds_read_b64_tr_b16 v[196:197], v241 offset:32768\n\t"
    "v_accvgpr_read_b32 v223, a59\n\t"
    "v_mfma_f32_16x16x32_bf16 a[212:215], v[176:179], v[116:119], a[212:215]\n\t"
    "ds_read_b64_tr_b16 v[198:199], v241 offset:36352\n\t"
    "v_cvt_pk_bf16_f32 v216, v216, v217\n\t"
    "v_mfma_f32_16x16x32_bf16 a[216:219], v[176:179], v[120:123], a[216:219]\n\t"
    "v_cvt_pk_bf16_f32 v217, v218, v219\n\t"
    "v_cvt_pk_bf16_f32 v218, v220, v221\n\t"
    "v_mfma_f32_16x16x32_bf16 a[216:219], v[180:183], v[136:139], a[216:219]\n\t"
    "v_cvt_pk_bf16_f32 v219, v222, v223\n\t"
    "v_accvgpr_write_b32 a40, v216\n\t"
    "v_mfma_f32_16x16x32_bf16 a[220:223], v[180:183], v[140:143], a[220:223]\n\t"
    "v_accvgpr_write_b32 a41, v217\n\t"
    "v_accvgpr_write_b32 a42, v218\n\t"
    "v_mfma_f32_16x16x32_bf16 a[220:223], v[176:179], v[124:127], a[220:223]\n\t"
    "s_waitcnt lgkmcnt(4)\n\t"
    "v_accvgpr_write_b32 a43, v219\n\t"
    "v_accvgpr_read_b32 v208, a72\n\t"
    "v_mfma_f32_16x16x32_bf16 a[236:239], v[184:187], v[124:127], a[236:239]\n\t"
    "ds_read_b64_tr_b16 v[200:201], v241 offset:16384\n\t"
    "v_accvgpr_read_b32 v209, a73\n\t"
    "v_mfma_f32_16x16x32_bf16 a[236:239], v[188:191], v[140:143], a[236:239]\n\t"
    "ds_read_b64_tr_b16 v[202:203], v241 offset:19968\n\t"
    "v_accvgpr_read_b32 v210, a74\n\t"
    "v_mfma_f32_16x16x32_bf16 a[232:235], v[188:191], v[136:139], a[232:235]\n\t"
    "ds_read_b64_tr_b16 v[204:205], v241 offset:40960\n\t"
    "v_accvgpr_read_b32 v211, a75\n\t"
    "v_mfma_f32_16x16x32_bf16 a[232:235], v[184:187], v[120:123], a[232:235]\n\t"
    "ds_read_b64_tr_b16 v[206:207], v241 offset:44544\n\t"
    "v_accvgpr_read_b32 v212, a88\n\t"
    "v_mfma_f32_16x16x32_bf16 a[228:231], v[184:187], v[116:119], a[228:231]\n\t"
    "s_waitcnt vmcnt(0)\n\t"
    "s_barrier\n\t"
    "s_add_u32 m0, s85, 81920\n\t"
    "v_mfma_f32_16x16x32_bf16 a[228:231], v[188:191], v[132:135], a[228:231]\n\t"
    "buffer_load_dwordx4 v248, s[80:83], s84 offen sc1 lds\n\t"
    "v_mfma_f32_16x16x32_bf16 a[224:227], v[188:191], v[128:131], a[224:227]\n\t"
    "ds_read_b128 a[16:19], v248 offset:49152\n\t"
    "v_accvgpr_read_b32 v213, a89\n\t"
    "v_mfma_f32_16x16x32_bf16 a[224:227], v[184:187], v[112:115], a[224:227]\n\t"
    "s_waitcnt lgkmcnt(5)\n\t"
    "v_accvgpr_read_b32 v214, a90\n\t"
    "v_accvgpr_read_b32 v215, a91\n\t"
    "v_mfma_f32_16x16x32_bf16 v[0:3], v[192:195], v[144:147], v[0:3]\n\t"
    "ds_read_b64_tr_b16 v[176:177], v241 offset:8224\n\t"
    "s_mul_i32 s10, s3, 2816\n\t"
    "v_mfma_f32_16x16x32_bf16 v[0:3], v[196:199], v[160:163], v[0:3]\n\t"
    "ds_read_b64_tr_b16 v[178:179], v241 offset:11808\n\t"
    "s_add_u32 s10, s10, 11264\n\t"
    "v_mfma_f32_16x16x32_bf16 v[4:7], v[196:199], v[164:167], v[4:7]\n\t"
    "ds_read_b64_tr_b16 v[180:181], v241 offset:32800\n\t"
    "s_min_u32 s10, s10, s26\n\t"
    "v_mfma_f32_16x16x32_bf16 v[4:7], v[192:195], v[148:151], v[4:7]\n\t"
    "ds_read_b64_tr_b16 v[182:183], v241 offset:36384\n\t"
    "v_min_u32 v226, 336, v248\n\t"
    "v_mfma_f32_16x16x32_bf16 v[8:11], v[192:195], v[152:155], v[8:11]\n\t"
    "buffer_load_dwordx4 v248, s[80:83], s84 offen offset:1024 sc1 lds\n\t"
    "v_mfma_f32_16x16x32_bf16 v[8:11], v[196:199], v[168:171], v[8:11]\n\t"
    "ds_read_b128 a[48:51], v248 offset:50176\n\t"
    "v_lshlrev_b32 v226, 3, v226\n\t"
    "v_mfma_f32_16x16x32_bf16 v[12:15], v[196:199], v[172:175], v[12:15]\n\t"
    "buffer_load_dword a245, v226, s[24:27], s10 offen\n\t"
    "v_mfma_f32_16x16x32_bf16 v[12:15], v[192:195], v[156:159], v[12:15]\n\t"
    "s_waitcnt lgkmcnt(6)\n\t"
    "v_cvt_pk_bf16_f32 v208, v208, v209\n\t"
    "v_cvt_pk_bf16_f32 v209, v210, v211\n\t"
    "v_mfma_f32_16x16x32_bf16 v[76:79], v[200:203], v[156:159], v[76:79]\n\t"
    "ds_read_b64_tr_b16 v[184:185], v241 offset:16416\n\t"
    "v_cvt_pk_bf16_f32 v210, v212, v213\n\t"
    "v_mfma_f32_16x16x32_bf16 v[76:79], v[204:207], v[172:175], v[76:79]\n\t"
    "ds_read_b64_tr_b16 v[186:187], v241 offset:20000\n\t"
    "v_cvt_pk_bf16_f32 v211, v214, v215\n\t"
    "v_mfma_f32_16x16x32_bf16 v[72:75], v[204:207], v[168:171], v[72:75]\n\t"
    "ds_read_b64_tr_b16 v[188:189], v241 offset:40992\n\t"
    "v_accvgpr_write_b32 a72, v208\n\t"
    "v_mfma_f32_16x16x32_bf16 v[72:75], v[200:203], v[152:155], v[72:75]\n\t"
    "ds_read_b64_tr_b16 v[190:191], v241 offset:44576\n\t"
    "v_accvgpr_write_b32 a73, v209\n\t"
    "v_mfma_f32_16x16x32_bf16 v[68:71], v[200:203], v[148:151], v[68:71]\n\t"
    "buffer_load_dwordx4 v248, s[80:83], s84 offen offset:2048 sc1 lds\n\t"
    "v_mfma_f32_16x16x32_bf16 v[68:71], v[204:207], v[164:167], v[68:71]\n\t"
    "ds_read_b128 a[80:83], v248 offset:51200\n\t"
    "v_accvgpr_write_b32 a74, v210\n\t"
    "v_mfma_f32_16x16x32_bf16 v[64:67], v[204:207], v[160:163], v[64:67]\n\t"
    "v_accvgpr_write_b32 a75, v211\n\t"
    "v_accvgpr_read_b32 v216, a104\n\t"
    "v_mfma_f32_16x16x32_bf16 v[64:67], v[200:203], v[144:147], v[64:67]\n\t"
    "s_waitcnt lgkmcnt(6)\n\t"
    "v_accvgpr_read_b32 v217, a105\n\t"
    "v_accvgpr_read_b32 v218, a106\n\t"
    "v_mfma_f32_16x16x32_bf16 v[16:19], v[176:179], v[144:147], v[16:19]\n\t"
    "ds_read_b64_tr_b16 v[192:193], v241 offset:8256\n\t"
    "v_accvgpr_read_b32 v219, a107\n\t"
    "v_mfma_f32_16x16x32_bf16 v[16:19], v[180:183], v[160:163], v[16:19]\n\t"
    "ds_read_b64_tr_b16 v[194:195], v241 offset:11840\n\t"
    "v_accvgpr_read_b32 v220, a120\n\t"
    "v_mfma_f32_16x16x32_bf16 v[20:23], v[180:183], v[164:167], v[20:23]\n\t"
    "ds_read_b64_tr_b16 v[196:197], v241 offset:32832\n\t"
    "v_accvgpr_read_b32 v221, a121\n\t"
    "v_mfma_f32_16x16x32_bf16 v[20:23], v[176:179], v[148:151], v[20:23]\n\t"
    "ds_read_b64_tr_b16 v[198:199], v241 offset:36416\n\t"
    "v_accvgpr_read_b32 v222, a122\n\t"
    "v_mfma_f32_16x16x32_bf16 v[24:27], v[176:179], v[152:155], v[24:27]\n\t"
    "buffer_load_dwordx4 v248, s[80:83], s84 offen offset:3072 sc1 lds\n\t"
    "v_mfma_f32_16x16x32_bf16 v[24:27], v[180:183], v[168:171], v[24:27]\n\t"
    "s_add_u32 s84, s84, 16384\n\t"
    "s_add_u32 m0, s85, 98304\n\t"
    "v_accvgpr_read_b32 v223, a123\n\t"
    "v_mfma_f32_16x16x32_bf16 v[28:31], v[180:183], v[172:175], v[28:31]\n\t"
    "buffer_load_dwordx4 v248, s[80:83], s84 offen sc1 lds\n\t"
    "v_mfma_f32_16x16x32_bf16 v[28:31], v[176:179], v[156:159], v[28:31]\n\t"
    "ds_read_b128 a[112:115], v248 offset:52224\n\t"
    "s_waitcnt lgkmcnt(6)\n\t"
    "v_mfma_f32_16x16x32_bf16 v[92:95], v[184:187], v[156:159], v[92:95]\n\t"
    "ds_read_b64_tr_b16 v[200:201], v241 offset:16448\n\t"
    "v_cvt_pk_bf16_f32 v216, v216, v217\n\t"
    "v_mfma_f32_16x16x32_bf16 v[92:95], v[188:191], v[172:175], v[92:95]\n\t"
    "ds_read_b64_tr_b16 v[202:203], v241 offset:20032\n\t"
    "v_cvt_pk_bf16_f32 v217, v218, v219\n\t"
    "v_mfma_f32_16x16x32_bf16 v[88:91], v[188:191], v[168:171], v[88:91]\n\t"
    "ds_read_b64_tr_b16 v[204:205], v241 offset:41024\n\t"
    "v_cvt_pk_bf16_f32 v218, v220, v221\n\t"
    "v_mfma_f32_16x16x32_bf16 v[88:91], v[184:187], v[152:155], v[88:91]\n\t"
    "ds_read_b64_tr_b16 v[206:207], v241 offset:44608\n\t"
    "v_cvt_pk_bf16_f32 v219, v222, v223\n\t"
    "v_mfma_f32_16x16x32_bf16 v[84:87], v[184:187], v[148:151], v[84:87]\n\t"
    "buffer_load_dwordx4 v248, s[80:83], s84 offen offset:1024 sc1 lds\n\t"
    "v_mfma_f32_16x16x32_bf16 v[84:87], v[188:191], v[164:167], v[84:87]\n\t"
    "ds_read_b128 a[20:23], v248 offset:53248\n\t"
    "v_accvgpr_write_b32 a104, v216\n\t"
    "v_mfma_f32_16x16x32_bf16 v[80:83], v[188:191], v[160:163], v[80:83]\n\t"
    "v_accvgpr_write_b32 a105, v217\n\t"
    "v_accvgpr_write_b32 a106, v218\n\t"
    "v_mfma_f32_16x16x32_bf16 v[80:83], v[184:187], v[144:147], v[80:83]\n\t"
    "s_waitcnt lgkmcnt(6)\n\t"
    "v_accvgpr_write_b32 a107, v219\n\t"
    "v_accvgpr_read_b32 v208, a12\n\t"
    "v_mfma_f32_16x16x32_bf16 v[32:35], v[192:195], v[144:147], v[32:35]\n\t"
    "ds_read_b64_tr_b16 v[176:177], v241 offset:8288\n\t"
    "v_accvgpr_read_b32 v209, a13\n\t"
    "v_mfma_f32_16x16x32_bf16 v[32:35], v[196:199], v[160:163], v[32:35]\n\t"
    "ds_read_b64_tr_b16 v[178:179], v241 offset:11872\n\t"
    "v_accvgpr_read_b32 v210, a14\n\t"
    "v_mfma_f32_16x16x32_bf16 v[36:39], v[196:199], v[164:167], v[36:39]\n\t"
    "ds_read_b64_tr_b16 v[180:181], v241 offset:32864\n\t"
    "v_accvgpr_read_b32 v211, a15\n\t"
    "v_mfma_f32_16x16x32_bf16 v[36:39], v[192:195], v[148:151], v[36:39]\n\t"
    "ds_read_b64_tr_b16 v[182:183], v241 offset:36448\n\t"
    "v_accvgpr_read_b32 v212, a28\n\t"
    "v_mfma_f32_16x16x32_bf16 v[40:43], v[192:195], v[152:155], v[40:43]\n\t"
    "buffer_load_dwordx4 v248, s[80:83], s84 offen offset:2048 sc1 lds\n\t"
    "v_mfma_f32_16x16x32_bf16 v[40:43], v[196:199], v[168:171], v[40:43]\n\t"
    "ds_read_b128 a[52:55], v248 offset:54272\n\t"
    "v_accvgpr_read_b32 v213, a29\n\t"
    "v_mfma_f32_16x16x32_bf16 v[44:47], v[196:199], v[172:175], v[44:47]\n\t"
    "v_accvgpr_read_b32 v214, a30\n\t"
    "v_accvgpr_read_b32 v215, a31\n\t"
    "v_mfma_f32_16x16x32_bf16 v[44:47], v[192:195], v[156:159], v[44:47]\n\t"
    "s_waitcnt lgkmcnt(6)\n\t"
    "v_cvt_pk_bf16_f32 v208, v208, v209\n\t"
    "v_cvt_pk_bf16_f32 v209, v210, v211\n\t"
    "v_mfma_f32_16x16x32_bf16 v[108:111], v[200:203], v[156:159], v[108:111]\n\t"
    "ds_read_b64_tr_b16 v[184:185], v241 offset:16480\n\t"
    "v_cvt_pk_bf16_f32 v210, v212, v213\n\t"
    "v_mfma_f32_16x16x32_bf16 v[108:111], v[204:207], v[172:175], v[108:111]\n\t"
    "ds_read_b64_tr_b16 v[186:187], v241 offset:20064\n\t"
    "v_cvt_pk_bf16_f32 v211, v214, v215\n\t"
    "v_mfma_f32_16x16x32_bf16 v[104:107], v[204:207], v[168:171], v[104:107]\n\t"
    "ds_read_b64_tr_b16 v[188:189], v241 offset:41056\n\t"
    "v_accvgpr_write_b32 a12, v208\n\t"
    "v_mfma_f32_16x16x32_bf16 v[104:107], v[200:203], v[152:155], v[104:107]\n\t"
    "ds_read_b64_tr_b16 v[190:191], v241 offset:44640\n\t"
    "v_accvgpr_write_b32 a13, v209\n\t"
    "v_mfma_f32_16x16x32_bf16 v[100:103], v[200:203], v[148:151], v[100:103]\n\t"
    "buffer_load_dwordx4 v248, s[80:83], s84 offen offset:3072 sc1 lds\n\t"
    "v_mfma_f32_16x16x32_bf16 v[100:103], v[204:207], v[164:167], v[100:103]\n\t"
    "s_add_u32 s84, s84, 16384\n\t"
    "s_add_u32 m0, s85, 114688\n\t"
    "v_accvgpr_write_b32 a14, v210\n\t"
    "v_mfma_f32_16x16x32_bf16 v[96:99], v[204:207], v[160:163], v[96:99]\n\t"
    "buffer_load_dwordx4 v248, s[80:83], s84 offen sc1 lds\n\t"
    "v_mfma_f32_16x16x32_bf16 v[96:99], v[200:203], v[144:147], v[96:99]\n\t"
    "ds_read_b128 a[84:87], v248 offset:55296\n\t"
    "s_waitcnt lgkmcnt(6)\n\t"
    "v_mfma_f32_16x16x32_bf16 v[48:51], v[176:179], v[144:147], v[48:51]\n\t"
    "ds_read_b64_tr_b16 v[192:193], v241 offset:8320\n\t"
    "v_accvgpr_write_b32 a15, v211\n\t"
    "v_mfma_f32_16x16x32_bf16 v[48:51], v[180:183], v[160:163], v[48:51]\n\t"
    "ds_read_b64_tr_b16 v[194:195], v241 offset:11904\n\t"
    "v_accvgpr_read_b32 v216, a44\n\t"
    "v_mfma_f32_16x16x32_bf16 v[52:55], v[180:183], v[164:167], v[52:55]\n\t"
    "ds_read_b64_tr_b16 v[196:197], v241 offset:32896\n\t"
    "v_accvgpr_read_b32 v217, a45\n\t"
    "v_mfma_f32_16x16x32_bf16 v[52:55], v[176:179], v[148:151], v[52:55]\n\t"
    "ds_read_b64_tr_b16 v[198:199], v241 offset:36480\n\t"
    "v_accvgpr_read_b32 v218, a46\n\t"
    "v_mfma_f32_16x16x32_bf16 v[56:59], v[176:179], v[152:155], v[56:59]\n\t"
    "buffer_load_dwordx4 v248, s[80:83], s84 offen offset:1024 sc1 lds\n\t"
    "v_mfma_f32_16x16x32_bf16 v[56:59], v[180:183], v[168:171], v[56:59]\n\t"
    "ds_read_b128 a[116:119], v248 offset:56320\n\t"
    "v_accvgpr_read_b32 v219, a47\n\t"
    "v_mfma_f32_16x16x32_bf16 v[60:63], v[180:183], v[172:175], v[60:63]\n\t"
    "v_accvgpr_read_b32 v220, a60\n\t"
    "v_accvgpr_read_b32 v221, a61\n\t"
    "v_mfma_f32_16x16x32_bf16 v[60:63], v[176:179], v[156:159], v[60:63]\n\t"
    "s_waitcnt lgkmcnt(6)\n\t"
    "v_accvgpr_read_b32 v222, a62\n\t"
    "v_accvgpr_read_b32 v223, a63\n\t"
    "v_mfma_f32_16x16x32_bf16 a[140:143], v[184:187], v[156:159], a[140:143]\n\t"
    "ds_read_b64_tr_b16 v[200:201], v241 offset:16512\n\t"
    "v_cvt_pk_bf16_f32 v216, v216, v217\n\t"
    "v_mfma_f32_16x16x32_bf16 a[140:143], v[188:191], v[172:175], a[140:143]\n\t"
    "ds_read_b64_tr_b16 v[202:203], v241 offset:20096\n\t"
    "v_cvt_pk_bf16_f32 v217, v218, v219\n\t"
    "v_mfma_f32_16x16x32_bf16 a[136:139], v[188:191], v[168:171], a[136:139]\n\t"
    "ds_read_b64_tr_b16 v[204:205], v241 offset:41088\n\t"
    "v_cvt_pk_bf16_f32 v218, v220, v221\n\t"
    "v_mfma_f32_16x16x32_bf16 a[136:139], v[184:187], v[152:155], a[136:139]\n\t"
    "ds_read_b64_tr_b16 v[206:207], v241 offset:44672\n\t"
    "v_cvt_pk_bf16_f32 v219, v222, v223\n\t"
    "v_mfma_f32_16x16x32_bf16 a[132:135], v[184:187], v[148:151], a[132:135]\n\t"
    "buffer_load_dwordx4 v248, s[80:83], s84 offen offset:2048 sc1 lds\n\t"
    "v_mfma_f32_16x16x32_bf16 a[132:135], v[188:191], v[164:167], a[132:135]\n\t"
    "v_accvgpr_write_b32 a44, v216\n\t"
    "v_accvgpr_write_b32 a45, v217\n\t"
    "v_mfma_f32_16x16x32_bf16 a[128:131], v[188:191], v[160:163], a[128:131]\n\t"
    "v_accvgpr_write_b32 a46, v218\n\t"
    "v_accvgpr_write_b32 a47, v219\n\t"
    "v_mfma_f32_16x16x32_bf16 a[128:131], v[184:187], v[144:147], a[128:131]\n\t"
    "s_waitcnt lgkmcnt(5)\n\t"
    "v_accvgpr_read_b32 v208, a76\n\t"
    "v_accvgpr_read_b32 v209, a77\n\t"
    "v_mfma_f32_16x16x32_bf16 a[144:147], v[192:195], v[144:147], a[144:147]\n\t"
    "ds_read_b64_tr_b16 v[176:177], v241 offset:8352\n\t"
    "v_accvgpr_read_b32 v210, a78\n\t"
    "v_mfma_f32_16x16x32_bf16 a[144:147], v[196:199], v[160:163], a[144:147]\n\t"
    "ds_read_b64_tr_b16 v[178:179], v241 offset:11936\n\t"
    "v_accvgpr_read_b32 v211, a79\n\t"
    "v_mfma_f32_16x16x32_bf16 a[148:151], v[196:199], v[164:167], a[148:151]\n\t"
    "ds_read_b64_tr_b16 v[180:181], v241 offset:32928\n\t"
    "v_accvgpr_read_b32 v212, a92\n\t"
    "v_mfma_f32_16x16x32_bf16 a[148:151], v[192:195], v[148:151], a[148:151]\n\t"
    "ds_read_b64_tr_b16 v[182:183], v241 offset:36512\n\t"
    "v_accvgpr_read_b32 v213, a93\n\t"
    "v_mfma_f32_16x16x32_bf16 a[152:155], v[192:195], v[152:155], a[152:155]\n\t"
    "buffer_load_dwordx4 v248, s[80:83], s84 offen offset:3072 sc1 lds\n\t"
    "v_mfma_f32_16x16x32_bf16 a[152:155], v[196:199], v[168:171], a[152:155]\n\t"
    "s_add_u32 s84, s84, 16384\n\t"
    "s_getpc_b64 s[28:29]\n\t"
    ".Lfb_ct1:\n\t"
    "s_lshr_b32 s77, s2, 3\n\t"
    "v_mfma_f32_16x16x32_bf16 a[156:159], v[196:199], v[172:175], a[156:159]\n\t"
    "s_lshl_b32 s77, s77, 7\n\t"
    "s_lshl_b32 s78, s3, 12\n\t"
    "s_add_u32 s77, s77, s78\n\t"
    "v_mfma_f32_16x16x32_bf16 a[156:159], v[192:195], v[156:159], a[156:159]\n\t"
    "s_waitcnt lgkmcnt(4)\n\t"
    "s_add_u32 s77, s77, .Lfb_mid0-.Lfb_ct1+16384\n\t"
    "s_min_u32 s77, s77, .Lfb_end-.Lfb_ct1\n\t"
    "v_mfma_f32_16x16x32_bf16 a[204:207], v[200:203], v[156:159], a[204:207]\n\t"
    "ds_read_b64_tr_b16 v[184:185], v241 offset:16544\n\t"
    "v_mov_b32 v255, s77\n\t"
    "v_mfma_f32_16x16x32_bf16 a[204:207], v[204:207], v[172:175], a[204:207]\n\t"
    "ds_read_b64_tr_b16 v[186:187], v241 offset:20128\n\t"
    "v_accvgpr_read_b32 v214, a94\n\t"
    "v_mfma_f32_16x16x32_bf16 a[200:203], v[204:207], v[168:171], a[200:203]\n\t"
    "ds_read_b64_tr_b16 v[188:189], v241 offset:41120\n\t"
    "v_accvgpr_read_b32 v215, a95\n\t"
    "v_mfma_f32_16x16x32_bf16 a[200:203], v[200:203], v[152:155], a[200:203]\n\t"
    "ds_read_b64_tr_b16 v[190:191], v241 offset:44704\n\t"
    "v_cvt_pk_bf16_f32 v208, v208, v209\n\t"
    "v_mfma_f32_16x16x32_bf16 a[196:199], v[200:203], v[148:151], a[196:199]\n\t"
    "global_load_dword v255, v255, s[28:29]\n\t"
    "v_mfma_f32_16x16x32_bf16 a[196:199], v[204:207], v[164:167], a[196:199]\n\t"
    "v_cvt_pk_bf16_f32 v209, v210, v211\n\t"
    "v_cvt_pk_bf16_f32 v210, v212, v213\n\t"
    "v_mfma_f32_16x16x32_bf16 a[192:195], v[204:207], v[160:163], a[192:195]\n\t"
    "v_cvt_pk_bf16_f32 v211, v214, v215\n\t"
    "v_accvgpr_write_b32 a76, v208\n\t"
    "v_mfma_f32_16x16x32_bf16 a[192:195], v[200:203], v[144:147], a[192:195]\n\t"
    "s_waitcnt lgkmcnt(4)\n\t"
    "v_accvgpr_write_b32 a77, v209\n\t"
    "v_accvgpr_write_b32 a78, v210\n\t"
    "v_mfma_f32_16x16x32_bf16 a[160:163], v[176:179], v[144:147], a[160:163]\n\t"
    "ds_read_b64_tr_b16 v[192:193], v241 offset:8384\n\t"
    "v_accvgpr_write_b32 a79, v211\n\t"
    "v_mfma_f32_16x16x32_bf16 a[160:163], v[180:183], v[160:163], a[160:163]\n\t"
    "ds_read_b64_tr_b16 v[194:195], v241 offset:11968\n\t"
    "v_accvgpr_read_b32 v216, a108\n\t"
    "v_mfma_f32_16x16x32_bf16 a[164:167], v[180:183], v[164:167], a[164:167]\n\t"
    "ds_read_b64_tr_b16 v[196:197], v241 offset:32960\n\t"
    "v_accvgpr_read_b32 v217, a109\n\t"
    "v_mfma_f32_16x16x32_bf16 a[164:167], v[176:179], v[148:151], a[164:167]\n\t"
    "ds_read_b64_tr_b16 v[198:199], v241 offset:36544\n\t"
    "v_accvgpr_read_b32 v218, a110\n\t"
    "v_mfma_f32_16x16x32_bf16 a[168:171], v[176:179], v[152:155], a[168:171]\n\t"
    "v_accvgpr_read_b32 v219, a111\n\t"
    "v_accvgpr_read_b32 v220, a124\n\t"
    "v_mfma_f32_16x16x32_bf16 a[168:171], v[180:183], v[168:171], a[168:171]\n\t"
    "v_accvgpr_read_b32 v221, a125\n\t"
    "v_accvgpr_read_b32 v222, a126\n\t"
    "v_mfma_f32_16x16x32_bf16 a[172:175], v[180:183], v[172:175], a[172:175]\n\t"
    "v_accvgpr_read_b32 v223, a127\n\t"
    "v_cvt_pk_bf16_f32 v216, v216, v217\n\t"
    "v_mfma_f32_16x16x32_bf16 a[172:175], v[176:179], v[156:159], a[172:175]\n\t"
    "s_waitcnt lgkmcnt(4)\n\t"
    "v_cvt_pk_bf16_f32 v217, v218, v219\n\t"
    "v_cvt_pk_bf16_f32 v218, v220, v221\n\t"
    "v_mfma_f32_16x16x32_bf16 a[220:223], v[184:187], v[156:159], a[220:223]\n\t"
    "ds_read_b64_tr_b16 v[200:201], v241 offset:16576\n\t"
    "v_cvt_pk_bf16_f32 v219, v222, v223\n\t"
    "v_mfma_f32_16x16x32_bf16 a[220:223], v[188:191], v[172:175], a[220:223]\n\t"
    "ds_read_b64_tr_b16 v[202:203], v241 offset:20160\n\t"
    "v_accvgpr_write_b32 a108, v216\n\t"
    "v_mfma_f32_16x16x32_bf16 a[216:219], v[188:191], v[168:171], a[216:219]\n\t"
    "ds_read_b64_tr_b16 v[204:205], v241 offset:41152\n\t"
    "v_accvgpr_write_b32 a109, v217\n\t"
    "v_mfma_f32_16x16x32_bf16 a[216:219], v[184:187], v[152:155], a[216:219]\n\t"
    "ds_read_b64_tr_b16 v[206:207], v241 offset:44736\n\t"
    "v_accvgpr_write_b32 a110, v218\n\t"
    "v_mfma_f32_16x16x32_bf16 a[212:215], v[184:187], v[148:151], a[212:215]\n\t"
    "v_accvgpr_write_b32 a111, v219\n\t"
    "v_mul_f32 v8, v72, v8\n\t"
    "v_mfma_f32_16x16x32_bf16 a[212:215], v[188:191], v[164:167], a[212:215]\n\t"
    "v_mul_f32 v9, v73, v9\n\t"
    "v_mul_f32 v10, v74, v10\n\t"
    "v_mfma_f32_16x16x32_bf16 a[208:211], v[188:191], v[160:163], a[208:211]\n\t"
    "v_mul_f32 v11, v75, v11\n\t"
    "v_mul_f32 v24, v88, v24\n\t"
    "v_mfma_f32_16x16x32_bf16 a[208:211], v[184:187], v[144:147], a[208:211]\n\t"
    "s_waitcnt lgkmcnt(4)\n\t"
    "v_mul_f32 v25, v89, v25\n\t"
    "v_mul_f32 v26, v90, v26\n\t"
    "v_mfma_f32_16x16x32_bf16 a[176:179], v[192:195], v[144:147], a[176:179]\n\t"
    "v_mul_f32 v27, v91, v27\n\t"
    "v_cvt_pk_bf16_f32 v8, v8, v9\n\t"
    "v_mfma_f32_16x16x32_bf16 a[176:179], v[196:199], v[160:163], a[176:179]\n\t"
    "v_cvt_pk_bf16_f32 v9, v10, v11\n\t"
    "v_cvt_pk_bf16_f32 v10, v24, v25\n\t"
    "v_mfma_f32_16x16x32_bf16 a[180:183], v[196:199], v[164:167], a[180:183]\n\t"
    "v_cvt_pk_bf16_f32 v11, v26, v27\n\t"
    "v_mfma_f32_16x16x32_bf16 a[180:183], v[192:195], v[148:151], a[180:183]\n\t"
    "buffer_store_dwordx4 v[8:11], v248, s[88:91], s96 offen offset:2048 sc1\n\t"
    "v_mfma_f32_16x16x32_bf16 a[184:187], v[192:195], v[152:155], a[184:187]\n\t"
    "v_mul_f32 v12, v76, v12\n\t"
    "v_mul_f32 v13, v77, v13\n\t"
    "v_mfma_f32_16x16x32_bf16 a[184:187], v[196:199], v[168:171], a[184:187]\n\t"
    "v_mul_f32 v14, v78, v14\n\t"
    "v_mul_f32 v15, v79, v15\n\t"
    "v_mfma_f32_16x16x32_bf16 a[188:191], v[196:199], v[172:175], a[188:191]\n\t"
    "v_mul_f32 v28, v92, v28\n\t"
    "v_mul_f32 v29, v93, v29\n\t"
    "v_mfma_f32_16x16x32_bf16 a[188:191], v[192:195], v[156:159], a[188:191]\n\t"
    "s_waitcnt lgkmcnt(0)\n\t"
    "v_mul_f32 v30, v94, v30\n\t"
    "v_mul_f32 v31, v95, v31\n\t"
    "v_mfma_f32_16x16x32_bf16 a[236:239], v[200:203], v[156:159], a[236:239]\n\t"
    "v_cvt_pk_bf16_f32 v12, v12, v13\n\t"
    "v_cvt_pk_bf16_f32 v13, v14, v15\n\t"
    "v_mfma_f32_16x16x32_bf16 a[236:239], v[204:207], v[172:175], a[236:239]\n\t"
    "v_cvt_pk_bf16_f32 v14, v28, v29\n\t"
    "v_cvt_pk_bf16_f32 v15, v30, v31\n\t"
    "v_mfma_f32_16x16x32_bf16 a[232:235], v[204:207], v[168:171], a[232:235]\n\t"
    "buffer_store_dwordx4 v[12:15], v248, s[88:91], s96 offen offset:3072 sc1\n\t"
    "v_mfma_f32_16x16x32_bf16 a[232:235], v[200:203], v[152:155], a[232:235]\n\t"
    "v_mul_f32 v32, v96, v32\n\t"
    "v_mul_f32 v33, v97, v33\n\t"
    "v_mfma_f32_16x16x32_bf16 a[228:231], v[200:203], v[148:151], a[228:231]\n\t"
    "v_mul_f32 v34, v98, v34\n\t"
    "v_mul_f32 v35, v99, v35\n\t"
    "v_mfma_f32_16x16x32_bf16 a[228:231], v[204:207], v[164:167], a[228:231]\n\t"
    "v_accvgpr_read_b32 v112, a128\n\t"
    "v_accvgpr_read_b32 v113, a129\n\t"
    "v_mfma_f32_16x16x32_bf16 a[224:227], v[204:207], v[160:163], a[224:227]\n\t"
    "v_accvgpr_read_b32 v114, a130\n\t"
    "v_accvgpr_read_b32 v115, a131\n\t"
    "v_mfma_f32_16x16x32_bf16 a[224:227], v[200:203], v[144:147], a[224:227]\n\t"
    "v_mul_f32 v48, v112, v48\n\t"
    "v_mul_f32 v49, v113, v49\n\t"
    ".p2align 3\n\t"
    "v_mul_f32 v50, v114, v50\n\t"
    "v_mul_f32 v51, v115, v51\n\t"
    "v_cvt_pk_bf16_f32 v32, v32, v33\n\t"
    "v_cvt_pk_bf16_f32 v33, v34, v35\n\t"
    "v_cvt_pk_bf16_f32 v34, v48, v49\n\t"
    "v_cvt_pk_bf16_f32 v35, v50, v51\n\t"
    "buffer_store_dwordx4 v[32:35], v248, s[88:91], s97 offen sc1\n\t"
    "v_mov_b32 v254, 0x3f803f80\n\t"
    "v_pk_mul_f32 v[36:37], v[36:37], v[100:101]\n\t"
    "v_pk_mul_f32 v[38:39], v[38:39], v[102:103]\n\t"
    "v_accvgpr_read_b32 v180, a132\n\t"
    "v_accvgpr_read_b32 v181, a133\n\t"
    "v_accvgpr_read_b32 v182, a134\n\t"
    "v_accvgpr_read_b32 v183, a135\n\t"
    "v_pk_mul_f32 v[52:53], v[52:53], v[180:181]\n\t"
    "v_pk_mul_f32 v[54:55], v[54:55], v[182:183]\n\t"
    "v_pk_mul_f32 v[40:41], v[40:41], v[104:105]\n\t"
    "v_pk_mul_f32 v[42:43], v[42:43], v[106:107]\n\t"
    "v_accvgpr_read_b32 v184, a136\n\t"
    "v_accvgpr_read_b32 v185, a137\n\t"
    "v_accvgpr_read_b32 v186, a138\n\t"
    "v_accvgpr_read_b32 v187, a139\n\t"
    "v_pk_mul_f32 v[56:57], v[56:57], v[184:185]\n\t"
    "v_pk_mul_f32 v[58:59], v[58:59], v[186:187]\n\t"
    "v_pk_mul_f32 v[44:45], v[44:45], v[108:109]\n\t"
    "v_pk_mul_f32 v[46:47], v[46:47], v[110:111]\n\t"
    "v_accvgpr_read_b32 v188, a140\n\t"
    "v_accvgpr_read_b32 v189, a141\n\t"
    "v_accvgpr_read_b32 v190, a142\n\t"
    "v_accvgpr_read_b32 v191, a143\n\t"
    "v_pk_mul_f32 v[60:61], v[60:61], v[188:189]\n\t"
    "v_pk_mul_f32 v[62:63], v[62:63], v[190:191]\n\t"
    "v_mfma_f32_16x16x32_bf16 v[160:163], a[16:19], a[0:3], 0\n\t"
    "v_cvt_pk_bf16_f32 v36, v36, v37\n\t"
    "v_cvt_pk_bf16_f32 v37, v38, v39\n\t"
    "v_mfma_f32_16x16x32_bf16 v[160:163], a[48:51], a[32:35], v[160:163]\n\t"
    "v_cvt_pk_bf16_f32 v38, v52, v53\n\t"
    "v_cvt_pk_bf16_f32 v39, v54, v55\n\t"
    "v_mfma_f32_16x16x32_bf16 v[160:163], a[80:83], a[64:67], v[160:163]\n\t"
    "buffer_store_dwordx4 v[36:39], v248, s[88:91], s97 offen offset:1024 sc1\n\t"
    "v_cvt_pk_bf16_f32 v40, v40, v41\n\t"
    "v_mfma_f32_16x16x32_bf16 v[160:163], a[112:115], a[96:99], v[160:163]\n\t"
    "v_cvt_pk_bf16_f32 v41, v42, v43\n\t"
    "v_cvt_pk_bf16_f32 v42, v56, v57\n\t"
    "v_mfma_f32_16x16x32_bf16 v[164:167], a[112:115], a[100:103], 0\n\t"
    "v_cvt_pk_bf16_f32 v43, v58, v59\n\t"
    "buffer_store_dwordx4 v[40:43], v248, s[88:91], s97 offen offset:2048 sc1\n\t"
    "v_mfma_f32_16x16x32_bf16 v[164:167], a[80:83], a[68:71], v[164:167]\n\t"
    "v_cvt_pk_bf16_f32 v44, v44, v45\n\t"
    "v_cvt_pk_bf16_f32 v45, v46, v47\n\t"
    "v_mfma_f32_16x16x32_bf16 v[164:167], a[48:51], a[36:39], v[164:167]\n\t"
    "v_cvt_pk_bf16_f32 v46, v60, v61\n\t"
    "v_cvt_pk_bf16_f32 v47, v62, v63\n\t"
    "v_mfma_f32_16x16x32_bf16 v[164:167], a[16:19], a[4:7], v[164:167]\n\t"
    "buffer_store_dwordx4 v[44:47], v248, s[88:91], s97 offen offset:3072 sc1\n\t"
    "v_accvgpr_write_b32 a240, v254\n\t"
    "v_mfma_f32_16x16x32_bf16 v[168:171], a[16:19], a[8:11], 0\n\t"
    "v_accvgpr_write_b32 a241, v254\n\t"
    "v_accvgpr_write_b32 a242, v254\n\t"
    "v_mfma_f32_16x16x32_bf16 v[168:171], a[48:51], a[40:43], v[168:171]\n\t"
    "v_accvgpr_write_b32 a243, v254\n\t"
    "v_mfma_f32_16x16x32_bf16 v[168:171], a[80:83], a[72:75], v[168:171]\n\t"
    "v_mfma_f32_16x16x32_bf16 v[168:171], a[112:115], a[104:107], v[168:171]\n\t"
    "v_mfma_f32_16x16x32_bf16 v[172:175], a[112:115], a[108:111], 0\n\t"
    "v_mfma_f32_16x16x32_bf16 v[172:175], a[80:83], a[76:79], v[172:175]\n\t"
    "v_mfma_f32_16x16x32_bf16 v[172:175], a[48:51], a[44:47], v[172:175]\n\t"
    "v_mfma_f32_16x16x32_bf16 v[172:175], a[16:19], a[12:15], v[172:175]\n\t"
    "v_max3_f32 v208, v160, v161, v162\n\t"
    "v_max_f32_e64 v208, v208, v163\n\t"
    "v_max3_f32 v209, v164, v165, v166\n\t"
    "v_max_f32_e64 v209, v209, v167\n\t"
    "v_max3_f32 v210, v168, v169, v170\n\t"
    "v_max_f32 v210, v210, v171\n\t"
    "s_nop 1\n\t"
    "v_max3_f32 v211, v172, v173, v174\n\t"
    "v_max_f32 v211, v211, v175\n\t"
    ".Lfb_att:\n\t"
    "v_mov_b32 v212, v208\n\t"
    "v_mov_b32 v213, v209\n\t"
    "v_mov_b32 v214, v210\n\t"
    "v_mov_b32 v215, v211\n\t"
    "v_permlane16_swap_b32 v208, v212\n\t"
    "v_permlane16_swap_b32 v209, v213\n\t"
    "v_permlane16_swap_b32 v210, v214\n\t"
    "v_permlane16_swap_b32 v211, v215\n\t"
    "v_max_f32 v208, v208, v212\n\t"
    "v_max_f32 v209, v209, v213\n\t"
    "v_max_f32 v210, v210, v214\n\t"
    "v_max_f32 v211, v211, v215\n\t"
    "v_mov_b32 v212, v208\n\t"
    "v_mov_b32 v213, v209\n\t"
    "v_mov_b32 v214, v210\n\t"
    "v_mov_b32 v215, v211\n\t"
    "v_permlane32_swap_b32 v208, v212\n\t"
    "v_permlane32_swap_b32 v209, v213\n\t"
    "v_permlane32_swap_b32 v210, v214\n\t"
    "v_permlane32_swap_b32 v211, v215\n\t"
    "v_max_f32 v208, v208, v212\n\t"
    "v_max_f32 v209, v209, v213\n\t"
    "v_max_f32 v210, v210, v214\n\t"
    "v_max_f32 v211, v211, v215\n\t"
    "v_sub_f32 v224, 0, v208\n\t"
    "v_sub_f32 v225, 0, v208\n\t"
    "v_sub_f32 v226, 0, v208\n\t"
    "v_sub_f32 v227, 0, v208\n\t"
    "v_sub_f32 v228, 0, v209\n\t"
    "v_sub_f32 v229, 0, v209\n\t"
    "v_sub_f32 v230, 0, v209\n\t"
    "v_sub_f32 v231, 0, v209\n\t"
    "v_sub_f32 v232, 0, v210\n\t"
    "v_sub_f32 v233, 0, v210\n\t"
    "v_sub_f32 v234, 0, v210\n\t"
    "v_sub_f32 v235, 0, v210\n\t"
    "v_sub_f32 v236, 0, v211\n\t"
    "v_sub_f32 v237, 0, v211\n\t"
    "v_sub_f32 v238, 0, v211\n\t"
    "v_sub_f32 v239, 0, v211\n\t"
    "v_mfma_f32_16x16x32_bf16 v[128:131], a[16:19], a[0:3], v[224:227]\n\t"
    "s_bfe_u32 s49, s2, 131073\n\t"
    "v_mul_f32 v0, v64, v0\n\t"
    "v_mul_f32 v1, v65, v1\n\t"
    "v_mfma_f32_16x16x32_bf16 v[128:131], a[48:51], a[32:35], v[128:131]\n\t"
    "s_and_b32 s77, s2, 1\n\t"
    "v_mul_f32 v2, v66, v2\n\t"
    "v_mul_f32 v3, v67, v3\n\t"
    "v_mfma_f32_16x16x32_bf16 v[128:131], a[80:83], a[64:67], v[128:131]\n\t"
    "s_lshl_b32 s77, s77, 2\n\t"
    "v_mul_f32 v16, v80, v16\n\t"
    "v_mul_f32 v17, v81, v17\n\t"
    "v_mfma_f32_16x16x32_bf16 v[128:131], a[112:115], a[96:99], v[128:131]\n\t"
    "s_bfe_u32 s78, s2, 131075\n\t"
    "v_mul_f32 v18, v82, v18\n\t"
    "v_mul_f32 v19, v83, v19\n\t"
    "v_mfma_f32_16x16x32_bf16 v[132:135], a[112:115], a[100:103], v[228:231]\n\t"
    "s_add_u32 s50, s77, s78\n\t"
    "v_cvt_pk_bf16_f32 v0, v0, v1\n\t"
    "v_cvt_pk_bf16_f32 v1, v2, v3\n\t"
    "v_mfma_f32_16x16x32_bf16 v[132:135], a[80:83], a[68:71], v[132:135]\n\t"
    "s_add_u32 s84, s85, 81920\n\t"
    "v_cvt_pk_bf16_f32 v2, v16, v17\n\t"
    "v_cvt_pk_bf16_f32 v3, v18, v19\n\t"
    "v_mfma_f32_16x16x32_bf16 v[132:135], a[48:51], a[36:39], v[132:135]\n\t"
    "buffer_store_dwordx4 v[0:3], v248, s[88:91], s96 offen sc1\n\t"
    "v_mfma_f32_16x16x32_bf16 v[132:135], a[16:19], a[4:7], v[132:135]\n\t"
    "s_lshr_b32 s51, s2, 5\n\t"
    "v_mul_f32 v4, v68, v4\n\t"
    "v_mul_f32 v5, v69, v5\n\t"
    "v_mfma_f32_16x16x32_bf16 v[136:139], a[16:19], a[8:11], v[232:235]\n\t"
    "s_lshl_b32 s52, s50, 3\n\t"
    "v_mul_f32 v6, v70, v6\n\t"
    "v_mul_f32 v7, v71, v7\n\t"
    "v_mfma_f32_16x16x32_bf16 v[136:139], a[48:51], a[40:43], v[136:139]\n\t"
    "s_add_u32 s52, s52, s51\n\t"
    "v_mul_f32 v20, v84, v20\n\t"
    "v_mul_f32 v21, v85, v21\n\t"
    "v_mfma_f32_16x16x32_bf16 v[136:139], a[80:83], a[72:75], v[136:139]\n\t"
    "s_mul_i32 s77, s49, 1835008\n\t"
    "v_mul_f32 v22, v86, v22\n\t"
    "v_mul_f32 v23, v87, v23\n\t"
    "v_mfma_f32_16x16x32_bf16 v[136:139], a[112:115], a[104:107], v[136:139]\n\t"
    "v_cvt_pk_bf16_f32 v4, v4, v5\n\t"
    "v_cvt_pk_bf16_f32 v5, v6, v7\n\t"
    "v_mfma_f32_16x16x32_bf16 v[140:143], a[112:115], a[108:111], v[236:239]\n\t"
    "v_exp_f32 v128, v128\n\t"
    "v_mfma_f32_16x16x32_bf16 v[140:143], a[80:83], a[76:79], v[140:143]\n\t"
    "v_exp_f32 v129, v129\n\t"
    "v_mfma_f32_16x16x32_bf16 v[140:143], a[48:51], a[44:47], v[140:143]\n\t"
    "v_cvt_pk_bf16_f32 v6, v20, v21\n\t"
    "v_cvt_pk_bf16_f32 v7, v22, v23\n\t"
    "v_mfma_f32_16x16x32_bf16 v[140:143], a[16:19], a[12:15], v[140:143]\n\t"
    "v_exp_f32 v130, v130\n\t"
    "v_mfma_f32_16x16x32_bf16 v[156:159], a[20:23], a[12:15], v[236:239]\n\t"
    "ds_read_b128 a[16:19], v250 offset:0\n\t"
    "v_cvt_pk_bf16_f32 v192, v128, v129\n\t"
    "v_mfma_f32_16x16x32_bf16 v[156:159], a[52:55], a[44:47], v[156:159]\n\t"
    "v_exp_f32 v131, v131\n\t"
    "v_mfma_f32_16x16x32_bf16 v[156:159], a[84:87], a[76:79], v[156:159]\n\t"
    "ds_read_b128 a[48:51], v250 offset:1024\n\t"
    "v_mfma_f32_16x16x32_bf16 v[156:159], a[116:119], a[108:111], v[156:159]\n\t"
    "v_exp_f32 v132, v132\n\t"
    "v_mfma_f32_16x16x32_bf16 v[152:155], a[116:119], a[104:107], v[232:235]\n\t"
    "ds_read_b128 a[80:83], v250 offset:2048\n\t"
    "v_cvt_pk_bf16_f32 v193, v130, v131\n\t"
    "v_mfma_f32_16x16x32_bf16 v[152:155], a[84:87], a[72:75], v[152:155]\n\t"
    "v_exp_f32 v133, v133\n\t"
    "v_mfma_f32_16x16x32_bf16 v[152:155], a[52:55], a[40:43], v[152:155]\n\t"
    "ds_read_b128 a[112:115], v250 offset:3072\n\t"
    "v_mfma_f32_16x16x32_bf16 v[152:155], a[20:23], a[8:11], v[152:155]\n\t"
    "v_exp_f32 v134, v134\n\t"
    "v_mfma_f32_16x16x32_bf16 v[148:151], a[20:23], a[4:7], v[228:231]\n\t"
    "v_exp_f32 v135, v135\n\t"
    "v_mfma_f32_16x16x32_bf16 v[148:151], a[52:55], a[36:39], v[148:151]\n\t"
    "v_exp_f32 v136, v136\n\t"
    "v_mfma_f32_16x16x32_bf16 v[148:151], a[84:87], a[68:71], v[148:151]\n\t"
    "v_exp_f32 v137, v137\n\t"
    "v_mfma_f32_16x16x32_bf16 v[148:151], a[116:119], a[100:103], v[148:151]\n\t"
    "v_exp_f32 v138, v138\n\t"
    "v_mfma_f32_16x16x32_bf16 v[144:147], a[116:119], a[96:99], v[224:227]\n\t"
    "v_exp_f32 v139, v139\n\t"
    "v_mfma_f32_16x16x32_bf16 v[144:147], a[84:87], a[64:67], v[144:147]\n\t"
    "v_cvt_pk_bf16_f32 v196, v132, v133\n\t"
    "v_cvt_pk_bf16_f32 v197, v134, v135\n\t"
    "v_mfma_f32_16x16x32_bf16 v[144:147], a[52:55], a[32:35], v[144:147]\n\t"
    "v_exp_f32 v140, v140\n\t"
    "v_mfma_f32_16x16x32_bf16 v[144:147], a[20:23], a[0:3], v[144:147]\n\t"
    "s_waitcnt lgkmcnt(0)\n\t"
    "v_exp_f32 v141, v141\n\t"
    "v_mfma_f32_16x16x32_bf16 v[160:163], a[16:19], a[0:3], v[224:227]\n\t"
    "ds_read_b128 a[20:23], v250 offset:4096\n\t"
    "v_cvt_pk_bf16_f32 v200, v136, v137\n\t"
    "v_mfma_f32_16x16x32_bf16 v[160:163], a[48:51], a[32:35], v[160:163]\n\t"
    "v_exp_f32 v142, v142\n\t"
    "v_mfma_f32_16x16x32_bf16 v[160:163], a[80:83], a[64:67], v[160:163]\n\t"
    "ds_read_b128 a[52:55], v250 offset:5120\n\t"
    "v_cvt_pk_bf16_f32 v201, v138, v139\n\t"
    "v_mfma_f32_16x16x32_bf16 v[160:163], a[112:115], a[96:99], v[160:163]\n\t"
    "v_exp_f32 v143, v143\n\t"
    "v_mfma_f32_16x16x32_bf16 v[164:167], a[112:115], a[100:103], v[228:231]\n\t"
    "ds_read_b128 a[84:87], v250 offset:6144\n\t"
    "v_cvt_pk_bf16_f32 v204, v140, v141\n\t"
    "v_mfma_f32_16x16x32_bf16 v[164:167], a[80:83], a[68:71], v[164:167]\n\t"
    "v_exp_f32 v156, v156\n\t"
    "v_mfma_f32_16x16x32_bf16 v[164:167], a[48:51], a[36:39], v[164:167]\n\t"
    "ds_read_b128 a[116:119], v250 offset:7168\n\t"
    "v_cvt_pk_bf16_f32 v205, v142, v143\n\t"
    "v_mfma_f32_16x16x32_bf16 v[164:167], a[16:19], a[4:7], v[164:167]\n\t"
    "v_exp_f32 v157, v157\n\t"
    "v_mfma_f32_16x16x32_bf16 v[168:171], a[16:19], a[8:11], v[232:235]\n\t"
    "v_exp_f32 v158, v158\n\t"
    "v_mfma_f32_16x16x32_bf16 v[168:171], a[48:51], a[40:43], v[168:171]\n\t"
    "v_exp_f32 v159, v159\n\t"
    "v_mfma_f32_16x16x32_bf16 v[168:171], a[80:83], a[72:75], v[168:171]\n\t"
    "v_exp_f32 v152, v152\n\t"
    "v_mfma_f32_16x16x32_bf16 v[168:171], a[112:115], a[104:107], v[168:171]\n\t"
    "v_exp_f32 v153, v153\n\t"
    "v_mfma_f32_16x16x32_bf16 v[172:175], a[112:115], a[108:111], v[236:239]\n\t"
    "v_cvt_pk_bf16_f32 v206, v156, v157\n\t"
    "v_cvt_pk_bf16_f32 v207, v158, v159\n\t"
    "v_mfma_f32_16x16x32_bf16 v[172:175], a[80:83], a[76:79], v[172:175]\n\t"
    "v_exp_f32 v154, v154\n\t"
    "v_mfma_f32_16x16x32_bf16 v[172:175], a[48:51], a[44:47], v[172:175]\n\t"
    "v_exp_f32 v155, v155\n\t"
    "v_mfma_f32_16x16x32_bf16 v[172:175], a[16:19], a[12:15], v[172:175]\n\t"
    "s_waitcnt lgkmcnt(0)\n\t"
    "v_cvt_pk_bf16_f32 v202, v152, v153\n\t"
    "v_mfma_f32_16x16x32_bf16 v[188:191], a[20:23], a[12:15], v[236:239]\n\t"
    "ds_read_b128 a[16:19], v250 offset:16384\n\t"
    "v_cvt_pk_bf16_f32 v203, v154, v155\n\t"
    "v_mfma_f32_16x16x32_bf16 v[188:191], a[52:55], a[44:47], v[188:191]\n\t"
    "v_exp_f32 v148, v148\n\t"
    "v_mfma_f32_16x16x32_bf16 v[188:191], a[84:87], a[76:79], v[188:191]\n\t"
    "ds_read_b128 a[48:51], v250 offset:17408\n\t"
    "v_mfma_f32_16x16x32_bf16 v[188:191], a[116:119], a[108:111], v[188:191]\n\t"
    "v_exp_f32 v149, v149\n\t"
    "v_mfma_f32_16x16x32_bf16 v[184:187], a[116:119], a[104:107], v[232:235]\n\t"
    "ds_read_b128 a[80:83], v250 offset:18432\n\t"
    "v_mfma_f32_16x16x32_bf16 v[184:187], a[84:87], a[72:75], v[184:187]\n\t"
    "v_exp_f32 v150, v150\n\t"
    "v_mfma_f32_16x16x32_bf16 v[184:187], a[52:55], a[40:43], v[184:187]\n\t"
    "ds_read_b128 a[112:115], v250 offset:19456\n\t"
    "v_cvt_pk_bf16_f32 v198, v148, v149\n\t"
    "v_mfma_f32_16x16x32_bf16 v[184:187], a[20:23], a[8:11], v[184:187]\n\t"
    "v_exp_f32 v151, v151\n\t"
    "v_mfma_f32_16x16x32_bf16 v[180:183], a[20:23], a[4:7], v[228:231]\n\t"
    "v_exp_f32 v144, v144\n\t"
    "v_mfma_f32_16x16x32_bf16 v[180:183], a[52:55], a[36:39], v[180:183]\n\t"
    "v_exp_f32 v145, v145\n\t"
    "v_mfma_f32_16x16x32_bf16 v[180:183], a[84:87], a[68:71], v[180:183]\n\t"
    "v_exp_f32 v146, v146\n\t"
    "v_mfma_f32_16x16x32_bf16 v[180:183], a[116:119], a[100:103], v[180:183]\n\t"
    "v_exp_f32 v147, v147\n\t"
    "v_mfma_f32_16x16x32_bf16 v[176:179], a[116:119], a[96:99], v[224:227]\n\t"
    "v_cvt_pk_bf16_f32 v199, v150, v151\n\t"
    "v_cvt_pk_bf16_f32 v194, v144, v145\n\t"
    "v_mfma_f32_16x16x32_bf16 v[176:179], a[84:87], a[64:67], v[176:179]\n\t"
    "v_cvt_pk_bf16_f32 v195, v146, v147\n\t"
    "v_mfma_f32_16x16x32_bf16 v[176:179], a[52:55], a[32:35], v[176:179]\n\t"
    "buffer_store_dwordx4 v[4:7], v248, s[88:91], s96 offen offset:1024 sc1\n\t"
    "v_mfma_f32_16x16x32_bf16 v[176:179], a[20:23], a[0:3], v[176:179]\n\t"
    "s_waitcnt lgkmcnt(0)\n\t"
    "v_accvgpr_read_b32 v241, a144\n\t"
    "v_accvgpr_read_b32 v242, a192\n\t"
    "v_mfma_f32_16x16x32_bf16 v[128:131], a[16:19], a[0:3], v[224:227]\n\t"
    "ds_read_b128 a[20:23], v250 offset:20480\n\t"
    "v_accvgpr_read_b32 v243, a145\n\t"
    "v_mfma_f32_16x16x32_bf16 v[128:131], a[48:51], a[32:35], v[128:131]\n\t"
    "v_exp_f32 v160, v160\n\t"
    "v_mfma_f32_16x16x32_bf16 v[128:131], a[80:83], a[64:67], v[128:131]\n\t"
    "ds_read_b128 a[52:55], v250 offset:21504\n\t"
    "v_accvgpr_read_b32 v249, a193\n\t"
    "v_mfma_f32_16x16x32_bf16 v[128:131], a[112:115], a[96:99], v[128:131]\n\t"
    "s_waitcnt vmcnt(9)\n\t"
    "s_barrier\n\t"
    "v_mul_f32 v241, v241, v242\n\t"
    "v_mfma_f32_16x16x32_bf16 v[132:135], a[112:115], a[100:103], v[228:231]\n\t"
    "ds_read_b128 a[84:87], v250 offset:22528\n\t"
    "s_mul_i32 s10, s3, 2816\n\t"
    "v_mfma_f32_16x16x32_bf16 v[132:135], a[80:83], a[68:71], v[132:135]\n\t"
    "s_add_u32 s10, s10, 22528\n\t"
    "s_min_u32 s10, s10, s26\n\t"
    "v_min_u32 v252, 336, v248\n\t"
    "v_mfma_f32_16x16x32_bf16 v[132:135], a[48:51], a[36:39], v[132:135]\n\t"
    "ds_read_b128 a[116:119], v250 offset:23552\n\t"
    "v_lshlrev_b32 v252, 3, v252\n\t"
    "v_mfma_f32_16x16x32_bf16 v[132:135], a[16:19], a[4:7], v[132:135]\n\t"
    "buffer_load_dword a246, v252, s[24:27], s10 offen\n\t"
    "v_mfma_f32_16x16x32_bf16 v[136:139], a[16:19], a[8:11], v[232:235]\n\t"
    "v_exp_f32 v161, v161\n\t"
    "v_mfma_f32_16x16x32_bf16 v[136:139], a[48:51], a[40:43], v[136:139]\n\t"
    "s_add_u32 m0, s85, 0\n\t"
    "v_exp_f32 v162, v162\n\t"
    "v_mfma_f32_16x16x32_bf16 v[136:139], a[80:83], a[72:75], v[136:139]\n\t"
    "v_exp_f32 v163, v163\n\t"
    "v_mfma_f32_16x16x32_bf16 v[136:139], a[112:115], a[104:107], v[136:139]\n\t"
    "buffer_load_dwordx4 v248, s[80:83], s84 offen sc1 lds\n\t"
    "v_mfma_f32_16x16x32_bf16 v[140:143], a[112:115], a[108:111], v[236:239]\n\t"
    "ds_read_b128 a[24:27], v248 offset:57344\n\t"
    "v_cvt_pk_bf16_f32 v208, v160, v161\n\t"
    "v_mfma_f32_16x16x32_bf16 v[140:143], a[80:83], a[76:79], v[140:143]\n\t"
    "v_exp_f32 v164, v164\n\t"
    "v_mfma_f32_16x16x32_bf16 v[140:143], a[48:51], a[44:47], v[140:143]\n\t"
    "v_exp_f32 v165, v165\n\t"
    "v_mfma_f32_16x16x32_bf16 v[140:143], a[16:19], a[12:15], v[140:143]\n\t"
    "s_waitcnt lgkmcnt(1)\n\t"
    "v_exp_f32 v166, v166\n\t"
    "v_mfma_f32_16x16x32_bf16 v[156:159], a[20:23], a[12:15], v[236:239]\n\t"
    "ds_read_b128 a[56:59], v248 offset:58368\n\t"
    "v_cvt_pk_bf16_f32 v209, v162, v163\n\t"
    "v_mfma_f32_16x16x32_bf16 v[156:159], a[52:55], a[44:47], v[156:159]\n\t"
    "v_exp_f32 v167, v167\n\t"
    "v_mfma_f32_16x16x32_bf16 v[156:159], a[84:87], a[76:79], v[156:159]\n\t"
    "v_exp_f32 v168, v168\n\t"
    "v_mfma_f32_16x16x32_bf16 v[156:159], a[116:119], a[108:111], v[156:159]\n\t"
    "buffer_load_dwordx4 v248, s[80:83], s84 offen offset:1024 sc1 lds\n\t"
    "v_mfma_f32_16x16x32_bf16 v[152:155], a[116:119], a[104:107], v[232:235]\n\t"
    "ds_read_b128 a[88:91], v248 offset:59392\n\t"
    "v_cvt_pk_bf16_f32 v212, v164, v165\n\t"
    "v_mfma_f32_16x16x32_bf16 v[152:155], a[84:87], a[72:75], v[152:155]\n\t"
    "v_exp_f32 v169, v169\n\t"
    "v_mfma_f32_16x16x32_bf16 v[152:155], a[52:55], a[40:43], v[152:155]\n\t"
    "v_exp_f32 v170, v170\n\t"
    "v_mfma_f32_16x16x32_bf16 v[152:155], a[20:23], a[8:11], v[152:155]\n\t"
    "v_exp_f32 v171, v171\n\t"
    "v_mfma_f32_16x16x32_bf16 v[148:151], a[20:23], a[4:7], v[228:231]\n\t"
    "ds_read_b128 a[120:123], v248 offset:60416\n\t"
    "v_cvt_pk_bf16_f32 v213, v166, v167\n\t"
    "v_mfma_f32_16x16x32_bf16 v[148:151], a[52:55], a[36:39], v[148:151]\n\t"
    "v_exp_f32 v172, v172\n\t"
    "v_mfma_f32_16x16x32_bf16 v[148:151], a[84:87], a[68:71], v[148:151]\n\t"
    "v_exp_f32 v173, v173\n\t"
    "v_mfma_f32_16x16x32_bf16 v[148:151], a[116:119], a[100:103], v[148:151]\n\t"
    "buffer_load_dwordx4 v248, s[80:83], s84 offen offset:2048 sc1 lds\n\t"
    "v_mfma_f32_16x16x32_bf16 v[144:147], a[116:119], a[96:99], v[224:227]\n\t"
    "ds_read_b128 a[28:31], v248 offset:61440\n\t"
    "v_cvt_pk_bf16_f32 v216, v168, v169\n\t"
    "v_mfma_f32_16x16x32_bf16 v[144:147], a[84:87], a[64:67], v[144:147]\n\t"
    "v_exp_f32 v174, v174\n\t"
    "v_mfma_f32_16x16x32_bf16 v[144:147], a[52:55], a[32:35], v[144:147]\n\t"
    "v_exp_f32 v175, v175\n\t"
    "v_mfma_f32_16x16x32_bf16 v[144:147], a[20:23], a[0:3], v[144:147]\n\t"
    "s_waitcnt lgkmcnt(3)\n\t"
    "v_cvt_pk_bf16_f32 v217, v170, v171\n\t"
    "v_cvt_pk_bf16_f32 v220, v172, v173\n\t"
    "v_mfma_f32_16x16x32_bf16 v[0:3], a[24:27], v[192:195], 0\n\t"
    "ds_read_b128 a[60:63], v248 offset:62464\n\t"
    "v_cvt_pk_bf16_f32 v221, v174, v175\n\t"
    "v_mfma_f32_16x16x32_bf16 v[4:7], a[24:27], v[196:199], 0\n\t"
    "v_exp_f32 v188, v188\n\t"
    "v_mfma_f32_16x16x32_bf16 v[8:11], a[24:27], v[200:203], 0\n\t"
    "v_exp_f32 v189, v189\n\t"
    "v_mfma_f32_16x16x32_bf16 v[12:15], a[24:27], v[204:207], 0\n\t"
    "buffer_load_dwordx4 v248, s[80:83], s84 offen offset:3072 sc1 lds\n\t"
    "v_mfma_f32_16x16x32_bf16 v[28:31], a[56:59], v[204:207], 0\n\t"
    "ds_read_b128 a[92:95], v248 offset:63488\n\t"
    "v_cvt_pk_bf16_f32 v222, v188, v189\n\t"
    "v_mfma_f32_16x16x32_bf16 v[24:27], a[56:59], v[200:203], 0\n\t"
    "v_exp_f32 v190, v190\n\t"
    "v_mfma_f32_16x16x32_bf16 v[20:23], a[56:59], v[196:199], 0\n\t"
    "v_exp_f32 v191, v191\n\t"
    "v_mfma_f32_16x16x32_bf16 v[16:19], a[56:59], v[192:195], 0\n\t"
    "s_waitcnt lgkmcnt(3)\n\t"
    "v_mul_f32 v243, v243, v249\n\t"
    "v_cvt_pk_bf16_f32 v244, v241, v243\n\t"
    "v_mfma_f32_16x16x32_bf16 v[32:35], a[88:91], v[192:195], 0\n\t"
    "ds_read_b128 a[124:127], v248 offset:64512\n\t"
    "v_cvt_pk_bf16_f32 v223, v190, v191\n\t"
    "v_mfma_f32_16x16x32_bf16 v[36:39], a[88:91], v[196:199], 0\n\t"
    "v_exp_f32 v184, v184\n\t"
    "v_mfma_f32_16x16x32_bf16 v[40:43], a[88:91], v[200:203], 0\n\t"
    "v_exp_f32 v185, v185\n\t"
    "v_mfma_f32_16x16x32_bf16 v[44:47], a[88:91], v[204:207], 0\n\t"
    "s_add_u32 s84, s84, 16384\n\t"
    "v_accvgpr_read_b32 v241, a146\n\t"
    "v_accvgpr_read_b32 v242, a194\n\t"
    "v_mfma_f32_16x16x32_bf16 v[60:63], a[120:123], v[204:207], 0\n\t"
    "ds_read_b128 a[16:19], v250 offset:32768\n\t"
    "v_cvt_pk_bf16_f32 v218, v184, v185\n\t"
    "v_mfma_f32_16x16x32_bf16 v[56:59], a[120:123], v[200:203], 0\n\t"
    "v_exp_f32 v186, v186\n\t"
    "v_mfma_f32_16x16x32_bf16 v[52:55], a[120:123], v[196:199], 0\n\t"
    "v_exp_f32 v187, v187\n\t"
    "v_mfma_f32_16x16x32_bf16 v[48:51], a[120:123], v[192:195], 0\n\t"
    "s_waitcnt lgkmcnt(3)\n\t"
    "v_accvgpr_read_b32 v243, a147\n\t"
    "v_accvgpr_read_b32 v249, a195\n\t"
    "v_mfma_f32_16x16x32_bf16 v[64:67], a[28:31], v[192:195], 0\n\t"
    "ds_read_b128 a[48:51], v250 offset:33792\n\t"
    "v_cvt_pk_bf16_f32 v219, v186, v187\n\t"
    "v_mfma_f32_16x16x32_bf16 v[68:71], a[28:31], v[196:199], 0\n\t"
    "v_exp_f32 v180, v180\n\t"
    "v_mfma_f32_16x16x32_bf16 v[72:75], a[28:31], v[200:203], 0\n\t"
    "v_exp_f32 v181, v181\n\t"
    "v_mfma_f32_16x16x32_bf16 v[76:79], a[28:31], v[204:207], 0\n\t"
    "v_mul_f32 v241, v241, v242\n\t"
    "v_mul_f32 v243, v243, v249\n\t"
    "v_mfma_f32_16x16x32_bf16 v[92:95], a[60:63], v[204:207], 0\n\t"
    "ds_read_b128 a[80:83], v250 offset:34816\n\t"
    "v_cvt_pk_bf16_f32 v214, v180, v181\n\t"
    "v_mfma_f32_16x16x32_bf16 v[88:91], a[60:63], v[200:203], 0\n\t"
    "v_exp_f32 v182, v182\n\t"
    "v_mfma_f32_16x16x32_bf16 v[84:87], a[60:63], v[196:199], 0\n\t"
    "v_exp_f32 v183, v183\n\t"
    "v_mfma_f32_16x16x32_bf16 v[80:83], a[60:63], v[192:195], 0\n\t"
    "s_waitcnt lgkmcnt(3)\n\t"
    "v_cvt_pk_bf16_f32 v245, v241, v243\n\t"
    "v_accvgpr_read_b32 v241, a160\n\t"
    "v_mfma_f32_16x16x32_bf16 v[96:99], a[92:95], v[192:195], 0\n\t"
    "ds_read_b128 a[112:115], v250 offset:35840\n\t"
    "v_cvt_pk_bf16_f32 v215, v182, v183\n\t"
    "v_mfma_f32_16x16x32_bf16 v[100:103], a[92:95], v[196:199], 0\n\t"
    "v_exp_f32 v176, v176\n\t"
    "v_mfma_f32_16x16x32_bf16 v[104:107], a[92:95], v[200:203], 0\n\t"
    "v_exp_f32 v177, v177\n\t"
    "v_mfma_f32_16x16x32_bf16 v[108:111], a[92:95], v[204:207], 0\n\t"
    "v_accvgpr_read_b32 v242, a208\n\t"
    "v_accvgpr_read_b32 v243, a161\n\t"
    "v_mfma_f32_16x16x32_bf16 v[124:127], a[124:127], v[204:207], 0\n\t"
    "ds_read_b128 a[20:23], v250 offset:36864\n\t"
    "v_cvt_pk_bf16_f32 v210, v176, v177\n\t"
    "v_mfma_f32_16x16x32_bf16 v[120:123], a[124:127], v[200:203], 0\n\t"
    "v_exp_f32 v178, v178\n\t"
    "v_mfma_f32_16x16x32_bf16 v[116:119], a[124:127], v[196:199], 0\n\t"
    "v_exp_f32 v179, v179\n\t"
    "v_mfma_f32_16x16x32_bf16 v[112:115], a[124:127], v[192:195], 0\n\t"
    "v_accvgpr_read_b32 v249, a209\n\t"
    "v_mul_f32 v241, v241, v242\n\t"
    "v_mfma_f32_16x16x32_bf16 a[128:131], a[240:243], v[192:195], 0\n\t"
    "ds_read_b128 a[52:55], v250 offset:37888\n\t"
    "v_cvt_pk_bf16_f32 v211, v178, v179\n\t"
    "v_mfma_f32_16x16x32_bf16 a[132:135], a[240:243], v[196:199], 0\n\t"
    "v_mul_f32 v243, v243, v249\n\t"
    "v_cvt_pk_bf16_f32 v246, v241, v243\n\t"
    "v_mfma_f32_16x16x32_bf16 a[136:139], a[240:243], v[200:203], 0\n\t"
    "v_accvgpr_read_b32 v241, a162\n\t"
    "v_accvgpr_read_b32 v242, a210\n\t"
    "v_mfma_f32_16x16x32_bf16 a[140:143], a[240:243], v[204:207], 0\n\t"
    "s_waitcnt lgkmcnt(2)\n\t"
    "v_accvgpr_read_b32 v243, a163\n\t"
    "v_accvgpr_read_b32 v249, a211\n\t"
    "v_mfma_f32_16x16x32_bf16 v[160:163], a[16:19], a[0:3], v[224:227]\n\t"
    "ds_read_b128 a[84:87], v250 offset:38912\n\t"
    "v_mul_f32 v241, v241, v242\n\t"
    "v_mfma_f32_16x16x32_bf16 v[160:163], a[48:51], a[32:35], v[160:163]\n\t"
    "v_exp_f32 v128, v128\n\t"
    "v_mfma_f32_16x16x32_bf16 v[160:163], a[80:83], a[64:67], v[160:163]\n\t"
    "v_exp_f32 v129, v129\n\t"
    "v_mfma_f32_16x16x32_bf16 v[160:163], a[112:115], a[96:99], v[160:163]\n\t"
    "v_mul_f32 v243, v243, v249\n\t"
    "v_cvt_pk_bf16_f32 v247, v241, v243\n\t"
    "v_mfma_f32_16x16x32_bf16 v[164:167], a[112:115], a[100:103], v[228:231]\n\t"
    "ds_read_b128 a[116:119], v250 offset:39936\n\t"
    "v_cvt_pk_bf16_f32 v192, v128, v129\n\t"
    "v_mfma_f32_16x16x32_bf16 v[164:167], a[80:83], a[68:71], v[164:167]\n\t"
    "v_exp_f32 v130, v130\n\t"
    "v_mfma_f32_16x16x32_bf16 v[164:167], a[48:51], a[36:39], v[164:167]\n\t"
    "v_exp_f32 v131, v131\n\t"
    "v_mfma_f32_16x16x32_bf16 v[164:167], a[16:19], a[4:7], v[164:167]\n\t"
    "buffer_store_dwordx4 v[244:247], v248, s[88:91], s98 offen sc1\n\t"
    "v_mfma_f32_16x16x32_bf16 v[168:171], a[16:19], a[8:11], v[232:235]\n\t"
    "v_cvt_pk_bf16_f32 v193, v130, v131\n\t"
    "v_accvgpr_read_b32 v241, a148\n\t"
    "v_mfma_f32_16x16x32_bf16 v[168:171], a[48:51], a[40:43], v[168:171]\n\t"
    "s_add_u32 m0, s85, 16384\n\t"
    "v_exp_f32 v132, v132\n\t"
    "v_mfma_f32_16x16x32_bf16 v[168:171], a[80:83], a[72:75], v[168:171]\n\t"
    "v_exp_f32 v133, v133\n\t"
    "v_mfma_f32_16x16x32_bf16 v[168:171], a[112:115], a[104:107], v[168:171]\n\t"
    "buffer_load_dwordx4 v248, s[80:83], s84 offen sc1 lds\n\t"
    "v_mfma_f32_16x16x32_bf16 v[172:175], a[112:115], a[108:111], v[236:239]\n\t"
    "ds_read_b128 a[24:27], v250 offset:8192\n\t"
    "v_cvt_pk_bf16_f32 v196, v132, v133\n\t"
    "v_mfma_f32_16x16x32_bf16 v[172:175], a[80:83], a[76:79], v[172:175]\n\t"
    "v_exp_f32 v134, v134\n\t"
    "v_mfma_f32_16x16x32_bf16 v[172:175], a[48:51], a[44:47], v[172:175]\n\t"
    "v_exp_f32 v135, v135\n\t"
    "v_mfma_f32_16x16x32_bf16 v[172:175], a[16:19], a[12:15], v[172:175]\n\t"
    "s_waitcnt lgkmcnt(1)\n\t"
    "v_accvgpr_read_b32 v242, a196\n\t"
    "v_accvgpr_read_b32 v243, a149\n\t"
    "v_mfma_f32_16x16x32_bf16 v[188:191], a[20:23], a[12:15], v[236:239]\n\t"
    "ds_read_b128 a[56:59], v250 offset:9216\n\t"
    "v_cvt_pk_bf16_f32 v197, v134, v135\n\t"
    "v_mfma_f32_16x16x32_bf16 v[188:191], a[52:55], a[44:47], v[188:191]\n\t"
    "v_exp_f32 v136, v136\n\t"
    "v_mfma_f32_16x16x32_bf16 v[188:191], a[84:87], a[76:79], v[188:191]\n\t"
    "v_exp_f32 v137, v137\n\t"
    "v_mfma_f32_16x16x32_bf16 v[188:191], a[116:119], a[108:111], v[188:191]\n\t"
    "buffer_load_dwordx4 v248, s[80:83], s84 offen offset:1024 sc1 lds\n\t"
    "v_mfma_f32_16x16x32_bf16 v[184:187], a[116:119], a[104:107], v[232:235]\n\t"
    "ds_read_b128 a[88:91], v250 offset:10240\n\t"
    "v_cvt_pk_bf16_f32 v200, v136, v137\n\t"
    "v_mfma_f32_16x16x32_bf16 v[184:187], a[84:87], a[72:75], v[184:187]\n\t"
    "v_exp_f32 v138, v138\n\t"
    "v_mfma_f32_16x16x32_bf16 v[184:187], a[52:55], a[40:43], v[184:187]\n\t"
    "v_exp_f32 v139, v139\n\t"
    "v_mfma_f32_16x16x32_bf16 v[184:187], a[20:23], a[8:11], v[184:187]\n\t"
    "v_accvgpr_read_b32 v249, a197\n\t"
    "v_mul_f32 v241, v241, v242\n\t"
    "v_mfma_f32_16x16x32_bf16 v[180:183], a[20:23], a[4:7], v[228:231]\n\t"
    "ds_read_b128 a[120:123], v250 offset:11264\n\t"
    "v_cvt_pk_bf16_f32 v201, v138, v139\n\t"
    "v_mfma_f32_16x16x32_bf16 v[180:183], a[52:55], a[36:39], v[180:183]\n\t"
    "v_exp_f32 v140, v140\n\t"
    "v_mfma_f32_16x16x32_bf16 v[180:183], a[84:87], a[68:71], v[180:183]\n\t"
    "v_exp_f32 v141, v141\n\t"
    "v_mfma_f32_16x16x32_bf16 v[180:183], a[116:119], a[100:103], v[180:183]\n\t"
    "buffer_load_dwordx4 v248, s[80:83], s84 offen offset:2048 sc1 lds\n\t"
    "v_mfma_f32_16x16x32_bf16 v[176:179], a[116:119], a[96:99], v[224:227]\n\t"
    "ds_read_b128 a[28:31], v250 offset:12288\n\t"
    "v_cvt_pk_bf16_f32 v204, v140, v141\n\t"
    "v_mfma_f32_16x16x32_bf16 v[176:179], a[84:87], a[64:67], v[176:179]\n\t"
    "v_exp_f32 v142, v142\n\t"
    "v_mfma_f32_16x16x32_bf16 v[176:179], a[52:55], a[32:35], v[176:179]\n\t"
    "v_exp_f32 v143, v143\n\t"
    "v_mfma_f32_16x16x32_bf16 v[176:179], a[20:23], a[0:3], v[176:179]\n\t"
    "s_waitcnt lgkmcnt(3)\n\t"
    "v_mul_f32 v243, v243, v249\n\t"
    "v_cvt_pk_bf16_f32 v244, v241, v243\n\t"
    "v_mfma_f32_16x16x32_bf16 v[0:3], a[24:27], v[208:211], v[0:3]\n\t"
    "ds_read_b128 a[60:63], v250 offset:13312\n\t"
    "v_cvt_pk_bf16_f32 v205, v142, v143\n\t"
    "v_mfma_f32_16x16x32_bf16 v[4:7], a[24:27], v[212:215], v[4:7]\n\t"
    "v_exp_f32 v156, v156\n\t"
    "v_mfma_f32_16x16x32_bf16 v[8:11], a[24:27], v[216:219], v[8:11]\n\t"
    "v_exp_f32 v157, v157\n\t"
    "v_mfma_f32_16x16x32_bf16 v[12:15], a[24:27], v[220:223], v[12:15]\n\t"
    "buffer_load_dwordx4 v248, s[80:83], s84 offen offset:3072 sc1 lds\n\t"
    "v_mfma_f32_16x16x32_bf16 v[28:31], a[56:59], v[220:223], v[28:31]\n\t"
    "ds_read_b128 a[92:95], v250 offset:14336\n\t"
    "v_cvt_pk_bf16_f32 v206, v156, v157\n\t"
    "v_mfma_f32_16x16x32_bf16 v[24:27], a[56:59], v[216:219], v[24:27]\n\t"
    "v_exp_f32 v158, v158\n\t"
    "v_mfma_f32_16x16x32_bf16 v[20:23], a[56:59], v[212:215], v[20:23]\n\t"
    "v_exp_f32 v159, v159\n\t"
    "v_mfma_f32_16x16x32_bf16 v[16:19], a[56:59], v[208:211], v[16:19]\n\t"
    "s_waitcnt lgkmcnt(3)\n\t"
    "v_accvgpr_read_b32 v241, a150\n\t"
    "v_accvgpr_read_b32 v242, a198\n\t"
    "v_mfma_f32_16x16x32_bf16 v[32:35], a[88:91], v[208:211], v[32:35]\n\t"
    "ds_read_b128 a[124:127], v250 offset:15360\n\t"
    "v_cvt_pk_bf16_f32 v207, v158, v159\n\t"
    "v_mfma_f32_16x16x32_bf16 v[36:39], a[88:91], v[212:215], v[36:39]\n\t"
    "v_exp_f32 v152, v152\n\t"
    "v_mfma_f32_16x16x32_bf16 v[40:43], a[88:91], v[216:219], v[40:43]\n\t"
    "v_exp_f32 v153, v153\n\t"
    "v_mfma_f32_16x16x32_bf16 v[44:47], a[88:91], v[220:223], v[44:47]\n\t"
    "s_add_u32 s84, s84, 16384\n\t"
    "v_accvgpr_read_b32 v243, a151\n\t"
    "v_accvgpr_read_b32 v249, a199\n\t"
    "v_mfma_f32_16x16x32_bf16 v[60:63], a[120:123], v[220:223], v[60:63]\n\t"
    "ds_read_b128 a[16:19], v250 offset:49152\n\t"
    "v_cvt_pk_bf16_f32 v202, v152, v153\n\t"
    "v_mfma_f32_16x16x32_bf16 v[56:59], a[120:123], v[216:219], v[56:59]\n\t"
    "v_exp_f32 v154, v154\n\t"
    "v_mfma_f32_16x16x32_bf16 v[52:55], a[120:123], v[212:215], v[52:55]\n\t"
    "v_exp_f32 v155, v155\n\t"
    "v_mfma_f32_16x16x32_bf16 v[48:51], a[120:123], v[208:211], v[48:51]\n\t"
    "s_waitcnt lgkmcnt(3)\n\t"
    "v_mul_f32 v241, v241, v242\n\t"
    "v_mul_f32 v243, v243, v249\n\t"
    "v_mfma_f32_16x16x32_bf16 v[64:67], a[28:31], v[208:211], v[64:67]\n\t"
    "ds_read_b128 a[48:51], v250 offset:50176\n\t"
    "v_cvt_pk_bf16_f32 v203, v154, v155\n\t"
    "v_mfma_f32_16x16x32_bf16 v[68:71], a[28:31], v[212:215], v[68:71]\n\t"
    "v_exp_f32 v148, v148\n\t"
    "v_mfma_f32_16x16x32_bf16 v[72:75], a[28:31], v[216:219], v[72:75]\n\t"
    "v_exp_f32 v149, v149\n\t"
    "v_mfma_f32_16x16x32_bf16 v[76:79], a[28:31], v[220:223], v[76:79]\n\t"
    "v_cvt_pk_bf16_f32 v245, v241, v243\n\t"
    "v_accvgpr_read_b32 v241, a164\n\t"
    "v_mfma_f32_16x16x32_bf16 v[92:95], a[60:63], v[220:223], v[92:95]\n\t"
    "ds_read_b128 a[80:83], v250 offset:51200\n\t"
    "v_cvt_pk_bf16_f32 v198, v148, v149\n\t"
    "v_mfma_f32_16x16x32_bf16 v[88:91], a[60:63], v[216:219], v[88:91]\n\t"
    "v_exp_f32 v150, v150\n\t"
    "v_mfma_f32_16x16x32_bf16 v[84:87], a[60:63], v[212:215], v[84:87]\n\t"
    "v_exp_f32 v151, v151\n\t"
    "v_mfma_f32_16x16x32_bf16 v[80:83], a[60:63], v[208:211], v[80:83]\n\t"
    "s_waitcnt lgkmcnt(3)\n\t"
    "v_accvgpr_read_b32 v242, a212\n\t"
    "v_accvgpr_read_b32 v243, a165\n\t"
    "v_mfma_f32_16x16x32_bf16 v[96:99], a[92:95], v[208:211], v[96:99]\n\t"
    "ds_read_b128 a[112:115], v250 offset:52224\n\t"
    "v_cvt_pk_bf16_f32 v199, v150, v151\n\t"
    "v_mfma_f32_16x16x32_bf16 v[100:103], a[92:95], v[212:215], v[100:103]\n\t"
    "v_exp_f32 v144, v144\n\t"
    "v_mfma_f32_16x16x32_bf16 v[104:107], a[92:95], v[216:219], v[104:107]\n\t"
    "v_exp_f32 v145, v145\n\t"
    "v_mfma_f32_16x16x32_bf16 v[108:111], a[92:95], v[220:223], v[108:111]\n\t"
    "v_accvgpr_read_b32 v249, a213\n\t"
    "v_mul_f32 v241, v241, v242\n\t"
    "v_mfma_f32_16x16x32_bf16 v[124:127], a[124:127], v[220:223], v[124:127]\n\t"
    "ds_read_b128 a[20:23], v250 offset:53248\n\t"
    "v_cvt_pk_bf16_f32 v194, v144, v145\n\t"
    "v_mfma_f32_16x16x32_bf16 v[120:123], a[124:127], v[216:219], v[120:123]\n\t"
    "v_exp_f32 v146, v146\n\t"
    "v_mfma_f32_16x16x32_bf16 v[116:119], a[124:127], v[212:215], v[116:119]\n\t"
    "v_exp_f32 v147, v147\n\t"
    "v_mfma_f32_16x16x32_bf16 v[112:115], a[124:127], v[208:211], v[112:115]\n\t"
    "v_mul_f32 v243, v243, v249\n\t"
    "v_cvt_pk_bf16_f32 v246, v241, v243\n\t"
    "v_mfma_f32_16x16x32_bf16 a[128:131], a[240:243], v[208:211], a[128:131]\n\t"
    "ds_read_b128 a[52:55], v250 offset:54272\n\t"
    "v_cvt_pk_bf16_f32 v195, v146, v147\n\t"
    "v_mfma_f32_16x16x32_bf16 a[132:135], a[240:243], v[212:215], a[132:135]\n\t"
    "v_accvgpr_read_b32 v241, a166\n\t"
    "v_accvgpr_read_b32 v242, a214\n\t"
    "v_mfma_f32_16x16x32_bf16 a[136:139], a[240:243], v[216:219], a[136:139]\n\t"
    "v_accvgpr_read_b32 v243, a167\n\t"
    "v_accvgpr_read_b32 v249, a215\n\t"
    "v_mfma_f32_16x16x32_bf16 a[140:143], a[240:243], v[220:223], a[140:143]\n\t"
    "s_waitcnt lgkmcnt(2)\n\t"
    "v_mul_f32 v241, v241, v242\n\t"
    "v_mul_f32 v243, v243, v249\n\t"
    "v_mfma_f32_16x16x32_bf16 v[128:131], a[16:19], a[0:3], v[224:227]\n\t"
    "ds_read_b128 a[84:87], v250 offset:55296\n\t"
    "v_cvt_pk_bf16_f32 v247, v241, v243\n\t"
    "v_mfma_f32_16x16x32_bf16 v[128:131], a[48:51], a[32:35], v[128:131]\n\t"
    "v_exp_f32 v160, v160\n\t"
    "v_mfma_f32_16x16x32_bf16 v[128:131], a[80:83], a[64:67], v[128:131]\n\t"
    "v_exp_f32 v161, v161\n\t"
    "v_mfma_f32_16x16x32_bf16 v[128:131], a[112:115], a[96:99], v[128:131]\n\t"
    "s_waitcnt vmcnt(5)\n\t"
    "s_barrier\n\t"
    "v_mfma_f32_16x16x32_bf16 v[132:135], a[112:115], a[100:103], v[228:231]\n\t"
    "ds_read_b128 a[116:119], v250 offset:56320\n\t"
    "s_mul_i32 s10, s3, 2816\n\t"
    "v_mfma_f32_16x16x32_bf16 v[132:135], a[80:83], a[68:71], v[132:135]\n\t"
    "s_add_u32 s10, s10, 33792\n\t"
    "s_min_u32 s10, s10, s26\n\t"
    "v_min_u32 v252, 336, v248\n\t"
    "v_mfma_f32_16x16x32_bf16 v[132:135], a[48:51], a[36:39], v[132:135]\n\t"
    "v_lshlrev_b32 v252, 3, v252\n\t"
    "v_cvt_pk_bf16_f32 v208, v160, v161\n\t"
    "v_mfma_f32_16x16x32_bf16 v[132:135], a[16:19], a[4:7], v[132:135]\n\t"
    "buffer_load_dword a247, v252, s[24:27], s10 offen\n\t"
    "v_mfma_f32_16x16x32_bf16 v[136:139], a[16:19], a[8:11], v[232:235]\n\t"
    "v_exp_f32 v162, v162\n\t"
    "v_mfma_f32_16x16x32_bf16 v[136:139], a[48:51], a[40:43], v[136:139]\n\t"
    "s_add_u32 m0, s85, 32768\n\t"
    "v_exp_f32 v163, v163\n\t"
    "v_mfma_f32_16x16x32_bf16 v[136:139], a[80:83], a[72:75], v[136:139]\n\t"
    "v_exp_f32 v164, v164\n\t"
    "v_mfma_f32_16x16x32_bf16 v[136:139], a[112:115], a[104:107], v[136:139]\n\t"
    "buffer_load_dwordx4 v248, s[80:83], s84 offen sc1 lds\n\t"
    "v_mfma_f32_16x16x32_bf16 v[140:143], a[112:115], a[108:111], v[236:239]\n\t"
    "ds_read_b128 a[24:27], v250 offset:24576\n\t"
    "v_cvt_pk_bf16_f32 v209, v162, v163\n\t"
    "v_mfma_f32_16x16x32_bf16 v[140:143], a[80:83], a[76:79], v[140:143]\n\t"
    "v_exp_f32 v165, v165\n\t"
    "v_mfma_f32_16x16x32_bf16 v[140:143], a[48:51], a[44:47], v[140:143]\n\t"
    "v_exp_f32 v166, v166\n\t"
    "v_mfma_f32_16x16x32_bf16 v[140:143], a[16:19], a[12:15], v[140:143]\n\t"
    "s_waitcnt lgkmcnt(1)\n\t"
    "v_exp_f32 v167, v167\n\t"
    "v_mfma_f32_16x16x32_bf16 v[156:159], a[20:23], a[12:15], v[236:239]\n\t"
    "ds_read_b128 a[56:59], v250 offset:25600\n\t"
    "v_cvt_pk_bf16_f32 v212, v164, v165\n\t"
    "v_mfma_f32_16x16x32_bf16 v[156:159], a[52:55], a[44:47], v[156:159]\n\t"
    "v_exp_f32 v168, v168\n\t"
    "v_mfma_f32_16x16x32_bf16 v[156:159], a[84:87], a[76:79], v[156:159]\n\t"
    "v_exp_f32 v169, v169\n\t"
    "v_mfma_f32_16x16x32_bf16 v[156:159], a[116:119], a[108:111], v[156:159]\n\t"
    "buffer_load_dwordx4 v248, s[80:83], s84 offen offset:1024 sc1 lds\n\t"
    "v_mfma_f32_16x16x32_bf16 v[152:155], a[116:119], a[104:107], v[232:235]\n\t"
    "ds_read_b128 a[88:91], v250 offset:26624\n\t"
    "v_cvt_pk_bf16_f32 v213, v166, v167\n\t"
    "v_mfma_f32_16x16x32_bf16 v[152:155], a[84:87], a[72:75], v[152:155]\n\t"
    "v_exp_f32 v170, v170\n\t"
    "v_mfma_f32_16x16x32_bf16 v[152:155], a[52:55], a[40:43], v[152:155]\n\t"
    "v_exp_f32 v171, v171\n\t"
    "v_mfma_f32_16x16x32_bf16 v[152:155], a[20:23], a[8:11], v[152:155]\n\t"
    "v_cvt_pk_bf16_f32 v216, v168, v169\n\t"
    "v_mfma_f32_16x16x32_bf16 v[148:151], a[20:23], a[4:7], v[228:231]\n\t"
    "ds_read_b128 a[120:123], v250 offset:27648\n\t"
    "v_cvt_pk_bf16_f32 v217, v170, v171\n\t"
    "v_mfma_f32_16x16x32_bf16 v[148:151], a[52:55], a[36:39], v[148:151]\n\t"
    "v_exp_f32 v172, v172\n\t"
    "v_mfma_f32_16x16x32_bf16 v[148:151], a[84:87], a[68:71], v[148:151]\n\t"
    "v_exp_f32 v173, v173\n\t"
    "v_mfma_f32_16x16x32_bf16 v[148:151], a[116:119], a[100:103], v[148:151]\n\t"
    "buffer_load_dwordx4 v248, s[80:83], s84 offen offset:2048 sc1 lds\n\t"
    "v_mfma_f32_16x16x32_bf16 v[144:147], a[116:119], a[96:99], v[224:227]\n\t"
    "ds_read_b128 a[28:31], v250 offset:28672\n\t"
    "v_cvt_pk_bf16_f32 v220, v172, v173\n\t"
    "v_mfma_f32_16x16x32_bf16 v[144:147], a[84:87], a[64:67], v[144:147]\n\t"
    "v_exp_f32 v174, v174\n\t"
    "v_mfma_f32_16x16x32_bf16 v[144:147], a[52:55], a[32:35], v[144:147]\n\t"
    "v_exp_f32 v175, v175\n\t"
    "v_mfma_f32_16x16x32_bf16 v[144:147], a[20:23], a[0:3], v[144:147]\n\t"
    "s_waitcnt lgkmcnt(3)\n\t"
    "buffer_store_dwordx4 v[244:247], v248, s[88:91], s98 offen offset:1024 sc1\n\t"
    "v_mfma_f32_16x16x32_bf16 v[0:3], a[24:27], v[192:195], v[0:3]\n\t"
    "ds_read_b128 a[60:63], v250 offset:29696\n\t"
    "v_cvt_pk_bf16_f32 v221, v174, v175\n\t"
    "v_mfma_f32_16x16x32_bf16 v[4:7], a[24:27], v[196:199], v[4:7]\n\t"
    "v_exp_f32 v188, v188\n\t"
    "v_mfma_f32_16x16x32_bf16 v[8:11], a[24:27], v[200:203], v[8:11]\n\t"
    "v_exp_f32 v189, v189\n\t"
    "v_mfma_f32_16x16x32_bf16 v[12:15], a[24:27], v[204:207], v[12:15]\n\t"
    "buffer_load_dwordx4 v248, s[80:83], s84 offen offset:3072 sc1 lds\n\t"
    "v_mfma_f32_16x16x32_bf16 v[28:31], a[56:59], v[204:207], v[28:31]\n\t"
    "ds_read_b128 a[92:95], v250 offset:30720\n\t"
    "v_cvt_pk_bf16_f32 v222, v188, v189\n\t"
    "v_mfma_f32_16x16x32_bf16 v[24:27], a[56:59], v[200:203], v[24:27]\n\t"
    "v_exp_f32 v190, v190\n\t"
    "v_mfma_f32_16x16x32_bf16 v[20:23], a[56:59], v[196:199], v[20:23]\n\t"
    "v_exp_f32 v191, v191\n\t"
    "v_mfma_f32_16x16x32_bf16 v[16:19], a[56:59], v[192:195], v[16:19]\n\t"
    "s_waitcnt lgkmcnt(3)\n\t"
    "v_accvgpr_read_b32 v241, a152\n\t"
    "v_accvgpr_read_b32 v242, a200\n\t"
    "v_mfma_f32_16x16x32_bf16 v[32:35], a[88:91], v[192:195], v[32:35]\n\t"
    "ds_read_b128 a[124:127], v250 offset:31744\n\t"
    "v_cvt_pk_bf16_f32 v223, v190, v191\n\t"
    "v_mfma_f32_16x16x32_bf16 v[36:39], a[88:91], v[196:199], v[36:39]\n\t"
    "v_exp_f32 v184, v184\n\t"
    "v_mfma_f32_16x16x32_bf16 v[40:43], a[88:91], v[200:203], v[40:43]\n\t"
    "v_exp_f32 v185, v185\n\t"
    "v_mfma_f32_16x16x32_bf16 v[44:47], a[88:91], v[204:207], v[44:47]\n\t"
    "s_add_u32 s84, s84, 16384\n\t"
    "v_accvgpr_read_b32 v243, a153\n\t"
    "v_accvgpr_read_b32 v249, a201\n\t"
    "v_mfma_f32_16x16x32_bf16 v[60:63], a[120:123], v[204:207], v[60:63]\n\t"
    "ds_read_b128 a[16:19], v251 offset:0\n\t"
    "v_cvt_pk_bf16_f32 v218, v184, v185\n\t"
    "v_mfma_f32_16x16x32_bf16 v[56:59], a[120:123], v[200:203], v[56:59]\n\t"
    "v_exp_f32 v186, v186\n\t"
    "v_mfma_f32_16x16x32_bf16 v[52:55], a[120:123], v[196:199], v[52:55]\n\t"
    "v_exp_f32 v187, v187\n\t"
    "v_mfma_f32_16x16x32_bf16 v[48:51], a[120:123], v[192:195], v[48:51]\n\t"
    "s_waitcnt lgkmcnt(3)\n\t"
    "v_mul_f32 v241, v241, v242\n\t"
    "v_mul_f32 v243, v243, v249\n\t"
    "v_mfma_f32_16x16x32_bf16 v[64:67], a[28:31], v[192:195], v[64:67]\n\t"
    "ds_read_b128 a[48:51], v251 offset:1024\n\t"
    "v_cvt_pk_bf16_f32 v219, v186, v187\n\t"
    "v_mfma_f32_16x16x32_bf16 v[68:71], a[28:31], v[196:199], v[68:71]\n\t"
    "v_exp_f32 v180, v180\n\t"
    "v_mfma_f32_16x16x32_bf16 v[72:75], a[28:31], v[200:203], v[72:75]\n\t"
    "v_exp_f32 v181, v181\n\t"
    "v_mfma_f32_16x16x32_bf16 v[76:79], a[28:31], v[204:207], v[76:79]\n\t"
    "v_cvt_pk_bf16_f32 v244, v241, v243\n\t"
    "v_accvgpr_read_b32 v241, a154\n\t"
    "v_mfma_f32_16x16x32_bf16 v[92:95], a[60:63], v[204:207], v[92:95]\n\t"
    "ds_read_b128 a[80:83], v251 offset:2048\n\t"
    "v_cvt_pk_bf16_f32 v214, v180, v181\n\t"
    "v_mfma_f32_16x16x32_bf16 v[88:91], a[60:63], v[200:203], v[88:91]\n\t"
    "v_exp_f32 v182, v182\n\t"
    "v_mfma_f32_16x16x32_bf16 v[84:87], a[60:63], v[196:199], v[84:87]\n\t"
    "v_exp_f32 v183, v183\n\t"
    "v_mfma_f32_16x16x32_bf16 v[80:83], a[60:63], v[192:195], v[80:83]\n\t"
    "s_waitcnt lgkmcnt(3)\n\t"
    "v_accvgpr_read_b32 v242, a202\n\t"
    "v_accvgpr_read_b32 v243, a155\n\t"
    "v_mfma_f32_16x16x32_bf16 v[96:99], a[92:95], v[192:195], v[96:99]\n\t"
    "ds_read_b128 a[112:115], v251 offset:3072\n\t"
    "v_cvt_pk_bf16_f32 v215, v182, v183\n\t"
    "v_mfma_f32_16x16x32_bf16 v[100:103], a[92:95], v[196:199], v[100:103]\n\t"
    "v_exp_f32 v176, v176\n\t"
    "v_mfma_f32_16x16x32_bf16 v[104:107], a[92:95], v[200:203], v[104:107]\n\t"
    "v_exp_f32 v177, v177\n\t"
    "v_mfma_f32_16x16x32_bf16 v[108:111], a[92:95], v[204:207], v[108:111]\n\t"
    "v_accvgpr_read_b32 v249, a203\n\t"
    "v_mul_f32 v241, v241, v242\n\t"
    "v_mfma_f32_16x16x32_bf16 v[124:127], a[124:127], v[204:207], v[124:127]\n\t"
    "ds_read_b128 a[20:23], v251 offset:4096\n\t"
    "v_cvt_pk_bf16_f32 v210, v176, v177\n\t"
    "v_mfma_f32_16x16x32_bf16 v[120:123], a[124:127], v[200:203], v[120:123]\n\t"
    "v_exp_f32 v178, v178\n\t"
    "v_mfma_f32_16x16x32_bf16 v[116:119], a[124:127], v[196:199], v[116:119]\n\t"
    "v_exp_f32 v179, v179\n\t"
    "v_mfma_f32_16x16x32_bf16 v[112:115], a[124:127], v[192:195], v[112:115]\n\t"
    "v_mul_f32 v243, v243, v249\n\t"
    "v_cvt_pk_bf16_f32 v245, v241, v243\n\t"
    "v_mfma_f32_16x16x32_bf16 a[128:131], a[240:243], v[192:195], a[128:131]\n\t"
    "ds_read_b128 a[52:55], v251 offset:5120\n\t"
    "v_cvt_pk_bf16_f32 v211, v178, v179\n\t"
    "v_mfma_f32_16x16x32_bf16 a[132:135], a[240:243], v[196:199], a[132:135]\n\t"
    "v_accvgpr_read_b32 v241, a168\n\t"
    "v_accvgpr_read_b32 v242, a216\n\t"
    "v_mfma_f32_16x16x32_bf16 a[136:139], a[240:243], v[200:203], a[136:139]\n\t"
    "v_accvgpr_read_b32 v243, a169\n\t"
    "v_accvgpr_read_b32 v249, a217\n\t"
    "v_mfma_f32_16x16x32_bf16 a[140:143], a[240:243], v[204:207], a[140:143]\n\t"
    "s_waitcnt lgkmcnt(2)\n\t"
    "v_mul_f32 v241, v241, v242\n\t"
    "v_mul_f32 v243, v243, v249\n\t"
    "v_mfma_f32_16x16x32_bf16 v[160:163], a[16:19], a[0:3], v[224:227]\n\t"
    "ds_read_b128 a[84:87], v251 offset:6144\n\t"
    "v_cvt_pk_bf16_f32 v246, v241, v243\n\t"
    "v_mfma_f32_16x16x32_bf16 v[160:163], a[48:51], a[32:35], v[160:163]\n\t"
    "v_exp_f32 v128, v128\n\t"
    "v_mfma_f32_16x16x32_bf16 v[160:163], a[80:83], a[64:67], v[160:163]\n\t"
    "v_exp_f32 v129, v129\n\t"
    "v_mfma_f32_16x16x32_bf16 v[160:163], a[112:115], a[96:99], v[160:163]\n\t"
    "v_accvgpr_read_b32 v241, a170\n\t"
    "v_accvgpr_read_b32 v242, a218\n\t"
    "v_mfma_f32_16x16x32_bf16 v[164:167], a[112:115], a[100:103], v[228:231]\n\t"
    "ds_read_b128 a[116:119], v251 offset:7168\n\t"
    "v_cvt_pk_bf16_f32 v192, v128, v129\n\t"
    "v_mfma_f32_16x16x32_bf16 v[164:167], a[80:83], a[68:71], v[164:167]\n\t"
    "v_exp_f32 v130, v130\n\t"
    "v_mfma_f32_16x16x32_bf16 v[164:167], a[48:51], a[36:39], v[164:167]\n\t"
    "v_exp_f32 v131, v131\n\t"
    "v_mfma_f32_16x16x32_bf16 v[164:167], a[16:19], a[4:7], v[164:167]\n\t"
    "v_accvgpr_read_b32 v243, a171\n\t"
    "v_accvgpr_read_b32 v249, a219\n\t"
    "v_mfma_f32_16x16x32_bf16 v[168:171], a[16:19], a[8:11], v[232:235]\n\t"
    "v_cvt_pk_bf16_f32 v193, v130, v131\n\t"
    "v_mul_f32 v241, v241, v242\n\t"
    "v_mfma_f32_16x16x32_bf16 v[168:171], a[48:51], a[40:43], v[168:171]\n\t"
    "s_add_u32 m0, s85, 49152\n\t"
    "v_exp_f32 v132, v132\n\t"
    "v_mfma_f32_16x16x32_bf16 v[168:171], a[80:83], a[72:75], v[168:171]\n\t"
    "v_exp_f32 v133, v133\n\t"
    "v_mfma_f32_16x16x32_bf16 v[168:171], a[112:115], a[104:107], v[168:171]\n\t"
    "buffer_load_dwordx4 v248, s[80:83], s84 offen sc1 lds\n\t"
    "v_mfma_f32_16x16x32_bf16 v[172:175], a[112:115], a[108:111], v[236:239]\n\t"
    "ds_read_b128 a[24:27], v250 offset:40960\n\t"
    "v_cvt_pk_bf16_f32 v196, v132, v133\n\t"
    "v_mfma_f32_16x16x32_bf16 v[172:175], a[80:83], a[76:79], v[172:175]\n\t"
    "v_exp_f32 v134, v134\n\t"
    "v_mfma_f32_16x16x32_bf16 v[172:175], a[48:51], a[44:47], v[172:175]\n\t"
    "v_exp_f32 v135, v135\n\t"
    "v_mfma_f32_16x16x32_bf16 v[172:175], a[16:19], a[12:15], v[172:175]\n\t"
    "s_waitcnt lgkmcnt(1)\n\t"
    "v_mul_f32 v243, v243, v249\n\t"
    "v_cvt_pk_bf16_f32 v247, v241, v243\n\t"
    "v_mfma_f32_16x16x32_bf16 v[188:191], a[20:23], a[12:15], v[236:239]\n\t"
    "ds_read_b128 a[56:59], v250 offset:41984\n\t"
    "v_cvt_pk_bf16_f32 v197, v134, v135\n\t"
    "v_mfma_f32_16x16x32_bf16 v[188:191], a[52:55], a[44:47], v[188:191]\n\t"
    "v_exp_f32 v136, v136\n\t"
    "v_mfma_f32_16x16x32_bf16 v[188:191], a[84:87], a[76:79], v[188:191]\n\t"
    "v_exp_f32 v137, v137\n\t"
    "v_mfma_f32_16x16x32_bf16 v[188:191], a[116:119], a[108:111], v[188:191]\n\t"
    "buffer_load_dwordx4 v248, s[80:83], s84 offen offset:1024 sc1 lds\n\t"
    "v_mfma_f32_16x16x32_bf16 v[184:187], a[116:119], a[104:107], v[232:235]\n\t"
    "ds_read_b128 a[88:91], v250 offset:43008\n\t"
    "v_cvt_pk_bf16_f32 v200, v136, v137\n\t"
    "v_mfma_f32_16x16x32_bf16 v[184:187], a[84:87], a[72:75], v[184:187]\n\t"
    "v_exp_f32 v138, v138\n\t"
    "v_mfma_f32_16x16x32_bf16 v[184:187], a[52:55], a[40:43], v[184:187]\n\t"
    "v_exp_f32 v139, v139\n\t"
    "v_mfma_f32_16x16x32_bf16 v[184:187], a[20:23], a[8:11], v[184:187]\n\t"
    "buffer_store_dwordx4 v[244:247], v248, s[88:91], s98 offen offset:2048 sc1\n\t"
    "v_mfma_f32_16x16x32_bf16 v[180:183], a[20:23], a[4:7], v[228:231]\n\t"
    "ds_read_b128 a[120:123], v250 offset:44032\n\t"
    "v_cvt_pk_bf16_f32 v201, v138, v139\n\t"
    "v_mfma_f32_16x16x32_bf16 v[180:183], a[52:55], a[36:39], v[180:183]\n\t"
    "v_exp_f32 v140, v140\n\t"
    "v_mfma_f32_16x16x32_bf16 v[180:183], a[84:87], a[68:71], v[180:183]\n\t"
    "v_exp_f32 v141, v141\n\t"
    "v_mfma_f32_16x16x32_bf16 v[180:183], a[116:119], a[100:103], v[180:183]\n\t"
    "buffer_load_dwordx4 v248, s[80:83], s84 offen offset:2048 sc1 lds\n\t"
    "v_mfma_f32_16x16x32_bf16 v[176:179], a[116:119], a[96:99], v[224:227]\n\t"
    "ds_read_b128 a[28:31], v250 offset:45056\n\t"
    "v_cvt_pk_bf16_f32 v204, v140, v141\n\t"
    "v_mfma_f32_16x16x32_bf16 v[176:179], a[84:87], a[64:67], v[176:179]\n\t"
    "v_exp_f32 v142, v142\n\t"
    "v_mfma_f32_16x16x32_bf16 v[176:179], a[52:55], a[32:35], v[176:179]\n\t"
    "v_exp_f32 v143, v143\n\t"
    "v_mfma_f32_16x16x32_bf16 v[176:179], a[20:23], a[0:3], v[176:179]\n\t"
    "s_waitcnt lgkmcnt(3)\n\t"
    "v_accvgpr_read_b32 v241, a156\n\t"
    "v_accvgpr_read_b32 v242, a204\n\t"
    "v_mfma_f32_16x16x32_bf16 v[0:3], a[24:27], v[208:211], v[0:3]\n\t"
    "ds_read_b128 a[60:63], v250 offset:46080\n\t"
    "v_cvt_pk_bf16_f32 v205, v142, v143\n\t"
    "v_mfma_f32_16x16x32_bf16 v[4:7], a[24:27], v[212:215], v[4:7]\n\t"
    "v_exp_f32 v156, v156\n\t"
    "v_mfma_f32_16x16x32_bf16 v[8:11], a[24:27], v[216:219], v[8:11]\n\t"
    "v_exp_f32 v157, v157\n\t"
    "v_mfma_f32_16x16x32_bf16 v[12:15], a[24:27], v[220:223], v[12:15]\n\t"
    "buffer_load_dwordx4 v248, s[80:83], s84 offen offset:3072 sc1 lds\n\t"
    "v_mfma_f32_16x16x32_bf16 v[28:31], a[56:59], v[220:223], v[28:31]\n\t"
    "ds_read_b128 a[92:95], v250 offset:47104\n\t"
    "v_cvt_pk_bf16_f32 v206, v156, v157\n\t"
    "v_mfma_f32_16x16x32_bf16 v[24:27], a[56:59], v[216:219], v[24:27]\n\t"
    "v_exp_f32 v158, v158\n\t"
    "v_mfma_f32_16x16x32_bf16 v[20:23], a[56:59], v[212:215], v[20:23]\n\t"
    "v_exp_f32 v159, v159\n\t"
    "v_mfma_f32_16x16x32_bf16 v[16:19], a[56:59], v[208:211], v[16:19]\n\t"
    "s_waitcnt lgkmcnt(3)\n\t"
    "v_accvgpr_read_b32 v243, a157\n\t"
    "v_accvgpr_read_b32 v249, a205\n\t"
    "v_mfma_f32_16x16x32_bf16 v[32:35], a[88:91], v[208:211], v[32:35]\n\t"
    "ds_read_b128 a[124:127], v250 offset:48128\n\t"
    "v_cvt_pk_bf16_f32 v207, v158, v159\n\t"
    "v_mfma_f32_16x16x32_bf16 v[36:39], a[88:91], v[212:215], v[36:39]\n\t"
    "v_exp_f32 v152, v152\n\t"
    "v_mfma_f32_16x16x32_bf16 v[40:43], a[88:91], v[216:219], v[40:43]\n\t"
    "v_exp_f32 v153, v153\n\t"
    "v_mfma_f32_16x16x32_bf16 v[44:47], a[88:91], v[220:223], v[44:47]\n\t"
    "s_add_u32 s84, s84, 16384\n\t"
    "v_mul_f32 v241, v241, v242\n\t"
    "v_mul_f32 v243, v243, v249\n\t"
    "v_mfma_f32_16x16x32_bf16 v[60:63], a[120:123], v[220:223], v[60:63]\n\t"
    "ds_read_b128 a[16:19], v248 offset:16384\n\t"
    "v_cvt_pk_bf16_f32 v202, v152, v153\n\t"
    "v_mfma_f32_16x16x32_bf16 v[56:59], a[120:123], v[216:219], v[56:59]\n\t"
    "v_exp_f32 v154, v154\n\t"
    "v_mfma_f32_16x16x32_bf16 v[52:55], a[120:123], v[212:215], v[52:55]\n\t"
    "v_exp_f32 v155, v155\n\t"
    "v_mfma_f32_16x16x32_bf16 v[48:51], a[120:123], v[208:211], v[48:51]\n\t"
    "s_waitcnt lgkmcnt(3)\n\t"
    "v_cvt_pk_bf16_f32 v244, v241, v243\n\t"
    "v_accvgpr_read_b32 v241, a158\n\t"
    "v_mfma_f32_16x16x32_bf16 v[64:67], a[28:31], v[208:211], v[64:67]\n\t"
    "ds_read_b128 a[48:51], v248 offset:17408\n\t"
    "v_cvt_pk_bf16_f32 v203, v154, v155\n\t"
    "v_mfma_f32_16x16x32_bf16 v[68:71], a[28:31], v[212:215], v[68:71]\n\t"
    "v_exp_f32 v148, v148\n\t"
    "v_mfma_f32_16x16x32_bf16 v[72:75], a[28:31], v[216:219], v[72:75]\n\t"
    "v_exp_f32 v149, v149\n\t"
    "v_mfma_f32_16x16x32_bf16 v[76:79], a[28:31], v[220:223], v[76:79]\n\t"
    "v_accvgpr_read_b32 v242, a206\n\t"
    "v_accvgpr_read_b32 v243, a159\n\t"
    "v_mfma_f32_16x16x32_bf16 v[92:95], a[60:63], v[220:223], v[92:95]\n\t"
    "ds_read_b128 a[80:83], v248 offset:18432\n\t"
    "v_cvt_pk_bf16_f32 v198, v148, v149\n\t"
    "v_mfma_f32_16x16x32_bf16 v[88:91], a[60:63], v[216:219], v[88:91]\n\t"
    "v_exp_f32 v150, v150\n\t"
    "v_mfma_f32_16x16x32_bf16 v[84:87], a[60:63], v[212:215], v[84:87]\n\t"
    "v_exp_f32 v151, v151\n\t"
    "v_mfma_f32_16x16x32_bf16 v[80:83], a[60:63], v[208:211], v[80:83]\n\t"
    "s_waitcnt lgkmcnt(3)\n\t"
    "v_accvgpr_read_b32 v249, a207\n\t"
    "v_mul_f32 v241, v241, v242\n\t"
    "v_mfma_f32_16x16x32_bf16 v[96:99], a[92:95], v[208:211], v[96:99]\n\t"
    "ds_read_b128 a[112:115], v248 offset:19456\n\t"
    "v_cvt_pk_bf16_f32 v199, v150, v151\n\t"
    "v_mfma_f32_16x16x32_bf16 v[100:103], a[92:95], v[212:215], v[100:103]\n\t"
    "v_exp_f32 v144, v144\n\t"
    "v_mfma_f32_16x16x32_bf16 v[104:107], a[92:95], v[216:219], v[104:107]\n\t"
    "v_exp_f32 v145, v145\n\t"
    "v_mfma_f32_16x16x32_bf16 v[108:111], a[92:95], v[220:223], v[108:111]\n\t"
    "v_mul_f32 v243, v243, v249\n\t"
    "v_cvt_pk_bf16_f32 v245, v241, v243\n\t"
    "v_mfma_f32_16x16x32_bf16 v[124:127], a[124:127], v[220:223], v[124:127]\n\t"
    "ds_read_b128 a[20:23], v248 offset:20480\n\t"
    "v_cvt_pk_bf16_f32 v194, v144, v145\n\t"
    "v_mfma_f32_16x16x32_bf16 v[120:123], a[124:127], v[216:219], v[120:123]\n\t"
    "v_exp_f32 v146, v146\n\t"
    "v_mfma_f32_16x16x32_bf16 v[116:119], a[124:127], v[212:215], v[116:119]\n\t"
    "v_exp_f32 v147, v147\n\t"
    "v_mfma_f32_16x16x32_bf16 v[112:115], a[124:127], v[208:211], v[112:115]\n\t"
    "v_accvgpr_read_b32 v241, a172\n\t"
    "v_accvgpr_read_b32 v242, a220\n\t"
    "v_mfma_f32_16x16x32_bf16 a[128:131], a[240:243], v[208:211], a[128:131]\n\t"
    "ds_read_b128 a[52:55], v248 offset:21504\n\t"
    "v_cvt_pk_bf16_f32 v195, v146, v147\n\t"
    "v_mfma_f32_16x16x32_bf16 a[132:135], a[240:243], v[212:215], a[132:135]\n\t"
    "v_accvgpr_read_b32 v243, a173\n\t"
    "v_accvgpr_read_b32 v249, a221\n\t"
    "v_mfma_f32_16x16x32_bf16 a[136:139], a[240:243], v[216:219], a[136:139]\n\t"
    "v_mul_f32 v241, v241, v242\n\t"
    "v_mul_f32 v243, v243, v249\n\t"
    "v_mfma_f32_16x16x32_bf16 a[140:143], a[240:243], v[220:223], a[140:143]\n\t"
    "s_waitcnt lgkmcnt(2)\n\t"
    "v_cvt_pk_bf16_f32 v246, v241, v243\n\t"
    "v_accvgpr_read_b32 v241, a174\n\t"
    "v_mfma_f32_16x16x32_bf16 v[128:131], a[16:19], a[0:3], v[224:227]\n\t"
    "ds_read_b128 a[84:87], v248 offset:22528\n\t"
    "v_accvgpr_read_b32 v242, a222\n\t"
    "v_mfma_f32_16x16x32_bf16 v[128:131], a[48:51], a[32:35], v[128:131]\n\t"
    "v_exp_f32 v160, v160\n\t"
    "v_mfma_f32_16x16x32_bf16 v[128:131], a[80:83], a[64:67], v[128:131]\n\t"
    "v_exp_f32 v161, v161\n\t"
    "v_mfma_f32_16x16x32_bf16 v[128:131], a[112:115], a[96:99], v[128:131]\n\t"
    "s_waitcnt vmcnt(5)\n\t"
    "s_barrier\n\t"
    "v_accvgpr_read_b32 v243, a175\n\t"
    "v_mfma_f32_16x16x32_bf16 v[132:135], a[112:115], a[100:103], v[228:231]\n\t"
    "ds_read_b128 a[116:119], v248 offset:23552\n\t"
    "s_mul_i32 s10, s3, 2816\n\t"
    "v_mfma_f32_16x16x32_bf16 v[132:135], a[80:83], a[68:71], v[132:135]\n\t"
    "s_add_u32 s10, s10, 45056\n\t"
    "s_min_u32 s10, s10, s26\n\t"
    "v_min_u32 v252, 336, v248\n\t"
    "v_mfma_f32_16x16x32_bf16 v[132:135], a[48:51], a[36:39], v[132:135]\n\t"
    "v_lshlrev_b32 v252, 3, v252\n\t"
    "v_cvt_pk_bf16_f32 v208, v160, v161\n\t"
    "v_mfma_f32_16x16x32_bf16 v[132:135], a[16:19], a[4:7], v[132:135]\n\t"
    "buffer_load_dword a248, v252, s[24:27], s10 offen\n\t"
    "v_mfma_f32_16x16x32_bf16 v[136:139], a[16:19], a[8:11], v[232:235]\n\t"
    "v_exp_f32 v162, v162\n\t"
    "v_mfma_f32_16x16x32_bf16 v[136:139], a[48:51], a[40:43], v[136:139]\n\t"
    "s_add_u32 m0, s85, 65536\n\t"
    "v_exp_f32 v163, v163\n\t"
    "v_mfma_f32_16x16x32_bf16 v[136:139], a[80:83], a[72:75], v[136:139]\n\t"
    "v_exp_f32 v164, v164\n\t"
    "v_mfma_f32_16x16x32_bf16 v[136:139], a[112:115], a[104:107], v[136:139]\n\t"
    "buffer_load_dwordx4 v248, s[80:83], s84 offen sc1 lds\n\t"
    "v_mfma_f32_16x16x32_bf16 v[140:143], a[112:115], a[108:111], v[236:239]\n\t"
    "ds_read_b128 a[24:27], v250 offset:57344\n\t"
    "v_cvt_pk_bf16_f32 v209, v162, v163\n\t"
    "v_mfma_f32_16x16x32_bf16 v[140:143], a[80:83], a[76:79], v[140:143]\n\t"
    "v_exp_f32 v165, v165\n\t"
    "v_mfma_f32_16x16x32_bf16 v[140:143], a[48:51], a[44:47], v[140:143]\n\t"
    "v_exp_f32 v166, v166\n\t"
    "v_mfma_f32_16x16x32_bf16 v[140:143], a[16:19], a[12:15], v[140:143]\n\t"
    "s_waitcnt lgkmcnt(1)\n\t"
    "v_exp_f32 v167, v167\n\t"
    "v_mfma_f32_16x16x32_bf16 v[156:159], a[20:23], a[12:15], v[236:239]\n\t"
    "ds_read_b128 a[56:59], v250 offset:58368\n\t"
    "v_cvt_pk_bf16_f32 v212, v164, v165\n\t"
    "v_mfma_f32_16x16x32_bf16 v[156:159], a[52:55], a[44:47], v[156:159]\n\t"
    "v_exp_f32 v168, v168\n\t"
    "v_mfma_f32_16x16x32_bf16 v[156:159], a[84:87], a[76:79], v[156:159]\n\t"
    "v_exp_f32 v169, v169\n\t"
    "v_mfma_f32_16x16x32_bf16 v[156:159], a[116:119], a[108:111], v[156:159]\n\t"
    "buffer_load_dwordx4 v248, s[80:83], s84 offen offset:1024 sc1 lds\n\t"
    "v_mfma_f32_16x16x32_bf16 v[152:155], a[116:119], a[104:107], v[232:235]\n\t"
    "ds_read_b128 a[88:91], v250 offset:59392\n\t"
    "v_cvt_pk_bf16_f32 v213, v166, v167\n\t"
    "v_mfma_f32_16x16x32_bf16 v[152:155], a[84:87], a[72:75], v[152:155]\n\t"
    "v_exp_f32 v170, v170\n\t"
    "v_mfma_f32_16x16x32_bf16 v[152:155], a[52:55], a[40:43], v[152:155]\n\t"
    "v_exp_f32 v171, v171\n\t"
    "v_mfma_f32_16x16x32_bf16 v[152:155], a[20:23], a[8:11], v[152:155]\n\t"
    "v_cvt_pk_bf16_f32 v216, v168, v169\n\t"
    "v_accvgpr_read_b32 v249, a223\n\t"
    "v_mfma_f32_16x16x32_bf16 v[148:151], a[20:23], a[4:7], v[228:231]\n\t"
    "ds_read_b128 a[120:123], v250 offset:60416\n\t"
    "v_cvt_pk_bf16_f32 v217, v170, v171\n\t"
    "v_mfma_f32_16x16x32_bf16 v[148:151], a[52:55], a[36:39], v[148:151]\n\t"
    "v_exp_f32 v172, v172\n\t"
    "v_mfma_f32_16x16x32_bf16 v[148:151], a[84:87], a[68:71], v[148:151]\n\t"
    "v_exp_f32 v173, v173\n\t"
    "v_mfma_f32_16x16x32_bf16 v[148:151], a[116:119], a[100:103], v[148:151]\n\t"
    "buffer_load_dwordx4 v248, s[80:83], s84 offen offset:2048 sc1 lds\n\t"
    "v_mfma_f32_16x16x32_bf16 v[144:147], a[116:119], a[96:99], v[224:227]\n\t"
    "ds_read_b128 a[28:31], v250 offset:61440\n\t"
    "v_cvt_pk_bf16_f32 v220, v172, v173\n\t"
    "v_mfma_f32_16x16x32_bf16 v[144:147], a[84:87], a[64:67], v[144:147]\n\t"
    "v_exp_f32 v174, v174\n\t"
    "v_mfma_f32_16x16x32_bf16 v[144:147], a[52:55], a[32:35], v[144:147]\n\t"
    "v_exp_f32 v175, v175\n\t"
    "v_mfma_f32_16x16x32_bf16 v[144:147], a[20:23], a[0:3], v[144:147]\n\t"
    "s_waitcnt lgkmcnt(3)\n\t"
    "v_mul_f32 v241, v241, v242\n\t"
    "v_mul_f32 v243, v243, v249\n\t"
    "v_mfma_f32_16x16x32_bf16 v[0:3], a[24:27], v[192:195], v[0:3]\n\t"
    "ds_read_b128 a[60:63], v250 offset:62464\n\t"
    "v_cvt_pk_bf16_f32 v221, v174, v175\n\t"
    "v_mfma_f32_16x16x32_bf16 v[4:7], a[24:27], v[196:199], v[4:7]\n\t"
    "v_exp_f32 v188, v188\n\t"
    "v_mfma_f32_16x16x32_bf16 v[8:11], a[24:27], v[200:203], v[8:11]\n\t"
    "v_exp_f32 v189, v189\n\t"
    "v_mfma_f32_16x16x32_bf16 v[12:15], a[24:27], v[204:207], v[12:15]\n\t"
    "buffer_load_dwordx4 v248, s[80:83], s84 offen offset:3072 sc1 lds\n\t"
    "v_mfma_f32_16x16x32_bf16 v[28:31], a[56:59], v[204:207], v[28:31]\n\t"
    "ds_read_b128 a[92:95], v250 offset:63488\n\t"
    "v_cvt_pk_bf16_f32 v222, v188, v189\n\t"
    "v_mfma_f32_16x16x32_bf16 v[24:27], a[56:59], v[200:203], v[24:27]\n\t"
    "v_exp_f32 v190, v190\n\t"
    "v_mfma_f32_16x16x32_bf16 v[20:23], a[56:59], v[196:199], v[20:23]\n\t"
    "v_exp_f32 v191, v191\n\t"
    "v_mfma_f32_16x16x32_bf16 v[16:19], a[56:59], v[192:195], v[16:19]\n\t"
    "s_waitcnt lgkmcnt(3)\n\t"
    "v_cvt_pk_bf16_f32 v247, v241, v243\n\t"
    "v_mfma_f32_16x16x32_bf16 v[32:35], a[88:91], v[192:195], v[32:35]\n\t"
    "ds_read_b128 a[124:127], v250 offset:64512\n\t"
    "v_cvt_pk_bf16_f32 v223, v190, v191\n\t"
    "v_mfma_f32_16x16x32_bf16 v[36:39], a[88:91], v[196:199], v[36:39]\n\t"
    "v_exp_f32 v184, v184\n\t"
    "v_mfma_f32_16x16x32_bf16 v[40:43], a[88:91], v[200:203], v[40:43]\n\t"
    "v_exp_f32 v185, v185\n\t"
    "v_mfma_f32_16x16x32_bf16 v[44:47], a[88:91], v[204:207], v[44:47]\n\t"
    "s_add_u32 s84, s84, 16384\n\t"
    "buffer_store_dwordx4 v[244:247], v248, s[88:91], s98 offen offset:3072 sc1\n\t"
    "v_mfma_f32_16x16x32_bf16 v[60:63], a[120:123], v[204:207], v[60:63]\n\t"
    "ds_read_b128 a[16:19], v248 offset:32768\n\t"
    "v_cvt_pk_bf16_f32 v218, v184, v185\n\t"
    "v_mfma_f32_16x16x32_bf16 v[56:59], a[120:123], v[200:203], v[56:59]\n\t"
    "v_exp_f32 v186, v186\n\t"
    "v_mfma_f32_16x16x32_bf16 v[52:55], a[120:123], v[196:199], v[52:55]\n\t"
    "v_exp_f32 v187, v187\n\t"
    "v_mfma_f32_16x16x32_bf16 v[48:51], a[120:123], v[192:195], v[48:51]\n\t"
    "s_waitcnt lgkmcnt(3)\n\t"
    "v_accvgpr_read_b32 v241, a176\n\t"
    "v_accvgpr_read_b32 v242, a224\n\t"
    "v_mfma_f32_16x16x32_bf16 v[64:67], a[28:31], v[192:195], v[64:67]\n\t"
    "ds_read_b128 a[48:51], v248 offset:33792\n\t"
    "v_cvt_pk_bf16_f32 v219, v186, v187\n\t"
    "v_mfma_f32_16x16x32_bf16 v[68:71], a[28:31], v[196:199], v[68:71]\n\t"
    "v_exp_f32 v180, v180\n\t"
    "v_mfma_f32_16x16x32_bf16 v[72:75], a[28:31], v[200:203], v[72:75]\n\t"
    "v_exp_f32 v181, v181\n\t"
    "v_mfma_f32_16x16x32_bf16 v[76:79], a[28:31], v[204:207], v[76:79]\n\t"
    "v_accvgpr_read_b32 v243, a177\n\t"
    "v_accvgpr_read_b32 v249, a225\n\t"
    "v_mfma_f32_16x16x32_bf16 v[92:95], a[60:63], v[204:207], v[92:95]\n\t"
    "ds_read_b128 a[80:83], v248 offset:34816\n\t"
    "v_cvt_pk_bf16_f32 v214, v180, v181\n\t"
    "v_mfma_f32_16x16x32_bf16 v[88:91], a[60:63], v[200:203], v[88:91]\n\t"
    "v_exp_f32 v182, v182\n\t"
    "v_mfma_f32_16x16x32_bf16 v[84:87], a[60:63], v[196:199], v[84:87]\n\t"
    "v_exp_f32 v183, v183\n\t"
    "v_mfma_f32_16x16x32_bf16 v[80:83], a[60:63], v[192:195], v[80:83]\n\t"
    "s_waitcnt lgkmcnt(3)\n\t"
    "v_mul_f32 v241, v241, v242\n\t"
    "v_mul_f32 v243, v243, v249\n\t"
    "v_mfma_f32_16x16x32_bf16 v[96:99], a[92:95], v[192:195], v[96:99]\n\t"
    "ds_read_b128 a[112:115], v248 offset:35840\n\t"
    "v_cvt_pk_bf16_f32 v215, v182, v183\n\t"
    "v_mfma_f32_16x16x32_bf16 v[100:103], a[92:95], v[196:199], v[100:103]\n\t"
    "v_exp_f32 v176, v176\n\t"
    "v_mfma_f32_16x16x32_bf16 v[104:107], a[92:95], v[200:203], v[104:107]\n\t"
    "v_exp_f32 v177, v177\n\t"
    "v_mfma_f32_16x16x32_bf16 v[108:111], a[92:95], v[204:207], v[108:111]\n\t"
    "v_cvt_pk_bf16_f32 v244, v241, v243\n\t"
    "v_accvgpr_read_b32 v241, a178\n\t"
    "v_mfma_f32_16x16x32_bf16 v[124:127], a[124:127], v[204:207], v[124:127]\n\t"
    "ds_read_b128 a[20:23], v248 offset:36864\n\t"
    "v_cvt_pk_bf16_f32 v210, v176, v177\n\t"
    "v_mfma_f32_16x16x32_bf16 v[120:123], a[124:127], v[200:203], v[120:123]\n\t"
    "v_exp_f32 v178, v178\n\t"
    "v_mfma_f32_16x16x32_bf16 v[116:119], a[124:127], v[196:199], v[116:119]\n\t"
    "v_exp_f32 v179, v179\n\t"
    "v_mfma_f32_16x16x32_bf16 v[112:115], a[124:127], v[192:195], v[112:115]\n\t"
    "v_accvgpr_read_b32 v242, a226\n\t"
    "v_accvgpr_read_b32 v243, a179\n\t"
    "v_mfma_f32_16x16x32_bf16 a[128:131], a[240:243], v[192:195], a[128:131]\n\t"
    "ds_read_b128 a[52:55], v248 offset:37888\n\t"
    "v_cvt_pk_bf16_f32 v211, v178, v179\n\t"
    "v_mfma_f32_16x16x32_bf16 a[132:135], a[240:243], v[196:199], a[132:135]\n\t"
    "v_accvgpr_read_b32 v249, a227\n\t"
    "v_mul_f32 v241, v241, v242\n\t"
    "v_mfma_f32_16x16x32_bf16 a[136:139], a[240:243], v[200:203], a[136:139]\n\t"
    "v_mul_f32 v243, v243, v249\n\t"
    "v_cvt_pk_bf16_f32 v245, v241, v243\n\t"
    "v_mfma_f32_16x16x32_bf16 a[140:143], a[240:243], v[204:207], a[140:143]\n\t"
    "s_waitcnt lgkmcnt(2)\n\t"
    "buffer_store_dwordx2 v[244:245], v248, s[88:91], s99 offen sc1\n\t"
    "v_mfma_f32_16x16x32_bf16 v[160:163], a[16:19], a[0:3], v[224:227]\n\t"
    "ds_read_b128 a[84:87], v248 offset:38912\n\t"
    "v_accvgpr_read_b32 v241, a180\n\t"
    "v_mfma_f32_16x16x32_bf16 v[160:163], a[48:51], a[32:35], v[160:163]\n\t"
    "v_exp_f32 v128, v128\n\t"
    "v_mfma_f32_16x16x32_bf16 v[160:163], a[80:83], a[64:67], v[160:163]\n\t"
    "v_exp_f32 v129, v129\n\t"
    "v_mfma_f32_16x16x32_bf16 v[160:163], a[112:115], a[96:99], v[160:163]\n\t"
    "v_accvgpr_read_b32 v242, a228\n\t"
    "v_accvgpr_read_b32 v243, a181\n\t"
    "v_mfma_f32_16x16x32_bf16 v[164:167], a[112:115], a[100:103], v[228:231]\n\t"
    "ds_read_b128 a[116:119], v248 offset:39936\n\t"
    "v_cvt_pk_bf16_f32 v192, v128, v129\n\t"
    "v_mfma_f32_16x16x32_bf16 v[164:167], a[80:83], a[68:71], v[164:167]\n\t"
    "v_exp_f32 v130, v130\n\t"
    "v_mfma_f32_16x16x32_bf16 v[164:167], a[48:51], a[36:39], v[164:167]\n\t"
    "v_exp_f32 v131, v131\n\t"
    "v_mfma_f32_16x16x32_bf16 v[164:167], a[16:19], a[4:7], v[164:167]\n\t"
    "v_accvgpr_read_b32 v249, a229\n\t"
    "v_mul_f32 v241, v241, v242\n\t"
    "v_mfma_f32_16x16x32_bf16 v[168:171], a[16:19], a[8:11], v[232:235]\n\t"
    "v_cvt_pk_bf16_f32 v193, v130, v131\n\t"
    "v_mul_f32 v243, v243, v249\n\t"
    "v_mfma_f32_16x16x32_bf16 v[168:171], a[48:51], a[40:43], v[168:171]\n\t"
    "s_add_u32 m0, s85, 81920\n\t"
    "v_exp_f32 v132, v132\n\t"
    "v_mfma_f32_16x16x32_bf16 v[168:171], a[80:83], a[72:75], v[168:171]\n\t"
    "v_exp_f32 v133, v133\n\t"
    "v_mfma_f32_16x16x32_bf16 v[168:171], a[112:115], a[104:107], v[168:171]\n\t"
    "buffer_load_dwordx4 v248, s[80:83], s84 offen sc1 lds\n\t"
    "v_mfma_f32_16x16x32_bf16 v[172:175], a[112:115], a[108:111], v[236:239]\n\t"
    "ds_read_b128 a[24:27], v251 offset:8192\n\t"
    "v_cvt_pk_bf16_f32 v196, v132, v133\n\t"
    "v_mfma_f32_16x16x32_bf16 v[172:175], a[80:83], a[76:79], v[172:175]\n\t"
    "v_exp_f32 v134, v134\n\t"
    "v_mfma_f32_16x16x32_bf16 v[172:175], a[48:51], a[44:47], v[172:175]\n\t"
    "v_exp_f32 v135, v135\n\t"
    "v_mfma_f32_16x16x32_bf16 v[172:175], a[16:19], a[12:15], v[172:175]\n\t"
    "s_waitcnt lgkmcnt(1)\n\t"
    "v_cvt_pk_bf16_f32 v244, v241, v243\n\t"
    "v_accvgpr_read_b32 v241, a182\n\t"
    "v_mfma_f32_16x16x32_bf16 v[188:191], a[20:23], a[12:15], v[236:239]\n\t"
    "ds_read_b128 a[56:59], v251 offset:9216\n\t"
    "v_cvt_pk_bf16_f32 v197, v134, v135\n\t"
    "v_mfma_f32_16x16x32_bf16 v[188:191], a[52:55], a[44:47], v[188:191]\n\t"
    "v_exp_f32 v136, v136\n\t"
    "v_mfma_f32_16x16x32_bf16 v[188:191], a[84:87], a[76:79], v[188:191]\n\t"
    "v_exp_f32 v137, v137\n\t"
    "v_mfma_f32_16x16x32_bf16 v[188:191], a[116:119], a[108:111], v[188:191]\n\t"
    "buffer_load_dwordx4 v248, s[80:83], s84 offen offset:1024 sc1 lds\n\t"
    "v_mfma_f32_16x16x32_bf16 v[184:187], a[116:119], a[104:107], v[232:235]\n\t"
    "ds_read_b128 a[88:91], v251 offset:10240\n\t"
    "v_cvt_pk_bf16_f32 v200, v136, v137\n\t"
    "v_mfma_f32_16x16x32_bf16 v[184:187], a[84:87], a[72:75], v[184:187]\n\t"
    "v_exp_f32 v138, v138\n\t"
    "v_mfma_f32_16x16x32_bf16 v[184:187], a[52:55], a[40:43], v[184:187]\n\t"
    "v_exp_f32 v139, v139\n\t"
    "v_mfma_f32_16x16x32_bf16 v[184:187], a[20:23], a[8:11], v[184:187]\n\t"
    "v_accvgpr_read_b32 v242, a230\n\t"
    "v_accvgpr_read_b32 v243, a183\n\t"
    "v_mfma_f32_16x16x32_bf16 v[180:183], a[20:23], a[4:7], v[228:231]\n\t"
    "ds_read_b128 a[120:123], v251 offset:11264\n\t"
    "v_cvt_pk_bf16_f32 v201, v138, v139\n\t"
    "v_mfma_f32_16x16x32_bf16 v[180:183], a[52:55], a[36:39], v[180:183]\n\t"
    "v_exp_f32 v140, v140\n\t"
    "v_mfma_f32_16x16x32_bf16 v[180:183], a[84:87], a[68:71], v[180:183]\n\t"
    "v_exp_f32 v141, v141\n\t"
    "v_mfma_f32_16x16x32_bf16 v[180:183], a[116:119], a[100:103], v[180:183]\n\t"
    "buffer_load_dwordx4 v248, s[80:83], s84 offen offset:2048 sc1 lds\n\t"
    "v_mfma_f32_16x16x32_bf16 v[176:179], a[116:119], a[96:99], v[224:227]\n\t"
    "ds_read_b128 a[28:31], v251 offset:12288\n\t"
    "v_cvt_pk_bf16_f32 v204, v140, v141\n\t"
    "v_mfma_f32_16x16x32_bf16 v[176:179], a[84:87], a[64:67], v[176:179]\n\t"
    "v_exp_f32 v142, v142\n\t"
    "v_mfma_f32_16x16x32_bf16 v[176:179], a[52:55], a[32:35], v[176:179]\n\t"
    "v_exp_f32 v143, v143\n\t"
    "v_mfma_f32_16x16x32_bf16 v[176:179], a[20:23], a[0:3], v[176:179]\n\t"
    "s_waitcnt lgkmcnt(3)\n\t"
    "v_accvgpr_read_b32 v249, a231\n\t"
    "v_mul_f32 v241, v241, v242\n\t"
    "v_mfma_f32_16x16x32_bf16 v[0:3], a[24:27], v[208:211], v[0:3]\n\t"
    "ds_read_b128 a[60:63], v251 offset:13312\n\t"
    "v_cvt_pk_bf16_f32 v205, v142, v143\n\t"
    "v_mfma_f32_16x16x32_bf16 v[4:7], a[24:27], v[212:215], v[4:7]\n\t"
    "v_exp_f32 v156, v156\n\t"
    "v_mfma_f32_16x16x32_bf16 v[8:11], a[24:27], v[216:219], v[8:11]\n\t"
    "v_exp_f32 v157, v157\n\t"
    "v_mfma_f32_16x16x32_bf16 v[12:15], a[24:27], v[220:223], v[12:15]\n\t"
    "buffer_load_dwordx4 v248, s[80:83], s84 offen offset:3072 sc1 lds\n\t"
    "v_mfma_f32_16x16x32_bf16 v[28:31], a[56:59], v[220:223], v[28:31]\n\t"
    "ds_read_b128 a[92:95], v251 offset:14336\n\t"
    "v_cvt_pk_bf16_f32 v206, v156, v157\n\t"
    "v_mfma_f32_16x16x32_bf16 v[24:27], a[56:59], v[216:219], v[24:27]\n\t"
    "v_exp_f32 v158, v158\n\t"
    "v_mfma_f32_16x16x32_bf16 v[20:23], a[56:59], v[212:215], v[20:23]\n\t"
    "v_exp_f32 v159, v159\n\t"
    "v_mfma_f32_16x16x32_bf16 v[16:19], a[56:59], v[208:211], v[16:19]\n\t"
    "s_waitcnt lgkmcnt(3)\n\t"
    "v_mul_f32 v243, v243, v249\n\t"
    "v_cvt_pk_bf16_f32 v245, v241, v243\n\t"
    "v_mfma_f32_16x16x32_bf16 v[32:35], a[88:91], v[208:211], v[32:35]\n\t"
    "ds_read_b128 a[124:127], v251 offset:15360\n\t"
    "v_cvt_pk_bf16_f32 v207, v158, v159\n\t"
    "v_mfma_f32_16x16x32_bf16 v[36:39], a[88:91], v[212:215], v[36:39]\n\t"
    "v_exp_f32 v152, v152\n\t"
    "v_mfma_f32_16x16x32_bf16 v[40:43], a[88:91], v[216:219], v[40:43]\n\t"
    "v_exp_f32 v153, v153\n\t"
    "v_mfma_f32_16x16x32_bf16 v[44:47], a[88:91], v[220:223], v[44:47]\n\t"
    "s_add_u32 s84, s84, 16384\n\t"
    "buffer_store_dwordx2 v[244:245], v248, s[88:91], s99 offen offset:1024 sc1\n\t"
    "v_mfma_f32_16x16x32_bf16 v[60:63], a[120:123], v[220:223], v[60:63]\n\t"
    "ds_read_b128 a[16:19], v248 offset:49152\n\t"
    "v_cvt_pk_bf16_f32 v202, v152, v153\n\t"
    "v_mfma_f32_16x16x32_bf16 v[56:59], a[120:123], v[216:219], v[56:59]\n\t"
    "v_exp_f32 v154, v154\n\t"
    "v_mfma_f32_16x16x32_bf16 v[52:55], a[120:123], v[212:215], v[52:55]\n\t"
    "v_exp_f32 v155, v155\n\t"
    "v_mfma_f32_16x16x32_bf16 v[48:51], a[120:123], v[208:211], v[48:51]\n\t"
    "s_waitcnt lgkmcnt(3)\n\t"
    "v_accvgpr_read_b32 v241, a184\n\t"
    "v_accvgpr_read_b32 v242, a232\n\t"
    "v_mfma_f32_16x16x32_bf16 v[64:67], a[28:31], v[208:211], v[64:67]\n\t"
    "ds_read_b128 a[48:51], v248 offset:50176\n\t"
    "v_cvt_pk_bf16_f32 v203, v154, v155\n\t"
    "v_mfma_f32_16x16x32_bf16 v[68:71], a[28:31], v[212:215], v[68:71]\n\t"
    "v_exp_f32 v148, v148\n\t"
    "v_mfma_f32_16x16x32_bf16 v[72:75], a[28:31], v[216:219], v[72:75]\n\t"
    "v_exp_f32 v149, v149\n\t"
    "v_mfma_f32_16x16x32_bf16 v[76:79], a[28:31], v[220:223], v[76:79]\n\t"
    "v_accvgpr_read_b32 v243, a185\n\t"
    "v_accvgpr_read_b32 v249, a233\n\t"
    "v_mfma_f32_16x16x32_bf16 v[92:95], a[60:63], v[220:223], v[92:95]\n\t"
    "ds_read_b128 a[80:83], v248 offset:51200\n\t"
    "v_cvt_pk_bf16_f32 v198, v148, v149\n\t"
    "v_mfma_f32_16x16x32_bf16 v[88:91], a[60:63], v[216:219], v[88:91]\n\t"
    "v_exp_f32 v150, v150\n\t"
    "v_mfma_f32_16x16x32_bf16 v[84:87], a[60:63], v[212:215], v[84:87]\n\t"
    "v_exp_f32 v151, v151\n\t"
    "v_mfma_f32_16x16x32_bf16 v[80:83], a[60:63], v[208:211], v[80:83]\n\t"
    "s_waitcnt lgkmcnt(3)\n\t"
    "v_mul_f32 v241, v241, v242\n\t"
    "v_mul_f32 v243, v243, v249\n\t"
    "v_mfma_f32_16x16x32_bf16 v[96:99], a[92:95], v[208:211], v[96:99]\n\t"
    "ds_read_b128 a[112:115], v248 offset:52224\n\t"
    "v_cvt_pk_bf16_f32 v199, v150, v151\n\t"
    "v_mfma_f32_16x16x32_bf16 v[100:103], a[92:95], v[212:215], v[100:103]\n\t"
    "v_exp_f32 v144, v144\n\t"
    "v_mfma_f32_16x16x32_bf16 v[104:107], a[92:95], v[216:219], v[104:107]\n\t"
    "v_exp_f32 v145, v145\n\t"
    "v_mfma_f32_16x16x32_bf16 v[108:111], a[92:95], v[220:223], v[108:111]\n\t"
    "v_cvt_pk_bf16_f32 v244, v241, v243\n\t"
    "v_accvgpr_read_b32 v241, a186\n\t"
    "v_mfma_f32_16x16x32_bf16 v[124:127], a[124:127], v[220:223], v[124:127]\n\t"
    "ds_read_b128 a[20:23], v248 offset:53248\n\t"
    "v_cvt_pk_bf16_f32 v194, v144, v145\n\t"
    "v_mfma_f32_16x16x32_bf16 v[120:123], a[124:127], v[216:219], v[120:123]\n\t"
    "v_exp_f32 v146, v146\n\t"
    "v_mfma_f32_16x16x32_bf16 v[116:119], a[124:127], v[212:215], v[116:119]\n\t"
    "v_exp_f32 v147, v147\n\t"
    "v_mfma_f32_16x16x32_bf16 v[112:115], a[124:127], v[208:211], v[112:115]\n\t"
    "v_accvgpr_read_b32 v242, a234\n\t"
    "v_accvgpr_read_b32 v243, a187\n\t"
    "v_mfma_f32_16x16x32_bf16 a[128:131], a[240:243], v[208:211], a[128:131]\n\t"
    "ds_read_b128 a[52:55], v248 offset:54272\n\t"
    "v_cvt_pk_bf16_f32 v195, v146, v147\n\t"
    "v_mfma_f32_16x16x32_bf16 a[132:135], a[240:243], v[212:215], a[132:135]\n\t"
    "v_accvgpr_read_b32 v249, a235\n\t"
    "v_mul_f32 v241, v241, v242\n\t"
    "v_mfma_f32_16x16x32_bf16 a[136:139], a[240:243], v[216:219], a[136:139]\n\t"
    "v_mul_f32 v243, v243, v249\n\t"
    "v_cvt_pk_bf16_f32 v245, v241, v243\n\t"
    "v_mfma_f32_16x16x32_bf16 a[140:143], a[240:243], v[220:223], a[140:143]\n\t"
    "s_waitcnt lgkmcnt(2)\n\t"
    "buffer_store_dwordx2 v[244:245], v248, s[88:91], s99 offen offset:2048 sc1\n\t"
    "v_mfma_f32_16x16x32_bf16 v[128:131], a[16:19], a[0:3], v[224:227]\n\t"
    "ds_read_b128 a[84:87], v248 offset:55296\n\t"
    "v_accvgpr_read_b32 v241, a188\n\t"
    "v_mfma_f32_16x16x32_bf16 v[128:131], a[48:51], a[32:35], v[128:131]\n\t"
    "v_exp_f32 v160, v160\n\t"
    "v_mfma_f32_16x16x32_bf16 v[128:131], a[80:83], a[64:67], v[128:131]\n\t"
    "v_exp_f32 v161, v161\n\t"
    "v_mfma_f32_16x16x32_bf16 v[128:131], a[112:115], a[96:99], v[128:131]\n\t"
    "s_waitcnt vmcnt(8)\n\t"
    "s_barrier\n\t"
    "v_accvgpr_read_b32 v242, a236\n\t"
    "v_mfma_f32_16x16x32_bf16 v[132:135], a[112:115], a[100:103], v[228:231]\n\t"
    "ds_read_b128 a[116:119], v248 offset:56320\n\t"
    "s_mul_i32 s10, s3, 2816\n\t"
    "v_mfma_f32_16x16x32_bf16 v[132:135], a[80:83], a[68:71], v[132:135]\n\t"
    "s_add_u32 s10, s10, 56320\n\t"
    "s_min_u32 s10, s10, s26\n\t"
    "v_min_u32 v252, 336, v248\n\t"
    "v_mfma_f32_16x16x32_bf16 v[132:135], a[48:51], a[36:39], v[132:135]\n\t"
    "v_lshlrev_b32 v252, 3, v252\n\t"
    "v_cvt_pk_bf16_f32 v208, v160, v161\n\t"
    "v_mfma_f32_16x16x32_bf16 v[132:135], a[16:19], a[4:7], v[132:135]\n\t"
    "buffer_load_dword a249, v252, s[24:27], s10 offen\n\t"
    "v_mfma_f32_16x16x32_bf16 v[136:139], a[16:19], a[8:11], v[232:235]\n\t"
    "v_exp_f32 v162, v162\n\t"
    "v_mfma_f32_16x16x32_bf16 v[136:139], a[48:51], a[40:43], v[136:139]\n\t"
    "s_add_u32 m0, s85, 98304\n\t"
    "v_exp_f32 v163, v163\n\t"
    "v_mfma_f32_16x16x32_bf16 v[136:139], a[80:83], a[72:75], v[136:139]\n\t"
    "v_exp_f32 v164, v164\n\t"
    "v_mfma_f32_16x16x32_bf16 v[136:139], a[112:115], a[104:107], v[136:139]\n\t"
    "buffer_load_dwordx4 v248, s[80:83], s84 offen sc1 lds\n\t"
    "v_mfma_f32_16x16x32_bf16 v[140:143], a[112:115], a[108:111], v[236:239]\n\t"
    "ds_read_b128 a[24:27], v248 offset:24576\n\t"
    "v_cvt_pk_bf16_f32 v209, v162, v163\n\t"
    "v_mfma_f32_16x16x32_bf16 v[140:143], a[80:83], a[76:79], v[140:143]\n\t"
    "v_exp_f32 v165, v165\n\t"
    "v_mfma_f32_16x16x32_bf16 v[140:143], a[48:51], a[44:47], v[140:143]\n\t"
    "v_exp_f32 v166, v166\n\t"
    "v_mfma_f32_16x16x32_bf16 v[140:143], a[16:19], a[12:15], v[140:143]\n\t"
    "s_waitcnt lgkmcnt(1)\n\t"
    "v_exp_f32 v167, v167\n\t"
    "v_mfma_f32_16x16x32_bf16 v[156:159], a[20:23], a[12:15], v[236:239]\n\t"
    "ds_read_b128 a[56:59], v248 offset:25600\n\t"
    "v_cvt_pk_bf16_f32 v212, v164, v165\n\t"
    "v_mfma_f32_16x16x32_bf16 v[156:159], a[52:55], a[44:47], v[156:159]\n\t"
    "v_exp_f32 v168, v168\n\t"
    "v_mfma_f32_16x16x32_bf16 v[156:159], a[84:87], a[76:79], v[156:159]\n\t"
    "v_exp_f32 v169, v169\n\t"
    "v_mfma_f32_16x16x32_bf16 v[156:159], a[116:119], a[108:111], v[156:159]\n\t"
    "buffer_load_dwordx4 v248, s[80:83], s84 offen offset:1024 sc1 lds\n\t"
    "v_mfma_f32_16x16x32_bf16 v[152:155], a[116:119], a[104:107], v[232:235]\n\t"
    "ds_read_b128 a[88:91], v248 offset:26624\n\t"
    "v_cvt_pk_bf16_f32 v213, v166, v167\n\t"
    "v_mfma_f32_16x16x32_bf16 v[152:155], a[84:87], a[72:75], v[152:155]\n\t"
    "v_exp_f32 v170, v170\n\t"
    "v_mfma_f32_16x16x32_bf16 v[152:155], a[52:55], a[40:43], v[152:155]\n\t"
    "v_exp_f32 v171, v171\n\t"
    "v_mfma_f32_16x16x32_bf16 v[152:155], a[20:23], a[8:11], v[152:155]\n\t"
    "v_cvt_pk_bf16_f32 v216, v168, v169\n\t"
    "v_accvgpr_read_b32 v243, a189\n\t"
    "v_mfma_f32_16x16x32_bf16 v[148:151], a[20:23], a[4:7], v[228:231]\n\t"
    "ds_read_b128 a[120:123], v248 offset:27648\n\t"
    "v_cvt_pk_bf16_f32 v217, v170, v171\n\t"
    "v_mfma_f32_16x16x32_bf16 v[148:151], a[52:55], a[36:39], v[148:151]\n\t"
    "v_exp_f32 v172, v172\n\t"
    "v_mfma_f32_16x16x32_bf16 v[148:151], a[84:87], a[68:71], v[148:151]\n\t"
    "v_exp_f32 v173, v173\n\t"
    "v_mfma_f32_16x16x32_bf16 v[148:151], a[116:119], a[100:103], v[148:151]\n\t"
    "buffer_load_dwordx4 v248, s[80:83], s84 offen offset:2048 sc1 lds\n\t"
    "v_mfma_f32_16x16x32_bf16 v[144:147], a[116:119], a[96:99], v[224:227]\n\t"
    "ds_read_b128 a[28:31], v248 offset:28672\n\t"
    "v_cvt_pk_bf16_f32 v220, v172, v173\n\t"
    "v_mfma_f32_16x16x32_bf16 v[144:147], a[84:87], a[64:67], v[144:147]\n\t"
    "v_exp_f32 v174, v174\n\t"
    "v_mfma_f32_16x16x32_bf16 v[144:147], a[52:55], a[32:35], v[144:147]\n\t"
    "v_exp_f32 v175, v175\n\t"
    "v_mfma_f32_16x16x32_bf16 v[144:147], a[20:23], a[0:3], v[144:147]\n\t"
    "s_waitcnt lgkmcnt(3)\n\t"
    "v_accvgpr_read_b32 v249, a237\n\t"
    "v_mul_f32 v241, v241, v242\n\t"
    "v_mfma_f32_16x16x32_bf16 v[0:3], a[24:27], v[192:195], v[0:3]\n\t"
    "ds_read_b128 a[60:63], v248 offset:29696\n\t"
    "v_cvt_pk_bf16_f32 v221, v174, v175\n\t"
    "v_mfma_f32_16x16x32_bf16 v[4:7], a[24:27], v[196:199], v[4:7]\n\t"
    "v_exp_f32 v188, v188\n\t"
    "v_mfma_f32_16x16x32_bf16 v[8:11], a[24:27], v[200:203], v[8:11]\n\t"
    "v_exp_f32 v189, v189\n\t"
    "v_mfma_f32_16x16x32_bf16 v[12:15], a[24:27], v[204:207], v[12:15]\n\t"
    "buffer_load_dwordx4 v248, s[80:83], s84 offen offset:3072 sc1 lds\n\t"
    "v_mfma_f32_16x16x32_bf16 v[28:31], a[56:59], v[204:207], v[28:31]\n\t"
    "ds_read_b128 a[92:95], v248 offset:30720\n\t"
    "v_cvt_pk_bf16_f32 v222, v188, v189\n\t"
    "v_mfma_f32_16x16x32_bf16 v[24:27], a[56:59], v[200:203], v[24:27]\n\t"
    "v_exp_f32 v190, v190\n\t"
    "v_mfma_f32_16x16x32_bf16 v[20:23], a[56:59], v[196:199], v[20:23]\n\t"
    "v_exp_f32 v191, v191\n\t"
    "v_mfma_f32_16x16x32_bf16 v[16:19], a[56:59], v[192:195], v[16:19]\n\t"
    "s_waitcnt lgkmcnt(3)\n\t"
    "v_mul_f32 v243, v243, v249\n\t"
    "v_cvt_pk_bf16_f32 v244, v241, v243\n\t"
    "v_mfma_f32_16x16x32_bf16 v[32:35], a[88:91], v[192:195], v[32:35]\n\t"
    "ds_read_b128 a[124:127], v248 offset:31744\n\t"
    "v_cvt_pk_bf16_f32 v223, v190, v191\n\t"
    "v_mfma_f32_16x16x32_bf16 v[36:39], a[88:91], v[196:199], v[36:39]\n\t"
    "v_exp_f32 v184, v184\n\t"
    "v_mfma_f32_16x16x32_bf16 v[40:43], a[88:91], v[200:203], v[40:43]\n\t"
    "v_exp_f32 v185, v185\n\t"
    "v_mfma_f32_16x16x32_bf16 v[44:47], a[88:91], v[204:207], v[44:47]\n\t"
    "s_add_u32 s84, s84, 16384\n\t"
    "v_accvgpr_read_b32 v241, a190\n\t"
    "v_accvgpr_read_b32 v242, a238\n\t"
    "v_mfma_f32_16x16x32_bf16 v[60:63], a[120:123], v[204:207], v[60:63]\n\t"
    "ds_read_b128 a[16:19], v250 offset:0\n\t"
    "v_cvt_pk_bf16_f32 v218, v184, v185\n\t"
    "v_mfma_f32_16x16x32_bf16 v[56:59], a[120:123], v[200:203], v[56:59]\n\t"
    "v_exp_f32 v186, v186\n\t"
    "v_mfma_f32_16x16x32_bf16 v[52:55], a[120:123], v[196:199], v[52:55]\n\t"
    "v_exp_f32 v187, v187\n\t"
    "v_mfma_f32_16x16x32_bf16 v[48:51], a[120:123], v[192:195], v[48:51]\n\t"
    "s_waitcnt lgkmcnt(3)\n\t"
    "v_accvgpr_read_b32 v243, a191\n\t"
    "v_accvgpr_read_b32 v249, a239\n\t"
    "v_mfma_f32_16x16x32_bf16 v[64:67], a[28:31], v[192:195], v[64:67]\n\t"
    "ds_read_b128 a[48:51], v250 offset:1024\n\t"
    "v_cvt_pk_bf16_f32 v219, v186, v187\n\t"
    "v_mfma_f32_16x16x32_bf16 v[68:71], a[28:31], v[196:199], v[68:71]\n\t"
    "v_exp_f32 v180, v180\n\t"
    "v_mfma_f32_16x16x32_bf16 v[72:75], a[28:31], v[200:203], v[72:75]\n\t"
    "v_exp_f32 v181, v181\n\t"
    "v_mfma_f32_16x16x32_bf16 v[76:79], a[28:31], v[204:207], v[76:79]\n\t"
    "v_mul_f32 v241, v241, v242\n\t"
    "v_mul_f32 v243, v243, v249\n\t"
    "v_mfma_f32_16x16x32_bf16 v[92:95], a[60:63], v[204:207], v[92:95]\n\t"
    "ds_read_b128 a[80:83], v250 offset:2048\n\t"
    "v_cvt_pk_bf16_f32 v214, v180, v181\n\t"
    "v_mfma_f32_16x16x32_bf16 v[88:91], a[60:63], v[200:203], v[88:91]\n\t"
    "v_exp_f32 v182, v182\n\t"
    "v_mfma_f32_16x16x32_bf16 v[84:87], a[60:63], v[196:199], v[84:87]\n\t"
    "v_exp_f32 v183, v183\n\t"
    "v_mfma_f32_16x16x32_bf16 v[80:83], a[60:63], v[192:195], v[80:83]\n\t"
    "s_waitcnt lgkmcnt(3)\n\t"
    "v_cvt_pk_bf16_f32 v245, v241, v243\n\t"
    "v_mfma_f32_16x16x32_bf16 v[96:99], a[92:95], v[192:195], v[96:99]\n\t"
    "ds_read_b128 a[112:115], v250 offset:3072\n\t"
    "v_cvt_pk_bf16_f32 v215, v182, v183\n\t"
    "v_mfma_f32_16x16x32_bf16 v[100:103], a[92:95], v[196:199], v[100:103]\n\t"
    "v_exp_f32 v176, v176\n\t"
    "v_mfma_f32_16x16x32_bf16 v[104:107], a[92:95], v[200:203], v[104:107]\n\t"
    "v_exp_f32 v177, v177\n\t"
    "v_mfma_f32_16x16x32_bf16 v[108:111], a[92:95], v[204:207], v[108:111]\n\t"
    "buffer_store_dwordx2 v[244:245], v248, s[88:91], s99 offen offset:3072 sc1\n\t"
    "v_mfma_f32_16x16x32_bf16 v[124:127], a[124:127], v[204:207], v[124:127]\n\t"
    "ds_read_b128 a[20:23], v250 offset:4096\n\t"
    "v_cvt_pk_bf16_f32 v210, v176, v177\n\t"
    "v_mfma_f32_16x16x32_bf16 v[120:123], a[124:127], v[200:203], v[120:123]\n\t"
    "v_exp_f32 v178, v178\n\t"
    "v_mfma_f32_16x16x32_bf16 v[116:119], a[124:127], v[196:199], v[116:119]\n\t"
    "v_exp_f32 v179, v179\n\t"
    "v_mfma_f32_16x16x32_bf16 v[112:115], a[124:127], v[192:195], v[112:115]\n\t"
    "s_add_u32 s40, s18, s77\n\t"
    "s_addc_u32 s41, s19, 0\n\t"
    "s_lshl_b32 s77, s49, 21\n\t"
    "v_mfma_f32_16x16x32_bf16 a[128:131], a[240:243], v[192:195], a[128:131]\n\t"
    "ds_read_b128 a[52:55], v250 offset:5120\n\t"
    "v_cvt_pk_bf16_f32 v211, v178, v179\n\t"
    "v_mfma_f32_16x16x32_bf16 a[132:135], a[240:243], v[196:199], a[132:135]\n\t"
    "s_add_u32 s42, s12, s77\n\t"
    "s_addc_u32 s43, s13, 0\n\t"
    "s_mov_b32 s44, 2097152\n\t"
    "v_mfma_f32_16x16x32_bf16 a[136:139], a[240:243], v[200:203], a[136:139]\n\t"
    "s_lshl_b32 s78, s50, 2\n\t"
    "s_add_u32 s78, s78, s3\n\t"
    "s_mul_i32 s77, s78, 463104\n\t"
    "v_mfma_f32_16x16x32_bf16 a[140:143], a[240:243], v[204:207], a[140:143]\n\t"
    "s_waitcnt lgkmcnt(2)\n\t"
    "s_mul_i32 s46, s49, 114688\n\t"
    "s_add_u32 s46, s46, s77\n\t"
    "v_mfma_f32_16x16x32_bf16 v[160:163], a[16:19], a[0:3], v[224:227]\n\t"
    "ds_read_b128 a[84:87], v250 offset:6144\n\t"
    "s_add_u32 s46, s46, 26214400\n\t"
    "v_mfma_f32_16x16x32_bf16 v[160:163], a[48:51], a[32:35], v[160:163]\n\t"
    "s_add_u32 s47, s46, 4096\n\t"
    "v_exp_f32 v128, v128\n\t"
    "v_mfma_f32_16x16x32_bf16 v[160:163], a[80:83], a[64:67], v[160:163]\n\t"
    "s_mov_b32 s60, s46\n\t"
    "v_exp_f32 v129, v129\n\t"
    "v_mfma_f32_16x16x32_bf16 v[160:163], a[112:115], a[96:99], v[160:163]\n\t"
    "s_mul_i32 s77, s78, 528640\n\t"
    "s_lshl_b32 s45, s49, 17\n\t"
    "s_add_u32 s45, s45, s77\n\t"
    "v_mfma_f32_16x16x32_bf16 v[164:167], a[112:115], a[100:103], v[228:231]\n\t"
    "ds_read_b128 a[116:119], v250 offset:7168\n\t"
    "v_cvt_pk_bf16_f32 v192, v128, v129\n\t"
    "v_mfma_f32_16x16x32_bf16 v[164:167], a[80:83], a[68:71], v[164:167]\n\t"
    "s_add_u32 s45, s45, 8388608\n\t"
    "v_exp_f32 v130, v130\n\t"
    "v_mfma_f32_16x16x32_bf16 v[164:167], a[48:51], a[36:39], v[164:167]\n\t"
    "s_lshl_b32 s77, s78, 2\n\t"
    "v_exp_f32 v131, v131\n\t"
    "v_mfma_f32_16x16x32_bf16 v[164:167], a[16:19], a[4:7], v[164:167]\n\t"
    "s_add_u32 s77, s77, s49\n\t"
    "s_lshl_b32 s77, s77, 15\n\t"
    "s_lshl_b32 s53, s51, 8\n\t"
    "v_mfma_f32_16x16x32_bf16 v[168:171], a[16:19], a[8:11], v[232:235]\n\t"
    "s_add_u32 s53, s53, s77\n\t"
    "v_cvt_pk_bf16_f32 v193, v130, v131\n\t"
    "s_lshl_b32 s48, s3, 12\n\t"
    "v_mfma_f32_16x16x32_bf16 v[168:171], a[48:51], a[40:43], v[168:171]\n\t"
    "s_add_u32 m0, s85, 114688\n\t"
    "v_exp_f32 v132, v132\n\t"
    "v_mfma_f32_16x16x32_bf16 v[168:171], a[80:83], a[72:75], v[168:171]\n\t"
    "s_lshl_b32 s77, s52, 2\n\t"
    "v_exp_f32 v133, v133\n\t"
    "v_mfma_f32_16x16x32_bf16 v[168:171], a[112:115], a[104:107], v[168:171]\n\t"
    "s_add_u32 s77, s77, s3\n\t"
    "buffer_load_dwordx4 v248, s[80:83], s84 offen sc1 lds\n\t"
    "v_mfma_f32_16x16x32_bf16 v[172:175], a[112:115], a[108:111], v[236:239]\n\t"
    "ds_read_b128 a[24:27], v248 offset:40960\n\t"
    "v_cvt_pk_bf16_f32 v196, v132, v133\n\t"
    "v_mfma_f32_16x16x32_bf16 v[172:175], a[80:83], a[76:79], v[172:175]\n\t"
    "s_lshl_b32 s54, s77, 8\n\t"
    "v_exp_f32 v134, v134\n\t"
    "v_mfma_f32_16x16x32_bf16 v[172:175], a[48:51], a[44:47], v[172:175]\n\t"
    "s_add_u32 s54, s54, 50724864\n\t"
    "v_exp_f32 v135, v135\n\t"
    "v_mfma_f32_16x16x32_bf16 v[172:175], a[16:19], a[12:15], v[172:175]\n\t"
    "s_waitcnt lgkmcnt(1)\n\t"
    "s_xor_b32 s77, s49, 0\n\t"
    "s_lshl_b32 s78, s52, 2\n\t"
    "v_mfma_f32_16x16x32_bf16 v[188:191], a[20:23], a[12:15], v[236:239]\n\t"
    "ds_read_b128 a[56:59], v248 offset:41984\n\t"
    "v_cvt_pk_bf16_f32 v197, v134, v135\n\t"
    "v_mfma_f32_16x16x32_bf16 v[188:191], a[52:55], a[44:47], v[188:191]\n\t"
    "s_add_u32 s77, s77, s78\n\t"
    "v_exp_f32 v136, v136\n\t"
    "v_mfma_f32_16x16x32_bf16 v[188:191], a[84:87], a[76:79], v[188:191]\n\t"
    "s_lshl_b32 s77, s77, 2\n\t"
    "v_exp_f32 v137, v137\n\t"
    "v_mfma_f32_16x16x32_bf16 v[188:191], a[116:119], a[108:111], v[188:191]\n\t"
    "s_add_u32 s77, s77, s3\n\t"
    "buffer_load_dwordx4 v248, s[80:83], s84 offen offset:1024 sc1 lds\n\t"
    "v_mfma_f32_16x16x32_bf16 v[184:187], a[116:119], a[104:107], v[232:235]\n\t"
    "ds_read_b128 a[88:91], v248 offset:43008\n\t"
    "v_cvt_pk_bf16_f32 v200, v136, v137\n\t"
    "v_mfma_f32_16x16x32_bf16 v[184:187], a[84:87], a[72:75], v[184:187]\n\t"
    "s_mul_i32 s55, s77, 16640\n\t"
    "v_exp_f32 v138, v138\n\t"
    "v_mfma_f32_16x16x32_bf16 v[184:187], a[52:55], a[40:43], v[184:187]\n\t"
    "s_add_u32 s55, s55, 75497472\n\t"
    "v_exp_f32 v139, v139\n\t"
    "v_mfma_f32_16x16x32_bf16 v[184:187], a[20:23], a[8:11], v[184:187]\n\t"
    "s_mul_i32 s62, s77, 16640\n\t"
    "s_add_u32 s62, s62, 109051904\n\t"
    "s_xor_b32 s77, s49, 1\n\t"
    "v_mfma_f32_16x16x32_bf16 v[180:183], a[20:23], a[4:7], v[228:231]\n\t"
    "ds_read_b128 a[120:123], v248 offset:44032\n\t"
    "v_cvt_pk_bf16_f32 v201, v138, v139\n\t"
    "v_mfma_f32_16x16x32_bf16 v[180:183], a[52:55], a[36:39], v[180:183]\n\t"
    "s_lshl_b32 s78, s52, 2\n\t"
    "v_exp_f32 v140, v140\n\t"
    "v_mfma_f32_16x16x32_bf16 v[180:183], a[84:87], a[68:71], v[180:183]\n\t"
    "s_add_u32 s77, s77, s78\n\t"
    "v_exp_f32 v141, v141\n\t"
    "v_mfma_f32_16x16x32_bf16 v[180:183], a[116:119], a[100:103], v[180:183]\n\t"
    "s_lshl_b32 s77, s77, 2\n\t"
    "buffer_load_dwordx4 v248, s[80:83], s84 offen offset:2048 sc1 lds\n\t"
    "v_mfma_f32_16x16x32_bf16 v[176:179], a[116:119], a[96:99], v[224:227]\n\t"
    "ds_read_b128 a[28:31], v248 offset:45056\n\t"
    "v_cvt_pk_bf16_f32 v204, v140, v141\n\t"
    "v_mfma_f32_16x16x32_bf16 v[176:179], a[84:87], a[64:67], v[176:179]\n\t"
    "s_add_u32 s77, s77, s3\n\t"
    "v_exp_f32 v142, v142\n\t"
    "v_mfma_f32_16x16x32_bf16 v[176:179], a[52:55], a[32:35], v[176:179]\n\t"
    "s_mul_i32 s56, s77, 16640\n\t"
    "v_exp_f32 v143, v143\n\t"
    "v_mfma_f32_16x16x32_bf16 v[176:179], a[20:23], a[0:3], v[176:179]\n\t"
    "s_waitcnt lgkmcnt(3)\n\t"
    "s_add_u32 s56, s56, 75497472\n\t"
    "s_mul_i32 s63, s77, 16640\n\t"
    "v_mfma_f32_16x16x32_bf16 v[0:3], a[24:27], v[208:211], v[0:3]\n\t"
    "ds_read_b128 a[60:63], v248 offset:46080\n\t"
    "v_cvt_pk_bf16_f32 v205, v142, v143\n\t"
    "v_mfma_f32_16x16x32_bf16 v[4:7], a[24:27], v[212:215], v[4:7]\n\t"
    "s_add_u32 s63, s63, 109051904\n\t"
    "v_exp_f32 v156, v156\n\t"
    "v_mfma_f32_16x16x32_bf16 v[8:11], a[24:27], v[216:219], v[8:11]\n\t"
    "s_xor_b32 s77, s49, 2\n\t"
    "v_exp_f32 v157, v157\n\t"
    "v_mfma_f32_16x16x32_bf16 v[12:15], a[24:27], v[220:223], v[12:15]\n\t"
    "s_lshl_b32 s78, s52, 2\n\t"
    "buffer_load_dwordx4 v248, s[80:83], s84 offen offset:3072 sc1 lds\n\t"
    "v_mfma_f32_16x16x32_bf16 v[28:31], a[56:59], v[220:223], v[28:31]\n\t"
    "ds_read_b128 a[92:95], v248 offset:47104\n\t"
    "v_cvt_pk_bf16_f32 v206, v156, v157\n\t"
    "v_mfma_f32_16x16x32_bf16 v[24:27], a[56:59], v[216:219], v[24:27]\n\t"
    "s_add_u32 s77, s77, s78\n\t"
    "v_exp_f32 v158, v158\n\t"
    "v_mfma_f32_16x16x32_bf16 v[20:23], a[56:59], v[212:215], v[20:23]\n\t"
    "s_lshl_b32 s77, s77, 2\n\t"
    "v_exp_f32 v159, v159\n\t"
    "v_mfma_f32_16x16x32_bf16 v[16:19], a[56:59], v[208:211], v[16:19]\n\t"
    "s_waitcnt lgkmcnt(3)\n\t"
    "s_add_u32 s77, s77, s3\n\t"
    "s_mul_i32 s57, s77, 16640\n\t"
    "v_mfma_f32_16x16x32_bf16 v[32:35], a[88:91], v[208:211], v[32:35]\n\t"
    "ds_read_b128 a[124:127], v248 offset:48128\n\t"
    "v_cvt_pk_bf16_f32 v207, v158, v159\n\t"
    "v_mfma_f32_16x16x32_bf16 v[36:39], a[88:91], v[212:215], v[36:39]\n\t"
    "s_add_u32 s57, s57, 75501568\n\t"
    "v_exp_f32 v152, v152\n\t"
    "v_mfma_f32_16x16x32_bf16 v[40:43], a[88:91], v[216:219], v[40:43]\n\t"
    "s_mul_i32 s64, s77, 16640\n\t"
    "v_exp_f32 v153, v153\n\t"
    "v_mfma_f32_16x16x32_bf16 v[44:47], a[88:91], v[220:223], v[44:47]\n\t"
    "s_add_u32 s84, s84, 16384\n\t"
    "s_add_u32 s64, s64, 109056000\n\t"
    "s_xor_b32 s77, s49, 3\n\t"
    "v_mfma_f32_16x16x32_bf16 v[60:63], a[120:123], v[220:223], v[60:63]\n\t"
    "ds_read_b128 a[16:19], v250 offset:16384\n\t"
    "v_cvt_pk_bf16_f32 v202, v152, v153\n\t"
    "v_mfma_f32_16x16x32_bf16 v[56:59], a[120:123], v[216:219], v[56:59]\n\t"
    "s_lshl_b32 s78, s52, 2\n\t"
    "v_exp_f32 v154, v154\n\t"
    "v_mfma_f32_16x16x32_bf16 v[52:55], a[120:123], v[212:215], v[52:55]\n\t"
    "s_add_u32 s77, s77, s78\n\t"
    "v_exp_f32 v155, v155\n\t"
    "v_mfma_f32_16x16x32_bf16 v[48:51], a[120:123], v[208:211], v[48:51]\n\t"
    "s_waitcnt lgkmcnt(3)\n\t"
    "s_lshl_b32 s77, s77, 2\n\t"
    "s_add_u32 s77, s77, s3\n\t"
    "v_mfma_f32_16x16x32_bf16 v[64:67], a[28:31], v[208:211], v[64:67]\n\t"
    "ds_read_b128 a[48:51], v250 offset:17408\n\t"
    "v_cvt_pk_bf16_f32 v203, v154, v155\n\t"
    "v_mfma_f32_16x16x32_bf16 v[68:71], a[28:31], v[212:215], v[68:71]\n\t"
    "s_mul_i32 s58, s77, 16640\n\t"
    "v_exp_f32 v148, v148\n\t"
    "v_mfma_f32_16x16x32_bf16 v[72:75], a[28:31], v[216:219], v[72:75]\n\t"
    "s_add_u32 s58, s58, 75505664\n\t"
    "v_exp_f32 v149, v149\n\t"
    "v_mfma_f32_16x16x32_bf16 v[76:79], a[28:31], v[220:223], v[76:79]\n\t"
    "s_mul_i32 s65, s77, 16640\n\t"
    "s_add_u32 s65, s65, 109060096\n\t"
    "s_mov_b32 s38, 4194304\n\t"
    "v_mfma_f32_16x16x32_bf16 v[92:95], a[60:63], v[220:223], v[92:95]\n\t"
    "ds_read_b128 a[80:83], v250 offset:18432\n\t"
    "v_cvt_pk_bf16_f32 v198, v148, v149\n\t"
    "v_mfma_f32_16x16x32_bf16 v[88:91], a[60:63], v[216:219], v[88:91]\n\t"
    "s_mov_b32 s39, 131072\n\t"
    "v_exp_f32 v150, v150\n\t"
    "v_mfma_f32_16x16x32_bf16 v[84:87], a[60:63], v[212:215], v[84:87]\n\t"
    "s_mov_b32 s59, 0xffff0000\n\t"
    "v_exp_f32 v151, v151\n\t"
    "v_mfma_f32_16x16x32_bf16 v[80:83], a[60:63], v[208:211], v[80:83]\n\t"
    "s_waitcnt lgkmcnt(3)\n\t"
    "s_mov_b64 s[36:37], s[8:9]\n\t"
    "s_getpc_b64 s[28:29]\n\t"
    ".Lfb_ct2:\n\t"
    "v_mfma_f32_16x16x32_bf16 v[96:99], a[92:95], v[208:211], v[96:99]\n\t"
    "ds_read_b128 a[112:115], v250 offset:19456\n\t"
    "v_cvt_pk_bf16_f32 v199, v150, v151\n\t"
    "v_mfma_f32_16x16x32_bf16 v[100:103], a[92:95], v[212:215], v[100:103]\n\t"
    "s_lshr_b32 s77, s2, 3\n\t"
    "v_exp_f32 v144, v144\n\t"
    "v_mfma_f32_16x16x32_bf16 v[104:107], a[92:95], v[216:219], v[104:107]\n\t"
    "s_lshl_b32 s77, s77, 7\n\t"
    "v_exp_f32 v145, v145\n\t"
    "v_mfma_f32_16x16x32_bf16 v[108:111], a[92:95], v[220:223], v[108:111]\n\t"
    "s_lshl_b32 s78, s3, 12\n\t"
    "s_add_u32 s77, s77, s78\n\t"
    "s_add_u32 s77, s77, .Lfb_mid0-.Lfb_ct2+32768\n\t"
    "v_mfma_f32_16x16x32_bf16 v[124:127], a[124:127], v[220:223], v[124:127]\n\t"
    "ds_read_b128 a[20:23], v250 offset:20480\n\t"
    "v_cvt_pk_bf16_f32 v194, v144, v145\n\t"
    "v_mfma_f32_16x16x32_bf16 v[120:123], a[124:127], v[216:219], v[120:123]\n\t"
    "s_min_u32 s77, s77, .Lfb_end-.Lfb_ct2\n\t"
    "v_exp_f32 v146, v146\n\t"
    "v_mfma_f32_16x16x32_bf16 v[116:119], a[124:127], v[212:215], v[116:119]\n\t"
    "v_exp_f32 v147, v147\n\t"
    "v_mfma_f32_16x16x32_bf16 v[112:115], a[124:127], v[208:211], v[112:115]\n\t"
    "v_mov_b32 v253, s77\n\t"
    "v_mfma_f32_16x16x32_bf16 a[128:131], a[240:243], v[208:211], a[128:131]\n\t"
    "ds_read_b128 a[52:55], v250 offset:21504\n\t"
    "v_cvt_pk_bf16_f32 v195, v146, v147\n\t"
    "v_mfma_f32_16x16x32_bf16 a[132:135], a[240:243], v[212:215], a[132:135]\n\t"
    "v_exp_f32 v160, v160\n\t"
    "v_mfma_f32_16x16x32_bf16 a[136:139], a[240:243], v[216:219], a[136:139]\n\t"
    "v_exp_f32 v161, v161\n\t"
    "v_mfma_f32_16x16x32_bf16 a[140:143], a[240:243], v[220:223], a[140:143]\n\t"
    "s_waitcnt lgkmcnt(2)\n\t"
    "global_load_dword v253, v253, s[28:29]\n\t"
    "v_mfma_f32_16x16x32_bf16 v[128:131], a[16:19], a[0:3], v[224:227]\n\t"
    "ds_read_b128 a[84:87], v250 offset:22528\n\t"
    "v_cvt_pk_bf16_f32 v208, v160, v161\n\t"
    "v_mfma_f32_16x16x32_bf16 v[128:131], a[48:51], a[32:35], v[128:131]\n\t"
    "v_exp_f32 v162, v162\n\t"
    "v_mfma_f32_16x16x32_bf16 v[128:131], a[80:83], a[64:67], v[128:131]\n\t"
    "v_exp_f32 v163, v163\n\t"
    "v_mfma_f32_16x16x32_bf16 v[128:131], a[112:115], a[96:99], v[128:131]\n\t"
    "s_waitcnt vmcnt(5)\n\t"
    "s_barrier\n\t"
    "v_mfma_f32_16x16x32_bf16 v[132:135], a[112:115], a[100:103], v[228:231]\n\t"
    "ds_read_b128 a[116:119], v250 offset:23552\n\t"
    "v_cvt_pk_bf16_f32 v209, v162, v163\n\t"
    "v_mfma_f32_16x16x32_bf16 v[132:135], a[80:83], a[68:71], v[132:135]\n\t"
    "v_exp_f32 v164, v164\n\t"
    "v_mfma_f32_16x16x32_bf16 v[132:135], a[48:51], a[36:39], v[132:135]\n\t"
    "v_exp_f32 v165, v165\n\t"
    "v_mfma_f32_16x16x32_bf16 v[132:135], a[16:19], a[4:7], v[132:135]\n\t"
    "s_cmp_eq_u32 s3, 0\n\t"
    "s_cbranch_scc1 .Lfb_fh\n\t"
    ".Lfb_fh_r:\n\t"
    "s_mov_b64 s[32:33], s[40:41]\n\t"
    "v_mfma_f32_16x16x32_bf16 v[136:139], a[16:19], a[8:11], v[232:235]\n\t"
    "s_mov_b32 s34, 1835008\n\t"
    "v_cvt_pk_bf16_f32 v212, v164, v165\n\t"
    "s_mov_b32 s35, 131072\n\t"
    "v_mfma_f32_16x16x32_bf16 v[136:139], a[48:51], a[40:43], v[136:139]\n\t"
    "s_add_u32 m0, s85, 0\n\t"
    "v_exp_f32 v166, v166\n\t"
    "v_mfma_f32_16x16x32_bf16 v[136:139], a[80:83], a[72:75], v[136:139]\n\t"
    "s_mov_b32 s79, 131072\n\t"
    "v_exp_f32 v167, v167\n\t"
    "v_mfma_f32_16x16x32_bf16 v[136:139], a[112:115], a[104:107], v[136:139]\n\t"
    "s_lshl_b32 s77, s3, 15\n\t"
    "buffer_load_dwordx4 v248, s[80:83], s84 offen sc1 lds\n\t"
    "v_mfma_f32_16x16x32_bf16 v[140:143], a[112:115], a[108:111], v[236:239]\n\t"
    "ds_read_b128 a[24:27], v248 offset:57344\n\t"
    "v_cvt_pk_bf16_f32 v213, v166, v167\n\t"
    "v_mfma_f32_16x16x32_bf16 v[140:143], a[80:83], a[76:79], v[140:143]\n\t"
    "s_lshl_b32 s78, s51, 8\n\t"
    "v_exp_f32 v168, v168\n\t"
    "v_mfma_f32_16x16x32_bf16 v[140:143], a[48:51], a[44:47], v[140:143]\n\t"
    "s_add_u32 s77, s77, s78\n\t"
    "v_exp_f32 v169, v169\n\t"
    "v_mfma_f32_16x16x32_bf16 v[140:143], a[16:19], a[12:15], v[140:143]\n\t"
    "s_waitcnt lgkmcnt(1)\n\t"
    "v_lshrrev_b32 v254, 8, v248\n\t"
    "v_bfe_u32 v255, v248, 5, 3\n\t"
    "v_mfma_f32_16x16x32_bf16 v[156:159], a[20:23], a[12:15], v[236:239]\n\t"
    "ds_read_b128 a[56:59], v248 offset:58368\n\t"
    "v_cvt_pk_bf16_f32 v216, v168, v169\n\t"
    "v_mfma_f32_16x16x32_bf16 v[156:159], a[52:55], a[44:47], v[156:159]\n\t"
    "v_exp_f32 v170, v170\n\t"
    "v_mfma_f32_16x16x32_bf16 v[156:159], a[84:87], a[76:79], v[156:159]\n\t"
    "v_exp_f32 v171, v171\n\t"
    "v_mfma_f32_16x16x32_bf16 v[156:159], a[116:119], a[108:111], v[156:159]\n\t"
    "buffer_load_dwordx4 v248, s[80:83], s84 offen offset:1024 sc1 lds\n\t"
    "v_mfma_f32_16x16x32_bf16 v[152:155], a[116:119], a[104:107], v[232:235]\n\t"
    "ds_read_b128 a[88:91], v248 offset:59392\n\t"
    "v_cvt_pk_bf16_f32 v217, v170, v171\n\t"
    "v_mfma_f32_16x16x32_bf16 v[152:155], a[84:87], a[72:75], v[152:155]\n\t"
    "v_exp_f32 v172, v172\n\t"
    "v_mfma_f32_16x16x32_bf16 v[152:155], a[52:55], a[40:43], v[152:155]\n\t"
    "v_exp_f32 v173, v173\n\t"
    "v_mfma_f32_16x16x32_bf16 v[152:155], a[20:23], a[8:11], v[152:155]\n\t"
    "v_xor_b32 v255, v254, v255\n\t"
    "v_and_b32 v242, 16, v248\n\t"
    "v_mfma_f32_16x16x32_bf16 v[148:151], a[20:23], a[4:7], v[228:231]\n\t"
    "ds_read_b128 a[120:123], v248 offset:60416\n\t"
    "v_cvt_pk_bf16_f32 v220, v172, v173\n\t"
    "v_mfma_f32_16x16x32_bf16 v[148:151], a[52:55], a[36:39], v[148:151]\n\t"
    "v_exp_f32 v174, v174\n\t"
    "v_mfma_f32_16x16x32_bf16 v[148:151], a[84:87], a[68:71], v[148:151]\n\t"
    "v_exp_f32 v175, v175\n\t"
    "v_mfma_f32_16x16x32_bf16 v[148:151], a[116:119], a[100:103], v[148:151]\n\t"
    "buffer_load_dwordx4 v248, s[80:83], s84 offen offset:2048 sc1 lds\n\t"
    "v_mfma_f32_16x16x32_bf16 v[144:147], a[116:119], a[96:99], v[224:227]\n\t"
    "ds_read_b128 a[28:31], v248 offset:61440\n\t"
    "v_cvt_pk_bf16_f32 v221, v174, v175\n\t"
    "v_mfma_f32_16x16x32_bf16 v[144:147], a[84:87], a[64:67], v[144:147]\n\t"
    "v_exp_f32 v188, v188\n\t"
    "v_mfma_f32_16x16x32_bf16 v[144:147], a[52:55], a[32:35], v[144:147]\n\t"
    "v_exp_f32 v189, v189\n\t"
    "v_mfma_f32_16x16x32_bf16 v[144:147], a[20:23], a[0:3], v[144:147]\n\t"
    "s_waitcnt lgkmcnt(3)\n\t"
    "v_lshl_add_u32 v242, v255, 5, v242\n\t"
    "v_lshl_add_u32 v242, v254, 11, v242\n\t"
    "v_mfma_f32_16x16x32_bf16 v[0:3], a[24:27], v[192:195], v[0:3]\n\t"
    "ds_read_b128 a[60:63], v248 offset:62464\n\t"
    "v_cvt_pk_bf16_f32 v222, v188, v189\n\t"
    "v_mfma_f32_16x16x32_bf16 v[4:7], a[24:27], v[196:199], v[4:7]\n\t"
    "v_exp_f32 v190, v190\n\t"
    "v_mfma_f32_16x16x32_bf16 v[8:11], a[24:27], v[200:203], v[8:11]\n\t"
    "v_exp_f32 v191, v191\n\t"
    "v_mfma_f32_16x16x32_bf16 v[12:15], a[24:27], v[204:207], v[12:15]\n\t"
    "buffer_load_dwordx4 v248, s[80:83], s84 offen offset:3072 sc1 lds\n\t"
    "v_mfma_f32_16x16x32_bf16 v[28:31], a[56:59], v[204:207], v[28:31]\n\t"
    "ds_read_b128 a[92:95], v248 offset:63488\n\t"
    "v_cvt_pk_bf16_f32 v223, v190, v191\n\t"
    "v_mfma_f32_16x16x32_bf16 v[24:27], a[56:59], v[200:203], v[24:27]\n\t"
    "v_exp_f32 v184, v184\n\t"
    "v_mfma_f32_16x16x32_bf16 v[20:23], a[56:59], v[196:199], v[20:23]\n\t"
    "v_exp_f32 v185, v185\n\t"
    "v_mfma_f32_16x16x32_bf16 v[16:19], a[56:59], v[192:195], v[16:19]\n\t"
    "s_waitcnt lgkmcnt(3)\n\t"
    "v_add_u32 v242, s77, v242\n\t"
    "v_xor_b32 v243, 128, v242\n\t"
    "v_mfma_f32_16x16x32_bf16 v[32:35], a[88:91], v[192:195], v[32:35]\n\t"
    "ds_read_b128 a[124:127], v248 offset:64512\n\t"
    "v_cvt_pk_bf16_f32 v218, v184, v185\n\t"
    "v_mfma_f32_16x16x32_bf16 v[36:39], a[88:91], v[196:199], v[36:39]\n\t"
    "v_exp_f32 v186, v186\n\t"
    "v_mfma_f32_16x16x32_bf16 v[40:43], a[88:91], v[200:203], v[40:43]\n\t"
    "v_exp_f32 v187, v187\n\t"
    "v_mfma_f32_16x16x32_bf16 v[44:47], a[88:91], v[204:207], v[44:47]\n\t"
    "s_add_u32 s84, s84, 16384\n\t"
    "v_add_u32 v243, 7168, v243\n\t"
    "v_add_u32 v244, 14336, v242\n\t"
    "v_mfma_f32_16x16x32_bf16 v[60:63], a[120:123], v[204:207], v[60:63]\n\t"
    "ds_read_b128 a[16:19], v250 offset:32768\n\t"
    "v_cvt_pk_bf16_f32 v219, v186, v187\n\t"
    "v_mfma_f32_16x16x32_bf16 v[56:59], a[120:123], v[200:203], v[56:59]\n\t"
    "v_exp_f32 v180, v180\n\t"
    "v_mfma_f32_16x16x32_bf16 v[52:55], a[120:123], v[196:199], v[52:55]\n\t"
    "v_exp_f32 v181, v181\n\t"
    "v_mfma_f32_16x16x32_bf16 v[48:51], a[120:123], v[192:195], v[48:51]\n\t"
    "s_waitcnt lgkmcnt(3)\n\t"
    "v_add_u32 v245, 14336, v243\n\t"
    "s_xor_b32 s77, s49, 0\n\t"
    "v_mfma_f32_16x16x32_bf16 v[64:67], a[28:31], v[192:195], v[64:67]\n\t"
    "ds_read_b128 a[48:51], v250 offset:33792\n\t"
    "v_cvt_pk_bf16_f32 v214, v180, v181\n\t"
    "v_mfma_f32_16x16x32_bf16 v[68:71], a[28:31], v[196:199], v[68:71]\n\t"
    "s_lshl_b32 s77, s77, 10\n\t"
    "v_exp_f32 v182, v182\n\t"
    "v_mfma_f32_16x16x32_bf16 v[72:75], a[28:31], v[200:203], v[72:75]\n\t"
    "v_exp_f32 v183, v183\n\t"
    "v_mfma_f32_16x16x32_bf16 v[76:79], a[28:31], v[204:207], v[76:79]\n\t"
    "v_add_u32 v241, s77, v248\n\t"
    "s_xor_b32 s77, s49, 1\n\t"
    "v_mfma_f32_16x16x32_bf16 v[92:95], a[60:63], v[204:207], v[92:95]\n\t"
    "ds_read_b128 a[80:83], v250 offset:34816\n\t"
    "v_cvt_pk_bf16_f32 v215, v182, v183\n\t"
    "v_mfma_f32_16x16x32_bf16 v[88:91], a[60:63], v[200:203], v[88:91]\n\t"
    "s_lshl_b32 s77, s77, 10\n\t"
    "v_exp_f32 v176, v176\n\t"
    "v_mfma_f32_16x16x32_bf16 v[84:87], a[60:63], v[196:199], v[84:87]\n\t"
    "v_exp_f32 v177, v177\n\t"
    "v_mfma_f32_16x16x32_bf16 v[80:83], a[60:63], v[192:195], v[80:83]\n\t"
    "s_waitcnt lgkmcnt(3)\n\t"
    "v_add_u32 v246, s77, v248\n\t"
    "s_xor_b32 s77, s49, 2\n\t"
    "v_mfma_f32_16x16x32_bf16 v[96:99], a[92:95], v[192:195], v[96:99]\n\t"
    "ds_read_b128 a[112:115], v250 offset:35840\n\t"
    "v_cvt_pk_bf16_f32 v210, v176, v177\n\t"
    "v_mfma_f32_16x16x32_bf16 v[100:103], a[92:95], v[196:199], v[100:103]\n\t"
    "s_lshl_b32 s77, s77, 10\n\t"
    "v_exp_f32 v178, v178\n\t"
    "v_mfma_f32_16x16x32_bf16 v[104:107], a[92:95], v[200:203], v[104:107]\n\t"
    "v_exp_f32 v179, v179\n\t"
    "v_mfma_f32_16x16x32_bf16 v[108:111], a[92:95], v[204:207], v[108:111]\n\t"
    "v_add_u32 v247, s77, v248\n\t"
    "s_xor_b32 s77, s49, 3\n\t"
    "v_mfma_f32_16x16x32_bf16 v[124:127], a[124:127], v[204:207], v[124:127]\n\t"
    "ds_read_b128 a[20:23], v250 offset:36864\n\t"
    "v_cvt_pk_bf16_f32 v211, v178, v179\n\t"
    "v_mfma_f32_16x16x32_bf16 v[120:123], a[124:127], v[200:203], v[120:123]\n\t"
    "s_lshl_b32 s77, s77, 10\n\t"
    "v_exp_f32 v128, v128\n\t"
    "v_mfma_f32_16x16x32_bf16 v[116:119], a[124:127], v[196:199], v[116:119]\n\t"
    "v_exp_f32 v129, v129\n\t"
    "v_mfma_f32_16x16x32_bf16 v[112:115], a[124:127], v[192:195], v[112:115]\n\t"
    "v_add_u32 v249, s77, v248\n\t"
    "s_and_b32 s77, s52, 15\n\t"
    "v_mfma_f32_16x16x32_bf16 a[128:131], a[240:243], v[192:195], a[128:131]\n\t"
    "ds_read_b128 a[52:55], v250 offset:37888\n\t"
    "s_lshl_b32 s77, s77, 11\n\t"
    "v_mfma_f32_16x16x32_bf16 a[132:135], a[240:243], v[196:199], a[132:135]\n\t"
    "s_lshl_b32 s78, s50, 7\n\t"
    "v_exp_f32 v130, v130\n\t"
    "v_mfma_f32_16x16x32_bf16 a[136:139], a[240:243], v[200:203], a[136:139]\n\t"
    "s_add_u32 s77, s77, s78\n\t"
    "v_exp_f32 v131, v131\n\t"
    "v_mfma_f32_16x16x32_bf16 a[140:143], a[240:243], v[204:207], a[140:143]\n\t"
    "s_waitcnt lgkmcnt(2)\n\t"
    "s_lshl_b32 s78, s49, 5\n\t"
    "s_add_u32 s77, s77, s78\n\t"
    "v_mfma_f32_16x16x32_bf16 v[160:163], a[16:19], a[0:3], v[224:227]\n\t"
    "ds_read_b128 a[84:87], v250 offset:38912\n\t"
    "v_cvt_pk_bf16_f32 v192, v128, v129\n\t"
    "v_mfma_f32_16x16x32_bf16 v[160:163], a[48:51], a[32:35], v[160:163]\n\t"
    "s_add_u32 s77, s77, 50593792\n\t"
    "v_exp_f32 v132, v132\n\t"
    "v_mfma_f32_16x16x32_bf16 v[160:163], a[80:83], a[64:67], v[160:163]\n\t"
    "v_exp_f32 v133, v133\n\t"
    "v_mfma_f32_16x16x32_bf16 v[160:163], a[112:115], a[96:99], v[160:163]\n\t"
    "v_cvt_pk_bf16_f32 v193, v130, v131\n\t"
    "v_bfe_u32 v254, v248, 4, 3\n\t"
    "v_mfma_f32_16x16x32_bf16 v[164:167], a[112:115], a[100:103], v[228:231]\n\t"
    "ds_read_b128 a[116:119], v250 offset:39936\n\t"
    "v_cvt_pk_bf16_f32 v196, v132, v133\n\t"
    "v_mfma_f32_16x16x32_bf16 v[164:167], a[80:83], a[68:71], v[164:167]\n\t"
    "v_exp_f32 v134, v134\n\t"
    "v_mfma_f32_16x16x32_bf16 v[164:167], a[48:51], a[36:39], v[164:167]\n\t"
    "v_exp_f32 v135, v135\n\t"
    "v_mfma_f32_16x16x32_bf16 v[164:167], a[16:19], a[4:7], v[164:167]\n\t"
    "v_lshl_add_u32 v252, v254, 2, s77\n\t"
    "s_mov_b64 s[68:69], s[4:5]\n\t"
    "v_mfma_f32_16x16x32_bf16 v[168:171], a[16:19], a[8:11], v[232:235]\n\t"
    "s_mov_b32 s70, 4194304\n\t"
    "v_cvt_pk_bf16_f32 v197, v134, v135\n\t"
    "s_mov_b32 s71, 131072\n\t"
    "v_mfma_f32_16x16x32_bf16 v[168:171], a[48:51], a[40:43], v[168:171]\n\t"
    "s_add_u32 m0, s85, 16384\n\t"
    "v_exp_f32 v136, v136\n\t"
    "v_mfma_f32_16x16x32_bf16 v[168:171], a[80:83], a[72:75], v[168:171]\n\t"
    "v_exp_f32 v137, v137\n\t"
    "v_mfma_f32_16x16x32_bf16 v[168:171], a[112:115], a[104:107], v[168:171]\n\t"
    "buffer_load_dwordx4 v248, s[80:83], s84 offen sc1 lds\n\t"
    "v_mfma_f32_16x16x32_bf16 v[172:175], a[112:115], a[108:111], v[236:239]\n\t"
    "ds_read_b128 a[24:27], v250 offset:8192\n\t"
    "v_cvt_pk_bf16_f32 v200, v136, v137\n\t"
    "v_mfma_f32_16x16x32_bf16 v[172:175], a[80:83], a[76:79], v[172:175]\n\t"
    "v_exp_f32 v138, v138\n\t"
    "v_mfma_f32_16x16x32_bf16 v[172:175], a[48:51], a[44:47], v[172:175]\n\t"
    "v_exp_f32 v139, v139\n\t"
    "v_mfma_f32_16x16x32_bf16 v[172:175], a[16:19], a[12:15], v[172:175]\n\t"
    "s_waitcnt lgkmcnt(1)\n\t"
    "v_lshrrev_b32 v254, 8, v248\n\t"
    "v_and_b32 v255, 240, v248\n\t"
    "v_mfma_f32_16x16x32_bf16 v[188:191], a[20:23], a[12:15], v[236:239]\n\t"
    "ds_read_b128 a[56:59], v250 offset:9216\n\t"
    "v_cvt_pk_bf16_f32 v201, v138, v139\n\t"
    "v_mfma_f32_16x16x32_bf16 v[188:191], a[52:55], a[44:47], v[188:191]\n\t"
    "v_exp_f32 v140, v140\n\t"
    "v_mfma_f32_16x16x32_bf16 v[188:191], a[84:87], a[76:79], v[188:191]\n\t"
    "v_exp_f32 v141, v141\n\t"
    "v_mfma_f32_16x16x32_bf16 v[188:191], a[116:119], a[108:111], v[188:191]\n\t"
    "buffer_load_dwordx4 v248, s[80:83], s84 offen offset:1024 sc1 lds\n\t"
    "v_mfma_f32_16x16x32_bf16 v[184:187], a[116:119], a[104:107], v[232:235]\n\t"
    "ds_read_b128 a[88:91], v250 offset:10240\n\t"
    "v_cvt_pk_bf16_f32 v204, v140, v141\n\t"
    "v_mfma_f32_16x16x32_bf16 v[184:187], a[84:87], a[72:75], v[184:187]\n\t"
    "v_exp_f32 v142, v142\n\t"
    "v_mfma_f32_16x16x32_bf16 v[184:187], a[52:55], a[40:43], v[184:187]\n\t"
    "v_exp_f32 v143, v143\n\t"
    "v_mfma_f32_16x16x32_bf16 v[184:187], a[20:23], a[8:11], v[184:187]\n\t"
    "v_lshl_add_u32 v255, v254, 11, v255\n\t"
    "v_lshlrev_b32 v254, 4, v254\n\t"
    "v_mfma_f32_16x16x32_bf16 v[180:183], a[20:23], a[4:7], v[228:231]\n\t"
    "ds_read_b128 a[120:123], v250 offset:11264\n\t"
    "v_cvt_pk_bf16_f32 v205, v142, v143\n\t"
    "v_mfma_f32_16x16x32_bf16 v[180:183], a[52:55], a[36:39], v[180:183]\n\t"
    "v_exp_f32 v156, v156\n\t"
    "v_mfma_f32_16x16x32_bf16 v[180:183], a[84:87], a[68:71], v[180:183]\n\t"
    "v_exp_f32 v157, v157\n\t"
    "v_mfma_f32_16x16x32_bf16 v[180:183], a[116:119], a[100:103], v[180:183]\n\t"
    "buffer_load_dwordx4 v248, s[80:83], s84 offen offset:2048 sc1 lds\n\t"
    "v_mfma_f32_16x16x32_bf16 v[176:179], a[116:119], a[96:99], v[224:227]\n\t"
    "ds_read_b128 a[28:31], v250 offset:12288\n\t"
    "v_cvt_pk_bf16_f32 v206, v156, v157\n\t"
    "v_mfma_f32_16x16x32_bf16 v[176:179], a[84:87], a[64:67], v[176:179]\n\t"
    "v_exp_f32 v158, v158\n\t"
    "v_mfma_f32_16x16x32_bf16 v[176:179], a[52:55], a[32:35], v[176:179]\n\t"
    "v_exp_f32 v159, v159\n\t"
    "v_mfma_f32_16x16x32_bf16 v[176:179], a[20:23], a[0:3], v[176:179]\n\t"
    "s_waitcnt lgkmcnt(3)\n\t"
    "v_xor_b32 v255, v254, v255\n\t"
    "v_mfma_f32_16x16x32_bf16 v[0:3], a[24:27], v[208:211], v[0:3]\n\t"
    "ds_read_b128 a[60:63], v250 offset:13312\n\t"
    "v_cvt_pk_bf16_f32 v207, v158, v159\n\t"
    "v_mfma_f32_16x16x32_bf16 v[4:7], a[24:27], v[212:215], v[4:7]\n\t"
    "v_exp_f32 v152, v152\n\t"
    "v_mfma_f32_16x16x32_bf16 v[8:11], a[24:27], v[216:219], v[8:11]\n\t"
    "v_exp_f32 v153, v153\n\t"
    "v_mfma_f32_16x16x32_bf16 v[12:15], a[24:27], v[220:223], v[12:15]\n\t"
    "buffer_load_dwordx4 v248, s[80:83], s84 offen offset:3072 sc1 lds\n\t"
    "v_mfma_f32_16x16x32_bf16 v[28:31], a[56:59], v[220:223], v[28:31]\n\t"
    "ds_read_b128 a[92:95], v250 offset:14336\n\t"
    "v_cvt_pk_bf16_f32 v202, v152, v153\n\t"
    "v_mfma_f32_16x16x32_bf16 v[24:27], a[56:59], v[216:219], v[24:27]\n\t"
    "v_exp_f32 v154, v154\n\t"
    "v_mfma_f32_16x16x32_bf16 v[20:23], a[56:59], v[212:215], v[20:23]\n\t"
    "v_exp_f32 v155, v155\n\t"
    "v_mfma_f32_16x16x32_bf16 v[16:19], a[56:59], v[208:211], v[16:19]\n\t"
    "s_waitcnt lgkmcnt(3)\n\t"
    "v_mfma_f32_16x16x32_bf16 v[32:35], a[88:91], v[208:211], v[32:35]\n\t"
    "ds_read_b128 a[124:127], v250 offset:15360\n\t"
    "v_cvt_pk_bf16_f32 v203, v154, v155\n\t"
    "v_mfma_f32_16x16x32_bf16 v[36:39], a[88:91], v[212:215], v[36:39]\n\t"
    "v_exp_f32 v148, v148\n\t"
    "v_mfma_f32_16x16x32_bf16 v[40:43], a[88:91], v[216:219], v[40:43]\n\t"
    "v_exp_f32 v149, v149\n\t"
    "v_mfma_f32_16x16x32_bf16 v[44:47], a[88:91], v[220:223], v[44:47]\n\t"
    "s_mov_b32 s84, 0\n\t"
    "v_mfma_f32_16x16x32_bf16 v[60:63], a[120:123], v[220:223], v[60:63]\n\t"
    "ds_read_b128 a[16:19], v250 offset:49152\n\t"
    "v_cvt_pk_bf16_f32 v198, v148, v149\n\t"
    "v_mfma_f32_16x16x32_bf16 v[56:59], a[120:123], v[216:219], v[56:59]\n\t"
    "v_exp_f32 v150, v150\n\t"
    "v_mfma_f32_16x16x32_bf16 v[52:55], a[120:123], v[212:215], v[52:55]\n\t"
    "v_exp_f32 v151, v151\n\t"
    "v_mfma_f32_16x16x32_bf16 v[48:51], a[120:123], v[208:211], v[48:51]\n\t"
    "s_waitcnt lgkmcnt(3)\n\t"
    "v_mfma_f32_16x16x32_bf16 v[64:67], a[28:31], v[208:211], v[64:67]\n\t"
    "ds_read_b128 a[48:51], v250 offset:50176\n\t"
    "v_cvt_pk_bf16_f32 v199, v150, v151\n\t"
    "v_mfma_f32_16x16x32_bf16 v[68:71], a[28:31], v[212:215], v[68:71]\n\t"
    "v_exp_f32 v144, v144\n\t"
    "v_mfma_f32_16x16x32_bf16 v[72:75], a[28:31], v[216:219], v[72:75]\n\t"
    "v_exp_f32 v145, v145\n\t"
    "v_mfma_f32_16x16x32_bf16 v[76:79], a[28:31], v[220:223], v[76:79]\n\t"
    "v_mfma_f32_16x16x32_bf16 v[92:95], a[60:63], v[220:223], v[92:95]\n\t"
    "ds_read_b128 a[80:83], v250 offset:51200\n\t"
    "v_cvt_pk_bf16_f32 v194, v144, v145\n\t"
    "v_mfma_f32_16x16x32_bf16 v[88:91], a[60:63], v[216:219], v[88:91]\n\t"
    "v_exp_f32 v146, v146\n\t"
    "v_mfma_f32_16x16x32_bf16 v[84:87], a[60:63], v[212:215], v[84:87]\n\t"
    "v_exp_f32 v147, v147\n\t"
    "v_mfma_f32_16x16x32_bf16 v[80:83], a[60:63], v[208:211], v[80:83]\n\t"
    "s_waitcnt lgkmcnt(3)\n\t"
    "v_mfma_f32_16x16x32_bf16 v[96:99], a[92:95], v[208:211], v[96:99]\n\t"
    "ds_read_b128 a[112:115], v250 offset:52224\n\t"
    "v_cvt_pk_bf16_f32 v195, v146, v147\n\t"
    "v_mfma_f32_16x16x32_bf16 v[100:103], a[92:95], v[212:215], v[100:103]\n\t"
    "v_exp_f32 v160, v160\n\t"
    "v_mfma_f32_16x16x32_bf16 v[104:107], a[92:95], v[216:219], v[104:107]\n\t"
    "v_exp_f32 v161, v161\n\t"
    "v_mfma_f32_16x16x32_bf16 v[108:111], a[92:95], v[220:223], v[108:111]\n\t"
    "v_mfma_f32_16x16x32_bf16 v[124:127], a[124:127], v[220:223], v[124:127]\n\t"
    "ds_read_b128 a[20:23], v250 offset:53248\n\t"
    "v_mfma_f32_16x16x32_bf16 v[120:123], a[124:127], v[216:219], v[120:123]\n\t"
    "v_exp_f32 v162, v162\n\t"
    "v_mfma_f32_16x16x32_bf16 v[116:119], a[124:127], v[212:215], v[116:119]\n\t"
    "v_exp_f32 v163, v163\n\t"
    "v_mfma_f32_16x16x32_bf16 v[112:115], a[124:127], v[208:211], v[112:115]\n\t"
    "v_mfma_f32_16x16x32_bf16 a[128:131], a[240:243], v[208:211], a[128:131]\n\t"
    "ds_read_b128 a[52:55], v250 offset:54272\n\t"
    "v_mfma_f32_16x16x32_bf16 a[132:135], a[240:243], v[212:215], a[132:135]\n\t"
    "v_exp_f32 v164, v164\n\t"
    "v_mfma_f32_16x16x32_bf16 a[136:139], a[240:243], v[216:219], a[136:139]\n\t"
    "v_exp_f32 v165, v165\n\t"
    "v_mfma_f32_16x16x32_bf16 a[140:143], a[240:243], v[220:223], a[140:143]\n\t"
    "s_waitcnt lgkmcnt(2)\n\t"
    "v_mfma_f32_16x16x32_bf16 v[128:131], a[16:19], a[0:3], v[224:227]\n\t"
    "ds_read_b128 a[84:87], v250 offset:55296\n\t"
    "v_cvt_pk_bf16_f32 v208, v160, v161\n\t"
    "v_mfma_f32_16x16x32_bf16 v[128:131], a[48:51], a[32:35], v[128:131]\n\t"
    "v_exp_f32 v166, v166\n\t"
    "v_mfma_f32_16x16x32_bf16 v[128:131], a[80:83], a[64:67], v[128:131]\n\t"
    "v_exp_f32 v167, v167\n\t"
    "v_mfma_f32_16x16x32_bf16 v[128:131], a[112:115], a[96:99], v[128:131]\n\t"
    "s_waitcnt vmcnt(4)\n\t"
    "s_barrier\n\t"
    "v_cvt_pk_bf16_f32 v209, v162, v163\n\t"
    "v_mfma_f32_16x16x32_bf16 v[132:135], a[112:115], a[100:103], v[228:231]\n\t"
    "ds_read_b128 a[116:119], v250 offset:56320\n\t"
    "v_cvt_pk_bf16_f32 v212, v164, v165\n\t"
    "v_mfma_f32_16x16x32_bf16 v[132:135], a[80:83], a[68:71], v[132:135]\n\t"
    "v_exp_f32 v168, v168\n\t"
    "v_mfma_f32_16x16x32_bf16 v[132:135], a[48:51], a[36:39], v[132:135]\n\t"
    "v_exp_f32 v169, v169\n\t"
    "v_mfma_f32_16x16x32_bf16 v[132:135], a[16:19], a[4:7], v[132:135]\n\t"
    "v_cvt_pk_bf16_f32 v213, v166, v167\n\t"
    "v_mfma_f32_16x16x32_bf16 v[136:139], a[16:19], a[8:11], v[232:235]\n\t"
    "v_cvt_pk_bf16_f32 v216, v168, v169\n\t"
    "v_mfma_f32_16x16x32_bf16 v[136:139], a[48:51], a[40:43], v[136:139]\n\t"
    "s_add_u32 m0, s85, 32768\n\t"
    "v_exp_f32 v170, v170\n\t"
    "v_mfma_f32_16x16x32_bf16 v[136:139], a[80:83], a[72:75], v[136:139]\n\t"
    "v_exp_f32 v171, v171\n\t"
    "v_mfma_f32_16x16x32_bf16 v[136:139], a[112:115], a[104:107], v[136:139]\n\t"
    "buffer_load_dwordx4 v242, s[32:35], s84 offen sc1 lds\n\t"
    "v_mfma_f32_16x16x32_bf16 v[140:143], a[112:115], a[108:111], v[236:239]\n\t"
    "ds_read_b128 a[24:27], v250 offset:24576\n\t"
    "v_cvt_pk_bf16_f32 v217, v170, v171\n\t"
    "v_mfma_f32_16x16x32_bf16 v[140:143], a[80:83], a[76:79], v[140:143]\n\t"
    "v_exp_f32 v172, v172\n\t"
    "v_mfma_f32_16x16x32_bf16 v[140:143], a[48:51], a[44:47], v[140:143]\n\t"
    "v_exp_f32 v173, v173\n\t"
    "v_mfma_f32_16x16x32_bf16 v[140:143], a[16:19], a[12:15], v[140:143]\n\t"
    "s_waitcnt lgkmcnt(1)\n\t"
    "v_mfma_f32_16x16x32_bf16 v[156:159], a[20:23], a[12:15], v[236:239]\n\t"
    "ds_read_b128 a[56:59], v250 offset:25600\n\t"
    "v_cvt_pk_bf16_f32 v220, v172, v173\n\t"
    "v_mfma_f32_16x16x32_bf16 v[156:159], a[52:55], a[44:47], v[156:159]\n\t"
    "v_exp_f32 v174, v174\n\t"
    "v_mfma_f32_16x16x32_bf16 v[156:159], a[84:87], a[76:79], v[156:159]\n\t"
    "v_exp_f32 v175, v175\n\t"
    "v_mfma_f32_16x16x32_bf16 v[156:159], a[116:119], a[108:111], v[156:159]\n\t"
    "buffer_load_dwordx4 v243, s[32:35], s84 offen offset:1024 sc1 lds\n\t"
    "v_mfma_f32_16x16x32_bf16 v[152:155], a[116:119], a[104:107], v[232:235]\n\t"
    "ds_read_b128 a[88:91], v250 offset:26624\n\t"
    "v_cvt_pk_bf16_f32 v221, v174, v175\n\t"
    "v_mfma_f32_16x16x32_bf16 v[152:155], a[84:87], a[72:75], v[152:155]\n\t"
    "v_exp_f32 v188, v188\n\t"
    "v_mfma_f32_16x16x32_bf16 v[152:155], a[52:55], a[40:43], v[152:155]\n\t"
    "v_exp_f32 v189, v189\n\t"
    "v_mfma_f32_16x16x32_bf16 v[152:155], a[20:23], a[8:11], v[152:155]\n\t"
    "v_mfma_f32_16x16x32_bf16 v[148:151], a[20:23], a[4:7], v[228:231]\n\t"
    "ds_read_b128 a[120:123], v250 offset:27648\n\t"
    "v_cvt_pk_bf16_f32 v222, v188, v189\n\t"
    "v_mfma_f32_16x16x32_bf16 v[148:151], a[52:55], a[36:39], v[148:151]\n\t"
    "v_exp_f32 v190, v190\n\t"
    "v_mfma_f32_16x16x32_bf16 v[148:151], a[84:87], a[68:71], v[148:151]\n\t"
    "v_exp_f32 v191, v191\n\t"
    "v_mfma_f32_16x16x32_bf16 v[148:151], a[116:119], a[100:103], v[148:151]\n\t"
    "buffer_load_dwordx4 v244, s[32:35], s84 offen offset:2048 sc1 lds\n\t"
    "v_mfma_f32_16x16x32_bf16 v[144:147], a[116:119], a[96:99], v[224:227]\n\t"
    "ds_read_b128 a[28:31], v250 offset:28672\n\t"
    "v_cvt_pk_bf16_f32 v223, v190, v191\n\t"
    "v_mfma_f32_16x16x32_bf16 v[144:147], a[84:87], a[64:67], v[144:147]\n\t"
    "v_exp_f32 v184, v184\n\t"
    "v_mfma_f32_16x16x32_bf16 v[144:147], a[52:55], a[32:35], v[144:147]\n\t"
    "v_exp_f32 v185, v185\n\t"
    "v_mfma_f32_16x16x32_bf16 v[144:147], a[20:23], a[0:3], v[144:147]\n\t"
    "s_waitcnt lgkmcnt(3)\n\t"
    "v_mfma_f32_16x16x32_bf16 v[0:3], a[24:27], v[192:195], v[0:3]\n\t"
    "ds_read_b128 a[60:63], v250 offset:29696\n\t"
    "v_cvt_pk_bf16_f32 v218, v184, v185\n\t"
    "v_mfma_f32_16x16x32_bf16 v[4:7], a[24:27], v[196:199], v[4:7]\n\t"
    "v_exp_f32 v186, v186\n\t"
    "v_mfma_f32_16x16x32_bf16 v[8:11], a[24:27], v[200:203], v[8:11]\n\t"
    "v_exp_f32 v187, v187\n\t"
    "v_mfma_f32_16x16x32_bf16 v[12:15], a[24:27], v[204:207], v[12:15]\n\t"
    "buffer_load_dwordx4 v245, s[32:35], s84 offen offset:3072 sc1 lds\n\t"
    "v_mfma_f32_16x16x32_bf16 v[28:31], a[56:59], v[204:207], v[28:31]\n\t"
    "ds_read_b128 a[92:95], v250 offset:30720\n\t"
    "v_cvt_pk_bf16_f32 v219, v186, v187\n\t"
    "v_mfma_f32_16x16x32_bf16 v[24:27], a[56:59], v[200:203], v[24:27]\n\t"
    "v_exp_f32 v180, v180\n\t"
    "v_mfma_f32_16x16x32_bf16 v[20:23], a[56:59], v[196:199], v[20:23]\n\t"
    "v_exp_f32 v181, v181\n\t"
    "v_mfma_f32_16x16x32_bf16 v[16:19], a[56:59], v[192:195], v[16:19]\n\t"
    "s_waitcnt lgkmcnt(3)\n\t"
    "s_add_u32 m0, s48, 147456\n\t"
    "v_mfma_f32_16x16x32_bf16 v[32:35], a[88:91], v[192:195], v[32:35]\n\t"
    "ds_read_b128 a[124:127], v250 offset:31744\n\t"
    "v_cvt_pk_bf16_f32 v214, v180, v181\n\t"
    "v_mfma_f32_16x16x32_bf16 v[36:39], a[88:91], v[196:199], v[36:39]\n\t"
    "v_exp_f32 v182, v182\n\t"
    "v_mfma_f32_16x16x32_bf16 v[40:43], a[88:91], v[200:203], v[40:43]\n\t"
    "v_exp_f32 v183, v183\n\t"
    "v_mfma_f32_16x16x32_bf16 v[44:47], a[88:91], v[204:207], v[44:47]\n\t"
    "s_add_u32 s84, s84, s79\n\t"
    "buffer_load_dwordx4 v255, s[68:71], s53 offen lds\n\t"
    "v_mfma_f32_16x16x32_bf16 v[60:63], a[120:123], v[204:207], v[60:63]\n\t"
    "ds_read_b128 a[16:19], v251 offset:0\n\t"
    "v_cvt_pk_bf16_f32 v215, v182, v183\n\t"
    "v_mfma_f32_16x16x32_bf16 v[56:59], a[120:123], v[200:203], v[56:59]\n\t"
    "v_exp_f32 v176, v176\n\t"
    "v_mfma_f32_16x16x32_bf16 v[52:55], a[120:123], v[196:199], v[52:55]\n\t"
    "v_exp_f32 v177, v177\n\t"
    "v_mfma_f32_16x16x32_bf16 v[48:51], a[120:123], v[192:195], v[48:51]\n\t"
    "s_waitcnt lgkmcnt(3)\n\t"
    "v_xor_b32 v254, 64, v255\n\t"
    "v_add_u32 v254, 7168, v254\n\t"
    "v_mfma_f32_16x16x32_bf16 v[64:67], a[28:31], v[192:195], v[64:67]\n\t"
    "ds_read_b128 a[48:51], v251 offset:1024\n\t"
    "v_cvt_pk_bf16_f32 v210, v176, v177\n\t"
    "v_mfma_f32_16x16x32_bf16 v[68:71], a[28:31], v[196:199], v[68:71]\n\t"
    "v_exp_f32 v178, v178\n\t"
    "v_mfma_f32_16x16x32_bf16 v[72:75], a[28:31], v[200:203], v[72:75]\n\t"
    "v_exp_f32 v179, v179\n\t"
    "v_mfma_f32_16x16x32_bf16 v[76:79], a[28:31], v[204:207], v[76:79]\n\t"
    "buffer_load_dwordx4 v254, s[68:71], s53 offen offset:1024 lds\n\t"
    "v_mfma_f32_16x16x32_bf16 v[92:95], a[60:63], v[204:207], v[92:95]\n\t"
    "ds_read_b128 a[80:83], v251 offset:2048\n\t"
    "v_cvt_pk_bf16_f32 v211, v178, v179\n\t"
    "v_mfma_f32_16x16x32_bf16 v[88:91], a[60:63], v[200:203], v[88:91]\n\t"
    "v_exp_f32 v128, v128\n\t"
    "v_mfma_f32_16x16x32_bf16 v[84:87], a[60:63], v[196:199], v[84:87]\n\t"
    "v_exp_f32 v129, v129\n\t"
    "v_mfma_f32_16x16x32_bf16 v[80:83], a[60:63], v[192:195], v[80:83]\n\t"
    "s_waitcnt lgkmcnt(3)\n\t"
    "v_xor_b32 v254, 128, v255\n\t"
    "v_add_u32 v254, 14336, v254\n\t"
    "v_mfma_f32_16x16x32_bf16 v[96:99], a[92:95], v[192:195], v[96:99]\n\t"
    "ds_read_b128 a[112:115], v251 offset:3072\n\t"
    "v_mfma_f32_16x16x32_bf16 v[100:103], a[92:95], v[196:199], v[100:103]\n\t"
    "v_exp_f32 v130, v130\n\t"
    "v_mfma_f32_16x16x32_bf16 v[104:107], a[92:95], v[200:203], v[104:107]\n\t"
    "v_exp_f32 v131, v131\n\t"
    "v_mfma_f32_16x16x32_bf16 v[108:111], a[92:95], v[204:207], v[108:111]\n\t"
    "buffer_load_dwordx4 v254, s[68:71], s53 offen offset:2048 lds\n\t"
    "v_mfma_f32_16x16x32_bf16 v[124:127], a[124:127], v[204:207], v[124:127]\n\t"
    "ds_read_b128 a[20:23], v251 offset:4096\n\t"
    "v_xor_b32 v254, 192, v255\n\t"
    "v_mfma_f32_16x16x32_bf16 v[120:123], a[124:127], v[200:203], v[120:123]\n\t"
    "v_exp_f32 v132, v132\n\t"
    "v_mfma_f32_16x16x32_bf16 v[116:119], a[124:127], v[196:199], v[116:119]\n\t"
    "v_exp_f32 v133, v133\n\t"
    "v_mfma_f32_16x16x32_bf16 v[112:115], a[124:127], v[192:195], v[112:115]\n\t"
    "v_add_u32 v254, 21504, v254\n\t"
    "v_mfma_f32_16x16x32_bf16 a[128:131], a[240:243], v[192:195], a[128:131]\n\t"
    "ds_read_b128 a[52:55], v251 offset:5120\n\t"
    "v_mfma_f32_16x16x32_bf16 a[132:135], a[240:243], v[196:199], a[132:135]\n\t"
    "v_exp_f32 v134, v134\n\t"
    "v_mfma_f32_16x16x32_bf16 a[136:139], a[240:243], v[200:203], a[136:139]\n\t"
    "v_exp_f32 v135, v135\n\t"
    "v_mfma_f32_16x16x32_bf16 a[140:143], a[240:243], v[204:207], a[140:143]\n\t"
    "s_waitcnt lgkmcnt(2)\n\t"
    "buffer_load_dwordx4 v254, s[68:71], s53 offen offset:3072 lds\n\t"
    "v_mfma_f32_16x16x32_bf16 v[160:163], a[16:19], a[0:3], v[224:227]\n\t"
    "ds_read_b128 a[84:87], v251 offset:6144\n\t"
    "v_cvt_pk_bf16_f32 v192, v128, v129\n\t"
    "v_mfma_f32_16x16x32_bf16 v[160:163], a[48:51], a[32:35], v[160:163]\n\t"
    "v_exp_f32 v136, v136\n\t"
    "v_mfma_f32_16x16x32_bf16 v[160:163], a[80:83], a[64:67], v[160:163]\n\t"
    "v_exp_f32 v137, v137\n\t"
    "v_mfma_f32_16x16x32_bf16 v[160:163], a[112:115], a[96:99], v[160:163]\n\t"
    "v_cvt_pk_bf16_f32 v193, v130, v131\n\t"
    "v_cvt_pk_bf16_f32 v196, v132, v133\n\t"
    "v_mfma_f32_16x16x32_bf16 v[164:167], a[112:115], a[100:103], v[228:231]\n\t"
    "ds_read_b128 a[116:119], v251 offset:7168\n\t"
    "v_cvt_pk_bf16_f32 v197, v134, v135\n\t"
    "v_mfma_f32_16x16x32_bf16 v[164:167], a[80:83], a[68:71], v[164:167]\n\t"
    "v_exp_f32 v138, v138\n\t"
    "v_mfma_f32_16x16x32_bf16 v[164:167], a[48:51], a[36:39], v[164:167]\n\t"
    "v_exp_f32 v139, v139\n\t"
    "v_mfma_f32_16x16x32_bf16 v[164:167], a[16:19], a[4:7], v[164:167]\n\t"
    "v_cvt_pk_bf16_f32 v200, v136, v137\n\t"
    "v_mfma_f32_16x16x32_bf16 v[168:171], a[16:19], a[8:11], v[232:235]\n\t"
    "v_cvt_pk_bf16_f32 v201, v138, v139\n\t"
    "v_mfma_f32_16x16x32_bf16 v[168:171], a[48:51], a[40:43], v[168:171]\n\t"
    "s_add_u32 m0, s85, 49152\n\t"
    "v_exp_f32 v140, v140\n\t"
    "v_mfma_f32_16x16x32_bf16 v[168:171], a[80:83], a[72:75], v[168:171]\n\t"
    "v_exp_f32 v141, v141\n\t"
    "v_mfma_f32_16x16x32_bf16 v[168:171], a[112:115], a[104:107], v[168:171]\n\t"
    "buffer_load_dwordx4 v242, s[32:35], s84 offen sc1 lds\n\t"
    "v_mfma_f32_16x16x32_bf16 v[172:175], a[112:115], a[108:111], v[236:239]\n\t"
    "ds_read_b128 a[24:27], v250 offset:40960\n\t"
    "v_cvt_pk_bf16_f32 v204, v140, v141\n\t"
    "v_mfma_f32_16x16x32_bf16 v[172:175], a[80:83], a[76:79], v[172:175]\n\t"
    "v_exp_f32 v142, v142\n\t"
    "v_mfma_f32_16x16x32_bf16 v[172:175], a[48:51], a[44:47], v[172:175]\n\t"
    "v_exp_f32 v143, v143\n\t"
    "v_mfma_f32_16x16x32_bf16 v[172:175], a[16:19], a[12:15], v[172:175]\n\t"
    "s_waitcnt lgkmcnt(1)\n\t"
    "v_mfma_f32_16x16x32_bf16 v[188:191], a[20:23], a[12:15], v[236:239]\n\t"
    "ds_read_b128 a[56:59], v250 offset:41984\n\t"
    "v_cvt_pk_bf16_f32 v205, v142, v143\n\t"
    "v_mfma_f32_16x16x32_bf16 v[188:191], a[52:55], a[44:47], v[188:191]\n\t"
    "v_exp_f32 v156, v156\n\t"
    "v_mfma_f32_16x16x32_bf16 v[188:191], a[84:87], a[76:79], v[188:191]\n\t"
    "v_exp_f32 v157, v157\n\t"
    "v_mfma_f32_16x16x32_bf16 v[188:191], a[116:119], a[108:111], v[188:191]\n\t"
    "buffer_load_dwordx4 v243, s[32:35], s84 offen offset:1024 sc1 lds\n\t"
    "v_mfma_f32_16x16x32_bf16 v[184:187], a[116:119], a[104:107], v[232:235]\n\t"
    "ds_read_b128 a[88:91], v250 offset:43008\n\t"
    "v_cvt_pk_bf16_f32 v206, v156, v157\n\t"
    "v_mfma_f32_16x16x32_bf16 v[184:187], a[84:87], a[72:75], v[184:187]\n\t"
    "v_exp_f32 v158, v158\n\t"
    "v_mfma_f32_16x16x32_bf16 v[184:187], a[52:55], a[40:43], v[184:187]\n\t"
    "v_exp_f32 v159, v159\n\t"
    "v_mfma_f32_16x16x32_bf16 v[184:187], a[20:23], a[8:11], v[184:187]\n\t"
    "v_mfma_f32_16x16x32_bf16 v[180:183], a[20:23], a[4:7], v[228:231]\n\t"
    "ds_read_b128 a[120:123], v250 offset:44032\n\t"
    "v_cvt_pk_bf16_f32 v207, v158, v159\n\t"
    "v_mfma_f32_16x16x32_bf16 v[180:183], a[52:55], a[36:39], v[180:183]\n\t"
    "v_exp_f32 v152, v152\n\t"
    "v_mfma_f32_16x16x32_bf16 v[180:183], a[84:87], a[68:71], v[180:183]\n\t"
    "v_exp_f32 v153, v153\n\t"
    "v_mfma_f32_16x16x32_bf16 v[180:183], a[116:119], a[100:103], v[180:183]\n\t"
    "buffer_load_dwordx4 v244, s[32:35], s84 offen offset:2048 sc1 lds\n\t"
    "v_mfma_f32_16x16x32_bf16 v[176:179], a[116:119], a[96:99], v[224:227]\n\t"
    "ds_read_b128 a[28:31], v250 offset:45056\n\t"
    "v_cvt_pk_bf16_f32 v202, v152, v153\n\t"
    "v_mfma_f32_16x16x32_bf16 v[176:179], a[84:87], a[64:67], v[176:179]\n\t"
    "v_exp_f32 v154, v154\n\t"
    "v_mfma_f32_16x16x32_bf16 v[176:179], a[52:55], a[32:35], v[176:179]\n\t"
    "v_exp_f32 v155, v155\n\t"
    "v_mfma_f32_16x16x32_bf16 v[176:179], a[20:23], a[0:3], v[176:179]\n\t"
    "s_waitcnt lgkmcnt(3)\n\t"
    "v_mfma_f32_16x16x32_bf16 v[0:3], a[24:27], v[208:211], v[0:3]\n\t"
    "ds_read_b128 a[60:63], v250 offset:46080\n\t"
    "v_cvt_pk_bf16_f32 v203, v154, v155\n\t"
    "v_mfma_f32_16x16x32_bf16 v[4:7], a[24:27], v[212:215], v[4:7]\n\t"
    "v_exp_f32 v148, v148\n\t"
    "v_mfma_f32_16x16x32_bf16 v[8:11], a[24:27], v[216:219], v[8:11]\n\t"
    "v_exp_f32 v149, v149\n\t"
    "v_mfma_f32_16x16x32_bf16 v[12:15], a[24:27], v[220:223], v[12:15]\n\t"
    "buffer_load_dwordx4 v245, s[32:35], s84 offen offset:3072 sc1 lds\n\t"
    "v_mfma_f32_16x16x32_bf16 v[28:31], a[56:59], v[220:223], v[28:31]\n\t"
    "ds_read_b128 a[92:95], v250 offset:47104\n\t"
    "v_cvt_pk_bf16_f32 v198, v148, v149\n\t"
    "v_mfma_f32_16x16x32_bf16 v[24:27], a[56:59], v[216:219], v[24:27]\n\t"
    "v_exp_f32 v150, v150\n\t"
    "v_mfma_f32_16x16x32_bf16 v[20:23], a[56:59], v[212:215], v[20:23]\n\t"
    "v_exp_f32 v151, v151\n\t"
    "v_mfma_f32_16x16x32_bf16 v[16:19], a[56:59], v[208:211], v[16:19]\n\t"
    "s_waitcnt lgkmcnt(3)\n\t"
    "v_mfma_f32_16x16x32_bf16 v[32:35], a[88:91], v[208:211], v[32:35]\n\t"
    "ds_read_b128 a[124:127], v250 offset:48128\n\t"
    "v_cvt_pk_bf16_f32 v199, v150, v151\n\t"
    "v_mfma_f32_16x16x32_bf16 v[36:39], a[88:91], v[212:215], v[36:39]\n\t"
    "v_exp_f32 v144, v144\n\t"
    "v_mfma_f32_16x16x32_bf16 v[40:43], a[88:91], v[216:219], v[40:43]\n\t"
    "v_exp_f32 v145, v145\n\t"
    "v_mfma_f32_16x16x32_bf16 v[44:47], a[88:91], v[220:223], v[44:47]\n\t"
    "s_add_u32 s84, s84, s79\n\t"
    "v_mfma_f32_16x16x32_bf16 v[60:63], a[120:123], v[220:223], v[60:63]\n\t"
    "ds_read_b128 a[16:19], v248 offset:16384\n\t"
    "v_cvt_pk_bf16_f32 v194, v144, v145\n\t"
    "v_mfma_f32_16x16x32_bf16 v[56:59], a[120:123], v[216:219], v[56:59]\n\t"
    "v_exp_f32 v146, v146\n\t"
    "v_mfma_f32_16x16x32_bf16 v[52:55], a[120:123], v[212:215], v[52:55]\n\t"
    "v_exp_f32 v147, v147\n\t"
    "v_mfma_f32_16x16x32_bf16 v[48:51], a[120:123], v[208:211], v[48:51]\n\t"
    "s_waitcnt lgkmcnt(3)\n\t"
    "v_mfma_f32_16x16x32_bf16 v[64:67], a[28:31], v[208:211], v[64:67]\n\t"
    "ds_read_b128 a[48:51], v248 offset:17408\n\t"
    "v_cvt_pk_bf16_f32 v195, v146, v147\n\t"
    "v_mfma_f32_16x16x32_bf16 v[68:71], a[28:31], v[212:215], v[68:71]\n\t"
    "v_exp_f32 v160, v160\n\t"
    "v_mfma_f32_16x16x32_bf16 v[72:75], a[28:31], v[216:219], v[72:75]\n\t"
    "v_exp_f32 v161, v161\n\t"
    "v_mfma_f32_16x16x32_bf16 v[76:79], a[28:31], v[220:223], v[76:79]\n\t"
    "v_mfma_f32_16x16x32_bf16 v[92:95], a[60:63], v[220:223], v[92:95]\n\t"
    "ds_read_b128 a[80:83], v248 offset:18432\n\t"
    "v_mfma_f32_16x16x32_bf16 v[88:91], a[60:63], v[216:219], v[88:91]\n\t"
    "v_exp_f32 v162, v162\n\t"
    "v_mfma_f32_16x16x32_bf16 v[84:87], a[60:63], v[212:215], v[84:87]\n\t"
    "v_exp_f32 v163, v163\n\t"
    "v_mfma_f32_16x16x32_bf16 v[80:83], a[60:63], v[208:211], v[80:83]\n\t"
    "s_waitcnt lgkmcnt(3)\n\t"
    "v_mfma_f32_16x16x32_bf16 v[96:99], a[92:95], v[208:211], v[96:99]\n\t"
    "ds_read_b128 a[112:115], v248 offset:19456\n\t"
    "v_mfma_f32_16x16x32_bf16 v[100:103], a[92:95], v[212:215], v[100:103]\n\t"
    "v_exp_f32 v164, v164\n\t"
    "v_mfma_f32_16x16x32_bf16 v[104:107], a[92:95], v[216:219], v[104:107]\n\t"
    "v_exp_f32 v165, v165\n\t"
    "v_mfma_f32_16x16x32_bf16 v[108:111], a[92:95], v[220:223], v[108:111]\n\t"
    "v_mfma_f32_16x16x32_bf16 v[124:127], a[124:127], v[220:223], v[124:127]\n\t"
    "ds_read_b128 a[20:23], v248 offset:20480\n\t"
    "v_mfma_f32_16x16x32_bf16 v[120:123], a[124:127], v[216:219], v[120:123]\n\t"
    "v_exp_f32 v166, v166\n\t"
    "v_mfma_f32_16x16x32_bf16 v[116:119], a[124:127], v[212:215], v[116:119]\n\t"
    "v_exp_f32 v167, v167\n\t"
    "v_mfma_f32_16x16x32_bf16 v[112:115], a[124:127], v[208:211], v[112:115]\n\t"
    "v_mfma_f32_16x16x32_bf16 a[128:131], a[240:243], v[208:211], a[128:131]\n\t"
    "ds_read_b128 a[52:55], v248 offset:21504\n\t"
    "v_mfma_f32_16x16x32_bf16 a[132:135], a[240:243], v[212:215], a[132:135]\n\t"
    "v_exp_f32 v168, v168\n\t"
    "v_mfma_f32_16x16x32_bf16 a[136:139], a[240:243], v[216:219], a[136:139]\n\t"
    "v_exp_f32 v169, v169\n\t"
    "v_mfma_f32_16x16x32_bf16 a[140:143], a[240:243], v[220:223], a[140:143]\n\t"
    "s_waitcnt lgkmcnt(2)\n\t"
    "v_mfma_f32_16x16x32_bf16 v[128:131], a[16:19], a[0:3], v[224:227]\n\t"
    "ds_read_b128 a[84:87], v248 offset:22528\n\t"
    "v_cvt_pk_bf16_f32 v208, v160, v161\n\t"
    "v_mfma_f32_16x16x32_bf16 v[128:131], a[48:51], a[32:35], v[128:131]\n\t"
    "v_exp_f32 v170, v170\n\t"
    "v_mfma_f32_16x16x32_bf16 v[128:131], a[80:83], a[64:67], v[128:131]\n\t"
    "v_exp_f32 v171, v171\n\t"
    "v_mfma_f32_16x16x32_bf16 v[128:131], a[112:115], a[96:99], v[128:131]\n\t"
    "s_waitcnt vmcnt(8)\n\t"
    "s_barrier\n\t"
    "v_cvt_pk_bf16_f32 v209, v162, v163\n\t"
    "v_mfma_f32_16x16x32_bf16 v[132:135], a[112:115], a[100:103], v[228:231]\n\t"
    "ds_read_b128 a[116:119], v248 offset:23552\n\t"
    "v_cvt_pk_bf16_f32 v212, v164, v165\n\t"
    "v_mfma_f32_16x16x32_bf16 v[132:135], a[80:83], a[68:71], v[132:135]\n\t"
    "v_exp_f32 v172, v172\n\t"
    "v_mfma_f32_16x16x32_bf16 v[132:135], a[48:51], a[36:39], v[132:135]\n\t"
    "v_exp_f32 v173, v173\n\t"
    "v_mfma_f32_16x16x32_bf16 v[132:135], a[16:19], a[4:7], v[132:135]\n\t"
    "v_cvt_pk_bf16_f32 v213, v166, v167\n\t"
    "v_cvt_pk_bf16_f32 v216, v168, v169\n\t"
    "v_mfma_f32_16x16x32_bf16 v[136:139], a[16:19], a[8:11], v[232:235]\n\t"
    "v_cvt_pk_bf16_f32 v217, v170, v171\n\t"
    "v_cvt_pk_bf16_f32 v220, v172, v173\n\t"
    "v_mfma_f32_16x16x32_bf16 v[136:139], a[48:51], a[40:43], v[136:139]\n\t"
    "s_add_u32 m0, s85, 65536\n\t"
    "v_exp_f32 v174, v174\n\t"
    "v_mfma_f32_16x16x32_bf16 v[136:139], a[80:83], a[72:75], v[136:139]\n\t"
    "v_exp_f32 v175, v175\n\t"
    "v_mfma_f32_16x16x32_bf16 v[136:139], a[112:115], a[104:107], v[136:139]\n\t"
    "buffer_load_dwordx4 v242, s[32:35], s84 offen sc1 lds\n\t"
    "v_mfma_f32_16x16x32_bf16 v[140:143], a[112:115], a[108:111], v[236:239]\n\t"
    "ds_read_b128 a[24:27], v250 offset:57344\n\t"
    "v_cvt_pk_bf16_f32 v221, v174, v175\n\t"
    "v_mfma_f32_16x16x32_bf16 v[140:143], a[80:83], a[76:79], v[140:143]\n\t"
    "v_exp_f32 v188, v188\n\t"
    "v_mfma_f32_16x16x32_bf16 v[140:143], a[48:51], a[44:47], v[140:143]\n\t"
    "v_exp_f32 v189, v189\n\t"
    "v_mfma_f32_16x16x32_bf16 v[140:143], a[16:19], a[12:15], v[140:143]\n\t"
    "s_waitcnt lgkmcnt(1)\n\t"
    "buffer_load_dword v253, v252, s[88:91], 0 offen sc1\n\t"
    "v_mfma_f32_16x16x32_bf16 v[156:159], a[20:23], a[12:15], v[236:239]\n\t"
    "ds_read_b128 a[56:59], v250 offset:58368\n\t"
    "v_cvt_pk_bf16_f32 v222, v188, v189\n\t"
    "v_mfma_f32_16x16x32_bf16 v[156:159], a[52:55], a[44:47], v[156:159]\n\t"
    "v_exp_f32 v190, v190\n\t"
    "v_mfma_f32_16x16x32_bf16 v[156:159], a[84:87], a[76:79], v[156:159]\n\t"
    "v_exp_f32 v191, v191\n\t"
    "v_mfma_f32_16x16x32_bf16 v[156:159], a[116:119], a[108:111], v[156:159]\n\t"
    "buffer_load_dwordx4 v243, s[32:35], s84 offen offset:1024 sc1 lds\n\t"
    "v_mfma_f32_16x16x32_bf16 v[152:155], a[116:119], a[104:107], v[232:235]\n\t"
    "ds_read_b128 a[88:91], v250 offset:59392\n\t"
    "v_cvt_pk_bf16_f32 v223, v190, v191\n\t"
    "v_mfma_f32_16x16x32_bf16 v[152:155], a[84:87], a[72:75], v[152:155]\n\t"
    "v_exp_f32 v184, v184\n\t"
    "v_mfma_f32_16x16x32_bf16 v[152:155], a[52:55], a[40:43], v[152:155]\n\t"
    "v_exp_f32 v185, v185\n\t"
    "v_mfma_f32_16x16x32_bf16 v[152:155], a[20:23], a[8:11], v[152:155]\n\t"
    "v_mfma_f32_16x16x32_bf16 v[148:151], a[20:23], a[4:7], v[228:231]\n\t"
    "ds_read_b128 a[120:123], v250 offset:60416\n\t"
    "v_cvt_pk_bf16_f32 v218, v184, v185\n\t"
    "v_mfma_f32_16x16x32_bf16 v[148:151], a[52:55], a[36:39], v[148:151]\n\t"
    "v_exp_f32 v186, v186\n\t"
    "v_mfma_f32_16x16x32_bf16 v[148:151], a[84:87], a[68:71], v[148:151]\n\t"
    "v_exp_f32 v187, v187\n\t"
    "v_mfma_f32_16x16x32_bf16 v[148:151], a[116:119], a[100:103], v[148:151]\n\t"
    "buffer_load_dwordx4 v244, s[32:35], s84 offen offset:2048 sc1 lds\n\t"
    "v_mfma_f32_16x16x32_bf16 v[144:147], a[116:119], a[96:99], v[224:227]\n\t"
    "ds_read_b128 a[28:31], v250 offset:61440\n\t"
    "v_cvt_pk_bf16_f32 v219, v186, v187\n\t"
    "v_mfma_f32_16x16x32_bf16 v[144:147], a[84:87], a[64:67], v[144:147]\n\t"
    "v_exp_f32 v180, v180\n\t"
    "v_mfma_f32_16x16x32_bf16 v[144:147], a[52:55], a[32:35], v[144:147]\n\t"
    "v_exp_f32 v181, v181\n\t"
    "v_mfma_f32_16x16x32_bf16 v[144:147], a[20:23], a[0:3], v[144:147]\n\t"
    "s_waitcnt lgkmcnt(3)\n\t"
    "v_mfma_f32_16x16x32_bf16 v[0:3], a[24:27], v[192:195], v[0:3]\n\t"
    "ds_read_b128 a[60:63], v250 offset:62464\n\t"
    "v_cvt_pk_bf16_f32 v214, v180, v181\n\t"
    "v_mfma_f32_16x16x32_bf16 v[4:7], a[24:27], v[196:199], v[4:7]\n\t"
    "v_exp_f32 v182, v182\n\t"
    "v_mfma_f32_16x16x32_bf16 v[8:11], a[24:27], v[200:203], v[8:11]\n\t"
    "v_exp_f32 v183, v183\n\t"
    "v_mfma_f32_16x16x32_bf16 v[12:15], a[24:27], v[204:207], v[12:15]\n\t"
    "buffer_load_dwordx4 v245, s[32:35], s84 offen offset:3072 sc1 lds\n\t"
    "v_mfma_f32_16x16x32_bf16 v[28:31], a[56:59], v[204:207], v[28:31]\n\t"
    "ds_read_b128 a[92:95], v250 offset:63488\n\t"
    "v_cvt_pk_bf16_f32 v215, v182, v183\n\t"
    "v_mfma_f32_16x16x32_bf16 v[24:27], a[56:59], v[200:203], v[24:27]\n\t"
    "v_exp_f32 v176, v176\n\t"
    "v_mfma_f32_16x16x32_bf16 v[20:23], a[56:59], v[196:199], v[20:23]\n\t"
    "v_exp_f32 v177, v177\n\t"
    "v_mfma_f32_16x16x32_bf16 v[16:19], a[56:59], v[192:195], v[16:19]\n\t"
    "s_waitcnt lgkmcnt(3)\n\t"
    "v_mfma_f32_16x16x32_bf16 v[32:35], a[88:91], v[192:195], v[32:35]\n\t"
    "ds_read_b128 a[124:127], v250 offset:64512\n\t"
    "v_cvt_pk_bf16_f32 v210, v176, v177\n\t"
    "v_mfma_f32_16x16x32_bf16 v[36:39], a[88:91], v[196:199], v[36:39]\n\t"
    "v_exp_f32 v178, v178\n\t"
    "v_mfma_f32_16x16x32_bf16 v[40:43], a[88:91], v[200:203], v[40:43]\n\t"
    "v_exp_f32 v179, v179\n\t"
    "v_mfma_f32_16x16x32_bf16 v[44:47], a[88:91], v[204:207], v[44:47]\n\t"
    "s_add_u32 s84, s84, s79\n\t"
    "v_mfma_f32_16x16x32_bf16 v[60:63], a[120:123], v[204:207], v[60:63]\n\t"
    "ds_read_b128 a[16:19], v248 offset:32768\n\t"
    "v_cvt_pk_bf16_f32 v211, v178, v179\n\t"
    "v_mfma_f32_16x16x32_bf16 v[56:59], a[120:123], v[200:203], v[56:59]\n\t"
    "v_exp_f32 v128, v128\n\t"
    "v_mfma_f32_16x16x32_bf16 v[52:55], a[120:123], v[196:199], v[52:55]\n\t"
    "v_exp_f32 v129, v129\n\t"
    "v_mfma_f32_16x16x32_bf16 v[48:51], a[120:123], v[192:195], v[48:51]\n\t"
    "s_waitcnt lgkmcnt(3)\n\t"
    "v_mfma_f32_16x16x32_bf16 v[64:67], a[28:31], v[192:195], v[64:67]\n\t"
    "ds_read_b128 a[48:51], v248 offset:33792\n\t"
    "v_mfma_f32_16x16x32_bf16 v[68:71], a[28:31], v[196:199], v[68:71]\n\t"
    "v_exp_f32 v130, v130\n\t"
    "v_mfma_f32_16x16x32_bf16 v[72:75], a[28:31], v[200:203], v[72:75]\n\t"
    "v_exp_f32 v131, v131\n\t"
    "v_mfma_f32_16x16x32_bf16 v[76:79], a[28:31], v[204:207], v[76:79]\n\t"
    "v_mfma_f32_16x16x32_bf16 v[92:95], a[60:63], v[204:207], v[92:95]\n\t"
    "ds_read_b128 a[80:83], v248 offset:34816\n\t"
    "v_mfma_f32_16x16x32_bf16 v[88:91], a[60:63], v[200:203], v[88:91]\n\t"
    "v_exp_f32 v132, v132\n\t"
    "v_mfma_f32_16x16x32_bf16 v[84:87], a[60:63], v[196:199], v[84:87]\n\t"
    "v_exp_f32 v133, v133\n\t"
    "v_mfma_f32_16x16x32_bf16 v[80:83], a[60:63], v[192:195], v[80:83]\n\t"
    "s_waitcnt lgkmcnt(3)\n\t"
    "v_mfma_f32_16x16x32_bf16 v[96:99], a[92:95], v[192:195], v[96:99]\n\t"
    "ds_read_b128 a[112:115], v248 offset:35840\n\t"
    "v_mfma_f32_16x16x32_bf16 v[100:103], a[92:95], v[196:199], v[100:103]\n\t"
    "v_exp_f32 v134, v134\n\t"
    "v_mfma_f32_16x16x32_bf16 v[104:107], a[92:95], v[200:203], v[104:107]\n\t"
    "v_exp_f32 v135, v135\n\t"
    "v_mfma_f32_16x16x32_bf16 v[108:111], a[92:95], v[204:207], v[108:111]\n\t"
    "v_mfma_f32_16x16x32_bf16 v[124:127], a[124:127], v[204:207], v[124:127]\n\t"
    "ds_read_b128 a[20:23], v248 offset:36864\n\t"
    "v_mfma_f32_16x16x32_bf16 v[120:123], a[124:127], v[200:203], v[120:123]\n\t"
    "v_exp_f32 v136, v136\n\t"
    "v_mfma_f32_16x16x32_bf16 v[116:119], a[124:127], v[196:199], v[116:119]\n\t"
    "v_exp_f32 v137, v137\n\t"
    "v_mfma_f32_16x16x32_bf16 v[112:115], a[124:127], v[192:195], v[112:115]\n\t"
    "v_mfma_f32_16x16x32_bf16 a[128:131], a[240:243], v[192:195], a[128:131]\n\t"
    "ds_read_b128 a[52:55], v248 offset:37888\n\t"
    "v_mfma_f32_16x16x32_bf16 a[132:135], a[240:243], v[196:199], a[132:135]\n\t"
    "v_exp_f32 v138, v138\n\t"
    "v_mfma_f32_16x16x32_bf16 a[136:139], a[240:243], v[200:203], a[136:139]\n\t"
    "v_exp_f32 v139, v139\n\t"
    "v_mfma_f32_16x16x32_bf16 a[140:143], a[240:243], v[204:207], a[140:143]\n\t"
    "s_waitcnt lgkmcnt(2)\n\t"
    "v_mfma_f32_16x16x32_bf16 v[160:163], a[16:19], a[0:3], v[224:227]\n\t"
    "ds_read_b128 a[84:87], v248 offset:38912\n\t"
    "v_cvt_pk_bf16_f32 v192, v128, v129\n\t"
    "v_mfma_f32_16x16x32_bf16 v[160:163], a[48:51], a[32:35], v[160:163]\n\t"
    "v_exp_f32 v140, v140\n\t"
    "v_mfma_f32_16x16x32_bf16 v[160:163], a[80:83], a[64:67], v[160:163]\n\t"
    "v_exp_f32 v141, v141\n\t"
    "v_mfma_f32_16x16x32_bf16 v[160:163], a[112:115], a[96:99], v[160:163]\n\t"
    "v_cvt_pk_bf16_f32 v193, v130, v131\n\t"
    "v_cvt_pk_bf16_f32 v196, v132, v133\n\t"
    "v_mfma_f32_16x16x32_bf16 v[164:167], a[112:115], a[100:103], v[228:231]\n\t"
    "ds_read_b128 a[116:119], v248 offset:39936\n\t"
    "v_cvt_pk_bf16_f32 v197, v134, v135\n\t"
    "v_mfma_f32_16x16x32_bf16 v[164:167], a[80:83], a[68:71], v[164:167]\n\t"
    "v_exp_f32 v142, v142\n\t"
    "v_mfma_f32_16x16x32_bf16 v[164:167], a[48:51], a[36:39], v[164:167]\n\t"
    "v_exp_f32 v143, v143\n\t"
    "v_mfma_f32_16x16x32_bf16 v[164:167], a[16:19], a[4:7], v[164:167]\n\t"
    "v_cvt_pk_bf16_f32 v200, v136, v137\n\t"
    "v_cvt_pk_bf16_f32 v201, v138, v139\n\t"
    "v_mfma_f32_16x16x32_bf16 v[168:171], a[16:19], a[8:11], v[232:235]\n\t"
    "s_waitcnt vmcnt(3)\n\t"
    "v_cvt_pk_bf16_f32 v204, v140, v141\n\t"
    "v_cvt_pk_bf16_f32 v205, v142, v143\n\t"
    "v_mfma_f32_16x16x32_bf16 v[168:171], a[48:51], a[40:43], v[168:171]\n\t"
    "s_add_u32 m0, s85, 81920\n\t"
    "v_exp_f32 v156, v156\n\t"
    "v_mfma_f32_16x16x32_bf16 v[168:171], a[80:83], a[72:75], v[168:171]\n\t"
    "v_exp_f32 v157, v157\n\t"
    "v_mfma_f32_16x16x32_bf16 v[168:171], a[112:115], a[104:107], v[168:171]\n\t"
    "buffer_load_dwordx4 v242, s[32:35], s84 offen sc1 lds\n\t"
    "v_mfma_f32_16x16x32_bf16 v[172:175], a[112:115], a[108:111], v[236:239]\n\t"
    "ds_read_b128 a[24:27], v251 offset:8192\n\t"
    "v_cvt_pk_bf16_f32 v206, v156, v157\n\t"
    "v_mfma_f32_16x16x32_bf16 v[172:175], a[80:83], a[76:79], v[172:175]\n\t"
    "v_exp_f32 v158, v158\n\t"
    "v_mfma_f32_16x16x32_bf16 v[172:175], a[48:51], a[44:47], v[172:175]\n\t"
    "v_exp_f32 v159, v159\n\t"
    "v_mfma_f32_16x16x32_bf16 v[172:175], a[16:19], a[12:15], v[172:175]\n\t"
    "s_waitcnt lgkmcnt(1)\n\t"
    "v_cmp_ne_u32 vcc, s20, v253\n\t"
    "v_mfma_f32_16x16x32_bf16 v[188:191], a[20:23], a[12:15], v[236:239]\n\t"
    "ds_read_b128 a[56:59], v251 offset:9216\n\t"
    "v_cvt_pk_bf16_f32 v207, v158, v159\n\t"
    "v_mfma_f32_16x16x32_bf16 v[188:191], a[52:55], a[44:47], v[188:191]\n\t"
    "v_exp_f32 v152, v152\n\t"
    "v_mfma_f32_16x16x32_bf16 v[188:191], a[84:87], a[76:79], v[188:191]\n\t"
    "v_exp_f32 v153, v153\n\t"
    "v_mfma_f32_16x16x32_bf16 v[188:191], a[116:119], a[108:111], v[188:191]\n\t"
    "buffer_load_dwordx4 v243, s[32:35], s84 offen offset:1024 sc1 lds\n\t"
    "v_mfma_f32_16x16x32_bf16 v[184:187], a[116:119], a[104:107], v[232:235]\n\t"
    "ds_read_b128 a[88:91], v251 offset:10240\n\t"
    "v_cvt_pk_bf16_f32 v202, v152, v153\n\t"
    "v_mfma_f32_16x16x32_bf16 v[184:187], a[84:87], a[72:75], v[184:187]\n\t"
    "v_exp_f32 v154, v154\n\t"
    "v_mfma_f32_16x16x32_bf16 v[184:187], a[52:55], a[40:43], v[184:187]\n\t"
    "v_exp_f32 v155, v155\n\t"
    "v_mfma_f32_16x16x32_bf16 v[184:187], a[20:23], a[8:11], v[184:187]\n\t"
    "s_cbranch_vccnz .Lfb_wh\n\t"
    ".Lfb_wh_r:\n\t"
    "v_mfma_f32_16x16x32_bf16 v[180:183], a[20:23], a[4:7], v[228:231]\n\t"
    "ds_read_b128 a[120:123], v251 offset:11264\n\t"
    "v_cvt_pk_bf16_f32 v203, v154, v155\n\t"
    "v_mfma_f32_16x16x32_bf16 v[180:183], a[52:55], a[36:39], v[180:183]\n\t"
    "v_exp_f32 v148, v148\n\t"
    "v_mfma_f32_16x16x32_bf16 v[180:183], a[84:87], a[68:71], v[180:183]\n\t"
    "buffer_load_dwordx4 a[144:147], v241, s[88:91], s46 offen sc1\n\t"
    "v_mfma_f32_16x16x32_bf16 v[180:183], a[116:119], a[100:103], v[180:183]\n\t"
    "buffer_load_dwordx4 v244, s[32:35], s84 offen offset:2048 sc1 lds\n\t"
    "v_mfma_f32_16x16x32_bf16 v[176:179], a[116:119], a[96:99], v[224:227]\n\t"
    "ds_read_b128 a[28:31], v251 offset:12288\n\t"
    "v_mfma_f32_16x16x32_bf16 v[176:179], a[84:87], a[64:67], v[176:179]\n\t"
    "buffer_load_dwordx4 a[160:163], v241, s[88:91], s47 offen sc1\n\t"
    "v_mfma_f32_16x16x32_bf16 v[176:179], a[52:55], a[32:35], v[176:179]\n\t"
    "v_exp_f32 v149, v149\n\t"
    "v_mfma_f32_16x16x32_bf16 v[176:179], a[20:23], a[0:3], v[176:179]\n\t"
    "s_waitcnt lgkmcnt(3)\n\t"
    "v_exp_f32 v150, v150\n\t"
    "v_mfma_f32_16x16x32_bf16 v[0:3], a[24:27], v[208:211], v[0:3]\n\t"
    "ds_read_b128 a[60:63], v251 offset:13312\n\t"
    "v_cvt_pk_bf16_f32 v198, v148, v149\n\t"
    "v_mfma_f32_16x16x32_bf16 v[4:7], a[24:27], v[212:215], v[4:7]\n\t"
    "buffer_load_dwordx4 a[148:151], v246, s[88:91], s46 offen sc1\n\t"
    "v_mfma_f32_16x16x32_bf16 v[8:11], a[24:27], v[216:219], v[8:11]\n\t"
    "v_exp_f32 v151, v151\n\t"
    "v_mfma_f32_16x16x32_bf16 v[12:15], a[24:27], v[220:223], v[12:15]\n\t"
    "buffer_load_dwordx4 v245, s[32:35], s84 offen offset:3072 sc1 lds\n\t"
    "v_mfma_f32_16x16x32_bf16 v[28:31], a[56:59], v[220:223], v[28:31]\n\t"
    "ds_read_b128 a[92:95], v251 offset:14336\n\t"
    "v_cvt_pk_bf16_f32 v199, v150, v151\n\t"
    "v_mfma_f32_16x16x32_bf16 v[24:27], a[56:59], v[216:219], v[24:27]\n\t"
    "buffer_load_dwordx4 a[164:167], v246, s[88:91], s47 offen sc1\n\t"
    "v_mfma_f32_16x16x32_bf16 v[20:23], a[56:59], v[212:215], v[20:23]\n\t"
    "ds_read_b128 a[24:27], v248 offset:24576\n\t"
    "v_mfma_f32_16x16x32_bf16 v[16:19], a[56:59], v[208:211], v[16:19]\n\t"
    "s_waitcnt lgkmcnt(4)\n\t"
    "v_exp_f32 v144, v144\n\t"
    "v_mfma_f32_16x16x32_bf16 v[32:35], a[88:91], v[208:211], v[32:35]\n\t"
    "ds_read_b128 a[124:127], v251 offset:15360\n\t"
    "v_mfma_f32_16x16x32_bf16 v[36:39], a[88:91], v[212:215], v[36:39]\n\t"
    "buffer_load_dwordx4 a[152:155], v247, s[88:91], s46 offen sc1\n\t"
    "v_mfma_f32_16x16x32_bf16 v[40:43], a[88:91], v[216:219], v[40:43]\n\t"
    "ds_read_b128 a[56:59], v248 offset:25600\n\t"
    "v_mfma_f32_16x16x32_bf16 v[44:47], a[88:91], v[220:223], v[44:47]\n\t"
    "s_add_u32 s84, s84, s79\n\t"
    "v_exp_f32 v145, v145\n\t"
    "v_mfma_f32_16x16x32_bf16 v[60:63], a[120:123], v[220:223], v[60:63]\n\t"
    "buffer_load_dwordx4 a[168:171], v247, s[88:91], s47 offen sc1\n\t"
    "v_mfma_f32_16x16x32_bf16 v[56:59], a[120:123], v[216:219], v[56:59]\n\t"
    "v_exp_f32 v146, v146\n\t"
    "v_mfma_f32_16x16x32_bf16 v[52:55], a[120:123], v[212:215], v[52:55]\n\t"
    "ds_read_b128 a[88:91], v248 offset:26624\n\t"
    "v_cvt_pk_bf16_f32 v194, v144, v145\n\t"
    "v_mfma_f32_16x16x32_bf16 v[48:51], a[120:123], v[208:211], v[48:51]\n\t"
    "s_waitcnt lgkmcnt(5)\n\t"
    "buffer_load_dwordx4 a[156:159], v249, s[88:91], s46 offen sc1\n\t"
    "v_mfma_f32_16x16x32_bf16 v[64:67], a[28:31], v[208:211], v[64:67]\n\t"
    "v_exp_f32 v147, v147\n\t"
    "v_mfma_f32_16x16x32_bf16 v[68:71], a[28:31], v[212:215], v[68:71]\n\t"
    "v_exp_f32 v160, v160\n\t"
    "v_mfma_f32_16x16x32_bf16 v[72:75], a[28:31], v[216:219], v[72:75]\n\t"
    "ds_read_b128 a[120:123], v248 offset:27648\n\t"
    "v_cvt_pk_bf16_f32 v195, v146, v147\n\t"
    "v_mfma_f32_16x16x32_bf16 v[76:79], a[28:31], v[220:223], v[76:79]\n\t"
    "buffer_load_dwordx4 a[172:175], v249, s[88:91], s47 offen sc1\n\t"
    "v_mfma_f32_16x16x32_bf16 v[92:95], a[60:63], v[220:223], v[92:95]\n\t"
    "s_add_u32 s46, s46, 8192\n\t"
    "s_add_u32 s47, s47, 8192\n\t"
    "v_mfma_f32_16x16x32_bf16 v[88:91], a[60:63], v[216:219], v[88:91]\n\t"
    "v_exp_f32 v161, v161\n\t"
    "v_mfma_f32_16x16x32_bf16 v[84:87], a[60:63], v[212:215], v[84:87]\n\t"
    "ds_read_b128 a[28:31], v248 offset:28672\n\t"
    "v_mfma_f32_16x16x32_bf16 v[80:83], a[60:63], v[208:211], v[80:83]\n\t"
    "s_waitcnt lgkmcnt(4)\n\t"
    "buffer_load_dwordx4 a[176:179], v241, s[88:91], s46 offen sc1\n\t"
    "v_mfma_f32_16x16x32_bf16 v[96:99], a[92:95], v[208:211], v[96:99]\n\t"
    "v_exp_f32 v162, v162\n\t"
    "v_mfma_f32_16x16x32_bf16 v[100:103], a[92:95], v[212:215], v[100:103]\n\t"
    "v_exp_f32 v163, v163\n\t"
    "v_mfma_f32_16x16x32_bf16 v[104:107], a[92:95], v[216:219], v[104:107]\n\t"
    "ds_read_b128 a[60:63], v248 offset:29696\n\t"
    "v_mfma_f32_16x16x32_bf16 v[108:111], a[92:95], v[220:223], v[108:111]\n\t"
    "buffer_load_dwordx4 a[192:195], v241, s[88:91], s47 offen sc1\n\t"
    "v_mfma_f32_16x16x32_bf16 v[124:127], a[124:127], v[220:223], v[124:127]\n\t"
    "v_exp_f32 v164, v164\n\t"
    "v_mfma_f32_16x16x32_bf16 v[120:123], a[124:127], v[216:219], v[120:123]\n\t"
    "v_exp_f32 v165, v165\n\t"
    "v_mfma_f32_16x16x32_bf16 v[116:119], a[124:127], v[212:215], v[116:119]\n\t"
    "ds_read_b128 a[92:95], v248 offset:30720\n\t"
    "v_mfma_f32_16x16x32_bf16 v[112:115], a[124:127], v[208:211], v[112:115]\n\t"
    "buffer_load_dwordx4 a[180:183], v246, s[88:91], s46 offen sc1\n\t"
    "v_mfma_f32_16x16x32_bf16 a[128:131], a[240:243], v[208:211], a[128:131]\n\t"
    "v_exp_f32 v166, v166\n\t"
    "v_mfma_f32_16x16x32_bf16 a[132:135], a[240:243], v[212:215], a[132:135]\n\t"
    "v_exp_f32 v167, v167\n\t"
    "v_mfma_f32_16x16x32_bf16 a[136:139], a[240:243], v[216:219], a[136:139]\n\t"
    "ds_read_b128 a[124:127], v248 offset:31744\n\t"
    "v_mfma_f32_16x16x32_bf16 a[140:143], a[240:243], v[220:223], a[140:143]\n\t"
    "s_waitcnt lgkmcnt(6)\n\t"
    "buffer_load_dwordx4 a[196:199], v246, s[88:91], s47 offen sc1\n\t"
    "v_mfma_f32_16x16x32_bf16 v[0:3], a[24:27], v[192:195], v[0:3]\n\t"
    "v_exp_f32 v168, v168\n\t"
    "v_mfma_f32_16x16x32_bf16 v[4:7], a[24:27], v[196:199], v[4:7]\n\t"
    "v_exp_f32 v169, v169\n\t"
    "v_mfma_f32_16x16x32_bf16 v[8:11], a[24:27], v[200:203], v[8:11]\n\t"
    "buffer_load_dwordx4 a[184:187], v247, s[88:91], s46 offen sc1\n\t"
    "v_mfma_f32_16x16x32_bf16 v[12:15], a[24:27], v[204:207], v[12:15]\n\t"
    "v_exp_f32 v170, v170\n\t"
    "v_mfma_f32_16x16x32_bf16 v[28:31], a[56:59], v[204:207], v[28:31]\n\t"
    "ds_read_b128 a[24:27], v248 offset:40960\n\t"
    "v_cvt_pk_bf16_f32 v208, v160, v161\n\t"
    "v_mfma_f32_16x16x32_bf16 v[24:27], a[56:59], v[200:203], v[24:27]\n\t"
    "buffer_load_dwordx4 a[200:203], v247, s[88:91], s47 offen sc1\n\t"
    "v_mfma_f32_16x16x32_bf16 v[20:23], a[56:59], v[196:199], v[20:23]\n\t"
    "v_exp_f32 v171, v171\n\t"
    "v_mfma_f32_16x16x32_bf16 v[16:19], a[56:59], v[192:195], v[16:19]\n\t"
    "s_waitcnt lgkmcnt(5)\n\t"
    "v_exp_f32 v172, v172\n\t"
    "v_mfma_f32_16x16x32_bf16 v[32:35], a[88:91], v[192:195], v[32:35]\n\t"
    "ds_read_b128 a[56:59], v248 offset:41984\n\t"
    "v_cvt_pk_bf16_f32 v209, v162, v163\n\t"
    "v_mfma_f32_16x16x32_bf16 v[36:39], a[88:91], v[196:199], v[36:39]\n\t"
    "buffer_load_dwordx4 a[188:191], v249, s[88:91], s46 offen sc1\n\t"
    "v_mfma_f32_16x16x32_bf16 v[40:43], a[88:91], v[200:203], v[40:43]\n\t"
    "v_exp_f32 v173, v173\n\t"
    "v_mfma_f32_16x16x32_bf16 v[44:47], a[88:91], v[204:207], v[44:47]\n\t"
    "v_exp_f32 v174, v174\n\t"
    "v_mfma_f32_16x16x32_bf16 v[60:63], a[120:123], v[204:207], v[60:63]\n\t"
    "ds_read_b128 a[88:91], v248 offset:43008\n\t"
    "v_cvt_pk_bf16_f32 v212, v164, v165\n\t"
    "v_mfma_f32_16x16x32_bf16 v[56:59], a[120:123], v[200:203], v[56:59]\n\t"
    "buffer_load_dwordx4 a[204:207], v249, s[88:91], s47 offen sc1\n\t"
    "v_mfma_f32_16x16x32_bf16 v[52:55], a[120:123], v[196:199], v[52:55]\n\t"
    "s_add_u32 s46, s46, 8192\n\t"
    "s_add_u32 s47, s47, 8192\n\t"
    "v_cvt_pk_bf16_f32 v213, v166, v167\n\t"
    "v_mfma_f32_16x16x32_bf16 v[48:51], a[120:123], v[192:195], v[48:51]\n\t"
    "s_waitcnt lgkmcnt(5)\n\t"
    "v_exp_f32 v175, v175\n\t"
    "v_mfma_f32_16x16x32_bf16 v[64:67], a[28:31], v[192:195], v[64:67]\n\t"
    "ds_read_b128 a[120:123], v248 offset:44032\n\t"
    "v_cvt_pk_bf16_f32 v216, v168, v169\n\t"
    "v_mfma_f32_16x16x32_bf16 v[68:71], a[28:31], v[196:199], v[68:71]\n\t"
    "v_exp_f32 v188, v188\n\t"
    "v_mfma_f32_16x16x32_bf16 v[72:75], a[28:31], v[200:203], v[72:75]\n\t"
    "v_exp_f32 v189, v189\n\t"
    "v_mfma_f32_16x16x32_bf16 v[76:79], a[28:31], v[204:207], v[76:79]\n\t"
    "v_exp_f32 v190, v190\n\t"
    "v_mfma_f32_16x16x32_bf16 v[92:95], a[60:63], v[204:207], v[92:95]\n\t"
    "ds_read_b128 a[28:31], v248 offset:45056\n\t"
    "v_cvt_pk_bf16_f32 v217, v170, v171\n\t"
    "v_mfma_f32_16x16x32_bf16 v[88:91], a[60:63], v[200:203], v[88:91]\n\t"
    "v_exp_f32 v191, v191\n\t"
    "v_mfma_f32_16x16x32_bf16 v[84:87], a[60:63], v[196:199], v[84:87]\n\t"
    "v_exp_f32 v184, v184\n\t"
    "v_mfma_f32_16x16x32_bf16 v[80:83], a[60:63], v[192:195], v[80:83]\n\t"
    "s_waitcnt lgkmcnt(5)\n\t"
    "v_exp_f32 v185, v185\n\t"
    "v_mfma_f32_16x16x32_bf16 v[96:99], a[92:95], v[192:195], v[96:99]\n\t"
    "ds_read_b128 a[60:63], v248 offset:46080\n\t"
    "v_cvt_pk_bf16_f32 v220, v172, v173\n\t"
    "v_mfma_f32_16x16x32_bf16 v[100:103], a[92:95], v[196:199], v[100:103]\n\t"
    "v_exp_f32 v186, v186\n\t"
    "v_mfma_f32_16x16x32_bf16 v[104:107], a[92:95], v[200:203], v[104:107]\n\t"
    "v_exp_f32 v187, v187\n\t"
    "v_mfma_f32_16x16x32_bf16 v[108:111], a[92:95], v[204:207], v[108:111]\n\t"
    "v_exp_f32 v180, v180\n\t"
    "v_mfma_f32_16x16x32_bf16 v[124:127], a[124:127], v[204:207], v[124:127]\n\t"
    "ds_read_b128 a[92:95], v248 offset:47104\n\t"
    "v_cvt_pk_bf16_f32 v221, v174, v175\n\t"
    "v_mfma_f32_16x16x32_bf16 v[120:123], a[124:127], v[200:203], v[120:123]\n\t"
    "v_exp_f32 v181, v181\n\t"
    "v_mfma_f32_16x16x32_bf16 v[116:119], a[124:127], v[196:199], v[116:119]\n\t"
    "v_exp_f32 v182, v182\n\t"
    "v_mfma_f32_16x16x32_bf16 v[112:115], a[124:127], v[192:195], v[112:115]\n\t"
    "v_exp_f32 v183, v183\n\t"
    "v_mfma_f32_16x16x32_bf16 a[128:131], a[240:243], v[192:195], a[128:131]\n\t"
    "ds_read_b128 a[124:127], v248 offset:48128\n\t"
    "v_cvt_pk_bf16_f32 v222, v188, v189\n\t"
    "v_mfma_f32_16x16x32_bf16 a[132:135], a[240:243], v[196:199], a[132:135]\n\t"
    "v_exp_f32 v176, v176\n\t"
    "v_mfma_f32_16x16x32_bf16 a[136:139], a[240:243], v[200:203], a[136:139]\n\t"
    "v_exp_f32 v177, v177\n\t"
    "v_mfma_f32_16x16x32_bf16 a[140:143], a[240:243], v[204:207], a[140:143]\n\t"
    "v_exp_f32 v178, v178\n\t"
    "v_exp_f32 v179, v179\n\t"
    "s_nop 0\n\t"
    "v_cvt_pk_bf16_f32 v223, v190, v191\n\t"
    "v_cvt_pk_bf16_f32 v218, v184, v185\n\t"
    "v_cvt_pk_bf16_f32 v219, v186, v187\n\t"
    "v_cvt_pk_bf16_f32 v214, v180, v181\n\t"
    "v_cvt_pk_bf16_f32 v215, v182, v183\n\t"
    "v_cvt_pk_bf16_f32 v210, v176, v177\n\t"
    "v_cvt_pk_bf16_f32 v211, v178, v179\n\t"
    "s_waitcnt lgkmcnt(0)\n\t"
    "s_nop 1\n\t"
    "v_mfma_f32_16x16x32_bf16 a[128:131], a[240:243], v[208:211], a[128:131]\n\t"
    "v_lshrrev_b32 v254, 6, v248\n\t"
    "v_bfe_u32 v255, v248, 4, 2\n\t"
    "v_mfma_f32_16x16x32_bf16 a[132:135], a[240:243], v[212:215], a[132:135]\n\t"
    "v_lshlrev_b32 v255, 3, v255\n\t"
    "v_lshl_add_u32 v255, v254, 8, v255\n\t"
    "v_mfma_f32_16x16x32_bf16 a[136:139], a[240:243], v[216:219], a[136:139]\n\t"
    "v_and_b32 v254, 7, v254\n\t"
    "v_lshl_add_u32 v192, v254, 5, v255\n\t"
    "v_mfma_f32_16x16x32_bf16 a[140:143], a[240:243], v[220:223], a[140:143]\n\t"
    "v_add_u32 v192, 49152, v192\n\t"
    "v_xor_b32 v193, 32, v192\n\t"
    "v_mfma_f32_16x16x32_bf16 v[0:3], a[24:27], v[208:211], v[0:3]\n\t"
    "v_xor_b32 v194, 64, v192\n\t"
    "v_xor_b32 v195, 96, v192\n\t"
    "v_mfma_f32_16x16x32_bf16 v[16:19], a[56:59], v[208:211], v[16:19]\n\t"
    "v_xor_b32 v196, 128, v192\n\t"
    "v_xor_b32 v197, 160, v192\n\t"
    "v_mfma_f32_16x16x32_bf16 v[4:7], a[24:27], v[212:215], v[4:7]\n\t"
    "v_xor_b32 v198, 192, v192\n\t"
    "v_xor_b32 v199, 224, v192\n\t"
    "v_mfma_f32_16x16x32_bf16 v[20:23], a[56:59], v[212:215], v[20:23]\n\t"
    "v_accvgpr_read_b32 v226, a128\n\t"
    "v_accvgpr_read_b32 v230, a132\n\t"
    "v_mfma_f32_16x16x32_bf16 v[8:11], a[24:27], v[216:219], v[8:11]\n\t"
    "v_accvgpr_read_b32 v234, a136\n\t"
    "v_accvgpr_read_b32 v238, a140\n\t"
    "v_mfma_f32_16x16x32_bf16 v[24:27], a[56:59], v[216:219], v[24:27]\n\t"
    "s_mov_b64 s[30:31], 0\n\t"
    "v_cmp_ngt_f32 vcc, 0x67800000, v226\n\t"
    "v_mfma_f32_16x16x32_bf16 v[12:15], a[24:27], v[220:223], v[12:15]\n\t"
    "v_rcp_f32 v224, v226\n\t"
    "v_mfma_f32_16x16x32_bf16 v[28:31], a[56:59], v[220:223], v[28:31]\n\t"
    "s_or_b64 s[30:31], s[30:31], vcc\n\t"
    "v_cmp_ngt_f32 vcc, 0x67800000, v230\n\t"
    "v_mfma_f32_16x16x32_bf16 v[32:35], a[88:91], v[208:211], v[32:35]\n\t"
    "v_rcp_f32 v228, v230\n\t"
    "v_mfma_f32_16x16x32_bf16 v[48:51], a[120:123], v[208:211], v[48:51]\n\t"
    "s_or_b64 s[30:31], s[30:31], vcc\n\t"
    "v_cmp_ngt_f32 vcc, 0x67800000, v234\n\t"
    "v_mfma_f32_16x16x32_bf16 v[36:39], a[88:91], v[212:215], v[36:39]\n\t"
    "v_rcp_f32 v232, v234\n\t"
    "v_mfma_f32_16x16x32_bf16 v[52:55], a[120:123], v[212:215], v[52:55]\n\t"
    "s_or_b64 s[30:31], s[30:31], vcc\n\t"
    "v_cmp_ngt_f32 vcc, 0x67800000, v238\n\t"
    "v_mfma_f32_16x16x32_bf16 v[40:43], a[88:91], v[216:219], v[40:43]\n\t"
    "v_rcp_f32 v236, v238\n\t"
    "v_mfma_f32_16x16x32_bf16 v[56:59], a[120:123], v[216:219], v[56:59]\n\t"
    "s_or_b64 s[30:31], s[30:31], vcc\n\t"
    "s_or_b32 s77, s30, s31\n\t"
    "s_lshl_b32 s78, s3, 2\n\t"
    "v_mfma_f32_16x16x32_bf16 v[44:47], a[88:91], v[220:223], v[44:47]\n\t"
    "v_mov_b32 v254, s78\n\t"
    "v_mov_b32 v255, s77\n\t"
    "v_mfma_f32_16x16x32_bf16 v[60:63], a[120:123], v[220:223], v[60:63]\n\t"
    "ds_write_b32 v254, v255 offset:0\n\t"
    "v_mfma_f32_16x16x32_bf16 v[64:67], a[28:31], v[208:211], v[64:67]\n\t"
    "v_mov_b32 v254, 0\n\t"
    "v_mfma_f32_16x16x32_bf16 v[80:83], a[60:63], v[208:211], v[80:83]\n\t"
    "s_waitcnt vmcnt(13) lgkmcnt(0)\n\t"
    "s_barrier\n\t"
    "v_mfma_f32_16x16x32_bf16 v[68:71], a[28:31], v[212:215], v[68:71]\n\t"
    "ds_read_b64_tr_b16 v[128:129], v192 offset:0\n\t"
    "v_mfma_f32_16x16x32_bf16 v[84:87], a[60:63], v[212:215], v[84:87]\n\t"
    "ds_read_b64_tr_b16 v[130:131], v192 offset:4096\n\t"
    "v_mfma_f32_16x16x32_bf16 v[72:75], a[28:31], v[216:219], v[72:75]\n\t"
    "ds_read_b64_tr_b16 v[132:133], v192 offset:8192\n\t"
    "v_mfma_f32_16x16x32_bf16 v[88:91], a[60:63], v[216:219], v[88:91]\n\t"
    "ds_read_b64_tr_b16 v[134:135], v192 offset:12288\n\t"
    "v_mfma_f32_16x16x32_bf16 v[76:79], a[28:31], v[220:223], v[76:79]\n\t"
    "ds_read_b64_tr_b16 v[136:137], v193 offset:0\n\t"
    "v_mfma_f32_16x16x32_bf16 v[92:95], a[60:63], v[220:223], v[92:95]\n\t"
    "ds_read_b64_tr_b16 v[138:139], v193 offset:4096\n\t"
    "v_mfma_f32_16x16x32_bf16 v[96:99], a[92:95], v[208:211], v[96:99]\n\t"
    "ds_read_b64_tr_b16 v[140:141], v193 offset:8192\n\t"
    "v_mfma_f32_16x16x32_bf16 v[112:115], a[124:127], v[208:211], v[112:115]\n\t"
    "ds_read_b64_tr_b16 v[142:143], v193 offset:12288\n\t"
    "v_mfma_f32_16x16x32_bf16 v[100:103], a[92:95], v[212:215], v[100:103]\n\t"
    "ds_read_b128 v[176:179], v254 offset:0\n\t"
    "v_mul_f32 v0, v224, v0\n\t"
    "v_mfma_f32_16x16x32_bf16 v[116:119], a[124:127], v[212:215], v[116:119]\n\t"
    "v_mul_f32 v1, v224, v1\n\t"
    "v_mul_f32 v2, v224, v2\n\t"
    "v_mfma_f32_16x16x32_bf16 v[104:107], a[92:95], v[216:219], v[104:107]\n\t"
    "v_mul_f32 v3, v224, v3\n\t"
    "v_mul_f32 v16, v224, v16\n\t"
    "v_mfma_f32_16x16x32_bf16 v[120:123], a[124:127], v[216:219], v[120:123]\n\t"
    "v_mul_f32 v17, v224, v17\n\t"
    "v_mul_f32 v18, v224, v18\n\t"
    "v_mfma_f32_16x16x32_bf16 v[108:111], a[92:95], v[220:223], v[108:111]\n\t"
    "v_mul_f32 v19, v224, v19\n\t"
    "v_cvt_pk_bf16_f32 v0, v0, v1\n\t"
    "v_mfma_f32_16x16x32_bf16 v[124:127], a[124:127], v[220:223], v[124:127]\n\t"
    "s_waitcnt vmcnt(8) lgkmcnt(0)\n\t"
    "v_or3_b32 v254, v176, v177, v178\n\t"
    "v_mfma_f32_16x16x32_bf16 a[208:211], v[128:131], a[144:147], 0\n\t"
    "ds_read_b64_tr_b16 v[144:145], v194 offset:0\n\t"
    "v_or_b32 v254, v254, v179\n\t"
    "v_mfma_f32_16x16x32_bf16 a[208:211], v[132:135], a[160:163], a[208:211]\n\t"
    "ds_read_b64_tr_b16 v[146:147], v194 offset:4096\n\t"
    "v_readfirstlane_b32 s77, v254\n\t"
    "v_mfma_f32_16x16x32_bf16 a[212:215], v[132:135], a[164:167], 0\n\t"
    "v_cvt_pk_bf16_f32 v1, v2, v3\n\t"
    "v_cvt_pk_bf16_f32 v2, v16, v17\n\t"
    "v_mfma_f32_16x16x32_bf16 a[212:215], v[128:131], a[148:151], a[212:215]\n\t"
    "s_andn2_b32 s77, s77, s87\n\t"
    "s_cbranch_scc1 .Lfb_slow\n\t"
    "v_cvt_pk_bf16_f32 v3, v18, v19\n\t"
    "v_mfma_f32_16x16x32_bf16 a[216:219], v[128:131], a[152:155], 0\n\t"
    "ds_read_b64_tr_b16 v[148:149], v194 offset:8192\n\t"
    "v_mfma_f32_16x16x32_bf16 a[216:219], v[132:135], a[168:171], a[216:219]\n\t"
    "ds_read_b64_tr_b16 v[150:151], v194 offset:12288\n\t"
    "v_mfma_f32_16x16x32_bf16 a[220:223], v[132:135], a[172:175], 0\n\t"
    "buffer_store_dwordx4 v[0:3], v248, s[88:91], s92 offen sc1\n\t"
    "v_mfma_f32_16x16x32_bf16 a[220:223], v[128:131], a[156:159], a[220:223]\n\t"
    "v_mul_f32 v4, v228, v4\n\t"
    "v_mul_f32 v5, v228, v5\n\t"
    "v_mfma_f32_16x16x32_bf16 a[236:239], v[136:139], a[156:159], 0\n\t"
    "ds_read_b64_tr_b16 v[152:153], v195 offset:0\n\t"
    "v_mul_f32 v6, v228, v6\n\t"
    "v_mfma_f32_16x16x32_bf16 a[236:239], v[140:143], a[172:175], a[236:239]\n\t"
    "ds_read_b64_tr_b16 v[154:155], v195 offset:4096\n\t"
    "v_mul_f32 v7, v228, v7\n\t"
    "v_mfma_f32_16x16x32_bf16 a[232:235], v[140:143], a[168:171], 0\n\t"
    "v_mul_f32 v20, v228, v20\n\t"
    "v_mul_f32 v21, v228, v21\n\t"
    "v_mfma_f32_16x16x32_bf16 a[232:235], v[136:139], a[152:155], a[232:235]\n\t"
    "v_mul_f32 v22, v228, v22\n\t"
    "v_mul_f32 v23, v228, v23\n\t"
    "v_mfma_f32_16x16x32_bf16 a[228:231], v[136:139], a[148:151], 0\n\t"
    "ds_read_b64_tr_b16 v[156:157], v195 offset:8192\n\t"
    "v_cvt_pk_bf16_f32 v4, v4, v5\n\t"
    "v_mfma_f32_16x16x32_bf16 a[228:231], v[140:143], a[164:167], a[228:231]\n\t"
    "ds_read_b64_tr_b16 v[158:159], v195 offset:12288\n\t"
    "v_cvt_pk_bf16_f32 v5, v6, v7\n\t"
    "v_mfma_f32_16x16x32_bf16 a[224:227], v[140:143], a[160:163], 0\n\t"
    "v_cvt_pk_bf16_f32 v6, v20, v21\n\t"
    "v_cvt_pk_bf16_f32 v7, v22, v23\n\t"
    "v_mfma_f32_16x16x32_bf16 a[224:227], v[136:139], a[144:147], a[224:227]\n\t"
    "s_waitcnt lgkmcnt(4)\n\t"
    "v_mfma_f32_16x16x32_bf16 a[240:243], v[144:147], a[144:147], 0\n\t"
    "ds_read_b64_tr_b16 v[128:129], v196 offset:0\n\t"
    "v_mfma_f32_16x16x32_bf16 a[240:243], v[148:151], a[160:163], a[240:243]\n\t"
    "ds_read_b64_tr_b16 v[130:131], v196 offset:4096\n\t"
    "v_mfma_f32_16x16x32_bf16 a[244:247], v[148:151], a[164:167], 0\n\t"
    "v_mfma_f32_16x16x32_bf16 a[244:247], v[144:147], a[148:151], a[244:247]\n\t"
    "v_mfma_f32_16x16x32_bf16 a[248:251], v[144:147], a[152:155], 0\n\t"
    "ds_read_b64_tr_b16 v[132:133], v196 offset:8192\n\t"
    "v_mfma_f32_16x16x32_bf16 a[248:251], v[148:151], a[168:171], a[248:251]\n\t"
    "ds_read_b64_tr_b16 v[134:135], v196 offset:12288\n\t"
    "v_mfma_f32_16x16x32_bf16 a[252:255], v[148:151], a[172:175], 0\n\t"
    "buffer_load_dwordx4 a[0:3], v241, s[88:91], s46 offen sc1\n\t"
    "v_mfma_f32_16x16x32_bf16 a[252:255], v[144:147], a[156:159], a[252:255]\n\t"
    "s_waitcnt lgkmcnt(4)\n\t"
    "buffer_store_dwordx4 v[4:7], v248, s[88:91], s92 offen offset:1024 sc1\n\t"
    "v_mfma_f32_16x16x32_bf16 a[140:143], v[152:155], a[156:159], 0\n\t"
    "ds_read_b64_tr_b16 v[136:137], v197 offset:0\n\t"
    "v_mul_f32 v8, v232, v8\n\t"
    "v_mfma_f32_16x16x32_bf16 a[140:143], v[156:159], a[172:175], a[140:143]\n\t"
    "ds_read_b64_tr_b16 v[138:139], v197 offset:4096\n\t"
    "v_mul_f32 v9, v232, v9\n\t"
    "v_mfma_f32_16x16x32_bf16 a[136:139], v[156:159], a[168:171], 0\n\t"
    "buffer_load_dwordx4 a[32:35], v241, s[88:91], s47 offen sc1\n\t"
    "v_mfma_f32_16x16x32_bf16 a[136:139], v[152:155], a[152:155], a[136:139]\n\t"
    "v_mul_f32 v10, v232, v10\n\t"
    "v_mul_f32 v11, v232, v11\n\t"
    "v_mfma_f32_16x16x32_bf16 a[132:135], v[152:155], a[148:151], 0\n\t"
    "ds_read_b64_tr_b16 v[140:141], v197 offset:8192\n\t"
    "v_mul_f32 v24, v232, v24\n\t"
    "v_mfma_f32_16x16x32_bf16 a[132:135], v[156:159], a[164:167], a[132:135]\n\t"
    "ds_read_b64_tr_b16 v[142:143], v197 offset:12288\n\t"
    "v_mul_f32 v25, v232, v25\n\t"
    "v_mfma_f32_16x16x32_bf16 a[128:131], v[156:159], a[160:163], 0\n\t"
    "v_mul_f32 v26, v232, v26\n\t"
    "v_mul_f32 v27, v232, v27\n\t"
    "v_mfma_f32_16x16x32_bf16 a[128:131], v[152:155], a[144:147], a[128:131]\n\t"
    "s_waitcnt lgkmcnt(4)\n\t"
    "v_cvt_pk_bf16_f32 v8, v8, v9\n\t"
    "v_cvt_pk_bf16_f32 v9, v10, v11\n\t"
    "v_mfma_f32_16x16x32_bf16 a[16:19], v[128:131], a[144:147], 0\n\t"
    "ds_read_b64_tr_b16 v[144:145], v198 offset:0\n\t"
    "v_cvt_pk_bf16_f32 v10, v24, v25\n\t"
    "v_mfma_f32_16x16x32_bf16 a[16:19], v[132:135], a[160:163], a[16:19]\n\t"
    "ds_read_b64_tr_b16 v[146:147], v198 offset:4096\n\t"
    "v_cvt_pk_bf16_f32 v11, v26, v27\n\t"
    "v_mfma_f32_16x16x32_bf16 a[20:23], v[132:135], a[164:167], 0\n\t"
    "buffer_load_dwordx4 a[4:7], v246, s[88:91], s46 offen sc1\n\t"
    "v_mfma_f32_16x16x32_bf16 a[20:23], v[128:131], a[148:151], a[20:23]\n\t"
    "v_mfma_f32_16x16x32_bf16 a[24:27], v[128:131], a[152:155], 0\n\t"
    "ds_read_b64_tr_b16 v[148:149], v198 offset:8192\n\t"
    "v_mfma_f32_16x16x32_bf16 a[24:27], v[132:135], a[168:171], a[24:27]\n\t"
    "ds_read_b64_tr_b16 v[150:151], v198 offset:12288\n\t"
    "v_mfma_f32_16x16x32_bf16 a[28:31], v[132:135], a[172:175], 0\n\t"
    "buffer_load_dwordx4 a[36:39], v246, s[88:91], s47 offen sc1\n\t"
    "v_mfma_f32_16x16x32_bf16 a[28:31], v[128:131], a[156:159], a[28:31]\n\t"
    "s_waitcnt lgkmcnt(4)\n\t"
    "buffer_store_dwordx4 v[8:11], v248, s[88:91], s92 offen offset:2048 sc1\n\t"
    "v_mfma_f32_16x16x32_bf16 a[60:63], v[136:139], a[156:159], 0\n\t"
    "ds_read_b64_tr_b16 v[152:153], v199 offset:0\n\t"
    "v_mul_f32 v12, v236, v12\n\t"
    "v_mfma_f32_16x16x32_bf16 a[60:63], v[140:143], a[172:175], a[60:63]\n\t"
    "ds_read_b64_tr_b16 v[154:155], v199 offset:4096\n\t"
    "v_mul_f32 v13, v236, v13\n\t"
    "v_mfma_f32_16x16x32_bf16 a[56:59], v[140:143], a[168:171], 0\n\t"
    "buffer_load_dwordx4 a[8:11], v247, s[88:91], s46 offen sc1\n\t"
    "v_mfma_f32_16x16x32_bf16 a[56:59], v[136:139], a[152:155], a[56:59]\n\t"
    "v_mul_f32 v14, v236, v14\n\t"
    "v_mul_f32 v15, v236, v15\n\t"
    "v_mfma_f32_16x16x32_bf16 a[52:55], v[136:139], a[148:151], 0\n\t"
    "ds_read_b64_tr_b16 v[156:157], v199 offset:8192\n\t"
    "v_mul_f32 v28, v236, v28\n\t"
    "v_mfma_f32_16x16x32_bf16 a[52:55], v[140:143], a[164:167], a[52:55]\n\t"
    "ds_read_b64_tr_b16 v[158:159], v199 offset:12288\n\t"
    "v_mul_f32 v29, v236, v29\n\t"
    "v_mfma_f32_16x16x32_bf16 a[48:51], v[140:143], a[160:163], 0\n\t"
    "v_mul_f32 v30, v236, v30\n\t"
    "v_mul_f32 v31, v236, v31\n\t"
    "v_mfma_f32_16x16x32_bf16 a[48:51], v[136:139], a[144:147], a[48:51]\n\t"
    "s_waitcnt lgkmcnt(4)\n\t"
    "s_barrier\n\t"
    "v_mfma_f32_16x16x32_bf16 a[80:83], v[144:147], a[144:147], 0\n\t"
    "ds_read_b64_tr_b16 v[128:129], v192 offset:16384\n\t"
    "v_add_u32 v252, 1024, v252\n\t"
    "v_mfma_f32_16x16x32_bf16 a[80:83], v[148:151], a[160:163], a[80:83]\n\t"
    "ds_read_b64_tr_b16 v[130:131], v192 offset:20480\n\t"
    "v_cvt_pk_bf16_f32 v12, v12, v13\n\t"
    "v_mfma_f32_16x16x32_bf16 a[84:87], v[148:151], a[164:167], 0\n\t"
    "buffer_load_dwordx4 a[40:43], v247, s[88:91], s47 offen sc1\n\t"
    "v_mfma_f32_16x16x32_bf16 a[84:87], v[144:147], a[148:151], a[84:87]\n\t"
    "v_cvt_pk_bf16_f32 v13, v14, v15\n\t"
    "v_cvt_pk_bf16_f32 v14, v28, v29\n\t"
    "v_mfma_f32_16x16x32_bf16 a[88:91], v[144:147], a[152:155], 0\n\t"
    "ds_read_b64_tr_b16 v[132:133], v192 offset:24576\n\t"
    "v_cvt_pk_bf16_f32 v15, v30, v31\n\t"
    "v_mfma_f32_16x16x32_bf16 a[88:91], v[148:151], a[168:171], a[88:91]\n\t"
    "ds_read_b64_tr_b16 v[134:135], v192 offset:28672\n\t"
    "v_mfma_f32_16x16x32_bf16 a[92:95], v[148:151], a[172:175], 0\n\t"
    "buffer_load_dwordx4 a[12:15], v249, s[88:91], s46 offen sc1\n\t"
    "v_mfma_f32_16x16x32_bf16 a[92:95], v[144:147], a[156:159], a[92:95]\n\t"
    "s_waitcnt lgkmcnt(4)\n\t"
    "buffer_store_dwordx4 v[12:15], v248, s[88:91], s92 offen offset:3072 sc1\n\t"
    "v_mfma_f32_16x16x32_bf16 a[124:127], v[152:155], a[156:159], 0\n\t"
    "ds_read_b64_tr_b16 v[136:137], v193 offset:16384\n\t"
    "v_mul_f32 v32, v224, v32\n\t"
    "v_mfma_f32_16x16x32_bf16 a[124:127], v[156:159], a[172:175], a[124:127]\n\t"
    "ds_read_b64_tr_b16 v[138:139], v193 offset:20480\n\t"
    "v_mul_f32 v33, v224, v33\n\t"
    "v_mfma_f32_16x16x32_bf16 a[120:123], v[156:159], a[168:171], 0\n\t"
    "buffer_load_dwordx4 a[44:47], v249, s[88:91], s47 offen sc1\n\t"
    "v_mfma_f32_16x16x32_bf16 a[120:123], v[152:155], a[152:155], a[120:123]\n\t"
    "s_bfe_u32 s46, s14, 262156\n\t"
    "s_lshl_b32 s46, s46, 13\n\t"
    "s_add_u32 s46, s46, s60\n\t"
    "v_mfma_f32_16x16x32_bf16 a[116:119], v[152:155], a[148:151], 0\n\t"
    "ds_read_b64_tr_b16 v[140:141], v193 offset:24576\n\t"
    "s_add_u32 s47, s46, 4096\n\t"
    "v_mfma_f32_16x16x32_bf16 a[116:119], v[156:159], a[164:167], a[116:119]\n\t"
    "ds_read_b64_tr_b16 v[142:143], v193 offset:28672\n\t"
    "v_mul_f32 v34, v224, v34\n\t"
    "v_mfma_f32_16x16x32_bf16 a[112:115], v[156:159], a[160:163], 0\n\t"
    "v_mul_f32 v35, v224, v35\n\t"
    "v_mul_f32 v48, v224, v48\n\t"
    "v_mfma_f32_16x16x32_bf16 a[112:115], v[152:155], a[144:147], a[112:115]\n\t"
    "s_waitcnt vmcnt(12) lgkmcnt(4)\n\t"
    "v_mul_f32 v49, v224, v49\n\t"
    "v_mul_f32 v50, v224, v50\n\t"
    "v_mfma_f32_16x16x32_bf16 a[208:211], v[128:131], a[176:179], a[208:211]\n\t"
    "ds_read_b64_tr_b16 v[144:145], v194 offset:16384\n\t"
    "v_mul_f32 v51, v224, v51\n\t"
    "v_mfma_f32_16x16x32_bf16 a[208:211], v[132:135], a[192:195], a[208:211]\n\t"
    "ds_read_b64_tr_b16 v[146:147], v194 offset:20480\n\t"
    "v_cvt_pk_bf16_f32 v32, v32, v33\n\t"
    "v_mfma_f32_16x16x32_bf16 a[212:215], v[132:135], a[196:199], a[212:215]\n\t"
    "v_cvt_pk_bf16_f32 v33, v34, v35\n\t"
    "v_cvt_pk_bf16_f32 v34, v48, v49\n\t"
    "v_mfma_f32_16x16x32_bf16 a[212:215], v[128:131], a[180:183], a[212:215]\n\t"
    "v_cvt_pk_bf16_f32 v35, v50, v51\n\t"
    "v_mfma_f32_16x16x32_bf16 a[216:219], v[128:131], a[184:187], a[216:219]\n\t"
    "ds_read_b64_tr_b16 v[148:149], v194 offset:24576\n\t"
    "v_mfma_f32_16x16x32_bf16 a[216:219], v[132:135], a[200:203], a[216:219]\n\t"
    "ds_read_b64_tr_b16 v[150:151], v194 offset:28672\n\t"
    "v_mfma_f32_16x16x32_bf16 a[220:223], v[132:135], a[204:207], a[220:223]\n\t"
    "v_mfma_f32_16x16x32_bf16 a[220:223], v[128:131], a[188:191], a[220:223]\n\t"
    "s_waitcnt lgkmcnt(4)\n\t"
    "buffer_store_dwordx4 v[32:35], v248, s[88:91], s93 offen sc1\n\t"
    "v_mfma_f32_16x16x32_bf16 a[236:239], v[136:139], a[188:191], a[236:239]\n\t"
    "ds_read_b64_tr_b16 v[152:153], v195 offset:16384\n\t"
    "v_mul_f32 v36, v228, v36\n\t"
    "v_mfma_f32_16x16x32_bf16 a[236:239], v[140:143], a[204:207], a[236:239]\n\t"
    "ds_read_b64_tr_b16 v[154:155], v195 offset:20480\n\t"
    "v_mul_f32 v37, v228, v37\n\t"
    "v_mfma_f32_16x16x32_bf16 a[232:235], v[140:143], a[200:203], a[232:235]\n\t"
    "v_mul_f32 v38, v228, v38\n\t"
    "v_mul_f32 v39, v228, v39\n\t"
    "v_mfma_f32_16x16x32_bf16 a[232:235], v[136:139], a[184:187], a[232:235]\n\t"
    "v_mul_f32 v52, v228, v52\n\t"
    "v_mul_f32 v53, v228, v53\n\t"
    "v_mfma_f32_16x16x32_bf16 a[228:231], v[136:139], a[180:183], a[228:231]\n\t"
    "ds_read_b64_tr_b16 v[156:157], v195 offset:24576\n\t"
    "v_mul_f32 v54, v228, v54\n\t"
    "v_mfma_f32_16x16x32_bf16 a[228:231], v[140:143], a[196:199], a[228:231]\n\t"
    "ds_read_b64_tr_b16 v[158:159], v195 offset:28672\n\t"
    "v_mul_f32 v55, v228, v55\n\t"
    "v_mfma_f32_16x16x32_bf16 a[224:227], v[140:143], a[192:195], a[224:227]\n\t"
    "v_cvt_pk_bf16_f32 v36, v36, v37\n\t"
    "v_cvt_pk_bf16_f32 v37, v38, v39\n\t"
    "v_mfma_f32_16x16x32_bf16 a[224:227], v[136:139], a[176:179], a[224:227]\n\t"
    "s_waitcnt lgkmcnt(4)\n\t"
    "v_cvt_pk_bf16_f32 v38, v52, v53\n\t"
    "v_cvt_pk_bf16_f32 v39, v54, v55\n\t"
    "v_mfma_f32_16x16x32_bf16 a[240:243], v[144:147], a[176:179], a[240:243]\n\t"
    "ds_read_b64_tr_b16 v[128:129], v196 offset:16384\n\t"
    "v_mfma_f32_16x16x32_bf16 a[240:243], v[148:151], a[192:195], a[240:243]\n\t"
    "ds_read_b64_tr_b16 v[130:131], v196 offset:20480\n\t"
    "v_mfma_f32_16x16x32_bf16 a[244:247], v[148:151], a[196:199], a[244:247]\n\t"
    "v_mfma_f32_16x16x32_bf16 a[244:247], v[144:147], a[180:183], a[244:247]\n\t"
    "v_mfma_f32_16x16x32_bf16 a[248:251], v[144:147], a[184:187], a[248:251]\n\t"
    "ds_read_b64_tr_b16 v[132:133], v196 offset:24576\n\t"
    "v_mfma_f32_16x16x32_bf16 a[248:251], v[148:151], a[200:203], a[248:251]\n\t"
    "ds_read_b64_tr_b16 v[134:135], v196 offset:28672\n\t"
    "v_mfma_f32_16x16x32_bf16 a[252:255], v[148:151], a[204:207], a[252:255]\n\t"
    "buffer_load_dwordx4 a[64:67], v241, s[88:91], s46 offen sc1\n\t"
    "v_mfma_f32_16x16x32_bf16 a[252:255], v[144:147], a[188:191], a[252:255]\n\t"
    "s_waitcnt lgkmcnt(4)\n\t"
    "buffer_store_dwordx4 v[36:39], v248, s[88:91], s93 offen offset:1024 sc1\n\t"
    "v_mfma_f32_16x16x32_bf16 a[140:143], v[152:155], a[188:191], a[140:143]\n\t"
    "ds_read_b64_tr_b16 v[136:137], v197 offset:16384\n\t"
    "v_mul_f32 v40, v232, v40\n\t"
    "v_mfma_f32_16x16x32_bf16 a[140:143], v[156:159], a[204:207], a[140:143]\n\t"
    "ds_read_b64_tr_b16 v[138:139], v197 offset:20480\n\t"
    "v_mul_f32 v41, v232, v41\n\t"
    "v_mfma_f32_16x16x32_bf16 a[136:139], v[156:159], a[200:203], a[136:139]\n\t"
    "buffer_load_dwordx4 a[96:99], v241, s[88:91], s47 offen sc1\n\t"
    "v_mfma_f32_16x16x32_bf16 a[136:139], v[152:155], a[184:187], a[136:139]\n\t"
    "v_mul_f32 v42, v232, v42\n\t"
    "v_mul_f32 v43, v232, v43\n\t"
    "v_mfma_f32_16x16x32_bf16 a[132:135], v[152:155], a[180:183], a[132:135]\n\t"
    "ds_read_b64_tr_b16 v[140:141], v197 offset:24576\n\t"
    "v_mul_f32 v56, v232, v56\n\t"
    "v_mfma_f32_16x16x32_bf16 a[132:135], v[156:159], a[196:199], a[132:135]\n\t"
    "ds_read_b64_tr_b16 v[142:143], v197 offset:28672\n\t"
    "v_mul_f32 v57, v232, v57\n\t"
    "v_mfma_f32_16x16x32_bf16 a[128:131], v[156:159], a[192:195], a[128:131]\n\t"
    "v_mul_f32 v58, v232, v58\n\t"
    "v_mul_f32 v59, v232, v59\n\t"
    "v_mfma_f32_16x16x32_bf16 a[128:131], v[152:155], a[176:179], a[128:131]\n\t"
    "s_waitcnt lgkmcnt(4)\n\t"
    "v_cvt_pk_bf16_f32 v40, v40, v41\n\t"
    "v_cvt_pk_bf16_f32 v41, v42, v43\n\t"
    "v_mfma_f32_16x16x32_bf16 a[16:19], v[128:131], a[176:179], a[16:19]\n\t"
    "ds_read_b64_tr_b16 v[144:145], v198 offset:16384\n\t"
    "v_cvt_pk_bf16_f32 v42, v56, v57\n\t"
    "v_mfma_f32_16x16x32_bf16 a[16:19], v[132:135], a[192:195], a[16:19]\n\t"
    "ds_read_b64_tr_b16 v[146:147], v198 offset:20480\n\t"
    "v_cvt_pk_bf16_f32 v43, v58, v59\n\t"
    "v_mfma_f32_16x16x32_bf16 a[20:23], v[132:135], a[196:199], a[20:23]\n\t"
    "buffer_load_dwordx4 a[68:71], v246, s[88:91], s46 offen sc1\n\t"
    "v_mfma_f32_16x16x32_bf16 a[20:23], v[128:131], a[180:183], a[20:23]\n\t"
    "v_mfma_f32_16x16x32_bf16 a[24:27], v[128:131], a[184:187], a[24:27]\n\t"
    "ds_read_b64_tr_b16 v[148:149], v198 offset:24576\n\t"
    "v_mfma_f32_16x16x32_bf16 a[24:27], v[132:135], a[200:203], a[24:27]\n\t"
    "ds_read_b64_tr_b16 v[150:151], v198 offset:28672\n\t"
    "v_mfma_f32_16x16x32_bf16 a[28:31], v[132:135], a[204:207], a[28:31]\n\t"
    "buffer_load_dwordx4 a[100:103], v246, s[88:91], s47 offen sc1\n\t"
    "v_mfma_f32_16x16x32_bf16 a[28:31], v[128:131], a[188:191], a[28:31]\n\t"
    "s_waitcnt lgkmcnt(4)\n\t"
    "buffer_store_dwordx4 v[40:43], v248, s[88:91], s93 offen offset:2048 sc1\n\t"
    "v_mfma_f32_16x16x32_bf16 a[60:63], v[136:139], a[188:191], a[60:63]\n\t"
    "ds_read_b64_tr_b16 v[152:153], v199 offset:16384\n\t"
    "v_mul_f32 v44, v236, v44\n\t"
    "v_mfma_f32_16x16x32_bf16 a[60:63], v[140:143], a[204:207], a[60:63]\n\t"
    "ds_read_b64_tr_b16 v[154:155], v199 offset:20480\n\t"
    "v_mul_f32 v45, v236, v45\n\t"
    "v_mfma_f32_16x16x32_bf16 a[56:59], v[140:143], a[200:203], a[56:59]\n\t"
    "buffer_load_dwordx4 a[72:75], v247, s[88:91], s46 offen sc1\n\t"
    "v_mfma_f32_16x16x32_bf16 a[56:59], v[136:139], a[184:187], a[56:59]\n\t"
    "v_mul_f32 v46, v236, v46\n\t"
    "v_mul_f32 v47, v236, v47\n\t"
    "v_mfma_f32_16x16x32_bf16 a[52:55], v[136:139], a[180:183], a[52:55]\n\t"
    "ds_read_b64_tr_b16 v[156:157], v199 offset:24576\n\t"
    "v_mul_f32 v60, v236, v60\n\t"
    "v_mfma_f32_16x16x32_bf16 a[52:55], v[140:143], a[196:199], a[52:55]\n\t"
    "ds_read_b64_tr_b16 v[158:159], v199 offset:28672\n\t"
    "v_mul_f32 v61, v236, v61\n\t"
    "v_mfma_f32_16x16x32_bf16 a[48:51], v[140:143], a[192:195], a[48:51]\n\t"
    "v_mul_f32 v62, v236, v62\n\t"
    "v_mul_f32 v63, v236, v63\n\t"
    "v_mfma_f32_16x16x32_bf16 a[48:51], v[136:139], a[176:179], a[48:51]\n\t"
    "s_waitcnt lgkmcnt(4)\n\t"
    "s_barrier\n\t"
    "v_mfma_f32_16x16x32_bf16 a[80:83], v[144:147], a[176:179], a[80:83]\n\t"
    "ds_read_b64_tr_b16 v[128:129], v192 offset:32768\n\t"
    "v_cvt_pk_bf16_f32 v44, v44, v45\n\t"
    "v_mfma_f32_16x16x32_bf16 a[80:83], v[148:151], a[192:195], a[80:83]\n\t"
    "ds_read_b64_tr_b16 v[130:131], v192 offset:36864\n\t"
    "v_cvt_pk_bf16_f32 v45, v46, v47\n\t"
    "v_mfma_f32_16x16x32_bf16 a[84:87], v[148:151], a[196:199], a[84:87]\n\t"
    "buffer_load_dwordx4 a[104:107], v247, s[88:91], s47 offen sc1\n\t"
    "v_mfma_f32_16x16x32_bf16 a[84:87], v[144:147], a[180:183], a[84:87]\n\t"
    "v_cvt_pk_bf16_f32 v46, v60, v61\n\t"
    "v_cvt_pk_bf16_f32 v47, v62, v63\n\t"
    "v_mfma_f32_16x16x32_bf16 a[88:91], v[144:147], a[184:187], a[88:91]\n\t"
    "ds_read_b64_tr_b16 v[132:133], v192 offset:40960\n\t"
    "v_mfma_f32_16x16x32_bf16 a[88:91], v[148:151], a[200:203], a[88:91]\n\t"
    "ds_read_b64_tr_b16 v[134:135], v192 offset:45056\n\t"
    "v_mfma_f32_16x16x32_bf16 a[92:95], v[148:151], a[204:207], a[92:95]\n\t"
    "buffer_load_dwordx4 a[76:79], v249, s[88:91], s46 offen sc1\n\t"
    "v_mfma_f32_16x16x32_bf16 a[92:95], v[144:147], a[188:191], a[92:95]\n\t"
    "s_waitcnt lgkmcnt(4)\n\t"
    "v_mfma_f32_16x16x32_bf16 a[124:127], v[152:155], a[188:191], a[124:127]\n\t"
    "ds_read_b64_tr_b16 v[136:137], v193 offset:32768\n\t"
    "v_mfma_f32_16x16x32_bf16 a[124:127], v[156:159], a[204:207], a[124:127]\n\t"
    "ds_read_b64_tr_b16 v[138:139], v193 offset:36864\n\t"
    "v_mfma_f32_16x16x32_bf16 a[120:123], v[156:159], a[200:203], a[120:123]\n\t"
    "buffer_load_dwordx4 a[108:111], v249, s[88:91], s47 offen sc1\n\t"
    "v_mfma_f32_16x16x32_bf16 a[120:123], v[152:155], a[184:187], a[120:123]\n\t"
    "s_bfe_u32 s84, s14, 262160\n\t"
    "s_lshl_b32 s84, s84, 17\n\t"
    "s_bfe_u32 s46, s14, 262160\n\t"
    "v_mfma_f32_16x16x32_bf16 a[116:119], v[152:155], a[180:183], a[116:119]\n\t"
    "ds_read_b64_tr_b16 v[140:141], v193 offset:40960\n\t"
    "s_lshl_b32 s46, s46, 13\n\t"
    "v_mfma_f32_16x16x32_bf16 a[116:119], v[156:159], a[196:199], a[116:119]\n\t"
    "ds_read_b64_tr_b16 v[142:143], v193 offset:45056\n\t"
    "s_add_u32 s46, s46, s60\n\t"
    "v_mfma_f32_16x16x32_bf16 a[112:115], v[156:159], a[192:195], a[112:115]\n\t"
    "s_add_u32 s47, s46, 4096\n\t"
    "v_mfma_f32_16x16x32_bf16 a[112:115], v[152:155], a[176:179], a[112:115]\n\t"
    "s_waitcnt vmcnt(11) lgkmcnt(4)\n\t"
    "v_mfma_f32_16x16x32_bf16 a[208:211], v[128:131], a[0:3], a[208:211]\n\t"
    "ds_read_b64_tr_b16 v[144:145], v194 offset:32768\n\t"
    "v_mfma_f32_16x16x32_bf16 a[208:211], v[132:135], a[32:35], a[208:211]\n\t"
    "ds_read_b64_tr_b16 v[146:147], v194 offset:36864\n\t"
    "s_add_u32 m0, s48, 49152\n\t"
    "v_mfma_f32_16x16x32_bf16 a[212:215], v[132:135], a[36:39], a[212:215]\n\t"
    "buffer_load_dwordx4 v242, s[32:35], s84 offen sc1 lds\n\t"
    "v_mfma_f32_16x16x32_bf16 a[212:215], v[128:131], a[4:7], a[212:215]\n\t"
    "v_mfma_f32_16x16x32_bf16 a[216:219], v[128:131], a[8:11], a[216:219]\n\t"
    "ds_read_b64_tr_b16 v[148:149], v194 offset:40960\n\t"
    "v_mfma_f32_16x16x32_bf16 a[216:219], v[132:135], a[40:43], a[216:219]\n\t"
    "ds_read_b64_tr_b16 v[150:151], v194 offset:45056\n\t"
    "v_mfma_f32_16x16x32_bf16 a[220:223], v[132:135], a[44:47], a[220:223]\n\t"
    "buffer_load_dwordx4 v243, s[32:35], s84 offen offset:1024 sc1 lds\n\t"
    "v_mfma_f32_16x16x32_bf16 a[220:223], v[128:131], a[12:15], a[220:223]\n\t"
    "s_waitcnt lgkmcnt(4)\n\t"
    "v_mfma_f32_16x16x32_bf16 a[236:239], v[136:139], a[12:15], a[236:239]\n\t"
    "ds_read_b64_tr_b16 v[152:153], v195 offset:32768\n\t"
    "v_mfma_f32_16x16x32_bf16 a[236:239], v[140:143], a[44:47], a[236:239]\n\t"
    "ds_read_b64_tr_b16 v[154:155], v195 offset:36864\n\t"
    "v_mfma_f32_16x16x32_bf16 a[232:235], v[140:143], a[40:43], a[232:235]\n\t"
    "buffer_load_dwordx4 v244, s[32:35], s84 offen offset:2048 sc1 lds\n\t"
    "v_mfma_f32_16x16x32_bf16 a[232:235], v[136:139], a[8:11], a[232:235]\n\t"
    "buffer_store_dwordx4 v[44:47], v248, s[88:91], s93 offen offset:3072 sc1\n\t"
    "v_mfma_f32_16x16x32_bf16 a[228:231], v[136:139], a[4:7], a[228:231]\n\t"
    "ds_read_b64_tr_b16 v[156:157], v195 offset:40960\n\t"
    "v_mul_f32 v64, v224, v64\n\t"
    "v_mfma_f32_16x16x32_bf16 a[228:231], v[140:143], a[36:39], a[228:231]\n\t"
    "ds_read_b64_tr_b16 v[158:159], v195 offset:45056\n\t"
    "v_mul_f32 v65, v224, v65\n\t"
    "v_mfma_f32_16x16x32_bf16 a[224:227], v[140:143], a[32:35], a[224:227]\n\t"
    "v_mul_f32 v66, v224, v66\n\t"
    "v_mul_f32 v67, v224, v67\n\t"
    "v_mfma_f32_16x16x32_bf16 a[224:227], v[136:139], a[0:3], a[224:227]\n\t"
    "s_waitcnt lgkmcnt(4)\n\t"
    "v_mul_f32 v80, v224, v80\n\t"
    "v_mul_f32 v81, v224, v81\n\t"
    "v_mfma_f32_16x16x32_bf16 a[240:243], v[144:147], a[0:3], a[240:243]\n\t"
    "ds_read_b64_tr_b16 v[128:129], v196 offset:32768\n\t"
    "v_mul_f32 v82, v224, v82\n\t"
    "v_mfma_f32_16x16x32_bf16 a[240:243], v[148:151], a[32:35], a[240:243]\n\t"
    "ds_read_b64_tr_b16 v[130:131], v196 offset:36864\n\t"
    "v_mul_f32 v83, v224, v83\n\t"
    "v_mfma_f32_16x16x32_bf16 a[244:247], v[148:151], a[36:39], a[244:247]\n\t"
    "buffer_load_dwordx4 v245, s[32:35], s84 offen offset:3072 sc1 lds\n\t"
    "v_mfma_f32_16x16x32_bf16 a[244:247], v[144:147], a[4:7], a[244:247]\n\t"
    "v_cvt_pk_bf16_f32 v64, v64, v65\n\t"
    "v_cvt_pk_bf16_f32 v65, v66, v67\n\t"
    "v_mfma_f32_16x16x32_bf16 a[248:251], v[144:147], a[8:11], a[248:251]\n\t"
    "ds_read_b64_tr_b16 v[132:133], v196 offset:40960\n\t"
    "v_cvt_pk_bf16_f32 v66, v80, v81\n\t"
    "v_mfma_f32_16x16x32_bf16 a[248:251], v[148:151], a[40:43], a[248:251]\n\t"
    "ds_read_b64_tr_b16 v[134:135], v196 offset:45056\n\t"
    "v_cvt_pk_bf16_f32 v67, v82, v83\n\t"
    "v_mfma_f32_16x16x32_bf16 a[252:255], v[148:151], a[44:47], a[252:255]\n\t"
    "buffer_load_dwordx4 a[144:147], v241, s[88:91], s46 offen sc1\n\t"
    "v_mfma_f32_16x16x32_bf16 a[252:255], v[144:147], a[12:15], a[252:255]\n\t"
    "s_waitcnt lgkmcnt(4)\n\t"
    "v_mfma_f32_16x16x32_bf16 a[140:143], v[152:155], a[12:15], a[140:143]\n\t"
    "ds_read_b64_tr_b16 v[136:137], v197 offset:32768\n\t"
    "v_mfma_f32_16x16x32_bf16 a[140:143], v[156:159], a[44:47], a[140:143]\n\t"
    "ds_read_b64_tr_b16 v[138:139], v197 offset:36864\n\t"
    "v_mfma_f32_16x16x32_bf16 a[136:139], v[156:159], a[40:43], a[136:139]\n\t"
    "buffer_load_dwordx4 a[160:163], v241, s[88:91], s47 offen sc1\n\t"
    "v_mfma_f32_16x16x32_bf16 a[136:139], v[152:155], a[8:11], a[136:139]\n\t"
    "v_mfma_f32_16x16x32_bf16 a[132:135], v[152:155], a[4:7], a[132:135]\n\t"
    "ds_read_b64_tr_b16 v[140:141], v197 offset:40960\n\t"
    "v_mfma_f32_16x16x32_bf16 a[132:135], v[156:159], a[36:39], a[132:135]\n\t"
    "ds_read_b64_tr_b16 v[142:143], v197 offset:45056\n\t"
    "v_mfma_f32_16x16x32_bf16 a[128:131], v[156:159], a[32:35], a[128:131]\n\t"
    "v_mfma_f32_16x16x32_bf16 a[128:131], v[152:155], a[0:3], a[128:131]\n\t"
    "s_waitcnt lgkmcnt(4)\n\t"
    "v_mfma_f32_16x16x32_bf16 a[16:19], v[128:131], a[0:3], a[16:19]\n\t"
    "ds_read_b64_tr_b16 v[144:145], v198 offset:32768\n\t"
    "v_mfma_f32_16x16x32_bf16 a[16:19], v[132:135], a[32:35], a[16:19]\n\t"
    "ds_read_b64_tr_b16 v[146:147], v198 offset:36864\n\t"
    "v_mfma_f32_16x16x32_bf16 a[20:23], v[132:135], a[36:39], a[20:23]\n\t"
    "buffer_load_dwordx4 a[148:151], v246, s[88:91], s46 offen sc1\n\t"
    "v_mfma_f32_16x16x32_bf16 a[20:23], v[128:131], a[4:7], a[20:23]\n\t"
    "v_mfma_f32_16x16x32_bf16 a[24:27], v[128:131], a[8:11], a[24:27]\n\t"
    "ds_read_b64_tr_b16 v[148:149], v198 offset:40960\n\t"
    "v_mfma_f32_16x16x32_bf16 a[24:27], v[132:135], a[40:43], a[24:27]\n\t"
    "ds_read_b64_tr_b16 v[150:151], v198 offset:45056\n\t"
    "v_mfma_f32_16x16x32_bf16 a[28:31], v[132:135], a[44:47], a[28:31]\n\t"
    "buffer_load_dwordx4 a[164:167], v246, s[88:91], s47 offen sc1\n\t"
    "v_mfma_f32_16x16x32_bf16 a[28:31], v[128:131], a[12:15], a[28:31]\n\t"
    "s_waitcnt lgkmcnt(4)\n\t"
    "v_mfma_f32_16x16x32_bf16 a[60:63], v[136:139], a[12:15], a[60:63]\n\t"
    "ds_read_b64_tr_b16 v[152:153], v199 offset:32768\n\t"
    "v_mfma_f32_16x16x32_bf16 a[60:63], v[140:143], a[44:47], a[60:63]\n\t"
    "ds_read_b64_tr_b16 v[154:155], v199 offset:36864\n\t"
    "v_mfma_f32_16x16x32_bf16 a[56:59], v[140:143], a[40:43], a[56:59]\n\t"
    "buffer_load_dwordx4 a[152:155], v247, s[88:91], s46 offen sc1\n\t"
    "v_mfma_f32_16x16x32_bf16 a[56:59], v[136:139], a[8:11], a[56:59]\n\t"
    "v_mfma_f32_16x16x32_bf16 a[52:55], v[136:139], a[4:7], a[52:55]\n\t"
    "ds_read_b64_tr_b16 v[156:157], v199 offset:40960\n\t"
    "v_mfma_f32_16x16x32_bf16 a[52:55], v[140:143], a[36:39], a[52:55]\n\t"
    "ds_read_b64_tr_b16 v[158:159], v199 offset:45056\n\t"
    "v_mfma_f32_16x16x32_bf16 a[48:51], v[140:143], a[32:35], a[48:51]\n\t"
    "v_mfma_f32_16x16x32_bf16 a[48:51], v[136:139], a[0:3], a[48:51]\n\t"
    "s_waitcnt lgkmcnt(4)\n\t"
    "s_barrier\n\t"
    "v_mfma_f32_16x16x32_bf16 a[80:83], v[144:147], a[0:3], a[80:83]\n\t"
    "ds_read_b64_tr_b16 v[128:129], v192 offset:49152\n\t"
    "v_mfma_f32_16x16x32_bf16 a[80:83], v[148:151], a[32:35], a[80:83]\n\t"
    "ds_read_b64_tr_b16 v[130:131], v192 offset:53248\n\t"
    "v_mfma_f32_16x16x32_bf16 a[84:87], v[148:151], a[36:39], a[84:87]\n\t"
    "buffer_load_dwordx4 a[168:171], v247, s[88:91], s47 offen sc1\n\t"
    "v_mfma_f32_16x16x32_bf16 a[84:87], v[144:147], a[4:7], a[84:87]\n\t"
    "buffer_store_dwordx4 v[64:67], v248, s[88:91], s94 offen sc1\n\t"
    "v_mfma_f32_16x16x32_bf16 a[88:91], v[144:147], a[8:11], a[88:91]\n\t"
    "ds_read_b64_tr_b16 v[132:133], v192 offset:57344\n\t"
    "v_mul_f32 v68, v228, v68\n\t"
    "v_mfma_f32_16x16x32_bf16 a[88:91], v[148:151], a[40:43], a[88:91]\n\t"
    "ds_read_b64_tr_b16 v[134:135], v192 offset:61440\n\t"
    "v_mul_f32 v69, v228, v69\n\t"
    "v_mfma_f32_16x16x32_bf16 a[92:95], v[148:151], a[44:47], a[92:95]\n\t"
    "buffer_load_dwordx4 a[156:159], v249, s[88:91], s46 offen sc1\n\t"
    "v_mfma_f32_16x16x32_bf16 a[92:95], v[144:147], a[12:15], a[92:95]\n\t"
    "s_waitcnt lgkmcnt(4)\n\t"
    "v_mul_f32 v70, v228, v70\n\t"
    "v_mul_f32 v71, v228, v71\n\t"
    "v_mfma_f32_16x16x32_bf16 a[124:127], v[152:155], a[12:15], a[124:127]\n\t"
    "ds_read_b64_tr_b16 v[136:137], v193 offset:49152\n\t"
    "v_mul_f32 v84, v228, v84\n\t"
    "v_mfma_f32_16x16x32_bf16 a[124:127], v[156:159], a[44:47], a[124:127]\n\t"
    "ds_read_b64_tr_b16 v[138:139], v193 offset:53248\n\t"
    "v_mul_f32 v85, v228, v85\n\t"
    "v_mfma_f32_16x16x32_bf16 a[120:123], v[156:159], a[40:43], a[120:123]\n\t"
    "buffer_load_dwordx4 a[172:175], v249, s[88:91], s47 offen sc1\n\t"
    "v_mfma_f32_16x16x32_bf16 a[120:123], v[152:155], a[8:11], a[120:123]\n\t"
    "s_bfe_u32 s84, s14, 262164\n\t"
    "s_lshl_b32 s84, s84, 17\n\t"
    "s_bfe_u32 s46, s14, 262164\n\t"
    "v_mfma_f32_16x16x32_bf16 a[116:119], v[152:155], a[4:7], a[116:119]\n\t"
    "ds_read_b64_tr_b16 v[140:141], v193 offset:57344\n\t"
    "s_lshl_b32 s46, s46, 13\n\t"
    "v_mfma_f32_16x16x32_bf16 a[116:119], v[156:159], a[36:39], a[116:119]\n\t"
    "ds_read_b64_tr_b16 v[142:143], v193 offset:61440\n\t"
    "s_add_u32 s46, s46, s60\n\t"
    "v_mfma_f32_16x16x32_bf16 a[112:115], v[156:159], a[32:35], a[112:115]\n\t"
    "s_add_u32 s47, s46, 4096\n\t"
    "v_mul_f32 v86, v228, v86\n\t"
    "v_mul_f32 v87, v228, v87\n\t"
    "v_mfma_f32_16x16x32_bf16 a[112:115], v[152:155], a[0:3], a[112:115]\n\t"
    "s_waitcnt vmcnt(14) lgkmcnt(4)\n\t"
    "v_cvt_pk_bf16_f32 v68, v68, v69\n\t"
    "v_cvt_pk_bf16_f32 v69, v70, v71\n\t"
    "v_mfma_f32_16x16x32_bf16 a[208:211], v[128:131], a[64:67], a[208:211]\n\t"
    "ds_read_b64_tr_b16 v[144:145], v194 offset:49152\n\t"
    "v_cvt_pk_bf16_f32 v70, v84, v85\n\t"
    "v_mfma_f32_16x16x32_bf16 a[208:211], v[132:135], a[96:99], a[208:211]\n\t"
    "ds_read_b64_tr_b16 v[146:147], v194 offset:53248\n\t"
    "s_add_u32 m0, s48, 65536\n\t"
    "v_mfma_f32_16x16x32_bf16 a[212:215], v[132:135], a[100:103], a[212:215]\n\t"
    "buffer_load_dwordx4 v242, s[32:35], s84 offen sc1 lds\n\t"
    "v_mfma_f32_16x16x32_bf16 a[212:215], v[128:131], a[68:71], a[212:215]\n\t"
    "v_cvt_pk_bf16_f32 v71, v86, v87\n\t"
    "v_mfma_f32_16x16x32_bf16 a[216:219], v[128:131], a[72:75], a[216:219]\n\t"
    "ds_read_b64_tr_b16 v[148:149], v194 offset:57344\n\t"
    "v_mfma_f32_16x16x32_bf16 a[216:219], v[132:135], a[104:107], a[216:219]\n\t"
    "ds_read_b64_tr_b16 v[150:151], v194 offset:61440\n\t"
    "v_mfma_f32_16x16x32_bf16 a[220:223], v[132:135], a[108:111], a[220:223]\n\t"
    "buffer_load_dwordx4 v243, s[32:35], s84 offen offset:1024 sc1 lds\n\t"
    "v_mfma_f32_16x16x32_bf16 a[220:223], v[128:131], a[76:79], a[220:223]\n\t"
    "s_waitcnt lgkmcnt(4)\n\t"
    "v_mfma_f32_16x16x32_bf16 a[236:239], v[136:139], a[76:79], a[236:239]\n\t"
    "ds_read_b64_tr_b16 v[152:153], v195 offset:49152\n\t"
    "v_mfma_f32_16x16x32_bf16 a[236:239], v[140:143], a[108:111], a[236:239]\n\t"
    "ds_read_b64_tr_b16 v[154:155], v195 offset:53248\n\t"
    "v_mfma_f32_16x16x32_bf16 a[232:235], v[140:143], a[104:107], a[232:235]\n\t"
    "buffer_load_dwordx4 v244, s[32:35], s84 offen offset:2048 sc1 lds\n\t"
    "v_mfma_f32_16x16x32_bf16 a[232:235], v[136:139], a[72:75], a[232:235]\n\t"
    "v_mfma_f32_16x16x32_bf16 a[228:231], v[136:139], a[68:71], a[228:231]\n\t"
    "ds_read_b64_tr_b16 v[156:157], v195 offset:57344\n\t"
    "v_mfma_f32_16x16x32_bf16 a[228:231], v[140:143], a[100:103], a[228:231]\n\t"
    "ds_read_b64_tr_b16 v[158:159], v195 offset:61440\n\t"
    "v_mfma_f32_16x16x32_bf16 a[224:227], v[140:143], a[96:99], a[224:227]\n\t"
    "v_mfma_f32_16x16x32_bf16 a[224:227], v[136:139], a[64:67], a[224:227]\n\t"
    "s_waitcnt lgkmcnt(4)\n\t"
    "v_mfma_f32_16x16x32_bf16 a[240:243], v[144:147], a[64:67], a[240:243]\n\t"
    "ds_read_b64_tr_b16 v[128:129], v196 offset:49152\n\t"
    "v_mfma_f32_16x16x32_bf16 a[240:243], v[148:151], a[96:99], a[240:243]\n\t"
    "ds_read_b64_tr_b16 v[130:131], v196 offset:53248\n\t"
    "v_mfma_f32_16x16x32_bf16 a[244:247], v[148:151], a[100:103], a[244:247]\n\t"
    "buffer_load_dwordx4 v245, s[32:35], s84 offen offset:3072 sc1 lds\n\t"
    "v_mfma_f32_16x16x32_bf16 a[244:247], v[144:147], a[68:71], a[244:247]\n\t"
    "v_mfma_f32_16x16x32_bf16 a[248:251], v[144:147], a[72:75], a[248:251]\n\t"
    "ds_read_b64_tr_b16 v[132:133], v196 offset:57344\n\t"
    "v_mfma_f32_16x16x32_bf16 a[248:251], v[148:151], a[104:107], a[248:251]\n\t"
    "ds_read_b64_tr_b16 v[134:135], v196 offset:61440\n\t"
    "v_mfma_f32_16x16x32_bf16 a[252:255], v[148:151], a[108:111], a[252:255]\n\t"
    "buffer_load_dwordx4 a[176:179], v241, s[88:91], s46 offen sc1\n\t"
    "v_mfma_f32_16x16x32_bf16 a[252:255], v[144:147], a[76:79], a[252:255]\n\t"
    "s_waitcnt lgkmcnt(4)\n\t"
    "buffer_store_dwordx4 v[68:71], v248, s[88:91], s94 offen offset:1024 sc1\n\t"
    "v_mfma_f32_16x16x32_bf16 a[140:143], v[152:155], a[76:79], a[140:143]\n\t"
    "ds_read_b64_tr_b16 v[136:137], v197 offset:49152\n\t"
    "v_mul_f32 v72, v232, v72\n\t"
    "v_mfma_f32_16x16x32_bf16 a[140:143], v[156:159], a[108:111], a[140:143]\n\t"
    "ds_read_b64_tr_b16 v[138:139], v197 offset:53248\n\t"
    "v_mul_f32 v73, v232, v73\n\t"
    "v_mfma_f32_16x16x32_bf16 a[136:139], v[156:159], a[104:107], a[136:139]\n\t"
    "buffer_load_dwordx4 a[192:195], v241, s[88:91], s47 offen sc1\n\t"
    "v_mfma_f32_16x16x32_bf16 a[136:139], v[152:155], a[72:75], a[136:139]\n\t"
    "v_mul_f32 v74, v232, v74\n\t"
    "v_mul_f32 v75, v232, v75\n\t"
    "v_mfma_f32_16x16x32_bf16 a[132:135], v[152:155], a[68:71], a[132:135]\n\t"
    "ds_read_b64_tr_b16 v[140:141], v197 offset:57344\n\t"
    "v_mul_f32 v88, v232, v88\n\t"
    "v_mfma_f32_16x16x32_bf16 a[132:135], v[156:159], a[100:103], a[132:135]\n\t"
    "ds_read_b64_tr_b16 v[142:143], v197 offset:61440\n\t"
    "v_mul_f32 v89, v232, v89\n\t"
    "v_mfma_f32_16x16x32_bf16 a[128:131], v[156:159], a[96:99], a[128:131]\n\t"
    "v_mul_f32 v90, v232, v90\n\t"
    "v_mul_f32 v91, v232, v91\n\t"
    "v_mfma_f32_16x16x32_bf16 a[128:131], v[152:155], a[64:67], a[128:131]\n\t"
    "s_waitcnt lgkmcnt(4)\n\t"
    "v_cvt_pk_bf16_f32 v72, v72, v73\n\t"
    "v_cvt_pk_bf16_f32 v73, v74, v75\n\t"
    "v_mfma_f32_16x16x32_bf16 a[16:19], v[128:131], a[64:67], a[16:19]\n\t"
    "ds_read_b64_tr_b16 v[144:145], v198 offset:49152\n\t"
    "v_cvt_pk_bf16_f32 v74, v88, v89\n\t"
    "v_mfma_f32_16x16x32_bf16 a[16:19], v[132:135], a[96:99], a[16:19]\n\t"
    "ds_read_b64_tr_b16 v[146:147], v198 offset:53248\n\t"
    "v_cvt_pk_bf16_f32 v75, v90, v91\n\t"
    "v_mfma_f32_16x16x32_bf16 a[20:23], v[132:135], a[100:103], a[20:23]\n\t"
    "buffer_load_dwordx4 a[180:183], v246, s[88:91], s46 offen sc1\n\t"
    "v_mfma_f32_16x16x32_bf16 a[20:23], v[128:131], a[68:71], a[20:23]\n\t"
    "v_mfma_f32_16x16x32_bf16 a[24:27], v[128:131], a[72:75], a[24:27]\n\t"
    "ds_read_b64_tr_b16 v[148:149], v198 offset:57344\n\t"
    "v_mfma_f32_16x16x32_bf16 a[24:27], v[132:135], a[104:107], a[24:27]\n\t"
    "ds_read_b64_tr_b16 v[150:151], v198 offset:61440\n\t"
    "v_mfma_f32_16x16x32_bf16 a[28:31], v[132:135], a[108:111], a[28:31]\n\t"
    "buffer_load_dwordx4 a[196:199], v246, s[88:91], s47 offen sc1\n\t"
    "v_mfma_f32_16x16x32_bf16 a[28:31], v[128:131], a[76:79], a[28:31]\n\t"
    "s_waitcnt lgkmcnt(4)\n\t"
    "v_mfma_f32_16x16x32_bf16 a[60:63], v[136:139], a[76:79], a[60:63]\n\t"
    "ds_read_b64_tr_b16 v[152:153], v199 offset:49152\n\t"
    "v_mfma_f32_16x16x32_bf16 a[60:63], v[140:143], a[108:111], a[60:63]\n\t"
    "ds_read_b64_tr_b16 v[154:155], v199 offset:53248\n\t"
    "v_mfma_f32_16x16x32_bf16 a[56:59], v[140:143], a[104:107], a[56:59]\n\t"
    "buffer_load_dwordx4 a[184:187], v247, s[88:91], s46 offen sc1\n\t"
    "v_mfma_f32_16x16x32_bf16 a[56:59], v[136:139], a[72:75], a[56:59]\n\t"
    "v_mfma_f32_16x16x32_bf16 a[52:55], v[136:139], a[68:71], a[52:55]\n\t"
    "ds_read_b64_tr_b16 v[156:157], v199 offset:57344\n\t"
    "v_mfma_f32_16x16x32_bf16 a[52:55], v[140:143], a[100:103], a[52:55]\n\t"
    "ds_read_b64_tr_b16 v[158:159], v199 offset:61440\n\t"
    "v_mfma_f32_16x16x32_bf16 a[48:51], v[140:143], a[96:99], a[48:51]\n\t"
    "v_mfma_f32_16x16x32_bf16 a[48:51], v[136:139], a[64:67], a[48:51]\n\t"
    "s_waitcnt vmcnt(19) lgkmcnt(4)\n\t"
    "s_barrier\n\t"
    "v_mfma_f32_16x16x32_bf16 a[80:83], v[144:147], a[64:67], a[80:83]\n\t"
    "ds_read_b64_tr_b16 v[128:129], v192 offset:0\n\t"
    "v_mfma_f32_16x16x32_bf16 a[80:83], v[148:151], a[96:99], a[80:83]\n\t"
    "ds_read_b64_tr_b16 v[130:131], v192 offset:4096\n\t"
    "v_mfma_f32_16x16x32_bf16 a[84:87], v[148:151], a[100:103], a[84:87]\n\t"
    "buffer_load_dwordx4 a[200:203], v247, s[88:91], s47 offen sc1\n\t"
    "v_mfma_f32_16x16x32_bf16 a[84:87], v[144:147], a[68:71], a[84:87]\n\t"
    "v_mfma_f32_16x16x32_bf16 a[88:91], v[144:147], a[72:75], a[88:91]\n\t"
    "ds_read_b64_tr_b16 v[132:133], v192 offset:8192\n\t"
    "v_mfma_f32_16x16x32_bf16 a[88:91], v[148:151], a[104:107], a[88:91]\n\t"
    "ds_read_b64_tr_b16 v[134:135], v192 offset:12288\n\t"
    "v_mfma_f32_16x16x32_bf16 a[92:95], v[148:151], a[108:111], a[92:95]\n\t"
    "buffer_load_dwordx4 a[188:191], v249, s[88:91], s46 offen sc1\n\t"
    "v_mfma_f32_16x16x32_bf16 a[92:95], v[144:147], a[76:79], a[92:95]\n\t"
    "s_waitcnt lgkmcnt(4)\n\t"
    "v_mfma_f32_16x16x32_bf16 a[124:127], v[152:155], a[76:79], a[124:127]\n\t"
    "ds_read_b64_tr_b16 v[136:137], v193 offset:0\n\t"
    "v_mfma_f32_16x16x32_bf16 a[124:127], v[156:159], a[108:111], a[124:127]\n\t"
    "ds_read_b64_tr_b16 v[138:139], v193 offset:4096\n\t"
    "v_mfma_f32_16x16x32_bf16 a[120:123], v[156:159], a[104:107], a[120:123]\n\t"
    "buffer_load_dwordx4 a[204:207], v249, s[88:91], s47 offen sc1\n\t"
    "v_mfma_f32_16x16x32_bf16 a[120:123], v[152:155], a[72:75], a[120:123]\n\t"
    "s_bfe_u32 s84, s14, 262168\n\t"
    "s_lshl_b32 s84, s84, 17\n\t"
    "s_bfe_u32 s46, s14, 262168\n\t"
    "v_mfma_f32_16x16x32_bf16 a[116:119], v[152:155], a[68:71], a[116:119]\n\t"
    "ds_read_b64_tr_b16 v[140:141], v193 offset:8192\n\t"
    "s_lshl_b32 s46, s46, 13\n\t"
    "v_mfma_f32_16x16x32_bf16 a[116:119], v[156:159], a[100:103], a[116:119]\n\t"
    "ds_read_b64_tr_b16 v[142:143], v193 offset:12288\n\t"
    "s_add_u32 s46, s46, s60\n\t"
    "v_mfma_f32_16x16x32_bf16 a[112:115], v[156:159], a[96:99], a[112:115]\n\t"
    "s_add_u32 s47, s46, 4096\n\t"
    "buffer_store_dwordx4 v[72:75], v248, s[88:91], s94 offen offset:2048 sc1\n\t"
    "v_mfma_f32_16x16x32_bf16 a[112:115], v[152:155], a[64:67], a[112:115]\n\t"
    "s_waitcnt vmcnt(14) lgkmcnt(4)\n\t"
    "v_mul_f32 v76, v236, v76\n\t"
    "v_mul_f32 v77, v236, v77\n\t"
    "v_mfma_f32_16x16x32_bf16 a[208:211], v[128:131], a[144:147], a[208:211]\n\t"
    "ds_read_b64_tr_b16 v[144:145], v194 offset:0\n\t"
    "v_mul_f32 v78, v236, v78\n\t"
    "v_mfma_f32_16x16x32_bf16 a[208:211], v[132:135], a[160:163], a[208:211]\n\t"
    "ds_read_b64_tr_b16 v[146:147], v194 offset:4096\n\t"
    "s_add_u32 m0, s48, 81920\n\t"
    "v_mfma_f32_16x16x32_bf16 a[212:215], v[132:135], a[164:167], a[212:215]\n\t"
    "buffer_load_dwordx4 v242, s[32:35], s84 offen sc1 lds\n\t"
    "v_mfma_f32_16x16x32_bf16 a[212:215], v[128:131], a[148:151], a[212:215]\n\t"
    "v_mul_f32 v79, v236, v79\n\t"
    "v_mul_f32 v92, v236, v92\n\t"
    "v_mfma_f32_16x16x32_bf16 a[216:219], v[128:131], a[152:155], a[216:219]\n\t"
    "ds_read_b64_tr_b16 v[148:149], v194 offset:8192\n\t"
    "v_mul_f32 v93, v236, v93\n\t"
    "v_mfma_f32_16x16x32_bf16 a[216:219], v[132:135], a[168:171], a[216:219]\n\t"
    "ds_read_b64_tr_b16 v[150:151], v194 offset:12288\n\t"
    "v_mul_f32 v94, v236, v94\n\t"
    "v_mfma_f32_16x16x32_bf16 a[220:223], v[132:135], a[172:175], a[220:223]\n\t"
    "buffer_load_dwordx4 v243, s[32:35], s84 offen offset:1024 sc1 lds\n\t"
    "v_mfma_f32_16x16x32_bf16 a[220:223], v[128:131], a[156:159], a[220:223]\n\t"
    "s_waitcnt lgkmcnt(4)\n\t"
    "v_mul_f32 v95, v236, v95\n\t"
    "v_cvt_pk_bf16_f32 v76, v76, v77\n\t"
    "v_mfma_f32_16x16x32_bf16 a[236:239], v[136:139], a[156:159], a[236:239]\n\t"
    "ds_read_b64_tr_b16 v[152:153], v195 offset:0\n\t"
    "v_cvt_pk_bf16_f32 v77, v78, v79\n\t"
    "v_mfma_f32_16x16x32_bf16 a[236:239], v[140:143], a[172:175], a[236:239]\n\t"
    "ds_read_b64_tr_b16 v[154:155], v195 offset:4096\n\t"
    "v_cvt_pk_bf16_f32 v78, v92, v93\n\t"
    "v_mfma_f32_16x16x32_bf16 a[232:235], v[140:143], a[168:171], a[232:235]\n\t"
    "buffer_load_dwordx4 v244, s[32:35], s84 offen offset:2048 sc1 lds\n\t"
    "v_mfma_f32_16x16x32_bf16 a[232:235], v[136:139], a[152:155], a[232:235]\n\t"
    "v_cvt_pk_bf16_f32 v79, v94, v95\n\t"
    "v_mfma_f32_16x16x32_bf16 a[228:231], v[136:139], a[148:151], a[228:231]\n\t"
    "ds_read_b64_tr_b16 v[156:157], v195 offset:8192\n\t"
    "v_mfma_f32_16x16x32_bf16 a[228:231], v[140:143], a[164:167], a[228:231]\n\t"
    "ds_read_b64_tr_b16 v[158:159], v195 offset:12288\n\t"
    "v_mfma_f32_16x16x32_bf16 a[224:227], v[140:143], a[160:163], a[224:227]\n\t"
    "v_mfma_f32_16x16x32_bf16 a[224:227], v[136:139], a[144:147], a[224:227]\n\t"
    "s_waitcnt lgkmcnt(4)\n\t"
    "v_mfma_f32_16x16x32_bf16 a[240:243], v[144:147], a[144:147], a[240:243]\n\t"
    "ds_read_b64_tr_b16 v[128:129], v196 offset:0\n\t"
    "v_mfma_f32_16x16x32_bf16 a[240:243], v[148:151], a[160:163], a[240:243]\n\t"
    "ds_read_b64_tr_b16 v[130:131], v196 offset:4096\n\t"
    "v_mfma_f32_16x16x32_bf16 a[244:247], v[148:151], a[164:167], a[244:247]\n\t"
    "buffer_load_dwordx4 v245, s[32:35], s84 offen offset:3072 sc1 lds\n\t"
    "v_mfma_f32_16x16x32_bf16 a[244:247], v[144:147], a[148:151], a[244:247]\n\t"
    "v_mfma_f32_16x16x32_bf16 a[248:251], v[144:147], a[152:155], a[248:251]\n\t"
    "ds_read_b64_tr_b16 v[132:133], v196 offset:8192\n\t"
    "v_mfma_f32_16x16x32_bf16 a[248:251], v[148:151], a[168:171], a[248:251]\n\t"
    "ds_read_b64_tr_b16 v[134:135], v196 offset:12288\n\t"
    "v_mfma_f32_16x16x32_bf16 a[252:255], v[148:151], a[172:175], a[252:255]\n\t"
    "buffer_load_dwordx4 a[0:3], v241, s[88:91], s46 offen sc1\n\t"
    "v_mfma_f32_16x16x32_bf16 a[252:255], v[144:147], a[156:159], a[252:255]\n\t"
    "s_waitcnt lgkmcnt(4)\n\t"
    "v_mfma_f32_16x16x32_bf16 a[140:143], v[152:155], a[156:159], a[140:143]\n\t"
    "ds_read_b64_tr_b16 v[136:137], v197 offset:0\n\t"
    "v_mfma_f32_16x16x32_bf16 a[140:143], v[156:159], a[172:175], a[140:143]\n\t"
    "ds_read_b64_tr_b16 v[138:139], v197 offset:4096\n\t"
    "v_mfma_f32_16x16x32_bf16 a[136:139], v[156:159], a[168:171], a[136:139]\n\t"
    "buffer_load_dwordx4 a[32:35], v241, s[88:91], s47 offen sc1\n\t"
    "v_mfma_f32_16x16x32_bf16 a[136:139], v[152:155], a[152:155], a[136:139]\n\t"
    "v_mfma_f32_16x16x32_bf16 a[132:135], v[152:155], a[148:151], a[132:135]\n\t"
    "ds_read_b64_tr_b16 v[140:141], v197 offset:8192\n\t"
    "v_mfma_f32_16x16x32_bf16 a[132:135], v[156:159], a[164:167], a[132:135]\n\t"
    "ds_read_b64_tr_b16 v[142:143], v197 offset:12288\n\t"
    "v_mfma_f32_16x16x32_bf16 a[128:131], v[156:159], a[160:163], a[128:131]\n\t"
    "v_mfma_f32_16x16x32_bf16 a[128:131], v[152:155], a[144:147], a[128:131]\n\t"
    "s_waitcnt lgkmcnt(4)\n\t"
    "v_mfma_f32_16x16x32_bf16 a[16:19], v[128:131], a[144:147], a[16:19]\n\t"
    "ds_read_b64_tr_b16 v[144:145], v198 offset:0\n\t"
    "v_mfma_f32_16x16x32_bf16 a[16:19], v[132:135], a[160:163], a[16:19]\n\t"
    "ds_read_b64_tr_b16 v[146:147], v198 offset:4096\n\t"
    "v_mfma_f32_16x16x32_bf16 a[20:23], v[132:135], a[164:167], a[20:23]\n\t"
    "buffer_load_dwordx4 a[4:7], v246, s[88:91], s46 offen sc1\n\t"
    "v_mfma_f32_16x16x32_bf16 a[20:23], v[128:131], a[148:151], a[20:23]\n\t"
    "buffer_store_dwordx4 v[76:79], v248, s[88:91], s94 offen offset:3072 sc1\n\t"
    "v_mfma_f32_16x16x32_bf16 a[24:27], v[128:131], a[152:155], a[24:27]\n\t"
    "ds_read_b64_tr_b16 v[148:149], v198 offset:8192\n\t"
    "v_mul_f32 v96, v224, v96\n\t"
    "v_mfma_f32_16x16x32_bf16 a[24:27], v[132:135], a[168:171], a[24:27]\n\t"
    "ds_read_b64_tr_b16 v[150:151], v198 offset:12288\n\t"
    "v_mul_f32 v97, v224, v97\n\t"
    "v_mfma_f32_16x16x32_bf16 a[28:31], v[132:135], a[172:175], a[28:31]\n\t"
    "buffer_load_dwordx4 a[36:39], v246, s[88:91], s47 offen sc1\n\t"
    "v_mfma_f32_16x16x32_bf16 a[28:31], v[128:131], a[156:159], a[28:31]\n\t"
    "s_waitcnt lgkmcnt(4)\n\t"
    "v_mul_f32 v98, v224, v98\n\t"
    "v_mul_f32 v99, v224, v99\n\t"
    "v_mfma_f32_16x16x32_bf16 a[60:63], v[136:139], a[156:159], a[60:63]\n\t"
    "ds_read_b64_tr_b16 v[152:153], v199 offset:0\n\t"
    "v_mul_f32 v112, v224, v112\n\t"
    "v_mfma_f32_16x16x32_bf16 a[60:63], v[140:143], a[172:175], a[60:63]\n\t"
    "ds_read_b64_tr_b16 v[154:155], v199 offset:4096\n\t"
    "v_mul_f32 v113, v224, v113\n\t"
    "v_mfma_f32_16x16x32_bf16 a[56:59], v[140:143], a[168:171], a[56:59]\n\t"
    "buffer_load_dwordx4 a[8:11], v247, s[88:91], s46 offen sc1\n\t"
    "v_mfma_f32_16x16x32_bf16 a[56:59], v[136:139], a[152:155], a[56:59]\n\t"
    "v_mul_f32 v114, v224, v114\n\t"
    "v_mul_f32 v115, v224, v115\n\t"
    "v_mfma_f32_16x16x32_bf16 a[52:55], v[136:139], a[148:151], a[52:55]\n\t"
    "ds_read_b64_tr_b16 v[156:157], v199 offset:8192\n\t"
    "v_cvt_pk_bf16_f32 v96, v96, v97\n\t"
    "v_mfma_f32_16x16x32_bf16 a[52:55], v[140:143], a[164:167], a[52:55]\n\t"
    "ds_read_b64_tr_b16 v[158:159], v199 offset:12288\n\t"
    "v_cvt_pk_bf16_f32 v97, v98, v99\n\t"
    "v_mfma_f32_16x16x32_bf16 a[48:51], v[140:143], a[160:163], a[48:51]\n\t"
    "v_cvt_pk_bf16_f32 v98, v112, v113\n\t"
    "v_cvt_pk_bf16_f32 v99, v114, v115\n\t"
    "v_mfma_f32_16x16x32_bf16 a[48:51], v[136:139], a[144:147], a[48:51]\n\t"
    "s_waitcnt vmcnt(20) lgkmcnt(4)\n\t"
    "s_barrier\n\t"
    "v_mfma_f32_16x16x32_bf16 a[80:83], v[144:147], a[144:147], a[80:83]\n\t"
    "ds_read_b64_tr_b16 v[128:129], v192 offset:16384\n\t"
    "v_mfma_f32_16x16x32_bf16 a[80:83], v[148:151], a[160:163], a[80:83]\n\t"
    "ds_read_b64_tr_b16 v[130:131], v192 offset:20480\n\t"
    "v_mfma_f32_16x16x32_bf16 a[84:87], v[148:151], a[164:167], a[84:87]\n\t"
    "buffer_load_dwordx4 a[40:43], v247, s[88:91], s47 offen sc1\n\t"
    "v_mfma_f32_16x16x32_bf16 a[84:87], v[144:147], a[148:151], a[84:87]\n\t"
    "v_mfma_f32_16x16x32_bf16 a[88:91], v[144:147], a[152:155], a[88:91]\n\t"
    "ds_read_b64_tr_b16 v[132:133], v192 offset:24576\n\t"
    "v_mfma_f32_16x16x32_bf16 a[88:91], v[148:151], a[168:171], a[88:91]\n\t"
    "ds_read_b64_tr_b16 v[134:135], v192 offset:28672\n\t"
    "v_mfma_f32_16x16x32_bf16 a[92:95], v[148:151], a[172:175], a[92:95]\n\t"
    "buffer_load_dwordx4 a[12:15], v249, s[88:91], s46 offen sc1\n\t"
    "v_mfma_f32_16x16x32_bf16 a[92:95], v[144:147], a[156:159], a[92:95]\n\t"
    "s_waitcnt lgkmcnt(4)\n\t"
    "v_mfma_f32_16x16x32_bf16 a[124:127], v[152:155], a[156:159], a[124:127]\n\t"
    "ds_read_b64_tr_b16 v[136:137], v193 offset:16384\n\t"
    "v_mfma_f32_16x16x32_bf16 a[124:127], v[156:159], a[172:175], a[124:127]\n\t"
    "ds_read_b64_tr_b16 v[138:139], v193 offset:20480\n\t"
    "v_mfma_f32_16x16x32_bf16 a[120:123], v[156:159], a[168:171], a[120:123]\n\t"
    "buffer_load_dwordx4 a[44:47], v249, s[88:91], s47 offen sc1\n\t"
    "v_mfma_f32_16x16x32_bf16 a[120:123], v[152:155], a[152:155], a[120:123]\n\t"
    "s_bfe_u32 s84, s14, 262172\n\t"
    "s_lshl_b32 s84, s84, 17\n\t"
    "s_bfe_u32 s46, s14, 262172\n\t"
    "v_mfma_f32_16x16x32_bf16 a[116:119], v[152:155], a[148:151], a[116:119]\n\t"
    "ds_read_b64_tr_b16 v[140:141], v193 offset:24576\n\t"
    "s_lshl_b32 s46, s46, 13\n\t"
    "v_mfma_f32_16x16x32_bf16 a[116:119], v[156:159], a[164:167], a[116:119]\n\t"
    "ds_read_b64_tr_b16 v[142:143], v193 offset:28672\n\t"
    "s_add_u32 s46, s46, s60\n\t"
    "v_mfma_f32_16x16x32_bf16 a[112:115], v[156:159], a[160:163], a[112:115]\n\t"
    "s_add_u32 s47, s46, 4096\n\t"
    "v_mfma_f32_16x16x32_bf16 a[112:115], v[152:155], a[144:147], a[112:115]\n\t"
    "s_waitcnt vmcnt(14) lgkmcnt(4)\n\t"
    "v_mfma_f32_16x16x32_bf16 a[208:211], v[128:131], a[176:179], a[208:211]\n\t"
    "ds_read_b64_tr_b16 v[144:145], v194 offset:16384\n\t"
    "v_mfma_f32_16x16x32_bf16 a[208:211], v[132:135], a[192:195], a[208:211]\n\t"
    "ds_read_b64_tr_b16 v[146:147], v194 offset:20480\n\t"
    "s_add_u32 m0, s48, 98304\n\t"
    "v_mfma_f32_16x16x32_bf16 a[212:215], v[132:135], a[196:199], a[212:215]\n\t"
    "buffer_load_dwordx4 v242, s[32:35], s84 offen sc1 lds\n\t"
    "v_mfma_f32_16x16x32_bf16 a[212:215], v[128:131], a[180:183], a[212:215]\n\t"
    "v_mfma_f32_16x16x32_bf16 a[216:219], v[128:131], a[184:187], a[216:219]\n\t"
    "ds_read_b64_tr_b16 v[148:149], v194 offset:24576\n\t"
    "v_mfma_f32_16x16x32_bf16 a[216:219], v[132:135], a[200:203], a[216:219]\n\t"
    "ds_read_b64_tr_b16 v[150:151], v194 offset:28672\n\t"
    "v_mfma_f32_16x16x32_bf16 a[220:223], v[132:135], a[204:207], a[220:223]\n\t"
    "buffer_load_dwordx4 v243, s[32:35], s84 offen offset:1024 sc1 lds\n\t"
    "v_mfma_f32_16x16x32_bf16 a[220:223], v[128:131], a[188:191], a[220:223]\n\t"
    "s_waitcnt lgkmcnt(4)\n\t"
    "buffer_store_dwordx4 v[96:99], v248, s[88:91], s95 offen sc1\n\t"
    "v_mfma_f32_16x16x32_bf16 a[236:239], v[136:139], a[188:191], a[236:239]\n\t"
    "ds_read_b64_tr_b16 v[152:153], v195 offset:16384\n\t"
    "v_mul_f32 v100, v228, v100\n\t"
    "v_mfma_f32_16x16x32_bf16 a[236:239], v[140:143], a[204:207], a[236:239]\n\t"
    "ds_read_b64_tr_b16 v[154:155], v195 offset:20480\n\t"
    "v_mul_f32 v101, v228, v101\n\t"
    "v_mfma_f32_16x16x32_bf16 a[232:235], v[140:143], a[200:203], a[232:235]\n\t"
    "buffer_load_dwordx4 v244, s[32:35], s84 offen offset:2048 sc1 lds\n\t"
    "v_mfma_f32_16x16x32_bf16 a[232:235], v[136:139], a[184:187], a[232:235]\n\t"
    "v_mul_f32 v102, v228, v102\n\t"
    "v_mul_f32 v103, v228, v103\n\t"
    "v_mfma_f32_16x16x32_bf16 a[228:231], v[136:139], a[180:183], a[228:231]\n\t"
    "ds_read_b64_tr_b16 v[156:157], v195 offset:24576\n\t"
    "v_mul_f32 v116, v228, v116\n\t"
    "v_mfma_f32_16x16x32_bf16 a[228:231], v[140:143], a[196:199], a[228:231]\n\t"
    "ds_read_b64_tr_b16 v[158:159], v195 offset:28672\n\t"
    "v_mul_f32 v117, v228, v117\n\t"
    "v_mfma_f32_16x16x32_bf16 a[224:227], v[140:143], a[192:195], a[224:227]\n\t"
    "v_mul_f32 v118, v228, v118\n\t"
    "v_mul_f32 v119, v228, v119\n\t"
    "v_mfma_f32_16x16x32_bf16 a[224:227], v[136:139], a[176:179], a[224:227]\n\t"
    "s_waitcnt lgkmcnt(4)\n\t"
    "v_cvt_pk_bf16_f32 v100, v100, v101\n\t"
    "v_cvt_pk_bf16_f32 v101, v102, v103\n\t"
    "v_mfma_f32_16x16x32_bf16 a[240:243], v[144:147], a[176:179], a[240:243]\n\t"
    "ds_read_b64_tr_b16 v[128:129], v196 offset:16384\n\t"
    "v_cvt_pk_bf16_f32 v102, v116, v117\n\t"
    "v_mfma_f32_16x16x32_bf16 a[240:243], v[148:151], a[192:195], a[240:243]\n\t"
    "ds_read_b64_tr_b16 v[130:131], v196 offset:20480\n\t"
    "v_cvt_pk_bf16_f32 v103, v118, v119\n\t"
    "v_mfma_f32_16x16x32_bf16 a[244:247], v[148:151], a[196:199], a[244:247]\n\t"
    "buffer_load_dwordx4 v245, s[32:35], s84 offen offset:3072 sc1 lds\n\t"
    "v_mfma_f32_16x16x32_bf16 a[244:247], v[144:147], a[180:183], a[244:247]\n\t"
    "v_mfma_f32_16x16x32_bf16 a[248:251], v[144:147], a[184:187], a[248:251]\n\t"
    "ds_read_b64_tr_b16 v[132:133], v196 offset:24576\n\t"
    "v_mfma_f32_16x16x32_bf16 a[248:251], v[148:151], a[200:203], a[248:251]\n\t"
    "ds_read_b64_tr_b16 v[134:135], v196 offset:28672\n\t"
    "v_mfma_f32_16x16x32_bf16 a[252:255], v[148:151], a[204:207], a[252:255]\n\t"
    "buffer_load_dwordx4 a[64:67], v241, s[88:91], s46 offen sc1\n\t"
    "v_mfma_f32_16x16x32_bf16 a[252:255], v[144:147], a[188:191], a[252:255]\n\t"
    "s_waitcnt lgkmcnt(4)\n\t"
    "v_mfma_f32_16x16x32_bf16 a[140:143], v[152:155], a[188:191], a[140:143]\n\t"
    "ds_read_b64_tr_b16 v[136:137], v197 offset:16384\n\t"
    "v_mfma_f32_16x16x32_bf16 a[140:143], v[156:159], a[204:207], a[140:143]\n\t"
    "ds_read_b64_tr_b16 v[138:139], v197 offset:20480\n\t"
    "v_mfma_f32_16x16x32_bf16 a[136:139], v[156:159], a[200:203], a[136:139]\n\t"
    "buffer_load_dwordx4 a[96:99], v241, s[88:91], s47 offen sc1\n\t"
    "v_mfma_f32_16x16x32_bf16 a[136:139], v[152:155], a[184:187], a[136:139]\n\t"
    "v_mfma_f32_16x16x32_bf16 a[132:135], v[152:155], a[180:183], a[132:135]\n\t"
    "ds_read_b64_tr_b16 v[140:141], v197 offset:24576\n\t"
    "v_mfma_f32_16x16x32_bf16 a[132:135], v[156:159], a[196:199], a[132:135]\n\t"
    "ds_read_b64_tr_b16 v[142:143], v197 offset:28672\n\t"
    "v_mfma_f32_16x16x32_bf16 a[128:131], v[156:159], a[192:195], a[128:131]\n\t"
    "v_mfma_f32_16x16x32_bf16 a[128:131], v[152:155], a[176:179], a[128:131]\n\t"
    "s_waitcnt lgkmcnt(4)\n\t"
    "v_mfma_f32_16x16x32_bf16 a[16:19], v[128:131], a[176:179], a[16:19]\n\t"
    "ds_read_b64_tr_b16 v[144:145], v198 offset:16384\n\t"
    "v_mfma_f32_16x16x32_bf16 a[16:19], v[132:135], a[192:195], a[16:19]\n\t"
    "ds_read_b64_tr_b16 v[146:147], v198 offset:20480\n\t"
    "v_mfma_f32_16x16x32_bf16 a[20:23], v[132:135], a[196:199], a[20:23]\n\t"
    "buffer_load_dwordx4 a[68:71], v246, s[88:91], s46 offen sc1\n\t"
    "v_mfma_f32_16x16x32_bf16 a[20:23], v[128:131], a[180:183], a[20:23]\n\t"
    "v_mfma_f32_16x16x32_bf16 a[24:27], v[128:131], a[184:187], a[24:27]\n\t"
    "ds_read_b64_tr_b16 v[148:149], v198 offset:24576\n\t"
    "v_mfma_f32_16x16x32_bf16 a[24:27], v[132:135], a[200:203], a[24:27]\n\t"
    "ds_read_b64_tr_b16 v[150:151], v198 offset:28672\n\t"
    "v_mfma_f32_16x16x32_bf16 a[28:31], v[132:135], a[204:207], a[28:31]\n\t"
    "buffer_load_dwordx4 a[100:103], v246, s[88:91], s47 offen sc1\n\t"
    "v_mfma_f32_16x16x32_bf16 a[28:31], v[128:131], a[188:191], a[28:31]\n\t"
    "s_waitcnt lgkmcnt(4)\n\t"
    "v_mfma_f32_16x16x32_bf16 a[60:63], v[136:139], a[188:191], a[60:63]\n\t"
    "ds_read_b64_tr_b16 v[152:153], v199 offset:16384\n\t"
    "v_mfma_f32_16x16x32_bf16 a[60:63], v[140:143], a[204:207], a[60:63]\n\t"
    "ds_read_b64_tr_b16 v[154:155], v199 offset:20480\n\t"
    "v_mfma_f32_16x16x32_bf16 a[56:59], v[140:143], a[200:203], a[56:59]\n\t"
    "buffer_load_dwordx4 a[72:75], v247, s[88:91], s46 offen sc1\n\t"
    "v_mfma_f32_16x16x32_bf16 a[56:59], v[136:139], a[184:187], a[56:59]\n\t"
    "buffer_store_dwordx4 v[100:103], v248, s[88:91], s95 offen offset:1024 sc1\n\t"
    "v_mfma_f32_16x16x32_bf16 a[52:55], v[136:139], a[180:183], a[52:55]\n\t"
    "ds_read_b64_tr_b16 v[156:157], v199 offset:24576\n\t"
    "v_mul_f32 v104, v232, v104\n\t"
    "v_mfma_f32_16x16x32_bf16 a[52:55], v[140:143], a[196:199], a[52:55]\n\t"
    "ds_read_b64_tr_b16 v[158:159], v199 offset:28672\n\t"
    "v_mul_f32 v105, v232, v105\n\t"
    "v_mfma_f32_16x16x32_bf16 a[48:51], v[140:143], a[192:195], a[48:51]\n\t"
    "v_mul_f32 v106, v232, v106\n\t"
    "v_mul_f32 v107, v232, v107\n\t"
    "v_mfma_f32_16x16x32_bf16 a[48:51], v[136:139], a[176:179], a[48:51]\n\t"
    "s_waitcnt vmcnt(20) lgkmcnt(4)\n\t"
    "s_barrier\n\t"
    "v_mfma_f32_16x16x32_bf16 a[80:83], v[144:147], a[176:179], a[80:83]\n\t"
    "ds_read_b64_tr_b16 v[128:129], v192 offset:32768\n\t"
    "v_mul_f32 v120, v232, v120\n\t"
    "v_mfma_f32_16x16x32_bf16 a[80:83], v[148:151], a[192:195], a[80:83]\n\t"
    "ds_read_b64_tr_b16 v[130:131], v192 offset:36864\n\t"
    "v_mul_f32 v121, v232, v121\n\t"
    "v_mfma_f32_16x16x32_bf16 a[84:87], v[148:151], a[196:199], a[84:87]\n\t"
    "buffer_load_dwordx4 a[104:107], v247, s[88:91], s47 offen sc1\n\t"
    "v_mfma_f32_16x16x32_bf16 a[84:87], v[144:147], a[180:183], a[84:87]\n\t"
    "v_mul_f32 v122, v232, v122\n\t"
    "v_mul_f32 v123, v232, v123\n\t"
    "v_mfma_f32_16x16x32_bf16 a[88:91], v[144:147], a[184:187], a[88:91]\n\t"
    "ds_read_b64_tr_b16 v[132:133], v192 offset:40960\n\t"
    "v_cvt_pk_bf16_f32 v104, v104, v105\n\t"
    "v_mfma_f32_16x16x32_bf16 a[88:91], v[148:151], a[200:203], a[88:91]\n\t"
    "ds_read_b64_tr_b16 v[134:135], v192 offset:45056\n\t"
    "v_cvt_pk_bf16_f32 v105, v106, v107\n\t"
    "v_mfma_f32_16x16x32_bf16 a[92:95], v[148:151], a[204:207], a[92:95]\n\t"
    "buffer_load_dwordx4 a[76:79], v249, s[88:91], s46 offen sc1\n\t"
    "v_mfma_f32_16x16x32_bf16 a[92:95], v[144:147], a[188:191], a[92:95]\n\t"
    "s_waitcnt lgkmcnt(4)\n\t"
    "v_cvt_pk_bf16_f32 v106, v120, v121\n\t"
    "v_cvt_pk_bf16_f32 v107, v122, v123\n\t"
    "v_mfma_f32_16x16x32_bf16 a[124:127], v[152:155], a[188:191], a[124:127]\n\t"
    "ds_read_b64_tr_b16 v[136:137], v193 offset:32768\n\t"
    "v_mfma_f32_16x16x32_bf16 a[124:127], v[156:159], a[204:207], a[124:127]\n\t"
    "ds_read_b64_tr_b16 v[138:139], v193 offset:36864\n\t"
    "v_mfma_f32_16x16x32_bf16 a[120:123], v[156:159], a[200:203], a[120:123]\n\t"
    "buffer_load_dwordx4 a[108:111], v249, s[88:91], s47 offen sc1\n\t"
    "v_mfma_f32_16x16x32_bf16 a[120:123], v[152:155], a[184:187], a[120:123]\n\t"
    "s_bfe_u32 s84, s15, 262144\n\t"
    "s_lshl_b32 s84, s84, 17\n\t"
    "s_bfe_u32 s46, s15, 262144\n\t"
    "v_mfma_f32_16x16x32_bf16 a[116:119], v[152:155], a[180:183], a[116:119]\n\t"
    "ds_read_b64_tr_b16 v[140:141], v193 offset:40960\n\t"
    "s_lshl_b32 s46, s46, 13\n\t"
    "v_mfma_f32_16x16x32_bf16 a[116:119], v[156:159], a[196:199], a[116:119]\n\t"
    "ds_read_b64_tr_b16 v[142:143], v193 offset:45056\n\t"
    "s_add_u32 s46, s46, s60\n\t"
    "v_mfma_f32_16x16x32_bf16 a[112:115], v[156:159], a[192:195], a[112:115]\n\t"
    "s_add_u32 s47, s46, 4096\n\t"
    "v_mfma_f32_16x16x32_bf16 a[112:115], v[152:155], a[176:179], a[112:115]\n\t"
    "s_waitcnt vmcnt(14) lgkmcnt(4)\n\t"
    "v_mfma_f32_16x16x32_bf16 a[208:211], v[128:131], a[0:3], a[208:211]\n\t"
    "ds_read_b64_tr_b16 v[144:145], v194 offset:32768\n\t"
    "v_mfma_f32_16x16x32_bf16 a[208:211], v[132:135], a[32:35], a[208:211]\n\t"
    "ds_read_b64_tr_b16 v[146:147], v194 offset:36864\n\t"
    "s_add_u32 m0, s48, 49152\n\t"
    "v_mfma_f32_16x16x32_bf16 a[212:215], v[132:135], a[36:39], a[212:215]\n\t"
    "buffer_load_dwordx4 v242, s[32:35], s84 offen sc1 lds\n\t"
    "v_mfma_f32_16x16x32_bf16 a[212:215], v[128:131], a[4:7], a[212:215]\n\t"
    "v_mfma_f32_16x16x32_bf16 a[216:219], v[128:131], a[8:11], a[216:219]\n\t"
    "ds_read_b64_tr_b16 v[148:149], v194 offset:40960\n\t"
    "v_mfma_f32_16x16x32_bf16 a[216:219], v[132:135], a[40:43], a[216:219]\n\t"
    "ds_read_b64_tr_b16 v[150:151], v194 offset:45056\n\t"
    "v_mfma_f32_16x16x32_bf16 a[220:223], v[132:135], a[44:47], a[220:223]\n\t"
    "buffer_load_dwordx4 v243, s[32:35], s84 offen offset:1024 sc1 lds\n\t"
    "v_mfma_f32_16x16x32_bf16 a[220:223], v[128:131], a[12:15], a[220:223]\n\t"
    "s_waitcnt lgkmcnt(4)\n\t"
    "v_mfma_f32_16x16x32_bf16 a[236:239], v[136:139], a[12:15], a[236:239]\n\t"
    "ds_read_b64_tr_b16 v[152:153], v195 offset:32768\n\t"
    "v_mfma_f32_16x16x32_bf16 a[236:239], v[140:143], a[44:47], a[236:239]\n\t"
    "ds_read_b64_tr_b16 v[154:155], v195 offset:36864\n\t"
    "v_mfma_f32_16x16x32_bf16 a[232:235], v[140:143], a[40:43], a[232:235]\n\t"
    "buffer_load_dwordx4 v244, s[32:35], s84 offen offset:2048 sc1 lds\n\t"
    "v_mfma_f32_16x16x32_bf16 a[232:235], v[136:139], a[8:11], a[232:235]\n\t"
    "v_mfma_f32_16x16x32_bf16 a[228:231], v[136:139], a[4:7], a[228:231]\n\t"
    "ds_read_b64_tr_b16 v[156:157], v195 offset:40960\n\t"
    "v_mfma_f32_16x16x32_bf16 a[228:231], v[140:143], a[36:39], a[228:231]\n\t"
    "ds_read_b64_tr_b16 v[158:159], v195 offset:45056\n\t"
    "v_mfma_f32_16x16x32_bf16 a[224:227], v[140:143], a[32:35], a[224:227]\n\t"
    "v_mfma_f32_16x16x32_bf16 a[224:227], v[136:139], a[0:3], a[224:227]\n\t"
    "s_waitcnt lgkmcnt(4)\n\t"
    "buffer_store_dwordx4 v[104:107], v248, s[88:91], s95 offen offset:2048 sc1\n\t"
    "v_mfma_f32_16x16x32_bf16 a[240:243], v[144:147], a[0:3], a[240:243]\n\t"
    "ds_read_b64_tr_b16 v[128:129], v196 offset:32768\n\t"
    "v_mul_f32 v108, v236, v108\n\t"
    "v_mfma_f32_16x16x32_bf16 a[240:243], v[148:151], a[32:35], a[240:243]\n\t"
    "ds_read_b64_tr_b16 v[130:131], v196 offset:36864\n\t"
    "v_mul_f32 v109, v236, v109\n\t"
    "v_mfma_f32_16x16x32_bf16 a[244:247], v[148:151], a[36:39], a[244:247]\n\t"
    "buffer_load_dwordx4 v245, s[32:35], s84 offen offset:3072 sc1 lds\n\t"
    "v_mfma_f32_16x16x32_bf16 a[244:247], v[144:147], a[4:7], a[244:247]\n\t"
    "v_mul_f32 v110, v236, v110\n\t"
    "v_mul_f32 v111, v236, v111\n\t"
    "v_mfma_f32_16x16x32_bf16 a[248:251], v[144:147], a[8:11], a[248:251]\n\t"
    "ds_read_b64_tr_b16 v[132:133], v196 offset:40960\n\t"
    "v_mul_f32 v124, v236, v124\n\t"
    "v_mfma_f32_16x16x32_bf16 a[248:251], v[148:151], a[40:43], a[248:251]\n\t"
    "ds_read_b64_tr_b16 v[134:135], v196 offset:45056\n\t"
    "v_mul_f32 v125, v236, v125\n\t"
    "v_mfma_f32_16x16x32_bf16 a[252:255], v[148:151], a[44:47], a[252:255]\n\t"
    "buffer_load_dwordx4 a[144:147], v241, s[88:91], s46 offen sc1\n\t"
    "v_mfma_f32_16x16x32_bf16 a[252:255], v[144:147], a[12:15], a[252:255]\n\t"
    "s_waitcnt lgkmcnt(4)\n\t"
    "v_mul_f32 v126, v236, v126\n\t"
    "v_mul_f32 v127, v236, v127\n\t"
    "v_mfma_f32_16x16x32_bf16 a[140:143], v[152:155], a[12:15], a[140:143]\n\t"
    "ds_read_b64_tr_b16 v[136:137], v197 offset:32768\n\t"
    "v_cvt_pk_bf16_f32 v108, v108, v109\n\t"
    "v_mfma_f32_16x16x32_bf16 a[140:143], v[156:159], a[44:47], a[140:143]\n\t"
    "ds_read_b64_tr_b16 v[138:139], v197 offset:36864\n\t"
    "v_cvt_pk_bf16_f32 v109, v110, v111\n\t"
    "v_mfma_f32_16x16x32_bf16 a[136:139], v[156:159], a[40:43], a[136:139]\n\t"
    "buffer_load_dwordx4 a[160:163], v241, s[88:91], s47 offen sc1\n\t"
    "v_mfma_f32_16x16x32_bf16 a[136:139], v[152:155], a[8:11], a[136:139]\n\t"
    "v_cvt_pk_bf16_f32 v110, v124, v125\n\t"
    "v_cvt_pk_bf16_f32 v111, v126, v127\n\t"
    "v_mfma_f32_16x16x32_bf16 a[132:135], v[152:155], a[4:7], a[132:135]\n\t"
    "ds_read_b64_tr_b16 v[140:141], v197 offset:40960\n\t"
    "v_mfma_f32_16x16x32_bf16 a[132:135], v[156:159], a[36:39], a[132:135]\n\t"
    "ds_read_b64_tr_b16 v[142:143], v197 offset:45056\n\t"
    "v_mfma_f32_16x16x32_bf16 a[128:131], v[156:159], a[32:35], a[128:131]\n\t"
    "v_mfma_f32_16x16x32_bf16 a[128:131], v[152:155], a[0:3], a[128:131]\n\t"
    "s_waitcnt lgkmcnt(4)\n\t"
    "v_mfma_f32_16x16x32_bf16 a[16:19], v[128:131], a[0:3], a[16:19]\n\t"
    "ds_read_b64_tr_b16 v[144:145], v198 offset:32768\n\t"
    "v_mfma_f32_16x16x32_bf16 a[16:19], v[132:135], a[32:35], a[16:19]\n\t"
    "ds_read_b64_tr_b16 v[146:147], v198 offset:36864\n\t"
    "v_mfma_f32_16x16x32_bf16 a[20:23], v[132:135], a[36:39], a[20:23]\n\t"
    "buffer_load_dwordx4 a[148:151], v246, s[88:91], s46 offen sc1\n\t"
    "v_mfma_f32_16x16x32_bf16 a[20:23], v[128:131], a[4:7], a[20:23]\n\t"
    "v_mfma_f32_16x16x32_bf16 a[24:27], v[128:131], a[8:11], a[24:27]\n\t"
    "ds_read_b64_tr_b16 v[148:149], v198 offset:40960\n\t"
    "v_mfma_f32_16x16x32_bf16 a[24:27], v[132:135], a[40:43], a[24:27]\n\t"
    "ds_read_b64_tr_b16 v[150:151], v198 offset:45056\n\t"
    "v_mfma_f32_16x16x32_bf16 a[28:31], v[132:135], a[44:47], a[28:31]\n\t"
    "buffer_load_dwordx4 a[164:167], v246, s[88:91], s47 offen sc1\n\t"
    "v_mfma_f32_16x16x32_bf16 a[28:31], v[128:131], a[12:15], a[28:31]\n\t"
    "s_waitcnt lgkmcnt(4)\n\t"
    "v_mfma_f32_16x16x32_bf16 a[60:63], v[136:139], a[12:15], a[60:63]\n\t"
    "ds_read_b64_tr_b16 v[152:153], v199 offset:32768\n\t"
    "v_mfma_f32_16x16x32_bf16 a[60:63], v[140:143], a[44:47], a[60:63]\n\t"
    "ds_read_b64_tr_b16 v[154:155], v199 offset:36864\n\t"
    "v_mfma_f32_16x16x32_bf16 a[56:59], v[140:143], a[40:43], a[56:59]\n\t"
    "buffer_load_dwordx4 a[152:155], v247, s[88:91], s46 offen sc1\n\t"
    "v_mfma_f32_16x16x32_bf16 a[56:59], v[136:139], a[8:11], a[56:59]\n\t"
    "v_mfma_f32_16x16x32_bf16 a[52:55], v[136:139], a[4:7], a[52:55]\n\t"
    "ds_read_b64_tr_b16 v[156:157], v199 offset:40960\n\t"
    "v_mfma_f32_16x16x32_bf16 a[52:55], v[140:143], a[36:39], a[52:55]\n\t"
    "ds_read_b64_tr_b16 v[158:159], v199 offset:45056\n\t"
    "v_mfma_f32_16x16x32_bf16 a[48:51], v[140:143], a[32:35], a[48:51]\n\t"
    "v_mfma_f32_16x16x32_bf16 a[48:51], v[136:139], a[0:3], a[48:51]\n\t"
    "s_waitcnt vmcnt(19) lgkmcnt(4)\n\t"
    "s_barrier\n\t"
    "v_mfma_f32_16x16x32_bf16 a[80:83], v[144:147], a[0:3], a[80:83]\n\t"
    "ds_read_b64_tr_b16 v[128:129], v192 offset:49152\n\t"
    "v_mfma_f32_16x16x32_bf16 a[80:83], v[148:151], a[32:35], a[80:83]\n\t"
    "ds_read_b64_tr_b16 v[130:131], v192 offset:53248\n\t"
    "v_mfma_f32_16x16x32_bf16 a[84:87], v[148:151], a[36:39], a[84:87]\n\t"
    "buffer_load_dwordx4 a[168:171], v247, s[88:91], s47 offen sc1\n\t"
    "v_mfma_f32_16x16x32_bf16 a[84:87], v[144:147], a[4:7], a[84:87]\n\t"
    "buffer_store_dwordx4 v[108:111], v248, s[88:91], s95 offen offset:3072 sc1\n\t"
    "v_mfma_f32_16x16x32_bf16 a[88:91], v[144:147], a[8:11], a[88:91]\n\t"
    "ds_read_b64_tr_b16 v[132:133], v192 offset:57344\n\t"
    "v_mfma_f32_16x16x32_bf16 a[88:91], v[148:151], a[40:43], a[88:91]\n\t"
    "ds_read_b64_tr_b16 v[134:135], v192 offset:61440\n\t"
    "v_mfma_f32_16x16x32_bf16 a[92:95], v[148:151], a[44:47], a[92:95]\n\t"
    "buffer_load_dwordx4 a[156:159], v249, s[88:91], s46 offen sc1\n\t"
    "v_mfma_f32_16x16x32_bf16 a[92:95], v[144:147], a[12:15], a[92:95]\n\t"
    "s_waitcnt lgkmcnt(4)\n\t"
    "v_mfma_f32_16x16x32_bf16 a[124:127], v[152:155], a[12:15], a[124:127]\n\t"
    "ds_read_b64_tr_b16 v[136:137], v193 offset:49152\n\t"
    "v_mfma_f32_16x16x32_bf16 a[124:127], v[156:159], a[44:47], a[124:127]\n\t"
    "ds_read_b64_tr_b16 v[138:139], v193 offset:53248\n\t"
    "v_mfma_f32_16x16x32_bf16 a[120:123], v[156:159], a[40:43], a[120:123]\n\t"
    "buffer_load_dwordx4 a[172:175], v249, s[88:91], s47 offen sc1\n\t"
    "v_mfma_f32_16x16x32_bf16 a[120:123], v[152:155], a[8:11], a[120:123]\n\t"
    "s_bfe_u32 s84, s15, 262148\n\t"
    "s_lshl_b32 s84, s84, 17\n\t"
    "s_bfe_u32 s46, s15, 262148\n\t"
    "v_mfma_f32_16x16x32_bf16 a[116:119], v[152:155], a[4:7], a[116:119]\n\t"
    "ds_read_b64_tr_b16 v[140:141], v193 offset:57344\n\t"
    "s_lshl_b32 s46, s46, 13\n\t"
    "v_mfma_f32_16x16x32_bf16 a[116:119], v[156:159], a[36:39], a[116:119]\n\t"
    "ds_read_b64_tr_b16 v[142:143], v193 offset:61440\n\t"
    "s_add_u32 s46, s46, s60\n\t"
    "v_mfma_f32_16x16x32_bf16 a[112:115], v[156:159], a[32:35], a[112:115]\n\t"
    "s_add_u32 s47, s46, 4096\n\t"
    "v_mfma_f32_16x16x32_bf16 a[112:115], v[152:155], a[0:3], a[112:115]\n\t"
    "s_waitcnt vmcnt(14) lgkmcnt(4)\n\t"
    "v_mfma_f32_16x16x32_bf16 a[208:211], v[128:131], a[64:67], a[208:211]\n\t"
    "ds_read_b64_tr_b16 v[144:145], v194 offset:49152\n\t"
    "v_lshrrev_b32 v161, 8, v248\n\t"
    "v_mfma_f32_16x16x32_bf16 a[208:211], v[132:135], a[96:99], a[208:211]\n\t"
    "ds_read_b64_tr_b16 v[146:147], v194 offset:53248\n\t"
    "s_add_u32 m0, s48, 65536\n\t"
    "v_mfma_f32_16x16x32_bf16 a[212:215], v[132:135], a[100:103], a[212:215]\n\t"
    "buffer_load_dwordx4 v242, s[32:35], s84 offen sc1 lds\n\t"
    "v_mfma_f32_16x16x32_bf16 a[212:215], v[128:131], a[68:71], a[212:215]\n\t"
    "v_and_b32 v160, 240, v248\n\t"
    "v_lshlrev_b32 v200, 3, v161\n\t"
    "v_mfma_f32_16x16x32_bf16 a[216:219], v[128:131], a[72:75], a[216:219]\n\t"
    "ds_read_b64_tr_b16 v[148:149], v194 offset:57344\n\t"
    "v_xor_b32 v200, v160, v200\n\t"
    "v_mfma_f32_16x16x32_bf16 a[216:219], v[132:135], a[104:107], a[216:219]\n\t"
    "ds_read_b64_tr_b16 v[150:151], v194 offset:61440\n\t"
    "v_lshl_add_u32 v200, v160, 4, v200\n\t"
    "v_mfma_f32_16x16x32_bf16 a[220:223], v[132:135], a[108:111], a[220:223]\n\t"
    "buffer_load_dwordx4 v243, s[32:35], s84 offen offset:1024 sc1 lds\n\t"
    "v_mfma_f32_16x16x32_bf16 a[220:223], v[128:131], a[76:79], a[220:223]\n\t"
    "s_waitcnt lgkmcnt(4)\n\t"
    "v_add_u32 v200, s48, v200\n\t"
    "v_add_u32 v200, 147456, v200\n\t"
    "v_mfma_f32_16x16x32_bf16 a[236:239], v[136:139], a[76:79], a[236:239]\n\t"
    "ds_read_b64_tr_b16 v[152:153], v195 offset:49152\n\t"
    "v_xor_b32 v201, 32, v200\n\t"
    "v_mfma_f32_16x16x32_bf16 a[236:239], v[140:143], a[108:111], a[236:239]\n\t"
    "ds_read_b64_tr_b16 v[154:155], v195 offset:53248\n\t"
    "v_xor_b32 v202, 64, v200\n\t"
    "v_mfma_f32_16x16x32_bf16 a[232:235], v[140:143], a[104:107], a[232:235]\n\t"
    "buffer_load_dwordx4 v244, s[32:35], s84 offen offset:2048 sc1 lds\n\t"
    "v_mfma_f32_16x16x32_bf16 a[232:235], v[136:139], a[72:75], a[232:235]\n\t"
    "v_xor_b32 v203, 96, v200\n\t"
    "v_xor_b32 v204, 128, v200\n\t"
    "v_mfma_f32_16x16x32_bf16 a[228:231], v[136:139], a[68:71], a[228:231]\n\t"
    "ds_read_b64_tr_b16 v[156:157], v195 offset:57344\n\t"
    "v_xor_b32 v205, 160, v200\n\t"
    "v_mfma_f32_16x16x32_bf16 a[228:231], v[140:143], a[100:103], a[228:231]\n\t"
    "ds_read_b64_tr_b16 v[158:159], v195 offset:61440\n\t"
    "v_xor_b32 v206, 192, v200\n\t"
    "v_mfma_f32_16x16x32_bf16 a[224:227], v[140:143], a[96:99], a[224:227]\n\t"
    "v_xor_b32 v207, 224, v200\n\t"
    "v_mfma_f32_16x16x32_bf16 a[224:227], v[136:139], a[64:67], a[224:227]\n\t"
    "ds_read_b64 v[162:163], v200\n\t"
    "s_waitcnt lgkmcnt(5)\n\t"
    "v_mfma_f32_16x16x32_bf16 a[240:243], v[144:147], a[64:67], a[240:243]\n\t"
    "ds_read_b64_tr_b16 v[128:129], v196 offset:49152\n\t"
    "v_mfma_f32_16x16x32_bf16 a[240:243], v[148:151], a[96:99], a[240:243]\n\t"
    "ds_read_b64_tr_b16 v[130:131], v196 offset:53248\n\t"
    "v_mfma_f32_16x16x32_bf16 a[244:247], v[148:151], a[100:103], a[244:247]\n\t"
    "buffer_load_dwordx4 v245, s[32:35], s84 offen offset:3072 sc1 lds\n\t"
    "v_mfma_f32_16x16x32_bf16 a[244:247], v[144:147], a[68:71], a[244:247]\n\t"
    "ds_read_b64 v[166:167], v201\n\t"
    "v_mfma_f32_16x16x32_bf16 a[248:251], v[144:147], a[72:75], a[248:251]\n\t"
    "ds_read_b64_tr_b16 v[132:133], v196 offset:57344\n\t"
    "v_mfma_f32_16x16x32_bf16 a[248:251], v[148:151], a[104:107], a[248:251]\n\t"
    "ds_read_b64_tr_b16 v[134:135], v196 offset:61440\n\t"
    "v_mfma_f32_16x16x32_bf16 a[252:255], v[148:151], a[108:111], a[252:255]\n\t"
    "buffer_load_dwordx4 a[176:179], v241, s[88:91], s46 offen sc1\n\t"
    "v_mfma_f32_16x16x32_bf16 a[252:255], v[144:147], a[76:79], a[252:255]\n\t"
    "ds_read_b64 v[170:171], v202\n\t"
    "s_waitcnt lgkmcnt(7)\n\t"
    "v_mfma_f32_16x16x32_bf16 a[140:143], v[152:155], a[76:79], a[140:143]\n\t"
    "ds_read_b64_tr_b16 v[136:137], v197 offset:49152\n\t"
    "v_mfma_f32_16x16x32_bf16 a[140:143], v[156:159], a[108:111], a[140:143]\n\t"
    "ds_read_b64_tr_b16 v[138:139], v197 offset:53248\n\t"
    "v_mfma_f32_16x16x32_bf16 a[136:139], v[156:159], a[104:107], a[136:139]\n\t"
    "buffer_load_dwordx4 a[192:195], v241, s[88:91], s47 offen sc1\n\t"
    "v_mfma_f32_16x16x32_bf16 a[136:139], v[152:155], a[72:75], a[136:139]\n\t"
    "ds_read_b64 v[174:175], v203\n\t"
    "v_mfma_f32_16x16x32_bf16 a[132:135], v[152:155], a[68:71], a[132:135]\n\t"
    "ds_read_b64_tr_b16 v[140:141], v197 offset:57344\n\t"
    "v_mfma_f32_16x16x32_bf16 a[132:135], v[156:159], a[100:103], a[132:135]\n\t"
    "ds_read_b64_tr_b16 v[142:143], v197 offset:61440\n\t"
    "v_mfma_f32_16x16x32_bf16 a[128:131], v[156:159], a[96:99], a[128:131]\n\t"
    "ds_read_b64 v[178:179], v204\n\t"
    "v_mfma_f32_16x16x32_bf16 a[128:131], v[152:155], a[64:67], a[128:131]\n\t"
    "s_waitcnt lgkmcnt(7)\n\t"
    "v_mfma_f32_16x16x32_bf16 a[16:19], v[128:131], a[64:67], a[16:19]\n\t"
    "ds_read_b64_tr_b16 v[144:145], v198 offset:49152\n\t"
    "v_mfma_f32_16x16x32_bf16 a[16:19], v[132:135], a[96:99], a[16:19]\n\t"
    "ds_read_b64_tr_b16 v[146:147], v198 offset:53248\n\t"
    "v_mfma_f32_16x16x32_bf16 a[20:23], v[132:135], a[100:103], a[20:23]\n\t"
    "buffer_load_dwordx4 a[180:183], v246, s[88:91], s46 offen sc1\n\t"
    "v_mfma_f32_16x16x32_bf16 a[20:23], v[128:131], a[68:71], a[20:23]\n\t"
    "ds_read_b64 v[182:183], v205\n\t"
    "v_mfma_f32_16x16x32_bf16 a[24:27], v[128:131], a[72:75], a[24:27]\n\t"
    "ds_read_b64_tr_b16 v[148:149], v198 offset:57344\n\t"
    "v_mfma_f32_16x16x32_bf16 a[24:27], v[132:135], a[104:107], a[24:27]\n\t"
    "ds_read_b64_tr_b16 v[150:151], v198 offset:61440\n\t"
    "v_mfma_f32_16x16x32_bf16 a[28:31], v[132:135], a[108:111], a[28:31]\n\t"
    "buffer_load_dwordx4 a[196:199], v246, s[88:91], s47 offen sc1\n\t"
    "v_mfma_f32_16x16x32_bf16 a[28:31], v[128:131], a[76:79], a[28:31]\n\t"
    "ds_read_b64 v[186:187], v206\n\t"
    "s_waitcnt lgkmcnt(7)\n\t"
    "v_mfma_f32_16x16x32_bf16 a[60:63], v[136:139], a[76:79], a[60:63]\n\t"
    "ds_read_b64_tr_b16 v[152:153], v199 offset:49152\n\t"
    "v_mfma_f32_16x16x32_bf16 a[60:63], v[140:143], a[108:111], a[60:63]\n\t"
    "ds_read_b64_tr_b16 v[154:155], v199 offset:53248\n\t"
    "v_mfma_f32_16x16x32_bf16 a[56:59], v[140:143], a[104:107], a[56:59]\n\t"
    "buffer_load_dwordx4 a[184:187], v247, s[88:91], s46 offen sc1\n\t"
    "v_mfma_f32_16x16x32_bf16 a[56:59], v[136:139], a[72:75], a[56:59]\n\t"
    "ds_read_b64 v[190:191], v207\n\t"
    "v_mfma_f32_16x16x32_bf16 a[52:55], v[136:139], a[68:71], a[52:55]\n\t"
    "ds_read_b64_tr_b16 v[156:157], v199 offset:57344\n\t"
    "v_mfma_f32_16x16x32_bf16 a[52:55], v[140:143], a[100:103], a[52:55]\n\t"
    "ds_read_b64_tr_b16 v[158:159], v199 offset:61440\n\t"
    "v_mfma_f32_16x16x32_bf16 a[48:51], v[140:143], a[96:99], a[48:51]\n\t"
    "v_mfma_f32_16x16x32_bf16 a[48:51], v[136:139], a[64:67], a[48:51]\n\t"
    "s_waitcnt vmcnt(18) lgkmcnt(6)\n\t"
    "s_barrier\n\t"
    "v_mfma_f32_16x16x32_bf16 a[80:83], v[144:147], a[64:67], a[80:83]\n\t"
    "ds_read_b64_tr_b16 v[128:129], v192 offset:0\n\t"
    "v_mfma_f32_16x16x32_bf16 a[80:83], v[148:151], a[96:99], a[80:83]\n\t"
    "ds_read_b64_tr_b16 v[130:131], v192 offset:4096\n\t"
    "v_mfma_f32_16x16x32_bf16 a[84:87], v[148:151], a[100:103], a[84:87]\n\t"
    "buffer_load_dwordx4 a[200:203], v247, s[88:91], s47 offen sc1\n\t"
    "v_mfma_f32_16x16x32_bf16 a[84:87], v[144:147], a[68:71], a[84:87]\n\t"
    "s_waitcnt lgkmcnt(4)\n\t"
    "v_lshlrev_b32 v160, 16, v162\n\t"
    "v_and_b32 v161, s59, v162\n\t"
    "v_mfma_f32_16x16x32_bf16 a[88:91], v[144:147], a[72:75], a[88:91]\n\t"
    "ds_read_b64_tr_b16 v[132:133], v192 offset:8192\n\t"
    "v_lshlrev_b32 v162, 16, v163\n\t"
    "v_mfma_f32_16x16x32_bf16 a[88:91], v[148:151], a[104:107], a[88:91]\n\t"
    "ds_read_b64_tr_b16 v[134:135], v192 offset:12288\n\t"
    "v_and_b32 v163, s59, v163\n\t"
    "v_mfma_f32_16x16x32_bf16 a[92:95], v[148:151], a[108:111], a[92:95]\n\t"
    "buffer_load_dwordx4 a[188:191], v249, s[88:91], s46 offen sc1\n\t"
    "v_mfma_f32_16x16x32_bf16 a[92:95], v[144:147], a[76:79], a[92:95]\n\t"
    "s_waitcnt lgkmcnt(4)\n\t"
    "v_lshlrev_b32 v164, 16, v166\n\t"
    "v_and_b32 v165, s59, v166\n\t"
    "v_mfma_f32_16x16x32_bf16 a[124:127], v[152:155], a[76:79], a[124:127]\n\t"
    "ds_read_b64_tr_b16 v[136:137], v193 offset:0\n\t"
    "v_lshlrev_b32 v166, 16, v167\n\t"
    "v_mfma_f32_16x16x32_bf16 a[124:127], v[156:159], a[108:111], a[124:127]\n\t"
    "ds_read_b64_tr_b16 v[138:139], v193 offset:4096\n\t"
    "v_and_b32 v167, s59, v167\n\t"
    "v_mfma_f32_16x16x32_bf16 a[120:123], v[156:159], a[104:107], a[120:123]\n\t"
    "buffer_load_dwordx4 a[204:207], v249, s[88:91], s47 offen sc1\n\t"
    "v_mfma_f32_16x16x32_bf16 a[120:123], v[152:155], a[72:75], a[120:123]\n\t"
    "s_bfe_u32 s84, s15, 262152\n\t"
    "s_lshl_b32 s84, s84, 17\n\t"
    "s_bfe_u32 s46, s15, 262152\n\t"
    "v_mfma_f32_16x16x32_bf16 a[116:119], v[152:155], a[68:71], a[116:119]\n\t"
    "ds_read_b64_tr_b16 v[140:141], v193 offset:8192\n\t"
    "s_lshl_b32 s46, s46, 13\n\t"
    "v_mfma_f32_16x16x32_bf16 a[116:119], v[156:159], a[100:103], a[116:119]\n\t"
    "ds_read_b64_tr_b16 v[142:143], v193 offset:12288\n\t"
    "s_add_u32 s46, s46, s60\n\t"
    "v_mfma_f32_16x16x32_bf16 a[112:115], v[156:159], a[96:99], a[112:115]\n\t"
    "s_add_u32 s47, s46, 4096\n\t"
    "v_lshlrev_b32 v168, 16, v170\n\t"
    "v_and_b32 v169, s59, v170\n\t"
    "v_mfma_f32_16x16x32_bf16 a[112:115], v[152:155], a[64:67], a[112:115]\n\t"
    "s_waitcnt vmcnt(12) lgkmcnt(4)\n\t"
    "v_lshlrev_b32 v170, 16, v171\n\t"
    "v_and_b32 v171, s59, v171\n\t"
    "v_mfma_f32_16x16x32_bf16 a[208:211], v[128:131], a[144:147], a[208:211]\n\t"
    "ds_read_b64_tr_b16 v[144:145], v194 offset:0\n\t"
    "v_lshlrev_b32 v172, 16, v174\n\t"
    "v_mfma_f32_16x16x32_bf16 a[208:211], v[132:135], a[160:163], a[208:211]\n\t"
    "ds_read_b64_tr_b16 v[146:147], v194 offset:4096\n\t"
    "s_add_u32 m0, s48, 81920\n\t"
    "v_mfma_f32_16x16x32_bf16 a[212:215], v[132:135], a[164:167], a[212:215]\n\t"
    "buffer_load_dwordx4 v242, s[32:35], s84 offen sc1 lds\n\t"
    "v_mfma_f32_16x16x32_bf16 a[212:215], v[128:131], a[148:151], a[212:215]\n\t"
    "v_and_b32 v173, s59, v174\n\t"
    "v_lshlrev_b32 v174, 16, v175\n\t"
    "v_mfma_f32_16x16x32_bf16 a[216:219], v[128:131], a[152:155], a[216:219]\n\t"
    "ds_read_b64_tr_b16 v[148:149], v194 offset:8192\n\t"
    "v_and_b32 v175, s59, v175\n\t"
    "v_mfma_f32_16x16x32_bf16 a[216:219], v[132:135], a[168:171], a[216:219]\n\t"
    "ds_read_b64_tr_b16 v[150:151], v194 offset:12288\n\t"
    "v_lshlrev_b32 v176, 16, v178\n\t"
    "v_mfma_f32_16x16x32_bf16 a[220:223], v[132:135], a[172:175], a[220:223]\n\t"
    "buffer_load_dwordx4 v243, s[32:35], s84 offen offset:1024 sc1 lds\n\t"
    "v_mfma_f32_16x16x32_bf16 a[220:223], v[128:131], a[156:159], a[220:223]\n\t"
    "s_waitcnt lgkmcnt(4)\n\t"
    "v_and_b32 v177, s59, v178\n\t"
    "v_lshlrev_b32 v178, 16, v179\n\t"
    "v_mfma_f32_16x16x32_bf16 a[236:239], v[136:139], a[156:159], a[236:239]\n\t"
    "ds_read_b64_tr_b16 v[152:153], v195 offset:0\n\t"
    "v_and_b32 v179, s59, v179\n\t"
    "v_mfma_f32_16x16x32_bf16 a[236:239], v[140:143], a[172:175], a[236:239]\n\t"
    "ds_read_b64_tr_b16 v[154:155], v195 offset:4096\n\t"
    "v_lshlrev_b32 v180, 16, v182\n\t"
    "v_mfma_f32_16x16x32_bf16 a[232:235], v[140:143], a[168:171], a[232:235]\n\t"
    "buffer_load_dwordx4 v244, s[32:35], s84 offen offset:2048 sc1 lds\n\t"
    "v_mfma_f32_16x16x32_bf16 a[232:235], v[136:139], a[152:155], a[232:235]\n\t"
    "v_and_b32 v181, s59, v182\n\t"
    "v_lshlrev_b32 v182, 16, v183\n\t"
    "v_mfma_f32_16x16x32_bf16 a[228:231], v[136:139], a[148:151], a[228:231]\n\t"
    "ds_read_b64_tr_b16 v[156:157], v195 offset:8192\n\t"
    "v_and_b32 v183, s59, v183\n\t"
    "v_mfma_f32_16x16x32_bf16 a[228:231], v[140:143], a[164:167], a[228:231]\n\t"
    "ds_read_b64_tr_b16 v[158:159], v195 offset:12288\n\t"
    "v_lshlrev_b32 v184, 16, v186\n\t"
    "v_mfma_f32_16x16x32_bf16 a[224:227], v[140:143], a[160:163], a[224:227]\n\t"
    "v_and_b32 v185, s59, v186\n\t"
    "v_lshlrev_b32 v186, 16, v187\n\t"
    "v_mfma_f32_16x16x32_bf16 a[224:227], v[136:139], a[144:147], a[224:227]\n\t"
    "s_waitcnt lgkmcnt(4)\n\t"
    "v_and_b32 v187, s59, v187\n\t"
    "v_lshlrev_b32 v188, 16, v190\n\t"
    "v_mfma_f32_16x16x32_bf16 a[240:243], v[144:147], a[144:147], a[240:243]\n\t"
    "ds_read_b64_tr_b16 v[128:129], v196 offset:0\n\t"
    "v_and_b32 v189, s59, v190\n\t"
    "v_mfma_f32_16x16x32_bf16 a[240:243], v[148:151], a[160:163], a[240:243]\n\t"
    "ds_read_b64_tr_b16 v[130:131], v196 offset:4096\n\t"
    "v_lshlrev_b32 v190, 16, v191\n\t"
    "v_mfma_f32_16x16x32_bf16 a[244:247], v[148:151], a[164:167], a[244:247]\n\t"
    "buffer_load_dwordx4 v245, s[32:35], s84 offen offset:3072 sc1 lds\n\t"
    "v_mfma_f32_16x16x32_bf16 a[244:247], v[144:147], a[148:151], a[244:247]\n\t"
    "v_and_b32 v191, s59, v191\n\t"
    "v_mfma_f32_16x16x32_bf16 a[248:251], v[144:147], a[152:155], a[248:251]\n\t"
    "ds_read_b64_tr_b16 v[132:133], v196 offset:8192\n\t"
    "v_mfma_f32_16x16x32_bf16 a[248:251], v[148:151], a[168:171], a[248:251]\n\t"
    "ds_read_b64_tr_b16 v[134:135], v196 offset:12288\n\t"
    "v_mfma_f32_16x16x32_bf16 a[252:255], v[148:151], a[172:175], a[252:255]\n\t"
    "buffer_load_dwordx4 a[0:3], v241, s[88:91], s46 offen sc1\n\t"
    "v_mfma_f32_16x16x32_bf16 a[252:255], v[144:147], a[156:159], a[252:255]\n\t"
    "s_waitcnt lgkmcnt(4)\n\t"
    "v_mfma_f32_16x16x32_bf16 a[140:143], v[152:155], a[156:159], a[140:143]\n\t"
    "ds_read_b64_tr_b16 v[136:137], v197 offset:0\n\t"
    "v_mfma_f32_16x16x32_bf16 a[140:143], v[156:159], a[172:175], a[140:143]\n\t"
    "ds_read_b64_tr_b16 v[138:139], v197 offset:4096\n\t"
    "v_mfma_f32_16x16x32_bf16 a[136:139], v[156:159], a[168:171], a[136:139]\n\t"
    "buffer_load_dwordx4 a[32:35], v241, s[88:91], s47 offen sc1\n\t"
    "v_mfma_f32_16x16x32_bf16 a[136:139], v[152:155], a[152:155], a[136:139]\n\t"
    "v_mfma_f32_16x16x32_bf16 a[132:135], v[152:155], a[148:151], a[132:135]\n\t"
    "ds_read_b64_tr_b16 v[140:141], v197 offset:8192\n\t"
    "v_mfma_f32_16x16x32_bf16 a[132:135], v[156:159], a[164:167], a[132:135]\n\t"
    "ds_read_b64_tr_b16 v[142:143], v197 offset:12288\n\t"
    "v_mfma_f32_16x16x32_bf16 a[128:131], v[156:159], a[160:163], a[128:131]\n\t"
    "v_mfma_f32_16x16x32_bf16 a[128:131], v[152:155], a[144:147], a[128:131]\n\t"
    "s_waitcnt lgkmcnt(4)\n\t"
    "v_mfma_f32_16x16x32_bf16 a[16:19], v[128:131], a[144:147], a[16:19]\n\t"
    "ds_read_b64_tr_b16 v[144:145], v198 offset:0\n\t"
    "v_mfma_f32_16x16x32_bf16 a[16:19], v[132:135], a[160:163], a[16:19]\n\t"
    "ds_read_b64_tr_b16 v[146:147], v198 offset:4096\n\t"
    "v_mfma_f32_16x16x32_bf16 a[20:23], v[132:135], a[164:167], a[20:23]\n\t"
    "buffer_load_dwordx4 a[4:7], v246, s[88:91], s46 offen sc1\n\t"
    "v_mfma_f32_16x16x32_bf16 a[20:23], v[128:131], a[148:151], a[20:23]\n\t"
    "v_mfma_f32_16x16x32_bf16 a[24:27], v[128:131], a[152:155], a[24:27]\n\t"
    "ds_read_b64_tr_b16 v[148:149], v198 offset:8192\n\t"
    "v_mfma_f32_16x16x32_bf16 a[24:27], v[132:135], a[168:171], a[24:27]\n\t"
    "ds_read_b64_tr_b16 v[150:151], v198 offset:12288\n\t"
    "v_mfma_f32_16x16x32_bf16 a[28:31], v[132:135], a[172:175], a[28:31]\n\t"
    "buffer_load_dwordx4 a[36:39], v246, s[88:91], s47 offen sc1\n\t"
    "v_mfma_f32_16x16x32_bf16 a[28:31], v[128:131], a[156:159], a[28:31]\n\t"
    "s_waitcnt lgkmcnt(4)\n\t"
    "v_mfma_f32_16x16x32_bf16 a[60:63], v[136:139], a[156:159], a[60:63]\n\t"
    "ds_read_b64_tr_b16 v[152:153], v199 offset:0\n\t"
    "v_mfma_f32_16x16x32_bf16 a[60:63], v[140:143], a[172:175], a[60:63]\n\t"
    "ds_read_b64_tr_b16 v[154:155], v199 offset:4096\n\t"
    "v_mfma_f32_16x16x32_bf16 a[56:59], v[140:143], a[168:171], a[56:59]\n\t"
    "buffer_load_dwordx4 a[8:11], v247, s[88:91], s46 offen sc1\n\t"
    "v_mfma_f32_16x16x32_bf16 a[56:59], v[136:139], a[152:155], a[56:59]\n\t"
    "v_mfma_f32_16x16x32_bf16 a[52:55], v[136:139], a[148:151], a[52:55]\n\t"
    "ds_read_b64_tr_b16 v[156:157], v199 offset:8192\n\t"
    "v_mfma_f32_16x16x32_bf16 a[52:55], v[140:143], a[164:167], a[52:55]\n\t"
    "ds_read_b64_tr_b16 v[158:159], v199 offset:12288\n\t"
    "v_mfma_f32_16x16x32_bf16 a[48:51], v[140:143], a[160:163], a[48:51]\n\t"
    "v_mfma_f32_16x16x32_bf16 a[48:51], v[136:139], a[144:147], a[48:51]\n\t"
    "s_waitcnt vmcnt(17) lgkmcnt(4)\n\t"
    "s_barrier\n\t"
    "v_mfma_f32_16x16x32_bf16 a[80:83], v[144:147], a[144:147], a[80:83]\n\t"
    "ds_read_b64_tr_b16 v[128:129], v192 offset:16384\n\t"
    "v_mfma_f32_16x16x32_bf16 a[80:83], v[148:151], a[160:163], a[80:83]\n\t"
    "ds_read_b64_tr_b16 v[130:131], v192 offset:20480\n\t"
    "v_mfma_f32_16x16x32_bf16 a[84:87], v[148:151], a[164:167], a[84:87]\n\t"
    "buffer_load_dwordx4 a[40:43], v247, s[88:91], s47 offen sc1\n\t"
    "v_mfma_f32_16x16x32_bf16 a[84:87], v[144:147], a[148:151], a[84:87]\n\t"
    "s_cmp_eq_u32 s3, 0\n\t"
    "s_cbranch_scc1 .Lfb_fo\n\t"
    ".Lfb_fo_r:\n\t"
    "s_getpc_b64 s[28:29]\n\t"
    ".Lfb_ct3:\n\t"
    "v_mfma_f32_16x16x32_bf16 a[88:91], v[144:147], a[152:155], a[88:91]\n\t"
    "ds_read_b64_tr_b16 v[132:133], v192 offset:24576\n\t"
    "s_lshr_b32 s77, s2, 3\n\t"
    "v_mfma_f32_16x16x32_bf16 a[88:91], v[148:151], a[168:171], a[88:91]\n\t"
    "ds_read_b64_tr_b16 v[134:135], v192 offset:28672\n\t"
    "s_lshl_b32 s77, s77, 7\n\t"
    "v_mfma_f32_16x16x32_bf16 a[92:95], v[148:151], a[172:175], a[92:95]\n\t"
    "s_lshl_b32 s78, s3, 12\n\t"
    "buffer_load_dwordx4 a[12:15], v249, s[88:91], s46 offen sc1\n\t"
    "v_mfma_f32_16x16x32_bf16 a[92:95], v[144:147], a[156:159], a[92:95]\n\t"
    "s_waitcnt lgkmcnt(4)\n\t"
    "s_add_u32 s77, s77, s78\n\t"
    "s_add_u32 s77, s77, .Lfb_mid0-.Lfb_ct3+49152\n\t"
    "v_mfma_f32_16x16x32_bf16 a[124:127], v[152:155], a[156:159], a[124:127]\n\t"
    "ds_read_b64_tr_b16 v[136:137], v193 offset:16384\n\t"
    "s_min_u32 s77, s77, .Lfb_end-.Lfb_ct3\n\t"
    "v_mfma_f32_16x16x32_bf16 a[124:127], v[156:159], a[172:175], a[124:127]\n\t"
    "ds_read_b64_tr_b16 v[138:139], v193 offset:20480\n\t"
    "v_mov_b32 v251, s77\n\t"
    "v_mfma_f32_16x16x32_bf16 a[120:123], v[156:159], a[168:171], a[120:123]\n\t"
    "buffer_load_dwordx4 a[44:47], v249, s[88:91], s47 offen sc1\n\t"
    "v_mfma_f32_16x16x32_bf16 a[120:123], v[152:155], a[152:155], a[120:123]\n\t"
    "s_bfe_u32 s84, s15, 262156\n\t"
    "s_lshl_b32 s84, s84, 17\n\t"
    "s_bfe_u32 s46, s15, 262156\n\t"
    "v_mfma_f32_16x16x32_bf16 a[116:119], v[152:155], a[148:151], a[116:119]\n\t"
    "ds_read_b64_tr_b16 v[140:141], v193 offset:24576\n\t"
    "s_lshl_b32 s46, s46, 13\n\t"
    "v_mfma_f32_16x16x32_bf16 a[116:119], v[156:159], a[164:167], a[116:119]\n\t"
    "ds_read_b64_tr_b16 v[142:143], v193 offset:28672\n\t"
    "s_add_u32 s46, s46, s60\n\t"
    "v_mfma_f32_16x16x32_bf16 a[112:115], v[156:159], a[160:163], a[112:115]\n\t"
    "s_add_u32 s47, s46, 4096\n\t"
    "global_load_dword v251, v251, s[28:29]\n\t"
    "v_mfma_f32_16x16x32_bf16 a[112:115], v[152:155], a[144:147], a[112:115]\n\t"
    "s_waitcnt vmcnt(13) lgkmcnt(4)\n\t"
    "v_mfma_f32_16x16x32_bf16 a[208:211], v[128:131], a[176:179], a[208:211]\n\t"
    "ds_read_b64_tr_b16 v[144:145], v194 offset:16384\n\t"
    "v_mfma_f32_16x16x32_bf16 a[208:211], v[132:135], a[192:195], a[208:211]\n\t"
    "ds_read_b64_tr_b16 v[146:147], v194 offset:20480\n\t"
    "s_add_u32 m0, s48, 98304\n\t"
    "v_mfma_f32_16x16x32_bf16 a[212:215], v[132:135], a[196:199], a[212:215]\n\t"
    "buffer_load_dwordx4 v242, s[32:35], s84 offen sc1 lds\n\t"
    "v_mfma_f32_16x16x32_bf16 a[212:215], v[128:131], a[180:183], a[212:215]\n\t"
    "v_mfma_f32_16x16x32_bf16 a[216:219], v[128:131], a[184:187], a[216:219]\n\t"
    "ds_read_b64_tr_b16 v[148:149], v194 offset:24576\n\t"
    "v_mfma_f32_16x16x32_bf16 a[216:219], v[132:135], a[200:203], a[216:219]\n\t"
    "ds_read_b64_tr_b16 v[150:151], v194 offset:28672\n\t"
    "v_mfma_f32_16x16x32_bf16 a[220:223], v[132:135], a[204:207], a[220:223]\n\t"
    "buffer_load_dwordx4 v243, s[32:35], s84 offen offset:1024 sc1 lds\n\t"
    "v_mfma_f32_16x16x32_bf16 a[220:223], v[128:131], a[188:191], a[220:223]\n\t"
    "s_waitcnt lgkmcnt(4)\n\t"
    "v_mfma_f32_16x16x32_bf16 a[236:239], v[136:139], a[188:191], a[236:239]\n\t"
    "ds_read_b64_tr_b16 v[152:153], v195 offset:16384\n\t"
    "v_mfma_f32_16x16x32_bf16 a[236:239], v[140:143], a[204:207], a[236:239]\n\t"
    "ds_read_b64_tr_b16 v[154:155], v195 offset:20480\n\t"
    "v_mfma_f32_16x16x32_bf16 a[232:235], v[140:143], a[200:203], a[232:235]\n\t"
    "buffer_load_dwordx4 v244, s[32:35], s84 offen offset:2048 sc1 lds\n\t"
    "v_mfma_f32_16x16x32_bf16 a[232:235], v[136:139], a[184:187], a[232:235]\n\t"
    "v_mfma_f32_16x16x32_bf16 a[228:231], v[136:139], a[180:183], a[228:231]\n\t"
    "ds_read_b64_tr_b16 v[156:157], v195 offset:24576\n\t"
    "v_mfma_f32_16x16x32_bf16 a[228:231], v[140:143], a[196:199], a[228:231]\n\t"
    "ds_read_b64_tr_b16 v[158:159], v195 offset:28672\n\t"
    "v_mfma_f32_16x16x32_bf16 a[224:227], v[140:143], a[192:195], a[224:227]\n\t"
    "v_mfma_f32_16x16x32_bf16 a[224:227], v[136:139], a[176:179], a[224:227]\n\t"
    "s_waitcnt lgkmcnt(4)\n\t"
    "v_mfma_f32_16x16x32_bf16 a[240:243], v[144:147], a[176:179], a[240:243]\n\t"
    "ds_read_b64_tr_b16 v[128:129], v196 offset:16384\n\t"
    "v_mfma_f32_16x16x32_bf16 a[240:243], v[148:151], a[192:195], a[240:243]\n\t"
    "ds_read_b64_tr_b16 v[130:131], v196 offset:20480\n\t"
    "v_mfma_f32_16x16x32_bf16 a[244:247], v[148:151], a[196:199], a[244:247]\n\t"
    "buffer_load_dwordx4 v245, s[32:35], s84 offen offset:3072 sc1 lds\n\t"
    "v_mfma_f32_16x16x32_bf16 a[244:247], v[144:147], a[180:183], a[244:247]\n\t"
    "v_mfma_f32_16x16x32_bf16 a[248:251], v[144:147], a[184:187], a[248:251]\n\t"
    "ds_read_b64_tr_b16 v[132:133], v196 offset:24576\n\t"
    "v_mfma_f32_16x16x32_bf16 a[248:251], v[148:151], a[200:203], a[248:251]\n\t"
    "ds_read_b64_tr_b16 v[134:135], v196 offset:28672\n\t"
    "v_mfma_f32_16x16x32_bf16 a[252:255], v[148:151], a[204:207], a[252:255]\n\t"
    "buffer_load_dwordx4 a[64:67], v241, s[88:91], s46 offen sc1\n\t"
    "v_mfma_f32_16x16x32_bf16 a[252:255], v[144:147], a[188:191], a[252:255]\n\t"
    "s_waitcnt lgkmcnt(4)\n\t"
    "v_mfma_f32_16x16x32_bf16 a[140:143], v[152:155], a[188:191], a[140:143]\n\t"
    "ds_read_b64_tr_b16 v[136:137], v197 offset:16384\n\t"
    "v_mfma_f32_16x16x32_bf16 a[140:143], v[156:159], a[204:207], a[140:143]\n\t"
    "ds_read_b64_tr_b16 v[138:139], v197 offset:20480\n\t"
    "v_mfma_f32_16x16x32_bf16 a[136:139], v[156:159], a[200:203], a[136:139]\n\t"
    "buffer_load_dwordx4 a[96:99], v241, s[88:91], s47 offen sc1\n\t"
    "v_mfma_f32_16x16x32_bf16 a[136:139], v[152:155], a[184:187], a[136:139]\n\t"
    "v_mfma_f32_16x16x32_bf16 a[132:135], v[152:155], a[180:183], a[132:135]\n\t"
    "ds_read_b64_tr_b16 v[140:141], v197 offset:24576\n\t"
    "v_mfma_f32_16x16x32_bf16 a[132:135], v[156:159], a[196:199], a[132:135]\n\t"
    "ds_read_b64_tr_b16 v[142:143], v197 offset:28672\n\t"
    "v_mfma_f32_16x16x32_bf16 a[128:131], v[156:159], a[192:195], a[128:131]\n\t"
    "v_mfma_f32_16x16x32_bf16 a[128:131], v[152:155], a[176:179], a[128:131]\n\t"
    "s_waitcnt lgkmcnt(4)\n\t"
    "v_mfma_f32_16x16x32_bf16 a[16:19], v[128:131], a[176:179], a[16:19]\n\t"
    "ds_read_b64_tr_b16 v[144:145], v198 offset:16384\n\t"
    "v_mfma_f32_16x16x32_bf16 a[16:19], v[132:135], a[192:195], a[16:19]\n\t"
    "ds_read_b64_tr_b16 v[146:147], v198 offset:20480\n\t"
    "v_mfma_f32_16x16x32_bf16 a[20:23], v[132:135], a[196:199], a[20:23]\n\t"
    "buffer_load_dwordx4 a[68:71], v246, s[88:91], s46 offen sc1\n\t"
    "v_mfma_f32_16x16x32_bf16 a[20:23], v[128:131], a[180:183], a[20:23]\n\t"
    "v_mfma_f32_16x16x32_bf16 a[24:27], v[128:131], a[184:187], a[24:27]\n\t"
    "ds_read_b64_tr_b16 v[148:149], v198 offset:24576\n\t"
    "v_mfma_f32_16x16x32_bf16 a[24:27], v[132:135], a[200:203], a[24:27]\n\t"
    "ds_read_b64_tr_b16 v[150:151], v198 offset:28672\n\t"
    "v_mfma_f32_16x16x32_bf16 a[28:31], v[132:135], a[204:207], a[28:31]\n\t"
    "buffer_load_dwordx4 a[100:103], v246, s[88:91], s47 offen sc1\n\t"
    "v_mfma_f32_16x16x32_bf16 a[28:31], v[128:131], a[188:191], a[28:31]\n\t"
    "s_waitcnt lgkmcnt(4)\n\t"
    "v_mfma_f32_16x16x32_bf16 a[60:63], v[136:139], a[188:191], a[60:63]\n\t"
    "ds_read_b64_tr_b16 v[152:153], v199 offset:16384\n\t"
    "v_mfma_f32_16x16x32_bf16 a[60:63], v[140:143], a[204:207], a[60:63]\n\t"
    "ds_read_b64_tr_b16 v[154:155], v199 offset:20480\n\t"
    "v_mfma_f32_16x16x32_bf16 a[56:59], v[140:143], a[200:203], a[56:59]\n\t"
    "buffer_load_dwordx4 a[72:75], v247, s[88:91], s46 offen sc1\n\t"
    "v_mfma_f32_16x16x32_bf16 a[56:59], v[136:139], a[184:187], a[56:59]\n\t"
    "v_mfma_f32_16x16x32_bf16 a[52:55], v[136:139], a[180:183], a[52:55]\n\t"
    "ds_read_b64_tr_b16 v[156:157], v199 offset:24576\n\t"
    "v_mfma_f32_16x16x32_bf16 a[52:55], v[140:143], a[196:199], a[52:55]\n\t"
    "ds_read_b64_tr_b16 v[158:159], v199 offset:28672\n\t"
    "v_mfma_f32_16x16x32_bf16 a[48:51], v[140:143], a[192:195], a[48:51]\n\t"
    "v_mfma_f32_16x16x32_bf16 a[48:51], v[136:139], a[176:179], a[48:51]\n\t"
    "s_waitcnt vmcnt(18) lgkmcnt(4)\n\t"
    "s_barrier\n\t"
    "v_mfma_f32_16x16x32_bf16 a[80:83], v[144:147], a[176:179], a[80:83]\n\t"
    "ds_read_b64_tr_b16 v[128:129], v192 offset:32768\n\t"
    "v_mfma_f32_16x16x32_bf16 a[80:83], v[148:151], a[192:195], a[80:83]\n\t"
    "ds_read_b64_tr_b16 v[130:131], v192 offset:36864\n\t"
    "v_mfma_f32_16x16x32_bf16 a[84:87], v[148:151], a[196:199], a[84:87]\n\t"
    "buffer_load_dwordx4 a[104:107], v247, s[88:91], s47 offen sc1\n\t"
    "v_mfma_f32_16x16x32_bf16 a[84:87], v[144:147], a[180:183], a[84:87]\n\t"
    "v_mfma_f32_16x16x32_bf16 a[88:91], v[144:147], a[184:187], a[88:91]\n\t"
    "ds_read_b64_tr_b16 v[132:133], v192 offset:40960\n\t"
    "v_mfma_f32_16x16x32_bf16 a[88:91], v[148:151], a[200:203], a[88:91]\n\t"
    "ds_read_b64_tr_b16 v[134:135], v192 offset:45056\n\t"
    "v_mfma_f32_16x16x32_bf16 a[92:95], v[148:151], a[204:207], a[92:95]\n\t"
    "buffer_load_dwordx4 a[76:79], v249, s[88:91], s46 offen sc1\n\t"
    "v_mfma_f32_16x16x32_bf16 a[92:95], v[144:147], a[188:191], a[92:95]\n\t"
    "s_waitcnt lgkmcnt(4)\n\t"
    "v_mfma_f32_16x16x32_bf16 a[124:127], v[152:155], a[188:191], a[124:127]\n\t"
    "ds_read_b64_tr_b16 v[136:137], v193 offset:32768\n\t"
    "v_mfma_f32_16x16x32_bf16 a[124:127], v[156:159], a[204:207], a[124:127]\n\t"
    "ds_read_b64_tr_b16 v[138:139], v193 offset:36864\n\t"
    "v_mfma_f32_16x16x32_bf16 a[120:123], v[156:159], a[200:203], a[120:123]\n\t"
    "buffer_load_dwordx4 a[108:111], v249, s[88:91], s47 offen sc1\n\t"
    "v_mfma_f32_16x16x32_bf16 a[120:123], v[152:155], a[184:187], a[120:123]\n\t"
    "s_bfe_u32 s84, s15, 262160\n\t"
    "s_lshl_b32 s84, s84, 17\n\t"
    "s_bfe_u32 s46, s15, 262160\n\t"
    "v_mfma_f32_16x16x32_bf16 a[116:119], v[152:155], a[180:183], a[116:119]\n\t"
    "ds_read_b64_tr_b16 v[140:141], v193 offset:40960\n\t"
    "s_lshl_b32 s46, s46, 13\n\t"
    "v_mfma_f32_16x16x32_bf16 a[116:119], v[156:159], a[196:199], a[116:119]\n\t"
    "ds_read_b64_tr_b16 v[142:143], v193 offset:45056\n\t"
    "s_add_u32 s46, s46, s60\n\t"
    "v_mfma_f32_16x16x32_bf16 a[112:115], v[156:159], a[192:195], a[112:115]\n\t"
    "s_add_u32 s47, s46, 4096\n\t"
    "v_mfma_f32_16x16x32_bf16 a[112:115], v[152:155], a[176:179], a[112:115]\n\t"
    "s_waitcnt vmcnt(13) lgkmcnt(4)\n\t"
    "v_mfma_f32_16x16x32_bf16 a[208:211], v[128:131], a[0:3], a[208:211]\n\t"
    "ds_read_b64_tr_b16 v[144:145], v194 offset:32768\n\t"
    "v_mfma_f32_16x16x32_bf16 a[208:211], v[132:135], a[32:35], a[208:211]\n\t"
    "ds_read_b64_tr_b16 v[146:147], v194 offset:36864\n\t"
    "s_add_u32 m0, s48, 49152\n\t"
    "v_mfma_f32_16x16x32_bf16 a[212:215], v[132:135], a[36:39], a[212:215]\n\t"
    "buffer_load_dwordx4 v242, s[32:35], s84 offen sc1 lds\n\t"
    "v_mfma_f32_16x16x32_bf16 a[212:215], v[128:131], a[4:7], a[212:215]\n\t"
    "v_mfma_f32_16x16x32_bf16 a[216:219], v[128:131], a[8:11], a[216:219]\n\t"
    "ds_read_b64_tr_b16 v[148:149], v194 offset:40960\n\t"
    "v_mfma_f32_16x16x32_bf16 a[216:219], v[132:135], a[40:43], a[216:219]\n\t"
    "ds_read_b64_tr_b16 v[150:151], v194 offset:45056\n\t"
    "v_mfma_f32_16x16x32_bf16 a[220:223], v[132:135], a[44:47], a[220:223]\n\t"
    "buffer_load_dwordx4 v243, s[32:35], s84 offen offset:1024 sc1 lds\n\t"
    "v_mfma_f32_16x16x32_bf16 a[220:223], v[128:131], a[12:15], a[220:223]\n\t"
    "s_waitcnt lgkmcnt(4)\n\t"
    "v_mfma_f32_16x16x32_bf16 a[236:239], v[136:139], a[12:15], a[236:239]\n\t"
    "ds_read_b64_tr_b16 v[152:153], v195 offset:32768\n\t"
    "v_mfma_f32_16x16x32_bf16 a[236:239], v[140:143], a[44:47], a[236:239]\n\t"
    "ds_read_b64_tr_b16 v[154:155], v195 offset:36864\n\t"
    "v_mfma_f32_16x16x32_bf16 a[232:235], v[140:143], a[40:43], a[232:235]\n\t"
    "buffer_load_dwordx4 v244, s[32:35], s84 offen offset:2048 sc1 lds\n\t"
    "v_mfma_f32_16x16x32_bf16 a[232:235], v[136:139], a[8:11], a[232:235]\n\t"
    "v_mfma_f32_16x16x32_bf16 a[228:231], v[136:139], a[4:7], a[228:231]\n\t"
    "ds_read_b64_tr_b16 v[156:157], v195 offset:40960\n\t"
    "v_mfma_f32_16x16x32_bf16 a[228:231], v[140:143], a[36:39], a[228:231]\n\t"
    "ds_read_b64_tr_b16 v[158:159], v195 offset:45056\n\t"
    "v_mfma_f32_16x16x32_bf16 a[224:227], v[140:143], a[32:35], a[224:227]\n\t"
    "v_mfma_f32_16x16x32_bf16 a[224:227], v[136:139], a[0:3], a[224:227]\n\t"
    "s_waitcnt lgkmcnt(4)\n\t"
    "v_mfma_f32_16x16x32_bf16 a[240:243], v[144:147], a[0:3], a[240:243]\n\t"
    "ds_read_b64_tr_b16 v[128:129], v196 offset:32768\n\t"
    "v_mfma_f32_16x16x32_bf16 a[240:243], v[148:151], a[32:35], a[240:243]\n\t"
    "ds_read_b64_tr_b16 v[130:131], v196 offset:36864\n\t"
    "v_mfma_f32_16x16x32_bf16 a[244:247], v[148:151], a[36:39], a[244:247]\n\t"
    "buffer_load_dwordx4 v245, s[32:35], s84 offen offset:3072 sc1 lds\n\t"
    "v_mfma_f32_16x16x32_bf16 a[244:247], v[144:147], a[4:7], a[244:247]\n\t"
    "v_mfma_f32_16x16x32_bf16 a[248:251], v[144:147], a[8:11], a[248:251]\n\t"
    "ds_read_b64_tr_b16 v[132:133], v196 offset:40960\n\t"
    "v_mfma_f32_16x16x32_bf16 a[248:251], v[148:151], a[40:43], a[248:251]\n\t"
    "ds_read_b64_tr_b16 v[134:135], v196 offset:45056\n\t"
    "v_mfma_f32_16x16x32_bf16 a[252:255], v[148:151], a[44:47], a[252:255]\n\t"
    "buffer_load_dwordx4 a[144:147], v241, s[88:91], s46 offen sc1\n\t"
    "v_mfma_f32_16x16x32_bf16 a[252:255], v[144:147], a[12:15], a[252:255]\n\t"
    "s_waitcnt lgkmcnt(4)\n\t"
    "v_mfma_f32_16x16x32_bf16 a[140:143], v[152:155], a[12:15], a[140:143]\n\t"
    "ds_read_b64_tr_b16 v[136:137], v197 offset:32768\n\t"
    "v_mfma_f32_16x16x32_bf16 a[140:143], v[156:159], a[44:47], a[140:143]\n\t"
    "ds_read_b64_tr_b16 v[138:139], v197 offset:36864\n\t"
    "v_mfma_f32_16x16x32_bf16 a[136:139], v[156:159], a[40:43], a[136:139]\n\t"
    "buffer_load_dwordx4 a[160:163], v241, s[88:91], s47 offen sc1\n\t"
    "v_mfma_f32_16x16x32_bf16 a[136:139], v[152:155], a[8:11], a[136:139]\n\t"
    "v_mfma_f32_16x16x32_bf16 a[132:135], v[152:155], a[4:7], a[132:135]\n\t"
    "ds_read_b64_tr_b16 v[140:141], v197 offset:40960\n\t"
    "v_mfma_f32_16x16x32_bf16 a[132:135], v[156:159], a[36:39], a[132:135]\n\t"
    "ds_read_b64_tr_b16 v[142:143], v197 offset:45056\n\t"
    "v_mfma_f32_16x16x32_bf16 a[128:131], v[156:159], a[32:35], a[128:131]\n\t"
    "v_mfma_f32_16x16x32_bf16 a[128:131], v[152:155], a[0:3], a[128:131]\n\t"
    "s_waitcnt lgkmcnt(4)\n\t"
    "v_mfma_f32_16x16x32_bf16 a[16:19], v[128:131], a[0:3], a[16:19]\n\t"
    "ds_read_b64_tr_b16 v[144:145], v198 offset:32768\n\t"
    "v_mfma_f32_16x16x32_bf16 a[16:19], v[132:135], a[32:35], a[16:19]\n\t"
    "ds_read_b64_tr_b16 v[146:147], v198 offset:36864\n\t"
    "v_mfma_f32_16x16x32_bf16 a[20:23], v[132:135], a[36:39], a[20:23]\n\t"
    "buffer_load_dwordx4 a[148:151], v246, s[88:91], s46 offen sc1\n\t"
    "v_mfma_f32_16x16x32_bf16 a[20:23], v[128:131], a[4:7], a[20:23]\n\t"
    "v_mfma_f32_16x16x32_bf16 a[24:27], v[128:131], a[8:11], a[24:27]\n\t"
    "ds_read_b64_tr_b16 v[148:149], v198 offset:40960\n\t"
    "v_mfma_f32_16x16x32_bf16 a[24:27], v[132:135], a[40:43], a[24:27]\n\t"
    "ds_read_b64_tr_b16 v[150:151], v198 offset:45056\n\t"
    "v_mfma_f32_16x16x32_bf16 a[28:31], v[132:135], a[44:47], a[28:31]\n\t"
    "buffer_load_dwordx4 a[164:167], v246, s[88:91], s47 offen sc1\n\t"
    "v_mfma_f32_16x16x32_bf16 a[28:31], v[128:131], a[12:15], a[28:31]\n\t"
    "s_waitcnt lgkmcnt(4)\n\t"
    "v_mfma_f32_16x16x32_bf16 a[60:63], v[136:139], a[12:15], a[60:63]\n\t"
    "ds_read_b64_tr_b16 v[152:153], v199 offset:32768\n\t"
    "v_mfma_f32_16x16x32_bf16 a[60:63], v[140:143], a[44:47], a[60:63]\n\t"
    "ds_read_b64_tr_b16 v[154:155], v199 offset:36864\n\t"
    "v_mfma_f32_16x16x32_bf16 a[56:59], v[140:143], a[40:43], a[56:59]\n\t"
    "buffer_load_dwordx4 a[152:155], v247, s[88:91], s46 offen sc1\n\t"
    "v_mfma_f32_16x16x32_bf16 a[56:59], v[136:139], a[8:11], a[56:59]\n\t"
    "v_mfma_f32_16x16x32_bf16 a[52:55], v[136:139], a[4:7], a[52:55]\n\t"
    "ds_read_b64_tr_b16 v[156:157], v199 offset:40960\n\t"
    "v_mfma_f32_16x16x32_bf16 a[52:55], v[140:143], a[36:39], a[52:55]\n\t"
    "ds_read_b64_tr_b16 v[158:159], v199 offset:45056\n\t"
    "v_mfma_f32_16x16x32_bf16 a[48:51], v[140:143], a[32:35], a[48:51]\n\t"
    "v_mfma_f32_16x16x32_bf16 a[48:51], v[136:139], a[0:3], a[48:51]\n\t"
    "s_waitcnt vmcnt(17) lgkmcnt(4)\n\t"
    "s_barrier\n\t"
    "v_mfma_f32_16x16x32_bf16 a[80:83], v[144:147], a[0:3], a[80:83]\n\t"
    "ds_read_b64_tr_b16 v[128:129], v192 offset:49152\n\t"
    "v_mfma_f32_16x16x32_bf16 a[80:83], v[148:151], a[32:35], a[80:83]\n\t"
    "ds_read_b64_tr_b16 v[130:131], v192 offset:53248\n\t"
    "v_mfma_f32_16x16x32_bf16 a[84:87], v[148:151], a[36:39], a[84:87]\n\t"
    "buffer_load_dwordx4 a[168:171], v247, s[88:91], s47 offen sc1\n\t"
    "v_mfma_f32_16x16x32_bf16 a[84:87], v[144:147], a[4:7], a[84:87]\n\t"
    "v_mfma_f32_16x16x32_bf16 a[88:91], v[144:147], a[8:11], a[88:91]\n\t"
    "ds_read_b64_tr_b16 v[132:133], v192 offset:57344\n\t"
    "v_mfma_f32_16x16x32_bf16 a[88:91], v[148:151], a[40:43], a[88:91]\n\t"
    "ds_read_b64_tr_b16 v[134:135], v192 offset:61440\n\t"
    "v_mfma_f32_16x16x32_bf16 a[92:95], v[148:151], a[44:47], a[92:95]\n\t"
    "buffer_load_dwordx4 a[156:159], v249, s[88:91], s46 offen sc1\n\t"
    "v_mfma_f32_16x16x32_bf16 a[92:95], v[144:147], a[12:15], a[92:95]\n\t"
    "s_waitcnt lgkmcnt(4)\n\t"
    "v_mfma_f32_16x16x32_bf16 a[124:127], v[152:155], a[12:15], a[124:127]\n\t"
    "ds_read_b64_tr_b16 v[136:137], v193 offset:49152\n\t"
    "v_mfma_f32_16x16x32_bf16 a[124:127], v[156:159], a[44:47], a[124:127]\n\t"
    "ds_read_b64_tr_b16 v[138:139], v193 offset:53248\n\t"
    "v_mfma_f32_16x16x32_bf16 a[120:123], v[156:159], a[40:43], a[120:123]\n\t"
    "buffer_load_dwordx4 a[172:175], v249, s[88:91], s47 offen sc1\n\t"
    "v_mfma_f32_16x16x32_bf16 a[120:123], v[152:155], a[8:11], a[120:123]\n\t"
    "s_bfe_u32 s84, s15, 262164\n\t"
    "s_lshl_b32 s84, s84, 17\n\t"
    "s_bfe_u32 s46, s15, 262164\n\t"
    "v_mfma_f32_16x16x32_bf16 a[116:119], v[152:155], a[4:7], a[116:119]\n\t"
    "ds_read_b64_tr_b16 v[140:141], v193 offset:57344\n\t"
    "s_lshl_b32 s46, s46, 13\n\t"
    "v_mfma_f32_16x16x32_bf16 a[116:119], v[156:159], a[36:39], a[116:119]\n\t"
    "ds_read_b64_tr_b16 v[142:143], v193 offset:61440\n\t"
    "s_add_u32 s46, s46, s60\n\t"
    "v_mfma_f32_16x16x32_bf16 a[112:115], v[156:159], a[32:35], a[112:115]\n\t"
    "s_add_u32 s47, s46, 4096\n\t"
    "v_mfma_f32_16x16x32_bf16 a[112:115], v[152:155], a[0:3], a[112:115]\n\t"
    "s_waitcnt vmcnt(12) lgkmcnt(4)\n\t"
    "v_mfma_f32_16x16x32_bf16 a[208:211], v[128:131], a[64:67], a[208:211]\n\t"
    "ds_read_b64_tr_b16 v[144:145], v194 offset:49152\n\t"
    "v_mfma_f32_16x16x32_bf16 a[208:211], v[132:135], a[96:99], a[208:211]\n\t"
    "ds_read_b64_tr_b16 v[146:147], v194 offset:53248\n\t"
    "s_add_u32 m0, s48, 65536\n\t"
    "v_mfma_f32_16x16x32_bf16 a[212:215], v[132:135], a[100:103], a[212:215]\n\t"
    "buffer_load_dwordx4 v242, s[32:35], s84 offen sc1 lds\n\t"
    "v_mfma_f32_16x16x32_bf16 a[212:215], v[128:131], a[68:71], a[212:215]\n\t"
    "buffer_load_dword v253, v252, s[88:91], 0 offen sc1\n\t"
    "v_mfma_f32_16x16x32_bf16 a[216:219], v[128:131], a[72:75], a[216:219]\n\t"
    "ds_read_b64_tr_b16 v[148:149], v194 offset:57344\n\t"
    "v_mfma_f32_16x16x32_bf16 a[216:219], v[132:135], a[104:107], a[216:219]\n\t"
    "ds_read_b64_tr_b16 v[150:151], v194 offset:61440\n\t"
    "v_mfma_f32_16x16x32_bf16 a[220:223], v[132:135], a[108:111], a[220:223]\n\t"
    "buffer_load_dwordx4 v243, s[32:35], s84 offen offset:1024 sc1 lds\n\t"
    "v_mfma_f32_16x16x32_bf16 a[220:223], v[128:131], a[76:79], a[220:223]\n\t"
    "s_waitcnt lgkmcnt(4)\n\t"
    "v_mfma_f32_16x16x32_bf16 a[236:239], v[136:139], a[76:79], a[236:239]\n\t"
    "ds_read_b64_tr_b16 v[152:153], v195 offset:49152\n\t"
    "v_mfma_f32_16x16x32_bf16 a[236:239], v[140:143], a[108:111], a[236:239]\n\t"
    "ds_read_b64_tr_b16 v[154:155], v195 offset:53248\n\t"
    "v_mfma_f32_16x16x32_bf16 a[232:235], v[140:143], a[104:107], a[232:235]\n\t"
    "buffer_load_dwordx4 v244, s[32:35], s84 offen offset:2048 sc1 lds\n\t"
    "v_mfma_f32_16x16x32_bf16 a[232:235], v[136:139], a[72:75], a[232:235]\n\t"
    "v_mfma_f32_16x16x32_bf16 a[228:231], v[136:139], a[68:71], a[228:231]\n\t"
    "ds_read_b64_tr_b16 v[156:157], v195 offset:57344\n\t"
    "v_mfma_f32_16x16x32_bf16 a[228:231], v[140:143], a[100:103], a[228:231]\n\t"
    "ds_read_b64_tr_b16 v[158:159], v195 offset:61440\n\t"
    "v_mfma_f32_16x16x32_bf16 a[224:227], v[140:143], a[96:99], a[224:227]\n\t"
    "v_mfma_f32_16x16x32_bf16 a[224:227], v[136:139], a[64:67], a[224:227]\n\t"
    "s_waitcnt lgkmcnt(4)\n\t"
    "v_mfma_f32_16x16x32_bf16 a[240:243], v[144:147], a[64:67], a[240:243]\n\t"
    "ds_read_b64_tr_b16 v[128:129], v196 offset:49152\n\t"
    "v_mfma_f32_16x16x32_bf16 a[240:243], v[148:151], a[96:99], a[240:243]\n\t"
    "ds_read_b64_tr_b16 v[130:131], v196 offset:53248\n\t"
    "v_mfma_f32_16x16x32_bf16 a[244:247], v[148:151], a[100:103], a[244:247]\n\t"
    "buffer_load_dwordx4 v245, s[32:35], s84 offen offset:3072 sc1 lds\n\t"
    "v_mfma_f32_16x16x32_bf16 a[244:247], v[144:147], a[68:71], a[244:247]\n\t"
    "v_mfma_f32_16x16x32_bf16 a[248:251], v[144:147], a[72:75], a[248:251]\n\t"
    "ds_read_b64_tr_b16 v[132:133], v196 offset:57344\n\t"
    "v_mfma_f32_16x16x32_bf16 a[248:251], v[148:151], a[104:107], a[248:251]\n\t"
    "ds_read_b64_tr_b16 v[134:135], v196 offset:61440\n\t"
    "v_mfma_f32_16x16x32_bf16 a[252:255], v[148:151], a[108:111], a[252:255]\n\t"
    "buffer_load_dwordx4 a[176:179], v241, s[88:91], s46 offen sc1\n\t"
    "v_mfma_f32_16x16x32_bf16 a[252:255], v[144:147], a[76:79], a[252:255]\n\t"
    "s_waitcnt lgkmcnt(4)\n\t"
    "v_mfma_f32_16x16x32_bf16 a[140:143], v[152:155], a[76:79], a[140:143]\n\t"
    "ds_read_b64_tr_b16 v[136:137], v197 offset:49152\n\t"
    "v_mfma_f32_16x16x32_bf16 a[140:143], v[156:159], a[108:111], a[140:143]\n\t"
    "ds_read_b64_tr_b16 v[138:139], v197 offset:53248\n\t"
    "v_mfma_f32_16x16x32_bf16 a[136:139], v[156:159], a[104:107], a[136:139]\n\t"
    "buffer_load_dwordx4 a[192:195], v241, s[88:91], s47 offen sc1\n\t"
    "v_mfma_f32_16x16x32_bf16 a[136:139], v[152:155], a[72:75], a[136:139]\n\t"
    "v_mfma_f32_16x16x32_bf16 a[132:135], v[152:155], a[68:71], a[132:135]\n\t"
    "ds_read_b64_tr_b16 v[140:141], v197 offset:57344\n\t"
    "v_mfma_f32_16x16x32_bf16 a[132:135], v[156:159], a[100:103], a[132:135]\n\t"
    "ds_read_b64_tr_b16 v[142:143], v197 offset:61440\n\t"
    "v_mfma_f32_16x16x32_bf16 a[128:131], v[156:159], a[96:99], a[128:131]\n\t"
    "v_mfma_f32_16x16x32_bf16 a[128:131], v[152:155], a[64:67], a[128:131]\n\t"
    "s_waitcnt lgkmcnt(4)\n\t"
    "v_mfma_f32_16x16x32_bf16 a[16:19], v[128:131], a[64:67], a[16:19]\n\t"
    "ds_read_b64_tr_b16 v[144:145], v198 offset:49152\n\t"
    "v_mfma_f32_16x16x32_bf16 a[16:19], v[132:135], a[96:99], a[16:19]\n\t"
    "ds_read_b64_tr_b16 v[146:147], v198 offset:53248\n\t"
    "v_mfma_f32_16x16x32_bf16 a[20:23], v[132:135], a[100:103], a[20:23]\n\t"
    "buffer_load_dwordx4 a[180:183], v246, s[88:91], s46 offen sc1\n\t"
    "v_mfma_f32_16x16x32_bf16 a[20:23], v[128:131], a[68:71], a[20:23]\n\t"
    "v_mfma_f32_16x16x32_bf16 a[24:27], v[128:131], a[72:75], a[24:27]\n\t"
    "ds_read_b64_tr_b16 v[148:149], v198 offset:57344\n\t"
    "v_mfma_f32_16x16x32_bf16 a[24:27], v[132:135], a[104:107], a[24:27]\n\t"
    "ds_read_b64_tr_b16 v[150:151], v198 offset:61440\n\t"
    "v_mfma_f32_16x16x32_bf16 a[28:31], v[132:135], a[108:111], a[28:31]\n\t"
    "buffer_load_dwordx4 a[196:199], v246, s[88:91], s47 offen sc1\n\t"
    "v_mfma_f32_16x16x32_bf16 a[28:31], v[128:131], a[76:79], a[28:31]\n\t"
    "s_waitcnt lgkmcnt(4)\n\t"
    "v_mfma_f32_16x16x32_bf16 a[60:63], v[136:139], a[76:79], a[60:63]\n\t"
    "ds_read_b64_tr_b16 v[152:153], v199 offset:49152\n\t"
    "v_mfma_f32_16x16x32_bf16 a[60:63], v[140:143], a[108:111], a[60:63]\n\t"
    "ds_read_b64_tr_b16 v[154:155], v199 offset:53248\n\t"
    "v_mfma_f32_16x16x32_bf16 a[56:59], v[140:143], a[104:107], a[56:59]\n\t"
    "buffer_load_dwordx4 a[184:187], v247, s[88:91], s46 offen sc1\n\t"
    "v_mfma_f32_16x16x32_bf16 a[56:59], v[136:139], a[72:75], a[56:59]\n\t"
    "v_mfma_f32_16x16x32_bf16 a[52:55], v[136:139], a[68:71], a[52:55]\n\t"
    "ds_read_b64_tr_b16 v[156:157], v199 offset:57344\n\t"
    "v_mfma_f32_16x16x32_bf16 a[52:55], v[140:143], a[100:103], a[52:55]\n\t"
    "ds_read_b64_tr_b16 v[158:159], v199 offset:61440\n\t"
    "v_mfma_f32_16x16x32_bf16 a[48:51], v[140:143], a[96:99], a[48:51]\n\t"
    "v_mfma_f32_16x16x32_bf16 a[48:51], v[136:139], a[64:67], a[48:51]\n\t"
    "s_waitcnt vmcnt(18) lgkmcnt(4)\n\t"
    "s_barrier\n\t"
    "v_mfma_f32_16x16x32_bf16 a[80:83], v[144:147], a[64:67], a[80:83]\n\t"
    "ds_read_b64_tr_b16 v[128:129], v192 offset:0\n\t"
    "v_mfma_f32_16x16x32_bf16 a[80:83], v[148:151], a[96:99], a[80:83]\n\t"
    "ds_read_b64_tr_b16 v[130:131], v192 offset:4096\n\t"
    "v_mfma_f32_16x16x32_bf16 a[84:87], v[148:151], a[100:103], a[84:87]\n\t"
    "buffer_load_dwordx4 a[200:203], v247, s[88:91], s47 offen sc1\n\t"
    "v_mfma_f32_16x16x32_bf16 a[84:87], v[144:147], a[68:71], a[84:87]\n\t"
    "s_mov_b64 s[32:33], s[42:43]\n\t"
    "s_mov_b32 s34, s44\n\t"
    "v_mfma_f32_16x16x32_bf16 a[88:91], v[144:147], a[72:75], a[88:91]\n\t"
    "ds_read_b64_tr_b16 v[132:133], v192 offset:8192\n\t"
    "v_mfma_f32_16x16x32_bf16 a[88:91], v[148:151], a[104:107], a[88:91]\n\t"
    "ds_read_b64_tr_b16 v[134:135], v192 offset:12288\n\t"
    "v_mfma_f32_16x16x32_bf16 a[92:95], v[148:151], a[108:111], a[92:95]\n\t"
    "buffer_load_dwordx4 a[188:191], v249, s[88:91], s46 offen sc1\n\t"
    "v_mfma_f32_16x16x32_bf16 a[92:95], v[144:147], a[76:79], a[92:95]\n\t"
    "s_waitcnt lgkmcnt(4)\n\t"
    "v_mfma_f32_16x16x32_bf16 a[124:127], v[152:155], a[76:79], a[124:127]\n\t"
    "ds_read_b64_tr_b16 v[136:137], v193 offset:0\n\t"
    "v_mfma_f32_16x16x32_bf16 a[124:127], v[156:159], a[108:111], a[124:127]\n\t"
    "ds_read_b64_tr_b16 v[138:139], v193 offset:4096\n\t"
    "v_mfma_f32_16x16x32_bf16 a[120:123], v[156:159], a[104:107], a[120:123]\n\t"
    "buffer_load_dwordx4 a[204:207], v249, s[88:91], s47 offen sc1\n\t"
    "v_mfma_f32_16x16x32_bf16 a[120:123], v[152:155], a[72:75], a[120:123]\n\t"
    "s_bfe_u32 s84, s16, 262144\n\t"
    "s_lshl_b32 s84, s84, 17\n\t"
    "s_bfe_u32 s46, s16, 262144\n\t"
    "v_mfma_f32_16x16x32_bf16 a[116:119], v[152:155], a[68:71], a[116:119]\n\t"
    "ds_read_b64_tr_b16 v[140:141], v193 offset:8192\n\t"
    "s_lshl_b32 s46, s46, 13\n\t"
    "v_mfma_f32_16x16x32_bf16 a[116:119], v[156:159], a[100:103], a[116:119]\n\t"
    "ds_read_b64_tr_b16 v[142:143], v193 offset:12288\n\t"
    "s_add_u32 s46, s46, s45\n\t"
    "v_mfma_f32_16x16x32_bf16 a[112:115], v[156:159], a[96:99], a[112:115]\n\t"
    "s_add_u32 s47, s46, 4096\n\t"
    "v_mfma_f32_16x16x32_bf16 a[112:115], v[152:155], a[64:67], a[112:115]\n\t"
    "s_waitcnt vmcnt(13) lgkmcnt(4)\n\t"
    "v_mfma_f32_16x16x32_bf16 a[208:211], v[128:131], a[144:147], a[208:211]\n\t"
    "ds_read_b64_tr_b16 v[144:145], v194 offset:0\n\t"
    "v_mfma_f32_16x16x32_bf16 a[208:211], v[132:135], a[160:163], a[208:211]\n\t"
    "ds_read_b64_tr_b16 v[146:147], v194 offset:4096\n\t"
    "s_add_u32 m0, s48, 81920\n\t"
    "v_mfma_f32_16x16x32_bf16 a[212:215], v[132:135], a[164:167], a[212:215]\n\t"
    "buffer_load_dwordx4 v242, s[32:35], s84 offen sc1 lds\n\t"
    "v_mfma_f32_16x16x32_bf16 a[212:215], v[128:131], a[148:151], a[212:215]\n\t"
    "v_mfma_f32_16x16x32_bf16 a[216:219], v[128:131], a[152:155], a[216:219]\n\t"
    "ds_read_b64_tr_b16 v[148:149], v194 offset:8192\n\t"
    "v_mfma_f32_16x16x32_bf16 a[216:219], v[132:135], a[168:171], a[216:219]\n\t"
    "ds_read_b64_tr_b16 v[150:151], v194 offset:12288\n\t"
    "v_mfma_f32_16x16x32_bf16 a[220:223], v[132:135], a[172:175], a[220:223]\n\t"
    "buffer_load_dwordx4 v243, s[32:35], s84 offen offset:1024 sc1 lds\n\t"
    "v_mfma_f32_16x16x32_bf16 a[220:223], v[128:131], a[156:159], a[220:223]\n\t"
    "s_waitcnt lgkmcnt(4)\n\t"
    "v_mfma_f32_16x16x32_bf16 a[236:239], v[136:139], a[156:159], a[236:239]\n\t"
    "ds_read_b64_tr_b16 v[152:153], v195 offset:0\n\t"
    "v_mfma_f32_16x16x32_bf16 a[236:239], v[140:143], a[172:175], a[236:239]\n\t"
    "ds_read_b64_tr_b16 v[154:155], v195 offset:4096\n\t"
    "v_mfma_f32_16x16x32_bf16 a[232:235], v[140:143], a[168:171], a[232:235]\n\t"
    "buffer_load_dwordx4 v244, s[32:35], s84 offen offset:2048 sc1 lds\n\t"
    "v_mfma_f32_16x16x32_bf16 a[232:235], v[136:139], a[152:155], a[232:235]\n\t"
    "v_mfma_f32_16x16x32_bf16 a[228:231], v[136:139], a[148:151], a[228:231]\n\t"
    "ds_read_b64_tr_b16 v[156:157], v195 offset:8192\n\t"
    "v_mfma_f32_16x16x32_bf16 a[228:231], v[140:143], a[164:167], a[228:231]\n\t"
    "ds_read_b64_tr_b16 v[158:159], v195 offset:12288\n\t"
    "v_mfma_f32_16x16x32_bf16 a[224:227], v[140:143], a[160:163], a[224:227]\n\t"
    "s_waitcnt vmcnt(14)\n\t"
    "v_cmp_ne_u32 vcc, s20, v253\n\t"
    "v_mfma_f32_16x16x32_bf16 a[224:227], v[136:139], a[144:147], a[224:227]\n\t"
    "s_waitcnt lgkmcnt(4)\n\t"
    "v_mfma_f32_16x16x32_bf16 a[240:243], v[144:147], a[144:147], a[240:243]\n\t"
    "ds_read_b64_tr_b16 v[128:129], v196 offset:0\n\t"
    "s_cbranch_vccnz .Lfb_wo\n\t"
    ".Lfb_wo_r:\n\t"
    "v_mfma_f32_16x16x32_bf16 a[240:243], v[148:151], a[160:163], a[240:243]\n\t"
    "ds_read_b64_tr_b16 v[130:131], v196 offset:4096\n\t"
    "v_bfe_u32 v254, v248, 6, 2\n\t"
    "v_mfma_f32_16x16x32_bf16 a[244:247], v[148:151], a[164:167], a[244:247]\n\t"
    "buffer_load_dwordx4 v245, s[32:35], s84 offen offset:3072 sc1 lds\n\t"
    "v_mfma_f32_16x16x32_bf16 a[244:247], v[144:147], a[148:151], a[244:247]\n\t"
    "v_lshrrev_b32 v255, 8, v248\n\t"
    "v_cmp_eq_u32 vcc, v254, v255\n\t"
    "v_mfma_f32_16x16x32_bf16 a[248:251], v[144:147], a[152:155], a[248:251]\n\t"
    "ds_read_b64_tr_b16 v[132:133], v196 offset:8192\n\t"
    "v_and_b32 v254, 16, v248\n\t"
    "v_mfma_f32_16x16x32_bf16 a[248:251], v[148:151], a[168:171], a[248:251]\n\t"
    "ds_read_b64_tr_b16 v[134:135], v196 offset:12288\n\t"
    "v_mov_b32 v255, 16256\n\t"
    "v_mfma_f32_16x16x32_bf16 a[252:255], v[148:151], a[172:175], a[252:255]\n\t"
    "buffer_load_dwordx4 a[0:3], v241, s[88:91], s46 offen sc1\n\t"
    "v_mfma_f32_16x16x32_bf16 a[252:255], v[144:147], a[156:159], a[252:255]\n\t"
    "s_waitcnt lgkmcnt(4)\n\t"
    "v_lshlrev_b32 v255, v254, v255\n\t"
    "v_cndmask_b32 v255, 0, v255, vcc\n\t"
    "v_mfma_f32_16x16x32_bf16 a[140:143], v[152:155], a[156:159], a[140:143]\n\t"
    "ds_read_b64_tr_b16 v[136:137], v197 offset:0\n\t"
    "v_and_b32 v254, 32, v248\n\t"
    "v_mfma_f32_16x16x32_bf16 a[140:143], v[156:159], a[172:175], a[140:143]\n\t"
    "ds_read_b64_tr_b16 v[138:139], v197 offset:4096\n\t"
    "v_cmp_eq_u32 vcc, 0, v254\n\t"
    "v_mfma_f32_16x16x32_bf16 a[136:139], v[156:159], a[168:171], a[136:139]\n\t"
    "buffer_load_dwordx4 a[32:35], v241, s[88:91], s47 offen sc1\n\t"
    "v_mfma_f32_16x16x32_bf16 a[136:139], v[152:155], a[152:155], a[136:139]\n\t"
    "v_mov_b32 v232, 0\n\t"
    "v_mov_b32 v233, 0\n\t"
    "v_mfma_f32_16x16x32_bf16 a[132:135], v[152:155], a[148:151], a[132:135]\n\t"
    "ds_read_b64_tr_b16 v[140:141], v197 offset:8192\n\t"
    "v_cndmask_b32 v234, 0, v255, vcc\n\t"
    "v_mfma_f32_16x16x32_bf16 a[132:135], v[156:159], a[164:167], a[132:135]\n\t"
    "ds_read_b64_tr_b16 v[142:143], v197 offset:12288\n\t"
    "v_sub_u32 v235, v255, v234\n\t"
    "v_mfma_f32_16x16x32_bf16 a[128:131], v[156:159], a[160:163], a[128:131]\n\t"
    "v_mov_b32 v236, 0\n\t"
    "v_mov_b32 v237, 0\n\t"
    "v_mfma_f32_16x16x32_bf16 a[128:131], v[152:155], a[144:147], a[128:131]\n\t"
    "s_waitcnt lgkmcnt(4)\n\t"
    "s_lshl_b32 s77, s3, 13\n\t"
    "s_add_u32 s77, s77, 114688\n\t"
    "v_mfma_f32_16x16x32_bf16 a[16:19], v[128:131], a[144:147], a[16:19]\n\t"
    "ds_read_b64_tr_b16 v[144:145], v198 offset:0\n\t"
    "v_lshrrev_b32 v255, 8, v248\n\t"
    "v_mfma_f32_16x16x32_bf16 a[16:19], v[132:135], a[160:163], a[16:19]\n\t"
    "ds_read_b64_tr_b16 v[146:147], v198 offset:4096\n\t"
    "v_lshl_add_u32 v238, v255, 3, s77\n\t"
    "v_mfma_f32_16x16x32_bf16 a[20:23], v[132:135], a[164:167], a[20:23]\n\t"
    "buffer_load_dwordx4 a[4:7], v246, s[88:91], s46 offen sc1\n\t"
    "v_mfma_f32_16x16x32_bf16 a[20:23], v[128:131], a[148:151], a[20:23]\n\t"
    "v_mul_u32_u24 v255, 272, v255\n\t"
    "v_add_u32 v239, s77, v255\n\t"
    "v_mfma_f32_16x16x32_bf16 a[24:27], v[128:131], a[152:155], a[24:27]\n\t"
    "ds_read_b64_tr_b16 v[148:149], v198 offset:8192\n\t"
    "v_and_b32 v255, 240, v248\n\t"
    "v_mfma_f32_16x16x32_bf16 a[24:27], v[132:135], a[168:171], a[24:27]\n\t"
    "ds_read_b64_tr_b16 v[150:151], v198 offset:12288\n\t"
    "v_add_u32 v239, v239, v255\n\t"
    "v_mfma_f32_16x16x32_bf16 a[28:31], v[132:135], a[172:175], a[28:31]\n\t"
    "buffer_load_dwordx4 a[36:39], v246, s[88:91], s47 offen sc1\n\t"
    "v_mfma_f32_16x16x32_bf16 a[28:31], v[128:131], a[156:159], a[28:31]\n\t"
    "s_waitcnt lgkmcnt(4)\n\t"
    "v_mul_u32_u24 v255, 17, v255\n\t"
    "v_add_u32 v238, v238, v255\n\t"
    "v_mfma_f32_16x16x32_bf16 a[60:63], v[136:139], a[156:159], a[60:63]\n\t"
    "ds_read_b64_tr_b16 v[152:153], v199 offset:0\n\t"
    "v_lshrrev_b32 v252, 4, v248\n\t"
    "v_mfma_f32_16x16x32_bf16 a[60:63], v[140:143], a[172:175], a[60:63]\n\t"
    "ds_read_b64_tr_b16 v[154:155], v199 offset:4096\n\t"
    "v_min_u32 v252, 2, v252\n\t"
    "v_mfma_f32_16x16x32_bf16 a[56:59], v[140:143], a[168:171], a[56:59]\n\t"
    "buffer_load_dwordx4 a[8:11], v247, s[88:91], s46 offen sc1\n\t"
    "v_mfma_f32_16x16x32_bf16 a[56:59], v[136:139], a[152:155], a[56:59]\n\t"
    "v_add_u32 v252, 1, v252\n\t"
    "v_xor_b32 v252, s49, v252\n\t"
    "v_mfma_f32_16x16x32_bf16 a[52:55], v[136:139], a[148:151], a[52:55]\n\t"
    "ds_read_b64_tr_b16 v[156:157], v199 offset:8192\n\t"
    "v_lshlrev_b32 v252, 2, v252\n\t"
    "v_mfma_f32_16x16x32_bf16 a[52:55], v[140:143], a[164:167], a[52:55]\n\t"
    "ds_read_b64_tr_b16 v[158:159], v199 offset:12288\n\t"
    "v_mfma_f32_16x16x32_bf16 a[48:51], v[140:143], a[160:163], a[48:51]\n\t"
    "v_mfma_f32_16x16x32_bf16 a[48:51], v[136:139], a[144:147], a[48:51]\n\t"
    "s_waitcnt vmcnt(17) lgkmcnt(4)\n\t"
    "s_barrier\n\t"
    "v_mfma_f32_16x16x32_bf16 a[80:83], v[144:147], a[144:147], a[80:83]\n\t"
    "ds_read_b64_tr_b16 v[128:129], v192 offset:16384\n\t"
    "v_mfma_f32_16x16x32_bf16 a[80:83], v[148:151], a[160:163], a[80:83]\n\t"
    "ds_read_b64_tr_b16 v[130:131], v192 offset:20480\n\t"
    "v_mfma_f32_16x16x32_bf16 a[84:87], v[148:151], a[164:167], a[84:87]\n\t"
    "buffer_load_dwordx4 a[40:43], v247, s[88:91], s47 offen sc1\n\t"
    "v_mfma_f32_16x16x32_bf16 a[84:87], v[144:147], a[148:151], a[84:87]\n\t"
    "v_mfma_f32_16x16x32_bf16 a[88:91], v[144:147], a[152:155], a[88:91]\n\t"
    "ds_read_b64_tr_b16 v[132:133], v192 offset:24576\n\t"
    "v_mfma_f32_16x16x32_bf16 a[88:91], v[148:151], a[168:171], a[88:91]\n\t"
    "ds_read_b64_tr_b16 v[134:135], v192 offset:28672\n\t"
    "v_mfma_f32_16x16x32_bf16 a[92:95], v[148:151], a[172:175], a[92:95]\n\t"
    "buffer_load_dwordx4 a[12:15], v249, s[88:91], s46 offen sc1\n\t"
    "v_mfma_f32_16x16x32_bf16 a[92:95], v[144:147], a[156:159], a[92:95]\n\t"
    "s_waitcnt lgkmcnt(4)\n\t"
    "v_mfma_f32_16x16x32_bf16 a[124:127], v[152:155], a[156:159], a[124:127]\n\t"
    "ds_read_b64_tr_b16 v[136:137], v193 offset:16384\n\t"
    "v_mfma_f32_16x16x32_bf16 a[124:127], v[156:159], a[172:175], a[124:127]\n\t"
    "ds_read_b64_tr_b16 v[138:139], v193 offset:20480\n\t"
    "v_mfma_f32_16x16x32_bf16 a[120:123], v[156:159], a[168:171], a[120:123]\n\t"
    "buffer_load_dwordx4 a[44:47], v249, s[88:91], s47 offen sc1\n\t"
    "v_mfma_f32_16x16x32_bf16 a[120:123], v[152:155], a[152:155], a[120:123]\n\t"
    "s_bfe_u32 s84, s16, 262148\n\t"
    "s_lshl_b32 s84, s84, 17\n\t"
    "s_bfe_u32 s46, s16, 262148\n\t"
    "v_mfma_f32_16x16x32_bf16 a[116:119], v[152:155], a[148:151], a[116:119]\n\t"
    "ds_read_b64_tr_b16 v[140:141], v193 offset:24576\n\t"
    "s_lshl_b32 s46, s46, 13\n\t"
    "v_mfma_f32_16x16x32_bf16 a[116:119], v[156:159], a[164:167], a[116:119]\n\t"
    "ds_read_b64_tr_b16 v[142:143], v193 offset:28672\n\t"
    "s_add_u32 s46, s46, s45\n\t"
    "v_mfma_f32_16x16x32_bf16 a[112:115], v[156:159], a[160:163], a[112:115]\n\t"
    "s_add_u32 s47, s46, 4096\n\t"
    "v_mfma_f32_16x16x32_bf16 a[112:115], v[152:155], a[144:147], a[112:115]\n\t"
    "s_waitcnt vmcnt(12) lgkmcnt(4)\n\t"
    "v_mfma_f32_16x16x32_bf16 a[208:211], v[128:131], a[176:179], a[208:211]\n\t"
    "ds_read_b64_tr_b16 v[144:145], v194 offset:16384\n\t"
    "v_mfma_f32_16x16x32_bf16 a[208:211], v[132:135], a[192:195], a[208:211]\n\t"
    "ds_read_b64_tr_b16 v[146:147], v194 offset:20480\n\t"
    "s_add_u32 m0, s48, 98304\n\t"
    "v_mfma_f32_16x16x32_bf16 a[212:215], v[132:135], a[196:199], a[212:215]\n\t"
    "buffer_load_dwordx4 v242, s[32:35], s84 offen sc1 lds\n\t"
    "v_mfma_f32_16x16x32_bf16 a[212:215], v[128:131], a[180:183], a[212:215]\n\t"
    "s_or_b32 s77, s2, s3\n\t"
    "s_cbranch_scc0 .Lfb_e1\n\t"
    ".Lfb_e1_r:\n\t"
    "v_mfma_f32_16x16x32_bf16 a[216:219], v[128:131], a[184:187], a[216:219]\n\t"
    "ds_read_b64_tr_b16 v[148:149], v194 offset:24576\n\t"
    "v_mfma_f32_16x16x32_bf16 a[216:219], v[132:135], a[200:203], a[216:219]\n\t"
    "ds_read_b64_tr_b16 v[150:151], v194 offset:28672\n\t"
    "v_mfma_f32_16x16x32_bf16 a[220:223], v[132:135], a[204:207], a[220:223]\n\t"
    "buffer_load_dwordx4 v243, s[32:35], s84 offen offset:1024 sc1 lds\n\t"
    "v_mfma_f32_16x16x32_bf16 a[220:223], v[128:131], a[188:191], a[220:223]\n\t"
    "s_waitcnt lgkmcnt(4)\n\t"
    "v_mfma_f32_16x16x32_bf16 a[236:239], v[136:139], a[188:191], a[236:239]\n\t"
    "ds_read_b64_tr_b16 v[152:153], v195 offset:16384\n\t"
    "v_mfma_f32_16x16x32_bf16 a[236:239], v[140:143], a[204:207], a[236:239]\n\t"
    "ds_read_b64_tr_b16 v[154:155], v195 offset:20480\n\t"
    "v_mfma_f32_16x16x32_bf16 a[232:235], v[140:143], a[200:203], a[232:235]\n\t"
    "buffer_load_dwordx4 v244, s[32:35], s84 offen offset:2048 sc1 lds\n\t"
    "v_mfma_f32_16x16x32_bf16 a[232:235], v[136:139], a[184:187], a[232:235]\n\t"
    "v_mfma_f32_16x16x32_bf16 a[228:231], v[136:139], a[180:183], a[228:231]\n\t"
    "ds_read_b64_tr_b16 v[156:157], v195 offset:24576\n\t"
    "v_mfma_f32_16x16x32_bf16 a[228:231], v[140:143], a[196:199], a[228:231]\n\t"
    "ds_read_b64_tr_b16 v[158:159], v195 offset:28672\n\t"
    "v_mfma_f32_16x16x32_bf16 a[224:227], v[140:143], a[192:195], a[224:227]\n\t"
    "v_mfma_f32_16x16x32_bf16 a[224:227], v[136:139], a[176:179], a[224:227]\n\t"
    "s_waitcnt lgkmcnt(4)\n\t"
    "v_mfma_f32_16x16x32_bf16 a[240:243], v[144:147], a[176:179], a[240:243]\n\t"
    "ds_read_b64_tr_b16 v[128:129], v196 offset:16384\n\t"
    "v_mfma_f32_16x16x32_bf16 a[240:243], v[148:151], a[192:195], a[240:243]\n\t"
    "ds_read_b64_tr_b16 v[130:131], v196 offset:20480\n\t"
    "v_mfma_f32_16x16x32_bf16 a[244:247], v[148:151], a[196:199], a[244:247]\n\t"
    "buffer_load_dwordx4 v245, s[32:35], s84 offen offset:3072 sc1 lds\n\t"
    "v_mfma_f32_16x16x32_bf16 a[244:247], v[144:147], a[180:183], a[244:247]\n\t"
    "v_mfma_f32_16x16x32_bf16 a[248:251], v[144:147], a[184:187], a[248:251]\n\t"
    "ds_read_b64_tr_b16 v[132:133], v196 offset:24576\n\t"
    "v_mfma_f32_16x16x32_bf16 a[248:251], v[148:151], a[200:203], a[248:251]\n\t"
    "ds_read_b64_tr_b16 v[134:135], v196 offset:28672\n\t"
    "v_mfma_f32_16x16x32_bf16 a[252:255], v[148:151], a[204:207], a[252:255]\n\t"
    "buffer_load_dwordx4 a[64:67], v241, s[88:91], s46 offen sc1\n\t"
    "v_mfma_f32_16x16x32_bf16 a[252:255], v[144:147], a[188:191], a[252:255]\n\t"
    "s_waitcnt lgkmcnt(4)\n\t"
    "v_accvgpr_read_b32 v200, a212\n\t"
    "v_accvgpr_read_b32 v201, a213\n\t"
    "v_mfma_f32_16x16x32_bf16 a[140:143], v[152:155], a[188:191], a[140:143]\n\t"
    "ds_read_b64_tr_b16 v[136:137], v197 offset:16384\n\t"
    "v_accvgpr_read_b32 v202, a214\n\t"
    "v_mfma_f32_16x16x32_bf16 a[140:143], v[156:159], a[204:207], a[140:143]\n\t"
    "ds_read_b64_tr_b16 v[138:139], v197 offset:20480\n\t"
    "v_accvgpr_read_b32 v203, a215\n\t"
    "v_mfma_f32_16x16x32_bf16 a[136:139], v[156:159], a[200:203], a[136:139]\n\t"
    "buffer_load_dwordx4 a[96:99], v241, s[88:91], s47 offen sc1\n\t"
    "v_mfma_f32_16x16x32_bf16 a[136:139], v[152:155], a[184:187], a[136:139]\n\t"
    "v_accvgpr_read_b32 v204, a228\n\t"
    "v_accvgpr_read_b32 v205, a229\n\t"
    "v_mfma_f32_16x16x32_bf16 a[132:135], v[152:155], a[180:183], a[132:135]\n\t"
    "ds_read_b64_tr_b16 v[140:141], v197 offset:24576\n\t"
    "v_accvgpr_read_b32 v206, a230\n\t"
    "v_mfma_f32_16x16x32_bf16 a[132:135], v[156:159], a[196:199], a[132:135]\n\t"
    "ds_read_b64_tr_b16 v[142:143], v197 offset:28672\n\t"
    "v_accvgpr_read_b32 v207, a231\n\t"
    "v_mfma_f32_16x16x32_bf16 a[128:131], v[156:159], a[192:195], a[128:131]\n\t"
    "v_cvt_pk_bf16_f32 v200, v200, v201\n\t"
    "v_cvt_pk_bf16_f32 v201, v202, v203\n\t"
    "v_mfma_f32_16x16x32_bf16 a[128:131], v[152:155], a[176:179], a[128:131]\n\t"
    "s_waitcnt lgkmcnt(4)\n\t"
    "v_cvt_pk_bf16_f32 v202, v204, v205\n\t"
    "v_cvt_pk_bf16_f32 v203, v206, v207\n\t"
    "v_mfma_f32_16x16x32_bf16 a[16:19], v[128:131], a[176:179], a[16:19]\n\t"
    "ds_read_b64_tr_b16 v[144:145], v198 offset:16384\n\t"
    "v_mfma_f32_16x16x32_bf16 a[16:19], v[132:135], a[192:195], a[16:19]\n\t"
    "ds_read_b64_tr_b16 v[146:147], v198 offset:20480\n\t"
    "v_mfma_f32_16x16x32_bf16 a[20:23], v[132:135], a[196:199], a[20:23]\n\t"
    "buffer_load_dwordx4 a[68:71], v246, s[88:91], s46 offen sc1\n\t"
    "v_mfma_f32_16x16x32_bf16 a[20:23], v[128:131], a[180:183], a[20:23]\n\t"
    "buffer_store_dwordx4 v[200:203], v248, s[88:91], s56 offen sc1\n\t"
    "v_mfma_f32_16x16x32_bf16 a[24:27], v[128:131], a[184:187], a[24:27]\n\t"
    "ds_read_b64_tr_b16 v[148:149], v198 offset:24576\n\t"
    "v_accvgpr_read_b32 v208, a216\n\t"
    "v_mfma_f32_16x16x32_bf16 a[24:27], v[132:135], a[200:203], a[24:27]\n\t"
    "ds_read_b64_tr_b16 v[150:151], v198 offset:28672\n\t"
    "v_accvgpr_read_b32 v209, a217\n\t"
    "v_mfma_f32_16x16x32_bf16 a[28:31], v[132:135], a[204:207], a[28:31]\n\t"
    "buffer_load_dwordx4 a[100:103], v246, s[88:91], s47 offen sc1\n\t"
    "v_mfma_f32_16x16x32_bf16 a[28:31], v[128:131], a[188:191], a[28:31]\n\t"
    "s_waitcnt lgkmcnt(4)\n\t"
    "v_accvgpr_read_b32 v210, a218\n\t"
    "v_accvgpr_read_b32 v211, a219\n\t"
    "v_mfma_f32_16x16x32_bf16 a[60:63], v[136:139], a[188:191], a[60:63]\n\t"
    "ds_read_b64_tr_b16 v[152:153], v199 offset:16384\n\t"
    "v_accvgpr_read_b32 v212, a232\n\t"
    "v_mfma_f32_16x16x32_bf16 a[60:63], v[140:143], a[204:207], a[60:63]\n\t"
    "ds_read_b64_tr_b16 v[154:155], v199 offset:20480\n\t"
    "v_accvgpr_read_b32 v213, a233\n\t"
    "v_mfma_f32_16x16x32_bf16 a[56:59], v[140:143], a[200:203], a[56:59]\n\t"
    "buffer_load_dwordx4 a[72:75], v247, s[88:91], s46 offen sc1\n\t"
    "v_mfma_f32_16x16x32_bf16 a[56:59], v[136:139], a[184:187], a[56:59]\n\t"
    "v_accvgpr_read_b32 v214, a234\n\t"
    "v_accvgpr_read_b32 v215, a235\n\t"
    "v_mfma_f32_16x16x32_bf16 a[52:55], v[136:139], a[180:183], a[52:55]\n\t"
    "ds_read_b64_tr_b16 v[156:157], v199 offset:24576\n\t"
    "v_cvt_pk_bf16_f32 v208, v208, v209\n\t"
    "v_mfma_f32_16x16x32_bf16 a[52:55], v[140:143], a[196:199], a[52:55]\n\t"
    "ds_read_b64_tr_b16 v[158:159], v199 offset:28672\n\t"
    "v_cvt_pk_bf16_f32 v209, v210, v211\n\t"
    "v_mfma_f32_16x16x32_bf16 a[48:51], v[140:143], a[192:195], a[48:51]\n\t"
    "v_cvt_pk_bf16_f32 v210, v212, v213\n\t"
    "v_cvt_pk_bf16_f32 v211, v214, v215\n\t"
    "v_mfma_f32_16x16x32_bf16 a[48:51], v[136:139], a[176:179], a[48:51]\n\t"
    "s_waitcnt vmcnt(18) lgkmcnt(4)\n\t"
    "s_barrier\n\t"
    "v_mfma_f32_16x16x32_bf16 a[80:83], v[144:147], a[176:179], a[80:83]\n\t"
    "ds_read_b64_tr_b16 v[128:129], v192 offset:32768\n\t"
    "v_mfma_f32_16x16x32_bf16 a[80:83], v[148:151], a[192:195], a[80:83]\n\t"
    "ds_read_b64_tr_b16 v[130:131], v192 offset:36864\n\t"
    "v_mfma_f32_16x16x32_bf16 a[84:87], v[148:151], a[196:199], a[84:87]\n\t"
    "buffer_load_dwordx4 a[104:107], v247, s[88:91], s47 offen sc1\n\t"
    "v_mfma_f32_16x16x32_bf16 a[84:87], v[144:147], a[180:183], a[84:87]\n\t"
    "v_mfma_f32_16x16x32_bf16 a[88:91], v[144:147], a[184:187], a[88:91]\n\t"
    "ds_read_b64_tr_b16 v[132:133], v192 offset:40960\n\t"
    "v_mfma_f32_16x16x32_bf16 a[88:91], v[148:151], a[200:203], a[88:91]\n\t"
    "ds_read_b64_tr_b16 v[134:135], v192 offset:45056\n\t"
    "v_mfma_f32_16x16x32_bf16 a[92:95], v[148:151], a[204:207], a[92:95]\n\t"
    "buffer_load_dwordx4 a[76:79], v249, s[88:91], s46 offen sc1\n\t"
    "v_mfma_f32_16x16x32_bf16 a[92:95], v[144:147], a[188:191], a[92:95]\n\t"
    "s_waitcnt lgkmcnt(4)\n\t"
    "v_mfma_f32_16x16x32_bf16 a[124:127], v[152:155], a[188:191], a[124:127]\n\t"
    "ds_read_b64_tr_b16 v[136:137], v193 offset:32768\n\t"
    "v_mfma_f32_16x16x32_bf16 a[124:127], v[156:159], a[204:207], a[124:127]\n\t"
    "ds_read_b64_tr_b16 v[138:139], v193 offset:36864\n\t"
    "v_mfma_f32_16x16x32_bf16 a[120:123], v[156:159], a[200:203], a[120:123]\n\t"
    "buffer_load_dwordx4 a[108:111], v249, s[88:91], s47 offen sc1\n\t"
    "v_mfma_f32_16x16x32_bf16 a[120:123], v[152:155], a[184:187], a[120:123]\n\t"
    "s_bfe_u32 s84, s16, 262152\n\t"
    "s_lshl_b32 s84, s84, 17\n\t"
    "s_bfe_u32 s46, s16, 262152\n\t"
    "v_mfma_f32_16x16x32_bf16 a[116:119], v[152:155], a[180:183], a[116:119]\n\t"
    "ds_read_b64_tr_b16 v[140:141], v193 offset:40960\n\t"
    "s_lshl_b32 s46, s46, 13\n\t"
    "v_mfma_f32_16x16x32_bf16 a[116:119], v[156:159], a[196:199], a[116:119]\n\t"
    "ds_read_b64_tr_b16 v[142:143], v193 offset:45056\n\t"
    "s_add_u32 s46, s46, s45\n\t"
    "v_mfma_f32_16x16x32_bf16 a[112:115], v[156:159], a[192:195], a[112:115]\n\t"
    "s_add_u32 s47, s46, 4096\n\t"
    "v_mfma_f32_16x16x32_bf16 a[112:115], v[152:155], a[176:179], a[112:115]\n\t"
    "s_waitcnt vmcnt(13) lgkmcnt(4)\n\t"
    "v_mfma_f32_16x16x32_bf16 v[0:3], v[128:131], a[0:3], 0\n\t"
    "ds_read_b64_tr_b16 v[144:145], v194 offset:32768\n\t"
    "v_mfma_f32_16x16x32_bf16 v[0:3], v[132:135], a[32:35], v[0:3]\n\t"
    "ds_read_b64_tr_b16 v[146:147], v194 offset:36864\n\t"
    "s_add_u32 m0, s48, 49152\n\t"
    "v_mfma_f32_16x16x32_bf16 v[32:35], v[132:135], a[36:39], 0\n\t"
    "buffer_load_dwordx4 v242, s[32:35], s84 offen sc1 lds\n\t"
    "v_mfma_f32_16x16x32_bf16 v[32:35], v[128:131], a[4:7], v[32:35]\n\t"
    "v_mfma_f32_16x16x32_bf16 v[64:67], v[128:131], a[8:11], 0\n\t"
    "ds_read_b64_tr_b16 v[148:149], v194 offset:40960\n\t"
    "v_mfma_f32_16x16x32_bf16 v[64:67], v[132:135], a[40:43], v[64:67]\n\t"
    "ds_read_b64_tr_b16 v[150:151], v194 offset:45056\n\t"
    "v_mfma_f32_16x16x32_bf16 v[96:99], v[132:135], a[44:47], 0\n\t"
    "buffer_load_dwordx4 v243, s[32:35], s84 offen offset:1024 sc1 lds\n\t"
    "v_mfma_f32_16x16x32_bf16 v[96:99], v[128:131], a[12:15], v[96:99]\n\t"
    "s_waitcnt lgkmcnt(4)\n\t"
    "v_mfma_f32_16x16x32_bf16 v[100:103], v[136:139], a[12:15], 0\n\t"
    "ds_read_b64_tr_b16 v[152:153], v195 offset:32768\n\t"
    "v_mfma_f32_16x16x32_bf16 v[100:103], v[140:143], a[44:47], v[100:103]\n\t"
    "ds_read_b64_tr_b16 v[154:155], v195 offset:36864\n\t"
    "v_mfma_f32_16x16x32_bf16 v[68:71], v[140:143], a[40:43], 0\n\t"
    "buffer_load_dwordx4 v244, s[32:35], s84 offen offset:2048 sc1 lds\n\t"
    "v_mfma_f32_16x16x32_bf16 v[68:71], v[136:139], a[8:11], v[68:71]\n\t"
    "v_mfma_f32_16x16x32_bf16 v[36:39], v[136:139], a[4:7], 0\n\t"
    "ds_read_b64_tr_b16 v[156:157], v195 offset:40960\n\t"
    "v_mfma_f32_16x16x32_bf16 v[36:39], v[140:143], a[36:39], v[36:39]\n\t"
    "ds_read_b64_tr_b16 v[158:159], v195 offset:45056\n\t"
    "v_mfma_f32_16x16x32_bf16 v[4:7], v[140:143], a[32:35], 0\n\t"
    "v_mfma_f32_16x16x32_bf16 v[4:7], v[136:139], a[0:3], v[4:7]\n\t"
    "s_waitcnt lgkmcnt(4)\n\t"
    "v_mfma_f32_16x16x32_bf16 v[8:11], v[144:147], a[0:3], 0\n\t"
    "ds_read_b64_tr_b16 v[128:129], v196 offset:32768\n\t"
    "v_mfma_f32_16x16x32_bf16 v[8:11], v[148:151], a[32:35], v[8:11]\n\t"
    "ds_read_b64_tr_b16 v[130:131], v196 offset:36864\n\t"
    "v_mfma_f32_16x16x32_bf16 v[40:43], v[148:151], a[36:39], 0\n\t"
    "buffer_load_dwordx4 v245, s[32:35], s84 offen offset:3072 sc1 lds\n\t"
    "v_mfma_f32_16x16x32_bf16 v[40:43], v[144:147], a[4:7], v[40:43]\n\t"
    "buffer_store_dwordx4 v[208:211], v248, s[88:91], s57 offen sc1\n\t"
    "v_mfma_f32_16x16x32_bf16 v[72:75], v[144:147], a[8:11], 0\n\t"
    "ds_read_b64_tr_b16 v[132:133], v196 offset:40960\n\t"
    "v_accvgpr_read_b32 v216, a220\n\t"
    "v_mfma_f32_16x16x32_bf16 v[72:75], v[148:151], a[40:43], v[72:75]\n\t"
    "ds_read_b64_tr_b16 v[134:135], v196 offset:45056\n\t"
    "v_accvgpr_read_b32 v217, a221\n\t"
    "v_mfma_f32_16x16x32_bf16 v[104:107], v[148:151], a[44:47], 0\n\t"
    "buffer_load_dwordx4 a[144:147], v241, s[88:91], s46 offen sc1\n\t"
    "v_mfma_f32_16x16x32_bf16 v[104:107], v[144:147], a[12:15], v[104:107]\n\t"
    "s_waitcnt lgkmcnt(4)\n\t"
    "v_accvgpr_read_b32 v218, a222\n\t"
    "v_accvgpr_read_b32 v219, a223\n\t"
    "v_mfma_f32_16x16x32_bf16 v[108:111], v[152:155], a[12:15], 0\n\t"
    "ds_read_b64_tr_b16 v[136:137], v197 offset:32768\n\t"
    "v_accvgpr_read_b32 v220, a236\n\t"
    "v_mfma_f32_16x16x32_bf16 v[108:111], v[156:159], a[44:47], v[108:111]\n\t"
    "ds_read_b64_tr_b16 v[138:139], v197 offset:36864\n\t"
    "v_accvgpr_read_b32 v221, a237\n\t"
    "v_mfma_f32_16x16x32_bf16 v[76:79], v[156:159], a[40:43], 0\n\t"
    "buffer_load_dwordx4 a[160:163], v241, s[88:91], s47 offen sc1\n\t"
    "v_mfma_f32_16x16x32_bf16 v[76:79], v[152:155], a[8:11], v[76:79]\n\t"
    "v_accvgpr_read_b32 v222, a238\n\t"
    "v_accvgpr_read_b32 v223, a239\n\t"
    "v_mfma_f32_16x16x32_bf16 v[44:47], v[152:155], a[4:7], 0\n\t"
    "ds_read_b64_tr_b16 v[140:141], v197 offset:40960\n\t"
    "v_cvt_pk_bf16_f32 v216, v216, v217\n\t"
    "v_mfma_f32_16x16x32_bf16 v[44:47], v[156:159], a[36:39], v[44:47]\n\t"
    "ds_read_b64_tr_b16 v[142:143], v197 offset:45056\n\t"
    "v_cvt_pk_bf16_f32 v217, v218, v219\n\t"
    "v_mfma_f32_16x16x32_bf16 v[12:15], v[156:159], a[32:35], 0\n\t"
    "v_cvt_pk_bf16_f32 v218, v220, v221\n\t"
    "v_cvt_pk_bf16_f32 v219, v222, v223\n\t"
    "v_mfma_f32_16x16x32_bf16 v[12:15], v[152:155], a[0:3], v[12:15]\n\t"
    "s_waitcnt lgkmcnt(4)\n\t"
    "v_mfma_f32_16x16x32_bf16 v[16:19], v[128:131], a[0:3], 0\n\t"
    "ds_read_b64_tr_b16 v[144:145], v198 offset:32768\n\t"
    "v_mfma_f32_16x16x32_bf16 v[16:19], v[132:135], a[32:35], v[16:19]\n\t"
    "ds_read_b64_tr_b16 v[146:147], v198 offset:36864\n\t"
    "v_mfma_f32_16x16x32_bf16 v[48:51], v[132:135], a[36:39], 0\n\t"
    "buffer_load_dwordx4 a[148:151], v246, s[88:91], s46 offen sc1\n\t"
    "v_mfma_f32_16x16x32_bf16 v[48:51], v[128:131], a[4:7], v[48:51]\n\t"
    "v_mfma_f32_16x16x32_bf16 v[80:83], v[128:131], a[8:11], 0\n\t"
    "ds_read_b64_tr_b16 v[148:149], v198 offset:40960\n\t"
    "v_mfma_f32_16x16x32_bf16 v[80:83], v[132:135], a[40:43], v[80:83]\n\t"
    "ds_read_b64_tr_b16 v[150:151], v198 offset:45056\n\t"
    "v_mfma_f32_16x16x32_bf16 v[112:115], v[132:135], a[44:47], 0\n\t"
    "buffer_load_dwordx4 a[164:167], v246, s[88:91], s47 offen sc1\n\t"
    "v_mfma_f32_16x16x32_bf16 v[112:115], v[128:131], a[12:15], v[112:115]\n\t"
    "s_waitcnt lgkmcnt(4)\n\t"
    "v_mfma_f32_16x16x32_bf16 v[116:119], v[136:139], a[12:15], 0\n\t"
    "ds_read_b64_tr_b16 v[152:153], v199 offset:32768\n\t"
    "v_mfma_f32_16x16x32_bf16 v[116:119], v[140:143], a[44:47], v[116:119]\n\t"
    "ds_read_b64_tr_b16 v[154:155], v199 offset:36864\n\t"
    "v_mfma_f32_16x16x32_bf16 v[84:87], v[140:143], a[40:43], 0\n\t"
    "buffer_load_dwordx4 a[152:155], v247, s[88:91], s46 offen sc1\n\t"
    "v_mfma_f32_16x16x32_bf16 v[84:87], v[136:139], a[8:11], v[84:87]\n\t"
    "v_mfma_f32_16x16x32_bf16 v[52:55], v[136:139], a[4:7], 0\n\t"
    "ds_read_b64_tr_b16 v[156:157], v199 offset:40960\n\t"
    "v_mfma_f32_16x16x32_bf16 v[52:55], v[140:143], a[36:39], v[52:55]\n\t"
    "ds_read_b64_tr_b16 v[158:159], v199 offset:45056\n\t"
    "v_mfma_f32_16x16x32_bf16 v[20:23], v[140:143], a[32:35], 0\n\t"
    "v_mfma_f32_16x16x32_bf16 v[20:23], v[136:139], a[0:3], v[20:23]\n\t"
    "s_waitcnt vmcnt(19) lgkmcnt(4)\n\t"
    "s_barrier\n\t"
    "v_mfma_f32_16x16x32_bf16 v[24:27], v[144:147], a[0:3], 0\n\t"
    "ds_read_b64_tr_b16 v[128:129], v192 offset:49152\n\t"
    "v_mfma_f32_16x16x32_bf16 v[24:27], v[148:151], a[32:35], v[24:27]\n\t"
    "ds_read_b64_tr_b16 v[130:131], v192 offset:53248\n\t"
    "v_mfma_f32_16x16x32_bf16 v[56:59], v[148:151], a[36:39], 0\n\t"
    "buffer_load_dwordx4 a[168:171], v247, s[88:91], s47 offen sc1\n\t"
    "v_mfma_f32_16x16x32_bf16 v[56:59], v[144:147], a[4:7], v[56:59]\n\t"
    "v_mfma_f32_16x16x32_bf16 v[88:91], v[144:147], a[8:11], 0\n\t"
    "ds_read_b64_tr_b16 v[132:133], v192 offset:57344\n\t"
    "v_mfma_f32_16x16x32_bf16 v[88:91], v[148:151], a[40:43], v[88:91]\n\t"
    "ds_read_b64_tr_b16 v[134:135], v192 offset:61440\n\t"
    "v_mfma_f32_16x16x32_bf16 v[120:123], v[148:151], a[44:47], 0\n\t"
    "buffer_load_dwordx4 a[156:159], v249, s[88:91], s46 offen sc1\n\t"
    "v_mfma_f32_16x16x32_bf16 v[120:123], v[144:147], a[12:15], v[120:123]\n\t"
    "s_waitcnt lgkmcnt(4)\n\t"
    "v_mfma_f32_16x16x32_bf16 v[124:127], v[152:155], a[12:15], 0\n\t"
    "ds_read_b64_tr_b16 v[136:137], v193 offset:49152\n\t"
    "v_mfma_f32_16x16x32_bf16 v[124:127], v[156:159], a[44:47], v[124:127]\n\t"
    "ds_read_b64_tr_b16 v[138:139], v193 offset:53248\n\t"
    "v_mfma_f32_16x16x32_bf16 v[92:95], v[156:159], a[40:43], 0\n\t"
    "buffer_load_dwordx4 a[172:175], v249, s[88:91], s47 offen sc1\n\t"
    "v_mfma_f32_16x16x32_bf16 v[92:95], v[152:155], a[8:11], v[92:95]\n\t"
    "s_bfe_u32 s84, s16, 262156\n\t"
    "s_lshl_b32 s84, s84, 17\n\t"
    "s_bfe_u32 s46, s16, 262156\n\t"
    "v_mfma_f32_16x16x32_bf16 v[60:63], v[152:155], a[4:7], 0\n\t"
    "ds_read_b64_tr_b16 v[140:141], v193 offset:57344\n\t"
    "s_lshl_b32 s46, s46, 13\n\t"
    "v_mfma_f32_16x16x32_bf16 v[60:63], v[156:159], a[36:39], v[60:63]\n\t"
    "ds_read_b64_tr_b16 v[142:143], v193 offset:61440\n\t"
    "s_add_u32 s46, s46, s45\n\t"
    "v_mfma_f32_16x16x32_bf16 v[28:31], v[156:159], a[32:35], 0\n\t"
    "s_add_u32 s47, s46, 4096\n\t"
    "v_mfma_f32_16x16x32_bf16 v[28:31], v[152:155], a[0:3], v[28:31]\n\t"
    "s_waitcnt vmcnt(13) lgkmcnt(4)\n\t"
    "v_mfma_f32_16x16x32_bf16 v[0:3], v[128:131], a[64:67], v[0:3]\n\t"
    "ds_read_b64_tr_b16 v[144:145], v194 offset:49152\n\t"
    "v_mfma_f32_16x16x32_bf16 v[0:3], v[132:135], a[96:99], v[0:3]\n\t"
    "ds_read_b64_tr_b16 v[146:147], v194 offset:53248\n\t"
    "s_add_u32 m0, s48, 65536\n\t"
    "v_mfma_f32_16x16x32_bf16 v[32:35], v[132:135], a[100:103], v[32:35]\n\t"
    "buffer_load_dwordx4 v242, s[32:35], s84 offen sc1 lds\n\t"
    "v_mfma_f32_16x16x32_bf16 v[32:35], v[128:131], a[68:71], v[32:35]\n\t"
    "buffer_store_dwordx4 v[216:219], v248, s[88:91], s58 offen sc1\n\t"
    "v_mfma_f32_16x16x32_bf16 v[64:67], v[128:131], a[72:75], v[64:67]\n\t"
    "ds_read_b64_tr_b16 v[148:149], v194 offset:57344\n\t"
    "v_accvgpr_read_b32 v200, a244\n\t"
    "v_mfma_f32_16x16x32_bf16 v[64:67], v[132:135], a[104:107], v[64:67]\n\t"
    "ds_read_b64_tr_b16 v[150:151], v194 offset:61440\n\t"
    "v_accvgpr_read_b32 v201, a245\n\t"
    "v_mfma_f32_16x16x32_bf16 v[96:99], v[132:135], a[108:111], v[96:99]\n\t"
    "buffer_load_dwordx4 v243, s[32:35], s84 offen offset:1024 sc1 lds\n\t"
    "v_mfma_f32_16x16x32_bf16 v[96:99], v[128:131], a[76:79], v[96:99]\n\t"
    "s_waitcnt lgkmcnt(4)\n\t"
    "v_accvgpr_read_b32 v202, a246\n\t"
    "v_accvgpr_read_b32 v203, a247\n\t"
    "v_mfma_f32_16x16x32_bf16 v[100:103], v[136:139], a[76:79], v[100:103]\n\t"
    "ds_read_b64_tr_b16 v[152:153], v195 offset:49152\n\t"
    "v_accvgpr_read_b32 v204, a132\n\t"
    "v_mfma_f32_16x16x32_bf16 v[100:103], v[140:143], a[108:111], v[100:103]\n\t"
    "ds_read_b64_tr_b16 v[154:155], v195 offset:53248\n\t"
    "v_accvgpr_read_b32 v205, a133\n\t"
    "v_mfma_f32_16x16x32_bf16 v[68:71], v[140:143], a[104:107], v[68:71]\n\t"
    "buffer_load_dwordx4 v244, s[32:35], s84 offen offset:2048 sc1 lds\n\t"
    "v_mfma_f32_16x16x32_bf16 v[68:71], v[136:139], a[72:75], v[68:71]\n\t"
    "v_accvgpr_read_b32 v206, a134\n\t"
    "v_accvgpr_read_b32 v207, a135\n\t"
    "v_mfma_f32_16x16x32_bf16 v[36:39], v[136:139], a[68:71], v[36:39]\n\t"
    "ds_read_b64_tr_b16 v[156:157], v195 offset:57344\n\t"
    "v_cvt_pk_bf16_f32 v200, v200, v201\n\t"
    "v_mfma_f32_16x16x32_bf16 v[36:39], v[140:143], a[100:103], v[36:39]\n\t"
    "ds_read_b64_tr_b16 v[158:159], v195 offset:61440\n\t"
    "v_cvt_pk_bf16_f32 v201, v202, v203\n\t"
    "v_mfma_f32_16x16x32_bf16 v[4:7], v[140:143], a[96:99], v[4:7]\n\t"
    "v_cvt_pk_bf16_f32 v202, v204, v205\n\t"
    "v_cvt_pk_bf16_f32 v203, v206, v207\n\t"
    "v_mfma_f32_16x16x32_bf16 v[4:7], v[136:139], a[64:67], v[4:7]\n\t"
    "s_waitcnt lgkmcnt(4)\n\t"
    "v_mfma_f32_16x16x32_bf16 v[8:11], v[144:147], a[64:67], v[8:11]\n\t"
    "ds_read_b64_tr_b16 v[128:129], v196 offset:49152\n\t"
    "v_mfma_f32_16x16x32_bf16 v[8:11], v[148:151], a[96:99], v[8:11]\n\t"
    "ds_read_b64_tr_b16 v[130:131], v196 offset:53248\n\t"
    "v_mfma_f32_16x16x32_bf16 v[40:43], v[148:151], a[100:103], v[40:43]\n\t"
    "buffer_load_dwordx4 v245, s[32:35], s84 offen offset:3072 sc1 lds\n\t"
    "v_mfma_f32_16x16x32_bf16 v[40:43], v[144:147], a[68:71], v[40:43]\n\t"
    "v_mfma_f32_16x16x32_bf16 v[72:75], v[144:147], a[72:75], v[72:75]\n\t"
    "ds_read_b64_tr_b16 v[132:133], v196 offset:57344\n\t"
    "v_mfma_f32_16x16x32_bf16 v[72:75], v[148:151], a[104:107], v[72:75]\n\t"
    "ds_read_b64_tr_b16 v[134:135], v196 offset:61440\n\t"
    "v_mfma_f32_16x16x32_bf16 v[104:107], v[148:151], a[108:111], v[104:107]\n\t"
    "buffer_load_dwordx4 a[176:179], v241, s[88:91], s46 offen sc1\n\t"
    "v_mfma_f32_16x16x32_bf16 v[104:107], v[144:147], a[76:79], v[104:107]\n\t"
    "s_waitcnt lgkmcnt(4)\n\t"
    "v_mfma_f32_16x16x32_bf16 v[108:111], v[152:155], a[76:79], v[108:111]\n\t"
    "ds_read_b64_tr_b16 v[136:137], v197 offset:49152\n\t"
    "v_mfma_f32_16x16x32_bf16 v[108:111], v[156:159], a[108:111], v[108:111]\n\t"
    "ds_read_b64_tr_b16 v[138:139], v197 offset:53248\n\t"
    "v_mfma_f32_16x16x32_bf16 v[76:79], v[156:159], a[104:107], v[76:79]\n\t"
    "buffer_load_dwordx4 a[192:195], v241, s[88:91], s47 offen sc1\n\t"
    "v_mfma_f32_16x16x32_bf16 v[76:79], v[152:155], a[72:75], v[76:79]\n\t"
    "v_mfma_f32_16x16x32_bf16 v[44:47], v[152:155], a[68:71], v[44:47]\n\t"
    "ds_read_b64_tr_b16 v[140:141], v197 offset:57344\n\t"
    "v_mfma_f32_16x16x32_bf16 v[44:47], v[156:159], a[100:103], v[44:47]\n\t"
    "ds_read_b64_tr_b16 v[142:143], v197 offset:61440\n\t"
    "v_mfma_f32_16x16x32_bf16 v[12:15], v[156:159], a[96:99], v[12:15]\n\t"
    "v_mfma_f32_16x16x32_bf16 v[12:15], v[152:155], a[64:67], v[12:15]\n\t"
    "s_waitcnt lgkmcnt(4)\n\t"
    "v_mfma_f32_16x16x32_bf16 v[16:19], v[128:131], a[64:67], v[16:19]\n\t"
    "ds_read_b64_tr_b16 v[144:145], v198 offset:49152\n\t"
    "v_mfma_f32_16x16x32_bf16 v[16:19], v[132:135], a[96:99], v[16:19]\n\t"
    "ds_read_b64_tr_b16 v[146:147], v198 offset:53248\n\t"
    "v_mfma_f32_16x16x32_bf16 v[48:51], v[132:135], a[100:103], v[48:51]\n\t"
    "buffer_load_dwordx4 a[180:183], v246, s[88:91], s46 offen sc1\n\t"
    "v_mfma_f32_16x16x32_bf16 v[48:51], v[128:131], a[68:71], v[48:51]\n\t"
    "v_mfma_f32_16x16x32_bf16 v[80:83], v[128:131], a[72:75], v[80:83]\n\t"
    "ds_read_b64_tr_b16 v[148:149], v198 offset:57344\n\t"
    "v_mfma_f32_16x16x32_bf16 v[80:83], v[132:135], a[104:107], v[80:83]\n\t"
    "ds_read_b64_tr_b16 v[150:151], v198 offset:61440\n\t"
    "v_mfma_f32_16x16x32_bf16 v[112:115], v[132:135], a[108:111], v[112:115]\n\t"
    "buffer_load_dwordx4 a[196:199], v246, s[88:91], s47 offen sc1\n\t"
    "v_mfma_f32_16x16x32_bf16 v[112:115], v[128:131], a[76:79], v[112:115]\n\t"
    "s_waitcnt lgkmcnt(4)\n\t"
    "v_mfma_f32_16x16x32_bf16 v[116:119], v[136:139], a[76:79], v[116:119]\n\t"
    "ds_read_b64_tr_b16 v[152:153], v199 offset:49152\n\t"
    "v_mfma_f32_16x16x32_bf16 v[116:119], v[140:143], a[108:111], v[116:119]\n\t"
    "ds_read_b64_tr_b16 v[154:155], v199 offset:53248\n\t"
    "v_mfma_f32_16x16x32_bf16 v[84:87], v[140:143], a[104:107], v[84:87]\n\t"
    "buffer_load_dwordx4 a[184:187], v247, s[88:91], s46 offen sc1\n\t"
    "v_mfma_f32_16x16x32_bf16 v[84:87], v[136:139], a[72:75], v[84:87]\n\t"
    "v_mfma_f32_16x16x32_bf16 v[52:55], v[136:139], a[68:71], v[52:55]\n\t"
    "ds_read_b64_tr_b16 v[156:157], v199 offset:57344\n\t"
    "v_mfma_f32_16x16x32_bf16 v[52:55], v[140:143], a[100:103], v[52:55]\n\t"
    "ds_read_b64_tr_b16 v[158:159], v199 offset:61440\n\t"
    "v_mfma_f32_16x16x32_bf16 v[20:23], v[140:143], a[96:99], v[20:23]\n\t"
    "v_mfma_f32_16x16x32_bf16 v[20:23], v[136:139], a[64:67], v[20:23]\n\t"
    "s_waitcnt vmcnt(19) lgkmcnt(4)\n\t"
    "s_barrier\n\t"
    "v_mfma_f32_16x16x32_bf16 v[24:27], v[144:147], a[64:67], v[24:27]\n\t"
    "ds_read_b64_tr_b16 v[128:129], v192 offset:0\n\t"
    "v_mfma_f32_16x16x32_bf16 v[24:27], v[148:151], a[96:99], v[24:27]\n\t"
    "ds_read_b64_tr_b16 v[130:131], v192 offset:4096\n\t"
    "v_mfma_f32_16x16x32_bf16 v[56:59], v[148:151], a[100:103], v[56:59]\n\t"
    "buffer_load_dwordx4 a[200:203], v247, s[88:91], s47 offen sc1\n\t"
    "v_mfma_f32_16x16x32_bf16 v[56:59], v[144:147], a[68:71], v[56:59]\n\t"
    "buffer_store_dwordx4 v[200:203], v248, s[88:91], s56 offen offset:1024 sc1\n\t"
    "v_mfma_f32_16x16x32_bf16 v[88:91], v[144:147], a[72:75], v[88:91]\n\t"
    "ds_read_b64_tr_b16 v[132:133], v192 offset:8192\n\t"
    "v_accvgpr_read_b32 v208, a248\n\t"
    "v_mfma_f32_16x16x32_bf16 v[88:91], v[148:151], a[104:107], v[88:91]\n\t"
    "ds_read_b64_tr_b16 v[134:135], v192 offset:12288\n\t"
    "v_accvgpr_read_b32 v209, a249\n\t"
    "v_mfma_f32_16x16x32_bf16 v[120:123], v[148:151], a[108:111], v[120:123]\n\t"
    "buffer_load_dwordx4 a[188:191], v249, s[88:91], s46 offen sc1\n\t"
    "v_mfma_f32_16x16x32_bf16 v[120:123], v[144:147], a[76:79], v[120:123]\n\t"
    "s_waitcnt lgkmcnt(4)\n\t"
    "v_accvgpr_read_b32 v210, a250\n\t"
    "v_accvgpr_read_b32 v211, a251\n\t"
    "v_mfma_f32_16x16x32_bf16 v[124:127], v[152:155], a[76:79], v[124:127]\n\t"
    "ds_read_b64_tr_b16 v[136:137], v193 offset:0\n\t"
    "v_accvgpr_read_b32 v212, a136\n\t"
    "v_mfma_f32_16x16x32_bf16 v[124:127], v[156:159], a[108:111], v[124:127]\n\t"
    "ds_read_b64_tr_b16 v[138:139], v193 offset:4096\n\t"
    "v_accvgpr_read_b32 v213, a137\n\t"
    "v_mfma_f32_16x16x32_bf16 v[92:95], v[156:159], a[104:107], v[92:95]\n\t"
    "buffer_load_dwordx4 a[204:207], v249, s[88:91], s47 offen sc1\n\t"
    "v_mfma_f32_16x16x32_bf16 v[92:95], v[152:155], a[72:75], v[92:95]\n\t"
    "s_bfe_u32 s84, s16, 262160\n\t"
    "s_lshl_b32 s84, s84, 17\n\t"
    "s_bfe_u32 s46, s16, 262160\n\t"
    "v_mfma_f32_16x16x32_bf16 v[60:63], v[152:155], a[68:71], v[60:63]\n\t"
    "ds_read_b64_tr_b16 v[140:141], v193 offset:8192\n\t"
    "s_lshl_b32 s46, s46, 13\n\t"
    "v_mfma_f32_16x16x32_bf16 v[60:63], v[156:159], a[100:103], v[60:63]\n\t"
    "ds_read_b64_tr_b16 v[142:143], v193 offset:12288\n\t"
    "s_add_u32 s46, s46, s45\n\t"
    "v_mfma_f32_16x16x32_bf16 v[28:31], v[156:159], a[96:99], v[28:31]\n\t"
    "s_add_u32 s47, s46, 4096\n\t"
    "v_accvgpr_read_b32 v214, a138\n\t"
    "v_accvgpr_read_b32 v215, a139\n\t"
    "v_mfma_f32_16x16x32_bf16 v[28:31], v[152:155], a[64:67], v[28:31]\n\t"
    "s_waitcnt vmcnt(14) lgkmcnt(4)\n\t"
    "v_cvt_pk_bf16_f32 v208, v208, v209\n\t"
    "v_cvt_pk_bf16_f32 v209, v210, v211\n\t"
    "v_mfma_f32_16x16x32_bf16 v[0:3], v[128:131], a[144:147], v[0:3]\n\t"
    "ds_read_b64_tr_b16 v[144:145], v194 offset:0\n\t"
    "v_cvt_pk_bf16_f32 v210, v212, v213\n\t"
    "v_mfma_f32_16x16x32_bf16 v[0:3], v[132:135], a[160:163], v[0:3]\n\t"
    "ds_read_b64_tr_b16 v[146:147], v194 offset:4096\n\t"
    "s_add_u32 m0, s48, 81920\n\t"
    "v_mfma_f32_16x16x32_bf16 v[32:35], v[132:135], a[164:167], v[32:35]\n\t"
    "buffer_load_dwordx4 v242, s[32:35], s84 offen sc1 lds\n\t"
    "v_mfma_f32_16x16x32_bf16 v[32:35], v[128:131], a[148:151], v[32:35]\n\t"
    "s_or_b32 s77, s2, s3\n\t"
    "s_cbranch_scc0 .Lfb_e2\n\t"
    ".Lfb_e2_r:\n\t"
    "v_cvt_pk_bf16_f32 v211, v214, v215\n\t"
    "v_mfma_f32_16x16x32_bf16 v[64:67], v[128:131], a[152:155], v[64:67]\n\t"
    "ds_read_b64_tr_b16 v[148:149], v194 offset:8192\n\t"
    "v_mfma_f32_16x16x32_bf16 v[64:67], v[132:135], a[168:171], v[64:67]\n\t"
    "ds_read_b64_tr_b16 v[150:151], v194 offset:12288\n\t"
    "v_mfma_f32_16x16x32_bf16 v[96:99], v[132:135], a[172:175], v[96:99]\n\t"
    "buffer_load_dwordx4 v243, s[32:35], s84 offen offset:1024 sc1 lds\n\t"
    "v_mfma_f32_16x16x32_bf16 v[96:99], v[128:131], a[156:159], v[96:99]\n\t"
    "s_waitcnt lgkmcnt(4)\n\t"
    "v_mfma_f32_16x16x32_bf16 v[100:103], v[136:139], a[156:159], v[100:103]\n\t"
    "ds_read_b64_tr_b16 v[152:153], v195 offset:0\n\t"
    "v_mfma_f32_16x16x32_bf16 v[100:103], v[140:143], a[172:175], v[100:103]\n\t"
    "ds_read_b64_tr_b16 v[154:155], v195 offset:4096\n\t"
    "v_mfma_f32_16x16x32_bf16 v[68:71], v[140:143], a[168:171], v[68:71]\n\t"
    "buffer_load_dwordx4 v244, s[32:35], s84 offen offset:2048 sc1 lds\n\t"
    "v_mfma_f32_16x16x32_bf16 v[68:71], v[136:139], a[152:155], v[68:71]\n\t"
    "v_mfma_f32_16x16x32_bf16 v[36:39], v[136:139], a[148:151], v[36:39]\n\t"
    "ds_read_b64_tr_b16 v[156:157], v195 offset:8192\n\t"
    "v_mfma_f32_16x16x32_bf16 v[36:39], v[140:143], a[164:167], v[36:39]\n\t"
    "ds_read_b64_tr_b16 v[158:159], v195 offset:12288\n\t"
    "v_mfma_f32_16x16x32_bf16 v[4:7], v[140:143], a[160:163], v[4:7]\n\t"
    "v_mfma_f32_16x16x32_bf16 v[4:7], v[136:139], a[144:147], v[4:7]\n\t"
    "s_waitcnt lgkmcnt(4)\n\t"
    "v_mfma_f32_16x16x32_bf16 v[8:11], v[144:147], a[144:147], v[8:11]\n\t"
    "ds_read_b64_tr_b16 v[128:129], v196 offset:0\n\t"
    "v_mfma_f32_16x16x32_bf16 v[8:11], v[148:151], a[160:163], v[8:11]\n\t"
    "ds_read_b64_tr_b16 v[130:131], v196 offset:4096\n\t"
    "v_mfma_f32_16x16x32_bf16 v[40:43], v[148:151], a[164:167], v[40:43]\n\t"
    "buffer_load_dwordx4 v245, s[32:35], s84 offen offset:3072 sc1 lds\n\t"
    "v_mfma_f32_16x16x32_bf16 v[40:43], v[144:147], a[148:151], v[40:43]\n\t"
    "v_mfma_f32_16x16x32_bf16 v[72:75], v[144:147], a[152:155], v[72:75]\n\t"
    "ds_read_b64_tr_b16 v[132:133], v196 offset:8192\n\t"
    "v_mfma_f32_16x16x32_bf16 v[72:75], v[148:151], a[168:171], v[72:75]\n\t"
    "ds_read_b64_tr_b16 v[134:135], v196 offset:12288\n\t"
    "v_mfma_f32_16x16x32_bf16 v[104:107], v[148:151], a[172:175], v[104:107]\n\t"
    "buffer_load_dwordx4 a[0:3], v241, s[88:91], s46 offen sc1\n\t"
    "v_mfma_f32_16x16x32_bf16 v[104:107], v[144:147], a[156:159], v[104:107]\n\t"
    "s_waitcnt lgkmcnt(4)\n\t"
    "v_mfma_f32_16x16x32_bf16 v[108:111], v[152:155], a[156:159], v[108:111]\n\t"
    "ds_read_b64_tr_b16 v[136:137], v197 offset:0\n\t"
    "v_mfma_f32_16x16x32_bf16 v[108:111], v[156:159], a[172:175], v[108:111]\n\t"
    "ds_read_b64_tr_b16 v[138:139], v197 offset:4096\n\t"
    "v_mfma_f32_16x16x32_bf16 v[76:79], v[156:159], a[168:171], v[76:79]\n\t"
    "buffer_load_dwordx4 a[32:35], v241, s[88:91], s47 offen sc1\n\t"
    "v_mfma_f32_16x16x32_bf16 v[76:79], v[152:155], a[152:155], v[76:79]\n\t"
    "v_mfma_f32_16x16x32_bf16 v[44:47], v[152:155], a[148:151], v[44:47]\n\t"
    "ds_read_b64_tr_b16 v[140:141], v197 offset:8192\n\t"
    "v_mfma_f32_16x16x32_bf16 v[44:47], v[156:159], a[164:167], v[44:47]\n\t"
    "ds_read_b64_tr_b16 v[142:143], v197 offset:12288\n\t"
    "v_mfma_f32_16x16x32_bf16 v[12:15], v[156:159], a[160:163], v[12:15]\n\t"
    "v_mfma_f32_16x16x32_bf16 v[12:15], v[152:155], a[144:147], v[12:15]\n\t"
    "s_waitcnt lgkmcnt(4)\n\t"
    "v_mfma_f32_16x16x32_bf16 v[16:19], v[128:131], a[144:147], v[16:19]\n\t"
    "ds_read_b64_tr_b16 v[144:145], v198 offset:0\n\t"
    "v_mfma_f32_16x16x32_bf16 v[16:19], v[132:135], a[160:163], v[16:19]\n\t"
    "ds_read_b64_tr_b16 v[146:147], v198 offset:4096\n\t"
    "v_mfma_f32_16x16x32_bf16 v[48:51], v[132:135], a[164:167], v[48:51]\n\t"
    "buffer_load_dwordx4 a[4:7], v246, s[88:91], s46 offen sc1\n\t"
    "v_mfma_f32_16x16x32_bf16 v[48:51], v[128:131], a[148:151], v[48:51]\n\t"
    "buffer_store_dwordx4 v[208:211], v248, s[88:91], s57 offen offset:1024 sc1\n\t"
    "v_mfma_f32_16x16x32_bf16 v[80:83], v[128:131], a[152:155], v[80:83]\n\t"
    "ds_read_b64_tr_b16 v[148:149], v198 offset:8192\n\t"
    "v_accvgpr_read_b32 v216, a252\n\t"
    "v_mfma_f32_16x16x32_bf16 v[80:83], v[132:135], a[168:171], v[80:83]\n\t"
    "ds_read_b64_tr_b16 v[150:151], v198 offset:12288\n\t"
    "v_accvgpr_read_b32 v217, a253\n\t"
    "v_mfma_f32_16x16x32_bf16 v[112:115], v[132:135], a[172:175], v[112:115]\n\t"
    "buffer_load_dwordx4 a[36:39], v246, s[88:91], s47 offen sc1\n\t"
    "v_mfma_f32_16x16x32_bf16 v[112:115], v[128:131], a[156:159], v[112:115]\n\t"
    "s_waitcnt lgkmcnt(4)\n\t"
    "v_accvgpr_read_b32 v218, a254\n\t"
    "v_accvgpr_read_b32 v219, a255\n\t"
    "v_mfma_f32_16x16x32_bf16 v[116:119], v[136:139], a[156:159], v[116:119]\n\t"
    "ds_read_b64_tr_b16 v[152:153], v199 offset:0\n\t"
    "v_accvgpr_read_b32 v220, a140\n\t"
    "v_mfma_f32_16x16x32_bf16 v[116:119], v[140:143], a[172:175], v[116:119]\n\t"
    "ds_read_b64_tr_b16 v[154:155], v199 offset:4096\n\t"
    "v_accvgpr_read_b32 v221, a141\n\t"
    "v_mfma_f32_16x16x32_bf16 v[84:87], v[140:143], a[168:171], v[84:87]\n\t"
    "buffer_load_dwordx4 a[8:11], v247, s[88:91], s46 offen sc1\n\t"
    "v_mfma_f32_16x16x32_bf16 v[84:87], v[136:139], a[152:155], v[84:87]\n\t"
    "v_accvgpr_read_b32 v222, a142\n\t"
    "v_accvgpr_read_b32 v223, a143\n\t"
    "v_mfma_f32_16x16x32_bf16 v[52:55], v[136:139], a[148:151], v[52:55]\n\t"
    "ds_read_b64_tr_b16 v[156:157], v199 offset:8192\n\t"
    "v_cvt_pk_bf16_f32 v216, v216, v217\n\t"
    "v_mfma_f32_16x16x32_bf16 v[52:55], v[140:143], a[164:167], v[52:55]\n\t"
    "ds_read_b64_tr_b16 v[158:159], v199 offset:12288\n\t"
    "v_cvt_pk_bf16_f32 v217, v218, v219\n\t"
    "v_mfma_f32_16x16x32_bf16 v[20:23], v[140:143], a[160:163], v[20:23]\n\t"
    "v_cvt_pk_bf16_f32 v218, v220, v221\n\t"
    "v_cvt_pk_bf16_f32 v219, v222, v223\n\t"
    "v_mfma_f32_16x16x32_bf16 v[20:23], v[136:139], a[144:147], v[20:23]\n\t"
    "s_waitcnt vmcnt(19) lgkmcnt(4)\n\t"
    "s_barrier\n\t"
    "v_mfma_f32_16x16x32_bf16 v[24:27], v[144:147], a[144:147], v[24:27]\n\t"
    "ds_read_b64_tr_b16 v[128:129], v192 offset:16384\n\t"
    "v_mfma_f32_16x16x32_bf16 v[24:27], v[148:151], a[160:163], v[24:27]\n\t"
    "ds_read_b64_tr_b16 v[130:131], v192 offset:20480\n\t"
    "v_mfma_f32_16x16x32_bf16 v[56:59], v[148:151], a[164:167], v[56:59]\n\t"
    "buffer_load_dwordx4 a[40:43], v247, s[88:91], s47 offen sc1\n\t"
    "v_mfma_f32_16x16x32_bf16 v[56:59], v[144:147], a[148:151], v[56:59]\n\t"
    "v_mfma_f32_16x16x32_bf16 v[88:91], v[144:147], a[152:155], v[88:91]\n\t"
    "ds_read_b64_tr_b16 v[132:133], v192 offset:24576\n\t"
    "v_mfma_f32_16x16x32_bf16 v[88:91], v[148:151], a[168:171], v[88:91]\n\t"
    "ds_read_b64_tr_b16 v[134:135], v192 offset:28672\n\t"
    "v_mfma_f32_16x16x32_bf16 v[120:123], v[148:151], a[172:175], v[120:123]\n\t"
    "buffer_load_dwordx4 a[12:15], v249, s[88:91], s46 offen sc1\n\t"
    "v_mfma_f32_16x16x32_bf16 v[120:123], v[144:147], a[156:159], v[120:123]\n\t"
    "s_waitcnt lgkmcnt(4)\n\t"
    "v_mfma_f32_16x16x32_bf16 v[124:127], v[152:155], a[156:159], v[124:127]\n\t"
    "ds_read_b64_tr_b16 v[136:137], v193 offset:16384\n\t"
    "v_mfma_f32_16x16x32_bf16 v[124:127], v[156:159], a[172:175], v[124:127]\n\t"
    "ds_read_b64_tr_b16 v[138:139], v193 offset:20480\n\t"
    "v_mfma_f32_16x16x32_bf16 v[92:95], v[156:159], a[168:171], v[92:95]\n\t"
    "buffer_load_dwordx4 a[44:47], v249, s[88:91], s47 offen sc1\n\t"
    "v_mfma_f32_16x16x32_bf16 v[92:95], v[152:155], a[152:155], v[92:95]\n\t"
    "s_bfe_u32 s84, s16, 262164\n\t"
    "s_lshl_b32 s84, s84, 17\n\t"
    "s_bfe_u32 s46, s16, 262164\n\t"
    "v_mfma_f32_16x16x32_bf16 v[60:63], v[152:155], a[148:151], v[60:63]\n\t"
    "ds_read_b64_tr_b16 v[140:141], v193 offset:24576\n\t"
    "s_lshl_b32 s46, s46, 13\n\t"
    "v_mfma_f32_16x16x32_bf16 v[60:63], v[156:159], a[164:167], v[60:63]\n\t"
    "ds_read_b64_tr_b16 v[142:143], v193 offset:28672\n\t"
    "s_add_u32 s46, s46, s45\n\t"
    "v_mfma_f32_16x16x32_bf16 v[28:31], v[156:159], a[160:163], v[28:31]\n\t"
    "s_add_u32 s47, s46, 4096\n\t"
    "v_mfma_f32_16x16x32_bf16 v[28:31], v[152:155], a[144:147], v[28:31]\n\t"
    "s_waitcnt vmcnt(13) lgkmcnt(4)\n\t"
    "v_mfma_f32_16x16x32_bf16 v[0:3], v[128:131], a[176:179], v[0:3]\n\t"
    "ds_read_b64_tr_b16 v[144:145], v194 offset:16384\n\t"
    "v_mfma_f32_16x16x32_bf16 v[0:3], v[132:135], a[192:195], v[0:3]\n\t"
    "ds_read_b64_tr_b16 v[146:147], v194 offset:20480\n\t"
    "s_add_u32 m0, s48, 98304\n\t"
    "v_mfma_f32_16x16x32_bf16 v[32:35], v[132:135], a[196:199], v[32:35]\n\t"
    "buffer_load_dwordx4 v242, s[32:35], s84 offen sc1 lds\n\t"
    "v_mfma_f32_16x16x32_bf16 v[32:35], v[128:131], a[180:183], v[32:35]\n\t"
    "v_mfma_f32_16x16x32_bf16 v[64:67], v[128:131], a[184:187], v[64:67]\n\t"
    "ds_read_b64_tr_b16 v[148:149], v194 offset:24576\n\t"
    "v_mfma_f32_16x16x32_bf16 v[64:67], v[132:135], a[200:203], v[64:67]\n\t"
    "ds_read_b64_tr_b16 v[150:151], v194 offset:28672\n\t"
    "v_mfma_f32_16x16x32_bf16 v[96:99], v[132:135], a[204:207], v[96:99]\n\t"
    "buffer_load_dwordx4 v243, s[32:35], s84 offen offset:1024 sc1 lds\n\t"
    "v_mfma_f32_16x16x32_bf16 v[96:99], v[128:131], a[188:191], v[96:99]\n\t"
    "s_waitcnt lgkmcnt(4)\n\t"
    "v_mfma_f32_16x16x32_bf16 v[100:103], v[136:139], a[188:191], v[100:103]\n\t"
    "ds_read_b64_tr_b16 v[152:153], v195 offset:16384\n\t"
    "v_mfma_f32_16x16x32_bf16 v[100:103], v[140:143], a[204:207], v[100:103]\n\t"
    "ds_read_b64_tr_b16 v[154:155], v195 offset:20480\n\t"
    "v_mfma_f32_16x16x32_bf16 v[68:71], v[140:143], a[200:203], v[68:71]\n\t"
    "buffer_load_dwordx4 v244, s[32:35], s84 offen offset:2048 sc1 lds\n\t"
    "v_mfma_f32_16x16x32_bf16 v[68:71], v[136:139], a[184:187], v[68:71]\n\t"
    "v_mfma_f32_16x16x32_bf16 v[36:39], v[136:139], a[180:183], v[36:39]\n\t"
    "ds_read_b64_tr_b16 v[156:157], v195 offset:24576\n\t"
    "v_mfma_f32_16x16x32_bf16 v[36:39], v[140:143], a[196:199], v[36:39]\n\t"
    "ds_read_b64_tr_b16 v[158:159], v195 offset:28672\n\t"
    "v_mfma_f32_16x16x32_bf16 v[4:7], v[140:143], a[192:195], v[4:7]\n\t"
    "v_mfma_f32_16x16x32_bf16 v[4:7], v[136:139], a[176:179], v[4:7]\n\t"
    "s_waitcnt lgkmcnt(4)\n\t"
    "v_mfma_f32_16x16x32_bf16 v[8:11], v[144:147], a[176:179], v[8:11]\n\t"
    "ds_read_b64_tr_b16 v[128:129], v196 offset:16384\n\t"
    "v_mfma_f32_16x16x32_bf16 v[8:11], v[148:151], a[192:195], v[8:11]\n\t"
    "ds_read_b64_tr_b16 v[130:131], v196 offset:20480\n\t"
    "v_mfma_f32_16x16x32_bf16 v[40:43], v[148:151], a[196:199], v[40:43]\n\t"
    "buffer_load_dwordx4 v245, s[32:35], s84 offen offset:3072 sc1 lds\n\t"
    "v_mfma_f32_16x16x32_bf16 v[40:43], v[144:147], a[180:183], v[40:43]\n\t"
    "buffer_store_dwordx4 v[216:219], v248, s[88:91], s58 offen offset:1024 sc1\n\t"
    "v_mfma_f32_16x16x32_bf16 v[72:75], v[144:147], a[184:187], v[72:75]\n\t"
    "ds_read_b64_tr_b16 v[132:133], v196 offset:24576\n\t"
    "v_accvgpr_read_b32 v200, a20\n\t"
    "v_mfma_f32_16x16x32_bf16 v[72:75], v[148:151], a[200:203], v[72:75]\n\t"
    "ds_read_b64_tr_b16 v[134:135], v196 offset:28672\n\t"
    "v_accvgpr_read_b32 v201, a21\n\t"
    "v_mfma_f32_16x16x32_bf16 v[104:107], v[148:151], a[204:207], v[104:107]\n\t"
    "buffer_load_dwordx4 a[64:67], v241, s[88:91], s46 offen sc1\n\t"
    "v_mfma_f32_16x16x32_bf16 v[104:107], v[144:147], a[188:191], v[104:107]\n\t"
    "s_waitcnt lgkmcnt(4)\n\t"
    "v_accvgpr_read_b32 v202, a22\n\t"
    "v_accvgpr_read_b32 v203, a23\n\t"
    "v_mfma_f32_16x16x32_bf16 v[108:111], v[152:155], a[188:191], v[108:111]\n\t"
    "ds_read_b64_tr_b16 v[136:137], v197 offset:16384\n\t"
    "v_accvgpr_read_b32 v204, a52\n\t"
    "v_mfma_f32_16x16x32_bf16 v[108:111], v[156:159], a[204:207], v[108:111]\n\t"
    "ds_read_b64_tr_b16 v[138:139], v197 offset:20480\n\t"
    "v_accvgpr_read_b32 v205, a53\n\t"
    "v_mfma_f32_16x16x32_bf16 v[76:79], v[156:159], a[200:203], v[76:79]\n\t"
    "buffer_load_dwordx4 a[96:99], v241, s[88:91], s47 offen sc1\n\t"
    "v_mfma_f32_16x16x32_bf16 v[76:79], v[152:155], a[184:187], v[76:79]\n\t"
    "v_accvgpr_read_b32 v206, a54\n\t"
    "v_accvgpr_read_b32 v207, a55\n\t"
    "v_mfma_f32_16x16x32_bf16 v[44:47], v[152:155], a[180:183], v[44:47]\n\t"
    "ds_read_b64_tr_b16 v[140:141], v197 offset:24576\n\t"
    "v_cvt_pk_bf16_f32 v200, v200, v201\n\t"
    "v_mfma_f32_16x16x32_bf16 v[44:47], v[156:159], a[196:199], v[44:47]\n\t"
    "ds_read_b64_tr_b16 v[142:143], v197 offset:28672\n\t"
    "v_cvt_pk_bf16_f32 v201, v202, v203\n\t"
    "v_mfma_f32_16x16x32_bf16 v[12:15], v[156:159], a[192:195], v[12:15]\n\t"
    "v_cvt_pk_bf16_f32 v202, v204, v205\n\t"
    "v_cvt_pk_bf16_f32 v203, v206, v207\n\t"
    "v_mfma_f32_16x16x32_bf16 v[12:15], v[152:155], a[176:179], v[12:15]\n\t"
    "s_waitcnt lgkmcnt(4)\n\t"
    "v_mfma_f32_16x16x32_bf16 v[16:19], v[128:131], a[176:179], v[16:19]\n\t"
    "ds_read_b64_tr_b16 v[144:145], v198 offset:16384\n\t"
    "v_mfma_f32_16x16x32_bf16 v[16:19], v[132:135], a[192:195], v[16:19]\n\t"
    "ds_read_b64_tr_b16 v[146:147], v198 offset:20480\n\t"
    "v_mfma_f32_16x16x32_bf16 v[48:51], v[132:135], a[196:199], v[48:51]\n\t"
    "buffer_load_dwordx4 a[68:71], v246, s[88:91], s46 offen sc1\n\t"
    "v_mfma_f32_16x16x32_bf16 v[48:51], v[128:131], a[180:183], v[48:51]\n\t"
    "v_mfma_f32_16x16x32_bf16 v[80:83], v[128:131], a[184:187], v[80:83]\n\t"
    "ds_read_b64_tr_b16 v[148:149], v198 offset:24576\n\t"
    "v_mfma_f32_16x16x32_bf16 v[80:83], v[132:135], a[200:203], v[80:83]\n\t"
    "ds_read_b64_tr_b16 v[150:151], v198 offset:28672\n\t"
    "v_mfma_f32_16x16x32_bf16 v[112:115], v[132:135], a[204:207], v[112:115]\n\t"
    "buffer_load_dwordx4 a[100:103], v246, s[88:91], s47 offen sc1\n\t"
    "v_mfma_f32_16x16x32_bf16 v[112:115], v[128:131], a[188:191], v[112:115]\n\t"
    "s_waitcnt lgkmcnt(4)\n\t"
    "v_mfma_f32_16x16x32_bf16 v[116:119], v[136:139], a[188:191], v[116:119]\n\t"
    "ds_read_b64_tr_b16 v[152:153], v199 offset:16384\n\t"
    "v_mfma_f32_16x16x32_bf16 v[116:119], v[140:143], a[204:207], v[116:119]\n\t"
    "ds_read_b64_tr_b16 v[154:155], v199 offset:20480\n\t"
    "v_mfma_f32_16x16x32_bf16 v[84:87], v[140:143], a[200:203], v[84:87]\n\t"
    "buffer_load_dwordx4 a[72:75], v247, s[88:91], s46 offen sc1\n\t"
    "v_mfma_f32_16x16x32_bf16 v[84:87], v[136:139], a[184:187], v[84:87]\n\t"
    "v_mfma_f32_16x16x32_bf16 v[52:55], v[136:139], a[180:183], v[52:55]\n\t"
    "ds_read_b64_tr_b16 v[156:157], v199 offset:24576\n\t"
    "v_mfma_f32_16x16x32_bf16 v[52:55], v[140:143], a[196:199], v[52:55]\n\t"
    "ds_read_b64_tr_b16 v[158:159], v199 offset:28672\n\t"
    "v_mfma_f32_16x16x32_bf16 v[20:23], v[140:143], a[192:195], v[20:23]\n\t"
    "v_mfma_f32_16x16x32_bf16 v[20:23], v[136:139], a[176:179], v[20:23]\n\t"
    "s_waitcnt vmcnt(19) lgkmcnt(4)\n\t"
    "s_barrier\n\t"
    "v_mfma_f32_16x16x32_bf16 v[24:27], v[144:147], a[176:179], v[24:27]\n\t"
    "ds_read_b64_tr_b16 v[128:129], v192 offset:32768\n\t"
    "v_mfma_f32_16x16x32_bf16 v[24:27], v[148:151], a[192:195], v[24:27]\n\t"
    "ds_read_b64_tr_b16 v[130:131], v192 offset:36864\n\t"
    "v_mfma_f32_16x16x32_bf16 v[56:59], v[148:151], a[196:199], v[56:59]\n\t"
    "buffer_load_dwordx4 a[104:107], v247, s[88:91], s47 offen sc1\n\t"
    "v_mfma_f32_16x16x32_bf16 v[56:59], v[144:147], a[180:183], v[56:59]\n\t"
    "v_mfma_f32_16x16x32_bf16 v[88:91], v[144:147], a[184:187], v[88:91]\n\t"
    "ds_read_b64_tr_b16 v[132:133], v192 offset:40960\n\t"
    "v_mfma_f32_16x16x32_bf16 v[88:91], v[148:151], a[200:203], v[88:91]\n\t"
    "ds_read_b64_tr_b16 v[134:135], v192 offset:45056\n\t"
    "v_mfma_f32_16x16x32_bf16 v[120:123], v[148:151], a[204:207], v[120:123]\n\t"
    "buffer_load_dwordx4 a[76:79], v249, s[88:91], s46 offen sc1\n\t"
    "v_mfma_f32_16x16x32_bf16 v[120:123], v[144:147], a[188:191], v[120:123]\n\t"
    "s_waitcnt lgkmcnt(4)\n\t"
    "v_mfma_f32_16x16x32_bf16 v[124:127], v[152:155], a[188:191], v[124:127]\n\t"
    "ds_read_b64_tr_b16 v[136:137], v193 offset:32768\n\t"
    "v_mfma_f32_16x16x32_bf16 v[124:127], v[156:159], a[204:207], v[124:127]\n\t"
    "ds_read_b64_tr_b16 v[138:139], v193 offset:36864\n\t"
    "v_mfma_f32_16x16x32_bf16 v[92:95], v[156:159], a[200:203], v[92:95]\n\t"
    "buffer_load_dwordx4 a[108:111], v249, s[88:91], s47 offen sc1\n\t"
    "v_mfma_f32_16x16x32_bf16 v[92:95], v[152:155], a[184:187], v[92:95]\n\t"
    "s_bfe_u32 s84, s16, 262168\n\t"
    "s_lshl_b32 s84, s84, 17\n\t"
    "s_bfe_u32 s46, s16, 262168\n\t"
    "v_mfma_f32_16x16x32_bf16 v[60:63], v[152:155], a[180:183], v[60:63]\n\t"
    "ds_read_b64_tr_b16 v[140:141], v193 offset:40960\n\t"
    "s_lshl_b32 s46, s46, 13\n\t"
    "v_mfma_f32_16x16x32_bf16 v[60:63], v[156:159], a[196:199], v[60:63]\n\t"
    "ds_read_b64_tr_b16 v[142:143], v193 offset:45056\n\t"
    "s_add_u32 s46, s46, s45\n\t"
    "v_mfma_f32_16x16x32_bf16 v[28:31], v[156:159], a[192:195], v[28:31]\n\t"
    "s_add_u32 s47, s46, 4096\n\t"
    "v_mfma_f32_16x16x32_bf16 v[28:31], v[152:155], a[176:179], v[28:31]\n\t"
    "s_waitcnt vmcnt(13) lgkmcnt(4)\n\t"
    "v_mfma_f32_16x16x32_bf16 v[0:3], v[128:131], a[0:3], v[0:3]\n\t"
    "ds_read_b64_tr_b16 v[144:145], v194 offset:32768\n\t"
    "v_mfma_f32_16x16x32_bf16 v[0:3], v[132:135], a[32:35], v[0:3]\n\t"
    "ds_read_b64_tr_b16 v[146:147], v194 offset:36864\n\t"
    "s_add_u32 m0, s48, 49152\n\t"
    "v_mfma_f32_16x16x32_bf16 v[32:35], v[132:135], a[36:39], v[32:35]\n\t"
    "buffer_load_dwordx4 v242, s[32:35], s84 offen sc1 lds\n\t"
    "v_mfma_f32_16x16x32_bf16 v[32:35], v[128:131], a[4:7], v[32:35]\n\t"
    "buffer_store_dwordx4 v[200:203], v248, s[88:91], s56 offen offset:2048 sc1\n\t"
    "v_mfma_f32_16x16x32_bf16 v[64:67], v[128:131], a[8:11], v[64:67]\n\t"
    "ds_read_b64_tr_b16 v[148:149], v194 offset:40960\n\t"
    "v_accvgpr_read_b32 v208, a24\n\t"
    "v_mfma_f32_16x16x32_bf16 v[64:67], v[132:135], a[40:43], v[64:67]\n\t"
    "ds_read_b64_tr_b16 v[150:151], v194 offset:45056\n\t"
    "v_accvgpr_read_b32 v209, a25\n\t"
    "v_mfma_f32_16x16x32_bf16 v[96:99], v[132:135], a[44:47], v[96:99]\n\t"
    "buffer_load_dwordx4 v243, s[32:35], s84 offen offset:1024 sc1 lds\n\t"
    "v_mfma_f32_16x16x32_bf16 v[96:99], v[128:131], a[12:15], v[96:99]\n\t"
    "s_waitcnt lgkmcnt(4)\n\t"
    "v_accvgpr_read_b32 v210, a26\n\t"
    "v_accvgpr_read_b32 v211, a27\n\t"
    "v_mfma_f32_16x16x32_bf16 v[100:103], v[136:139], a[12:15], v[100:103]\n\t"
    "ds_read_b64_tr_b16 v[152:153], v195 offset:32768\n\t"
    "v_accvgpr_read_b32 v212, a56\n\t"
    "v_mfma_f32_16x16x32_bf16 v[100:103], v[140:143], a[44:47], v[100:103]\n\t"
    "ds_read_b64_tr_b16 v[154:155], v195 offset:36864\n\t"
    "v_accvgpr_read_b32 v213, a57\n\t"
    "v_mfma_f32_16x16x32_bf16 v[68:71], v[140:143], a[40:43], v[68:71]\n\t"
    "buffer_load_dwordx4 v244, s[32:35], s84 offen offset:2048 sc1 lds\n\t"
    "v_mfma_f32_16x16x32_bf16 v[68:71], v[136:139], a[8:11], v[68:71]\n\t"
    "v_accvgpr_read_b32 v214, a58\n\t"
    "v_accvgpr_read_b32 v215, a59\n\t"
    "v_mfma_f32_16x16x32_bf16 v[36:39], v[136:139], a[4:7], v[36:39]\n\t"
    "ds_read_b64_tr_b16 v[156:157], v195 offset:40960\n\t"
    "v_cvt_pk_bf16_f32 v208, v208, v209\n\t"
    "v_mfma_f32_16x16x32_bf16 v[36:39], v[140:143], a[36:39], v[36:39]\n\t"
    "ds_read_b64_tr_b16 v[158:159], v195 offset:45056\n\t"
    "v_cvt_pk_bf16_f32 v209, v210, v211\n\t"
    "v_mfma_f32_16x16x32_bf16 v[4:7], v[140:143], a[32:35], v[4:7]\n\t"
    "v_cvt_pk_bf16_f32 v210, v212, v213\n\t"
    "v_cvt_pk_bf16_f32 v211, v214, v215\n\t"
    "v_mfma_f32_16x16x32_bf16 v[4:7], v[136:139], a[0:3], v[4:7]\n\t"
    "s_waitcnt lgkmcnt(4)\n\t"
    "v_mfma_f32_16x16x32_bf16 v[8:11], v[144:147], a[0:3], v[8:11]\n\t"
    "ds_read_b64_tr_b16 v[128:129], v196 offset:32768\n\t"
    "v_mfma_f32_16x16x32_bf16 v[8:11], v[148:151], a[32:35], v[8:11]\n\t"
    "ds_read_b64_tr_b16 v[130:131], v196 offset:36864\n\t"
    "v_mfma_f32_16x16x32_bf16 v[40:43], v[148:151], a[36:39], v[40:43]\n\t"
    "buffer_load_dwordx4 v245, s[32:35], s84 offen offset:3072 sc1 lds\n\t"
    "v_mfma_f32_16x16x32_bf16 v[40:43], v[144:147], a[4:7], v[40:43]\n\t"
    "v_mfma_f32_16x16x32_bf16 v[72:75], v[144:147], a[8:11], v[72:75]\n\t"
    "ds_read_b64_tr_b16 v[132:133], v196 offset:40960\n\t"
    "v_mfma_f32_16x16x32_bf16 v[72:75], v[148:151], a[40:43], v[72:75]\n\t"
    "ds_read_b64_tr_b16 v[134:135], v196 offset:45056\n\t"
    "v_mfma_f32_16x16x32_bf16 v[104:107], v[148:151], a[44:47], v[104:107]\n\t"
    "buffer_load_dwordx4 a[144:147], v241, s[88:91], s46 offen sc1\n\t"
    "v_mfma_f32_16x16x32_bf16 v[104:107], v[144:147], a[12:15], v[104:107]\n\t"
    "s_waitcnt lgkmcnt(4)\n\t"
    "v_mfma_f32_16x16x32_bf16 v[108:111], v[152:155], a[12:15], v[108:111]\n\t"
    "ds_read_b64_tr_b16 v[136:137], v197 offset:32768\n\t"
    "v_mfma_f32_16x16x32_bf16 v[108:111], v[156:159], a[44:47], v[108:111]\n\t"
    "ds_read_b64_tr_b16 v[138:139], v197 offset:36864\n\t"
    "v_mfma_f32_16x16x32_bf16 v[76:79], v[156:159], a[40:43], v[76:79]\n\t"
    "buffer_load_dwordx4 a[160:163], v241, s[88:91], s47 offen sc1\n\t"
    "v_mfma_f32_16x16x32_bf16 v[76:79], v[152:155], a[8:11], v[76:79]\n\t"
    "v_mfma_f32_16x16x32_bf16 v[44:47], v[152:155], a[4:7], v[44:47]\n\t"
    "ds_read_b64_tr_b16 v[140:141], v197 offset:40960\n\t"
    "v_mfma_f32_16x16x32_bf16 v[44:47], v[156:159], a[36:39], v[44:47]\n\t"
    "ds_read_b64_tr_b16 v[142:143], v197 offset:45056\n\t"
    "v_mfma_f32_16x16x32_bf16 v[12:15], v[156:159], a[32:35], v[12:15]\n\t"
    "v_mfma_f32_16x16x32_bf16 v[12:15], v[152:155], a[0:3], v[12:15]\n\t"
    "s_waitcnt lgkmcnt(4)\n\t"
    "v_mfma_f32_16x16x32_bf16 v[16:19], v[128:131], a[0:3], v[16:19]\n\t"
    "ds_read_b64_tr_b16 v[144:145], v198 offset:32768\n\t"
    "v_mfma_f32_16x16x32_bf16 v[16:19], v[132:135], a[32:35], v[16:19]\n\t"
    "ds_read_b64_tr_b16 v[146:147], v198 offset:36864\n\t"
    "v_mfma_f32_16x16x32_bf16 v[48:51], v[132:135], a[36:39], v[48:51]\n\t"
    "buffer_load_dwordx4 a[148:151], v246, s[88:91], s46 offen sc1\n\t"
    "v_mfma_f32_16x16x32_bf16 v[48:51], v[128:131], a[4:7], v[48:51]\n\t"
    "v_mfma_f32_16x16x32_bf16 v[80:83], v[128:131], a[8:11], v[80:83]\n\t"
    "ds_read_b64_tr_b16 v[148:149], v198 offset:40960\n\t"
    "v_mfma_f32_16x16x32_bf16 v[80:83], v[132:135], a[40:43], v[80:83]\n\t"
    "ds_read_b64_tr_b16 v[150:151], v198 offset:45056\n\t"
    "v_mfma_f32_16x16x32_bf16 v[112:115], v[132:135], a[44:47], v[112:115]\n\t"
    "buffer_load_dwordx4 a[164:167], v246, s[88:91], s47 offen sc1\n\t"
    "v_mfma_f32_16x16x32_bf16 v[112:115], v[128:131], a[12:15], v[112:115]\n\t"
    "s_waitcnt lgkmcnt(4)\n\t"
    "v_mfma_f32_16x16x32_bf16 v[116:119], v[136:139], a[12:15], v[116:119]\n\t"
    "ds_read_b64_tr_b16 v[152:153], v199 offset:32768\n\t"
    "v_mfma_f32_16x16x32_bf16 v[116:119], v[140:143], a[44:47], v[116:119]\n\t"
    "ds_read_b64_tr_b16 v[154:155], v199 offset:36864\n\t"
    "v_mfma_f32_16x16x32_bf16 v[84:87], v[140:143], a[40:43], v[84:87]\n\t"
    "buffer_load_dwordx4 a[152:155], v247, s[88:91], s46 offen sc1\n\t"
    "v_mfma_f32_16x16x32_bf16 v[84:87], v[136:139], a[8:11], v[84:87]\n\t"
    "v_mfma_f32_16x16x32_bf16 v[52:55], v[136:139], a[4:7], v[52:55]\n\t"
    "ds_read_b64_tr_b16 v[156:157], v199 offset:40960\n\t"
    "v_mfma_f32_16x16x32_bf16 v[52:55], v[140:143], a[36:39], v[52:55]\n\t"
    "ds_read_b64_tr_b16 v[158:159], v199 offset:45056\n\t"
    "v_mfma_f32_16x16x32_bf16 v[20:23], v[140:143], a[32:35], v[20:23]\n\t"
    "v_mfma_f32_16x16x32_bf16 v[20:23], v[136:139], a[0:3], v[20:23]\n\t"
    "s_waitcnt vmcnt(19) lgkmcnt(4)\n\t"
    "s_barrier\n\t"
    "v_mfma_f32_16x16x32_bf16 v[24:27], v[144:147], a[0:3], v[24:27]\n\t"
    "ds_read_b64_tr_b16 v[128:129], v192 offset:49152\n\t"
    "v_mfma_f32_16x16x32_bf16 v[24:27], v[148:151], a[32:35], v[24:27]\n\t"
    "ds_read_b64_tr_b16 v[130:131], v192 offset:53248\n\t"
    "v_mfma_f32_16x16x32_bf16 v[56:59], v[148:151], a[36:39], v[56:59]\n\t"
    "buffer_load_dwordx4 a[168:171], v247, s[88:91], s47 offen sc1\n\t"
    "v_mfma_f32_16x16x32_bf16 v[56:59], v[144:147], a[4:7], v[56:59]\n\t"
    "buffer_store_dwordx4 v[208:211], v248, s[88:91], s57 offen offset:2048 sc1\n\t"
    "v_mfma_f32_16x16x32_bf16 v[88:91], v[144:147], a[8:11], v[88:91]\n\t"
    "ds_read_b64_tr_b16 v[132:133], v192 offset:57344\n\t"
    "v_accvgpr_read_b32 v216, a28\n\t"
    "v_mfma_f32_16x16x32_bf16 v[88:91], v[148:151], a[40:43], v[88:91]\n\t"
    "ds_read_b64_tr_b16 v[134:135], v192 offset:61440\n\t"
    "v_accvgpr_read_b32 v217, a29\n\t"
    "v_mfma_f32_16x16x32_bf16 v[120:123], v[148:151], a[44:47], v[120:123]\n\t"
    "buffer_load_dwordx4 a[156:159], v249, s[88:91], s46 offen sc1\n\t"
    "v_mfma_f32_16x16x32_bf16 v[120:123], v[144:147], a[12:15], v[120:123]\n\t"
    "s_waitcnt lgkmcnt(4)\n\t"
    "v_accvgpr_read_b32 v218, a30\n\t"
    "v_accvgpr_read_b32 v219, a31\n\t"
    "v_mfma_f32_16x16x32_bf16 v[124:127], v[152:155], a[12:15], v[124:127]\n\t"
    "ds_read_b64_tr_b16 v[136:137], v193 offset:49152\n\t"
    "v_accvgpr_read_b32 v220, a60\n\t"
    "v_mfma_f32_16x16x32_bf16 v[124:127], v[156:159], a[44:47], v[124:127]\n\t"
    "ds_read_b64_tr_b16 v[138:139], v193 offset:53248\n\t"
    "v_accvgpr_read_b32 v221, a61\n\t"
    "v_mfma_f32_16x16x32_bf16 v[92:95], v[156:159], a[40:43], v[92:95]\n\t"
    "buffer_load_dwordx4 a[172:175], v249, s[88:91], s47 offen sc1\n\t"
    "v_mfma_f32_16x16x32_bf16 v[92:95], v[152:155], a[8:11], v[92:95]\n\t"
    "s_bfe_u32 s84, s16, 262172\n\t"
    "s_lshl_b32 s84, s84, 17\n\t"
    "s_bfe_u32 s46, s16, 262172\n\t"
    "v_mfma_f32_16x16x32_bf16 v[60:63], v[152:155], a[4:7], v[60:63]\n\t"
    "ds_read_b64_tr_b16 v[140:141], v193 offset:57344\n\t"
    "s_lshl_b32 s46, s46, 13\n\t"
    "v_mfma_f32_16x16x32_bf16 v[60:63], v[156:159], a[36:39], v[60:63]\n\t"
    "ds_read_b64_tr_b16 v[142:143], v193 offset:61440\n\t"
    "s_add_u32 s46, s46, s45\n\t"
    "v_mfma_f32_16x16x32_bf16 v[28:31], v[156:159], a[32:35], v[28:31]\n\t"
    "s_add_u32 s47, s46, 4096\n\t"
    "v_accvgpr_read_b32 v222, a62\n\t"
    "v_accvgpr_read_b32 v223, a63\n\t"
    "v_mfma_f32_16x16x32_bf16 v[28:31], v[152:155], a[0:3], v[28:31]\n\t"
    "s_waitcnt vmcnt(14) lgkmcnt(4)\n\t"
    "v_cvt_pk_bf16_f32 v216, v216, v217\n\t"
    "v_cvt_pk_bf16_f32 v217, v218, v219\n\t"
    "v_mfma_f32_16x16x32_bf16 v[0:3], v[128:131], a[64:67], v[0:3]\n\t"
    "ds_read_b64_tr_b16 v[144:145], v194 offset:49152\n\t"
    "v_cvt_pk_bf16_f32 v218, v220, v221\n\t"
    "v_mfma_f32_16x16x32_bf16 v[0:3], v[132:135], a[96:99], v[0:3]\n\t"
    "ds_read_b64_tr_b16 v[146:147], v194 offset:53248\n\t"
    "s_add_u32 m0, s48, 65536\n\t"
    "v_mfma_f32_16x16x32_bf16 v[32:35], v[132:135], a[100:103], v[32:35]\n\t"
    "buffer_load_dwordx4 v242, s[32:35], s84 offen sc1 lds\n\t"
    "v_mfma_f32_16x16x32_bf16 v[32:35], v[128:131], a[68:71], v[32:35]\n\t"
    "v_cvt_pk_bf16_f32 v219, v222, v223\n\t"
    "v_mfma_f32_16x16x32_bf16 v[64:67], v[128:131], a[72:75], v[64:67]\n\t"
    "ds_read_b64_tr_b16 v[148:149], v194 offset:57344\n\t"
    "v_mfma_f32_16x16x32_bf16 v[64:67], v[132:135], a[104:107], v[64:67]\n\t"
    "ds_read_b64_tr_b16 v[150:151], v194 offset:61440\n\t"
    "v_mfma_f32_16x16x32_bf16 v[96:99], v[132:135], a[108:111], v[96:99]\n\t"
    "buffer_load_dwordx4 v243, s[32:35], s84 offen offset:1024 sc1 lds\n\t"
    "v_mfma_f32_16x16x32_bf16 v[96:99], v[128:131], a[76:79], v[96:99]\n\t"
    "s_waitcnt lgkmcnt(4)\n\t"
    "v_mfma_f32_16x16x32_bf16 v[100:103], v[136:139], a[76:79], v[100:103]\n\t"
    "ds_read_b64_tr_b16 v[152:153], v195 offset:49152\n\t"
    "v_mfma_f32_16x16x32_bf16 v[100:103], v[140:143], a[108:111], v[100:103]\n\t"
    "ds_read_b64_tr_b16 v[154:155], v195 offset:53248\n\t"
    "v_mfma_f32_16x16x32_bf16 v[68:71], v[140:143], a[104:107], v[68:71]\n\t"
    "buffer_load_dwordx4 v244, s[32:35], s84 offen offset:2048 sc1 lds\n\t"
    "v_mfma_f32_16x16x32_bf16 v[68:71], v[136:139], a[72:75], v[68:71]\n\t"
    "v_mfma_f32_16x16x32_bf16 v[36:39], v[136:139], a[68:71], v[36:39]\n\t"
    "ds_read_b64_tr_b16 v[156:157], v195 offset:57344\n\t"
    "v_mfma_f32_16x16x32_bf16 v[36:39], v[140:143], a[100:103], v[36:39]\n\t"
    "ds_read_b64_tr_b16 v[158:159], v195 offset:61440\n\t"
    "v_mfma_f32_16x16x32_bf16 v[4:7], v[140:143], a[96:99], v[4:7]\n\t"
    "v_mfma_f32_16x16x32_bf16 v[4:7], v[136:139], a[64:67], v[4:7]\n\t"
    "s_waitcnt lgkmcnt(4)\n\t"
    "v_mfma_f32_16x16x32_bf16 v[8:11], v[144:147], a[64:67], v[8:11]\n\t"
    "ds_read_b64_tr_b16 v[128:129], v196 offset:49152\n\t"
    "v_mfma_f32_16x16x32_bf16 v[8:11], v[148:151], a[96:99], v[8:11]\n\t"
    "ds_read_b64_tr_b16 v[130:131], v196 offset:53248\n\t"
    "v_mfma_f32_16x16x32_bf16 v[40:43], v[148:151], a[100:103], v[40:43]\n\t"
    "buffer_load_dwordx4 v245, s[32:35], s84 offen offset:3072 sc1 lds\n\t"
    "v_mfma_f32_16x16x32_bf16 v[40:43], v[144:147], a[68:71], v[40:43]\n\t"
    "v_mfma_f32_16x16x32_bf16 v[72:75], v[144:147], a[72:75], v[72:75]\n\t"
    "ds_read_b64_tr_b16 v[132:133], v196 offset:57344\n\t"
    "v_mfma_f32_16x16x32_bf16 v[72:75], v[148:151], a[104:107], v[72:75]\n\t"
    "ds_read_b64_tr_b16 v[134:135], v196 offset:61440\n\t"
    "v_mfma_f32_16x16x32_bf16 v[104:107], v[148:151], a[108:111], v[104:107]\n\t"
    "buffer_load_dwordx4 a[176:179], v241, s[88:91], s46 offen sc1\n\t"
    "v_mfma_f32_16x16x32_bf16 v[104:107], v[144:147], a[76:79], v[104:107]\n\t"
    "s_waitcnt lgkmcnt(4)\n\t"
    "v_mfma_f32_16x16x32_bf16 v[108:111], v[152:155], a[76:79], v[108:111]\n\t"
    "ds_read_b64_tr_b16 v[136:137], v197 offset:49152\n\t"
    "v_mfma_f32_16x16x32_bf16 v[108:111], v[156:159], a[108:111], v[108:111]\n\t"
    "ds_read_b64_tr_b16 v[138:139], v197 offset:53248\n\t"
    "v_mfma_f32_16x16x32_bf16 v[76:79], v[156:159], a[104:107], v[76:79]\n\t"
    "buffer_load_dwordx4 a[192:195], v241, s[88:91], s47 offen sc1\n\t"
    "v_mfma_f32_16x16x32_bf16 v[76:79], v[152:155], a[72:75], v[76:79]\n\t"
    "v_mfma_f32_16x16x32_bf16 v[44:47], v[152:155], a[68:71], v[44:47]\n\t"
    "ds_read_b64_tr_b16 v[140:141], v197 offset:57344\n\t"
    "v_mfma_f32_16x16x32_bf16 v[44:47], v[156:159], a[100:103], v[44:47]\n\t"
    "ds_read_b64_tr_b16 v[142:143], v197 offset:61440\n\t"
    "v_mfma_f32_16x16x32_bf16 v[12:15], v[156:159], a[96:99], v[12:15]\n\t"
    "v_mfma_f32_16x16x32_bf16 v[12:15], v[152:155], a[64:67], v[12:15]\n\t"
    "s_waitcnt lgkmcnt(4)\n\t"
    "v_mfma_f32_16x16x32_bf16 v[16:19], v[128:131], a[64:67], v[16:19]\n\t"
    "ds_read_b64_tr_b16 v[144:145], v198 offset:49152\n\t"
    "v_mfma_f32_16x16x32_bf16 v[16:19], v[132:135], a[96:99], v[16:19]\n\t"
    "ds_read_b64_tr_b16 v[146:147], v198 offset:53248\n\t"
    "v_mfma_f32_16x16x32_bf16 v[48:51], v[132:135], a[100:103], v[48:51]\n\t"
    "buffer_load_dwordx4 a[180:183], v246, s[88:91], s46 offen sc1\n\t"
    "v_mfma_f32_16x16x32_bf16 v[48:51], v[128:131], a[68:71], v[48:51]\n\t"
    "buffer_store_dwordx4 v[216:219], v248, s[88:91], s58 offen offset:2048 sc1\n\t"
    "v_mfma_f32_16x16x32_bf16 v[80:83], v[128:131], a[72:75], v[80:83]\n\t"
    "ds_read_b64_tr_b16 v[148:149], v198 offset:57344\n\t"
    "v_accvgpr_read_b32 v200, a84\n\t"
    "v_mfma_f32_16x16x32_bf16 v[80:83], v[132:135], a[104:107], v[80:83]\n\t"
    "ds_read_b64_tr_b16 v[150:151], v198 offset:61440\n\t"
    "v_accvgpr_read_b32 v201, a85\n\t"
    "v_mfma_f32_16x16x32_bf16 v[112:115], v[132:135], a[108:111], v[112:115]\n\t"
    "buffer_load_dwordx4 a[196:199], v246, s[88:91], s47 offen sc1\n\t"
    "v_mfma_f32_16x16x32_bf16 v[112:115], v[128:131], a[76:79], v[112:115]\n\t"
    "s_waitcnt lgkmcnt(4)\n\t"
    "v_accvgpr_read_b32 v202, a86\n\t"
    "v_accvgpr_read_b32 v203, a87\n\t"
    "v_mfma_f32_16x16x32_bf16 v[116:119], v[136:139], a[76:79], v[116:119]\n\t"
    "ds_read_b64_tr_b16 v[152:153], v199 offset:49152\n\t"
    "v_accvgpr_read_b32 v204, a116\n\t"
    "v_mfma_f32_16x16x32_bf16 v[116:119], v[140:143], a[108:111], v[116:119]\n\t"
    "ds_read_b64_tr_b16 v[154:155], v199 offset:53248\n\t"
    "v_accvgpr_read_b32 v205, a117\n\t"
    "v_mfma_f32_16x16x32_bf16 v[84:87], v[140:143], a[104:107], v[84:87]\n\t"
    "buffer_load_dwordx4 a[184:187], v247, s[88:91], s46 offen sc1\n\t"
    "v_mfma_f32_16x16x32_bf16 v[84:87], v[136:139], a[72:75], v[84:87]\n\t"
    "v_accvgpr_read_b32 v206, a118\n\t"
    "v_accvgpr_read_b32 v207, a119\n\t"
    "v_mfma_f32_16x16x32_bf16 v[52:55], v[136:139], a[68:71], v[52:55]\n\t"
    "ds_read_b64_tr_b16 v[156:157], v199 offset:57344\n\t"
    "v_cvt_pk_bf16_f32 v200, v200, v201\n\t"
    "v_mfma_f32_16x16x32_bf16 v[52:55], v[140:143], a[100:103], v[52:55]\n\t"
    "ds_read_b64_tr_b16 v[158:159], v199 offset:61440\n\t"
    "v_cvt_pk_bf16_f32 v201, v202, v203\n\t"
    "v_mfma_f32_16x16x32_bf16 v[20:23], v[140:143], a[96:99], v[20:23]\n\t"
    "v_cvt_pk_bf16_f32 v202, v204, v205\n\t"
    "v_cvt_pk_bf16_f32 v203, v206, v207\n\t"
    "v_mfma_f32_16x16x32_bf16 v[20:23], v[136:139], a[64:67], v[20:23]\n\t"
    "s_waitcnt vmcnt(19) lgkmcnt(4)\n\t"
    "s_barrier\n\t"
    "v_mfma_f32_16x16x32_bf16 v[24:27], v[144:147], a[64:67], v[24:27]\n\t"
    "ds_read_b64_tr_b16 v[128:129], v192 offset:0\n\t"
    "v_mfma_f32_16x16x32_bf16 v[24:27], v[148:151], a[96:99], v[24:27]\n\t"
    "ds_read_b64_tr_b16 v[130:131], v192 offset:4096\n\t"
    "v_mfma_f32_16x16x32_bf16 v[56:59], v[148:151], a[100:103], v[56:59]\n\t"
    "buffer_load_dwordx4 a[200:203], v247, s[88:91], s47 offen sc1\n\t"
    "v_mfma_f32_16x16x32_bf16 v[56:59], v[144:147], a[68:71], v[56:59]\n\t"
    "v_mfma_f32_16x16x32_bf16 v[88:91], v[144:147], a[72:75], v[88:91]\n\t"
    "ds_read_b64_tr_b16 v[132:133], v192 offset:8192\n\t"
    "v_mfma_f32_16x16x32_bf16 v[88:91], v[148:151], a[104:107], v[88:91]\n\t"
    "ds_read_b64_tr_b16 v[134:135], v192 offset:12288\n\t"
    "v_mfma_f32_16x16x32_bf16 v[120:123], v[148:151], a[108:111], v[120:123]\n\t"
    "buffer_load_dwordx4 a[188:191], v249, s[88:91], s46 offen sc1\n\t"
    "v_mfma_f32_16x16x32_bf16 v[120:123], v[144:147], a[76:79], v[120:123]\n\t"
    "s_waitcnt lgkmcnt(4)\n\t"
    "v_mfma_f32_16x16x32_bf16 v[124:127], v[152:155], a[76:79], v[124:127]\n\t"
    "ds_read_b64_tr_b16 v[136:137], v193 offset:0\n\t"
    "v_mfma_f32_16x16x32_bf16 v[124:127], v[156:159], a[108:111], v[124:127]\n\t"
    "ds_read_b64_tr_b16 v[138:139], v193 offset:4096\n\t"
    "v_mfma_f32_16x16x32_bf16 v[92:95], v[156:159], a[104:107], v[92:95]\n\t"
    "buffer_load_dwordx4 a[204:207], v249, s[88:91], s47 offen sc1\n\t"
    "v_mfma_f32_16x16x32_bf16 v[92:95], v[152:155], a[72:75], v[92:95]\n\t"
    "s_bfe_u32 s84, s17, 262144\n\t"
    "s_lshl_b32 s84, s84, 17\n\t"
    "s_bfe_u32 s46, s17, 262144\n\t"
    "v_mfma_f32_16x16x32_bf16 v[60:63], v[152:155], a[68:71], v[60:63]\n\t"
    "ds_read_b64_tr_b16 v[140:141], v193 offset:8192\n\t"
    "s_lshl_b32 s46, s46, 13\n\t"
    "v_mfma_f32_16x16x32_bf16 v[60:63], v[156:159], a[100:103], v[60:63]\n\t"
    "ds_read_b64_tr_b16 v[142:143], v193 offset:12288\n\t"
    "s_add_u32 s46, s46, s45\n\t"
    "v_mfma_f32_16x16x32_bf16 v[28:31], v[156:159], a[96:99], v[28:31]\n\t"
    "s_add_u32 s47, s46, 4096\n\t"
    "v_mfma_f32_16x16x32_bf16 v[28:31], v[152:155], a[64:67], v[28:31]\n\t"
    "s_waitcnt vmcnt(13) lgkmcnt(4)\n\t"
    "v_mfma_f32_16x16x32_bf16 v[0:3], v[128:131], a[144:147], v[0:3]\n\t"
    "ds_read_b64_tr_b16 v[144:145], v194 offset:0\n\t"
    "v_mfma_f32_16x16x32_bf16 v[0:3], v[132:135], a[160:163], v[0:3]\n\t"
    "ds_read_b64_tr_b16 v[146:147], v194 offset:4096\n\t"
    "s_add_u32 m0, s48, 81920\n\t"
    "v_mfma_f32_16x16x32_bf16 v[32:35], v[132:135], a[164:167], v[32:35]\n\t"
    "buffer_load_dwordx4 v242, s[32:35], s84 offen sc1 lds\n\t"
    "v_mfma_f32_16x16x32_bf16 v[32:35], v[128:131], a[148:151], v[32:35]\n\t"
    "v_mfma_f32_16x16x32_bf16 v[64:67], v[128:131], a[152:155], v[64:67]\n\t"
    "ds_read_b64_tr_b16 v[148:149], v194 offset:8192\n\t"
    "v_mfma_f32_16x16x32_bf16 v[64:67], v[132:135], a[168:171], v[64:67]\n\t"
    "ds_read_b64_tr_b16 v[150:151], v194 offset:12288\n\t"
    "v_mfma_f32_16x16x32_bf16 v[96:99], v[132:135], a[172:175], v[96:99]\n\t"
    "buffer_load_dwordx4 v243, s[32:35], s84 offen offset:1024 sc1 lds\n\t"
    "v_mfma_f32_16x16x32_bf16 v[96:99], v[128:131], a[156:159], v[96:99]\n\t"
    "s_waitcnt lgkmcnt(4)\n\t"
    "v_mfma_f32_16x16x32_bf16 v[100:103], v[136:139], a[156:159], v[100:103]\n\t"
    "ds_read_b64_tr_b16 v[152:153], v195 offset:0\n\t"
    "v_mfma_f32_16x16x32_bf16 v[100:103], v[140:143], a[172:175], v[100:103]\n\t"
    "ds_read_b64_tr_b16 v[154:155], v195 offset:4096\n\t"
    "v_mfma_f32_16x16x32_bf16 v[68:71], v[140:143], a[168:171], v[68:71]\n\t"
    "buffer_load_dwordx4 v244, s[32:35], s84 offen offset:2048 sc1 lds\n\t"
    "v_mfma_f32_16x16x32_bf16 v[68:71], v[136:139], a[152:155], v[68:71]\n\t"
    "v_mfma_f32_16x16x32_bf16 v[36:39], v[136:139], a[148:151], v[36:39]\n\t"
    "ds_read_b64_tr_b16 v[156:157], v195 offset:8192\n\t"
    "v_mfma_f32_16x16x32_bf16 v[36:39], v[140:143], a[164:167], v[36:39]\n\t"
    "ds_read_b64_tr_b16 v[158:159], v195 offset:12288\n\t"
    "v_mfma_f32_16x16x32_bf16 v[4:7], v[140:143], a[160:163], v[4:7]\n\t"
    "v_mfma_f32_16x16x32_bf16 v[4:7], v[136:139], a[144:147], v[4:7]\n\t"
    "s_waitcnt lgkmcnt(4)\n\t"
    "v_mfma_f32_16x16x32_bf16 v[8:11], v[144:147], a[144:147], v[8:11]\n\t"
    "ds_read_b64_tr_b16 v[128:129], v196 offset:0\n\t"
    "v_mfma_f32_16x16x32_bf16 v[8:11], v[148:151], a[160:163], v[8:11]\n\t"
    "ds_read_b64_tr_b16 v[130:131], v196 offset:4096\n\t"
    "v_mfma_f32_16x16x32_bf16 v[40:43], v[148:151], a[164:167], v[40:43]\n\t"
    "buffer_load_dwordx4 v245, s[32:35], s84 offen offset:3072 sc1 lds\n\t"
    "v_mfma_f32_16x16x32_bf16 v[40:43], v[144:147], a[148:151], v[40:43]\n\t"
    "buffer_store_dwordx4 v[200:203], v248, s[88:91], s56 offen offset:3072 sc1\n\t"
    "v_mfma_f32_16x16x32_bf16 v[72:75], v[144:147], a[152:155], v[72:75]\n\t"
    "ds_read_b64_tr_b16 v[132:133], v196 offset:8192\n\t"
    "v_accvgpr_read_b32 v208, a88\n\t"
    "v_mfma_f32_16x16x32_bf16 v[72:75], v[148:151], a[168:171], v[72:75]\n\t"
    "ds_read_b64_tr_b16 v[134:135], v196 offset:12288\n\t"
    "v_accvgpr_read_b32 v209, a89\n\t"
    "v_mfma_f32_16x16x32_bf16 v[104:107], v[148:151], a[172:175], v[104:107]\n\t"
    "buffer_load_dwordx4 a[0:3], v241, s[88:91], s46 offen sc1\n\t"
    "v_mfma_f32_16x16x32_bf16 v[104:107], v[144:147], a[156:159], v[104:107]\n\t"
    "s_waitcnt lgkmcnt(4)\n\t"
    "v_accvgpr_read_b32 v210, a90\n\t"
    "v_accvgpr_read_b32 v211, a91\n\t"
    "v_mfma_f32_16x16x32_bf16 v[108:111], v[152:155], a[156:159], v[108:111]\n\t"
    "ds_read_b64_tr_b16 v[136:137], v197 offset:0\n\t"
    "v_accvgpr_read_b32 v212, a120\n\t"
    "v_mfma_f32_16x16x32_bf16 v[108:111], v[156:159], a[172:175], v[108:111]\n\t"
    "ds_read_b64_tr_b16 v[138:139], v197 offset:4096\n\t"
    "v_accvgpr_read_b32 v213, a121\n\t"
    "v_mfma_f32_16x16x32_bf16 v[76:79], v[156:159], a[168:171], v[76:79]\n\t"
    "buffer_load_dwordx4 a[32:35], v241, s[88:91], s47 offen sc1\n\t"
    "v_mfma_f32_16x16x32_bf16 v[76:79], v[152:155], a[152:155], v[76:79]\n\t"
    "v_accvgpr_read_b32 v214, a122\n\t"
    "v_accvgpr_read_b32 v215, a123\n\t"
    "v_mfma_f32_16x16x32_bf16 v[44:47], v[152:155], a[148:151], v[44:47]\n\t"
    "ds_read_b64_tr_b16 v[140:141], v197 offset:8192\n\t"
    "v_cvt_pk_bf16_f32 v208, v208, v209\n\t"
    "v_mfma_f32_16x16x32_bf16 v[44:47], v[156:159], a[164:167], v[44:47]\n\t"
    "ds_read_b64_tr_b16 v[142:143], v197 offset:12288\n\t"
    "v_cvt_pk_bf16_f32 v209, v210, v211\n\t"
    "v_mfma_f32_16x16x32_bf16 v[12:15], v[156:159], a[160:163], v[12:15]\n\t"
    "v_cvt_pk_bf16_f32 v210, v212, v213\n\t"
    "v_cvt_pk_bf16_f32 v211, v214, v215\n\t"
    "v_mfma_f32_16x16x32_bf16 v[12:15], v[152:155], a[144:147], v[12:15]\n\t"
    "s_waitcnt lgkmcnt(4)\n\t"
    "v_mfma_f32_16x16x32_bf16 v[16:19], v[128:131], a[144:147], v[16:19]\n\t"
    "ds_read_b64_tr_b16 v[144:145], v198 offset:0\n\t"
    "v_mfma_f32_16x16x32_bf16 v[16:19], v[132:135], a[160:163], v[16:19]\n\t"
    "ds_read_b64_tr_b16 v[146:147], v198 offset:4096\n\t"
    "v_mfma_f32_16x16x32_bf16 v[48:51], v[132:135], a[164:167], v[48:51]\n\t"
    "buffer_load_dwordx4 a[4:7], v246, s[88:91], s46 offen sc1\n\t"
    "v_mfma_f32_16x16x32_bf16 v[48:51], v[128:131], a[148:151], v[48:51]\n\t"
    "v_mfma_f32_16x16x32_bf16 v[80:83], v[128:131], a[152:155], v[80:83]\n\t"
    "ds_read_b64_tr_b16 v[148:149], v198 offset:8192\n\t"
    "v_mfma_f32_16x16x32_bf16 v[80:83], v[132:135], a[168:171], v[80:83]\n\t"
    "ds_read_b64_tr_b16 v[150:151], v198 offset:12288\n\t"
    "v_mfma_f32_16x16x32_bf16 v[112:115], v[132:135], a[172:175], v[112:115]\n\t"
    "buffer_load_dwordx4 a[36:39], v246, s[88:91], s47 offen sc1\n\t"
    "v_mfma_f32_16x16x32_bf16 v[112:115], v[128:131], a[156:159], v[112:115]\n\t"
    "s_waitcnt lgkmcnt(4)\n\t"
    "v_mfma_f32_16x16x32_bf16 v[116:119], v[136:139], a[156:159], v[116:119]\n\t"
    "ds_read_b64_tr_b16 v[152:153], v199 offset:0\n\t"
    "v_mfma_f32_16x16x32_bf16 v[116:119], v[140:143], a[172:175], v[116:119]\n\t"
    "ds_read_b64_tr_b16 v[154:155], v199 offset:4096\n\t"
    "v_mfma_f32_16x16x32_bf16 v[84:87], v[140:143], a[168:171], v[84:87]\n\t"
    "buffer_load_dwordx4 a[8:11], v247, s[88:91], s46 offen sc1\n\t"
    "v_mfma_f32_16x16x32_bf16 v[84:87], v[136:139], a[152:155], v[84:87]\n\t"
    "v_mfma_f32_16x16x32_bf16 v[52:55], v[136:139], a[148:151], v[52:55]\n\t"
    "ds_read_b64_tr_b16 v[156:157], v199 offset:8192\n\t"
    "v_mfma_f32_16x16x32_bf16 v[52:55], v[140:143], a[164:167], v[52:55]\n\t"
    "ds_read_b64_tr_b16 v[158:159], v199 offset:12288\n\t"
    "v_mfma_f32_16x16x32_bf16 v[20:23], v[140:143], a[160:163], v[20:23]\n\t"
    "v_mfma_f32_16x16x32_bf16 v[20:23], v[136:139], a[144:147], v[20:23]\n\t"
    "s_waitcnt vmcnt(19) lgkmcnt(4)\n\t"
    "s_barrier\n\t"
    "v_mfma_f32_16x16x32_bf16 v[24:27], v[144:147], a[144:147], v[24:27]\n\t"
    "ds_read_b64_tr_b16 v[128:129], v192 offset:16384\n\t"
    "v_mfma_f32_16x16x32_bf16 v[24:27], v[148:151], a[160:163], v[24:27]\n\t"
    "ds_read_b64_tr_b16 v[130:131], v192 offset:20480\n\t"
    "v_mfma_f32_16x16x32_bf16 v[56:59], v[148:151], a[164:167], v[56:59]\n\t"
    "buffer_load_dwordx4 a[40:43], v247, s[88:91], s47 offen sc1\n\t"
    "v_mfma_f32_16x16x32_bf16 v[56:59], v[144:147], a[148:151], v[56:59]\n\t"
    "v_mfma_f32_16x16x32_bf16 v[88:91], v[144:147], a[152:155], v[88:91]\n\t"
    "ds_read_b64_tr_b16 v[132:133], v192 offset:24576\n\t"
    "v_mfma_f32_16x16x32_bf16 v[88:91], v[148:151], a[168:171], v[88:91]\n\t"
    "ds_read_b64_tr_b16 v[134:135], v192 offset:28672\n\t"
    "v_mfma_f32_16x16x32_bf16 v[120:123], v[148:151], a[172:175], v[120:123]\n\t"
    "buffer_load_dwordx4 a[12:15], v249, s[88:91], s46 offen sc1\n\t"
    "v_mfma_f32_16x16x32_bf16 v[120:123], v[144:147], a[156:159], v[120:123]\n\t"
    "s_waitcnt lgkmcnt(4)\n\t"
    "v_mfma_f32_16x16x32_bf16 v[124:127], v[152:155], a[156:159], v[124:127]\n\t"
    "ds_read_b64_tr_b16 v[136:137], v193 offset:16384\n\t"
    "v_mfma_f32_16x16x32_bf16 v[124:127], v[156:159], a[172:175], v[124:127]\n\t"
    "ds_read_b64_tr_b16 v[138:139], v193 offset:20480\n\t"
    "v_mfma_f32_16x16x32_bf16 v[92:95], v[156:159], a[168:171], v[92:95]\n\t"
    "buffer_load_dwordx4 a[44:47], v249, s[88:91], s47 offen sc1\n\t"
    "v_mfma_f32_16x16x32_bf16 v[92:95], v[152:155], a[152:155], v[92:95]\n\t"
    "s_bfe_u32 s84, s17, 262148\n\t"
    "s_lshl_b32 s84, s84, 17\n\t"
    "s_bfe_u32 s46, s17, 262148\n\t"
    "v_mfma_f32_16x16x32_bf16 v[60:63], v[152:155], a[148:151], v[60:63]\n\t"
    "ds_read_b64_tr_b16 v[140:141], v193 offset:24576\n\t"
    "s_lshl_b32 s46, s46, 13\n\t"
    "v_mfma_f32_16x16x32_bf16 v[60:63], v[156:159], a[164:167], v[60:63]\n\t"
    "ds_read_b64_tr_b16 v[142:143], v193 offset:28672\n\t"
    "s_add_u32 s46, s46, s45\n\t"
    "v_mfma_f32_16x16x32_bf16 v[28:31], v[156:159], a[160:163], v[28:31]\n\t"
    "s_add_u32 s47, s46, 4096\n\t"
    "v_mfma_f32_16x16x32_bf16 v[28:31], v[152:155], a[144:147], v[28:31]\n\t"
    "s_waitcnt vmcnt(13) lgkmcnt(4)\n\t"
    "v_mfma_f32_16x16x32_bf16 v[0:3], v[128:131], a[176:179], v[0:3]\n\t"
    "ds_read_b64_tr_b16 v[144:145], v194 offset:16384\n\t"
    "v_mfma_f32_16x16x32_bf16 v[0:3], v[132:135], a[192:195], v[0:3]\n\t"
    "ds_read_b64_tr_b16 v[146:147], v194 offset:20480\n\t"
    "s_add_u32 m0, s48, 98304\n\t"
    "v_mfma_f32_16x16x32_bf16 v[32:35], v[132:135], a[196:199], v[32:35]\n\t"
    "buffer_load_dwordx4 v242, s[32:35], s84 offen sc1 lds\n\t"
    "v_mfma_f32_16x16x32_bf16 v[32:35], v[128:131], a[180:183], v[32:35]\n\t"
    "buffer_store_dwordx4 v[208:211], v248, s[88:91], s57 offen offset:3072 sc1\n\t"
    "v_mfma_f32_16x16x32_bf16 v[64:67], v[128:131], a[184:187], v[64:67]\n\t"
    "ds_read_b64_tr_b16 v[148:149], v194 offset:24576\n\t"
    "v_accvgpr_read_b32 v216, a92\n\t"
    "v_mfma_f32_16x16x32_bf16 v[64:67], v[132:135], a[200:203], v[64:67]\n\t"
    "ds_read_b64_tr_b16 v[150:151], v194 offset:28672\n\t"
    "v_accvgpr_read_b32 v217, a93\n\t"
    "v_mfma_f32_16x16x32_bf16 v[96:99], v[132:135], a[204:207], v[96:99]\n\t"
    "buffer_load_dwordx4 v243, s[32:35], s84 offen offset:1024 sc1 lds\n\t"
    "v_mfma_f32_16x16x32_bf16 v[96:99], v[128:131], a[188:191], v[96:99]\n\t"
    "s_waitcnt lgkmcnt(4)\n\t"
    "v_accvgpr_read_b32 v218, a94\n\t"
    "v_accvgpr_read_b32 v219, a95\n\t"
    "v_mfma_f32_16x16x32_bf16 v[100:103], v[136:139], a[188:191], v[100:103]\n\t"
    "ds_read_b64_tr_b16 v[152:153], v195 offset:16384\n\t"
    "v_accvgpr_read_b32 v220, a124\n\t"
    "v_mfma_f32_16x16x32_bf16 v[100:103], v[140:143], a[204:207], v[100:103]\n\t"
    "ds_read_b64_tr_b16 v[154:155], v195 offset:20480\n\t"
    "v_accvgpr_read_b32 v221, a125\n\t"
    "v_mfma_f32_16x16x32_bf16 v[68:71], v[140:143], a[200:203], v[68:71]\n\t"
    "buffer_load_dwordx4 v244, s[32:35], s84 offen offset:2048 sc1 lds\n\t"
    "v_mfma_f32_16x16x32_bf16 v[68:71], v[136:139], a[184:187], v[68:71]\n\t"
    "v_accvgpr_read_b32 v222, a126\n\t"
    "v_accvgpr_read_b32 v223, a127\n\t"
    "v_mfma_f32_16x16x32_bf16 v[36:39], v[136:139], a[180:183], v[36:39]\n\t"
    "ds_read_b64_tr_b16 v[156:157], v195 offset:24576\n\t"
    "v_cvt_pk_bf16_f32 v216, v216, v217\n\t"
    "v_mfma_f32_16x16x32_bf16 v[36:39], v[140:143], a[196:199], v[36:39]\n\t"
    "ds_read_b64_tr_b16 v[158:159], v195 offset:28672\n\t"
    "v_cvt_pk_bf16_f32 v217, v218, v219\n\t"
    "v_mfma_f32_16x16x32_bf16 v[4:7], v[140:143], a[192:195], v[4:7]\n\t"
    "v_cvt_pk_bf16_f32 v218, v220, v221\n\t"
    "v_cvt_pk_bf16_f32 v219, v222, v223\n\t"
    "v_mfma_f32_16x16x32_bf16 v[4:7], v[136:139], a[176:179], v[4:7]\n\t"
    "s_waitcnt lgkmcnt(4)\n\t"
    "v_mfma_f32_16x16x32_bf16 v[8:11], v[144:147], a[176:179], v[8:11]\n\t"
    "ds_read_b64_tr_b16 v[128:129], v196 offset:16384\n\t"
    "v_mfma_f32_16x16x32_bf16 v[8:11], v[148:151], a[192:195], v[8:11]\n\t"
    "ds_read_b64_tr_b16 v[130:131], v196 offset:20480\n\t"
    "v_mfma_f32_16x16x32_bf16 v[40:43], v[148:151], a[196:199], v[40:43]\n\t"
    "buffer_load_dwordx4 v245, s[32:35], s84 offen offset:3072 sc1 lds\n\t"
    "v_mfma_f32_16x16x32_bf16 v[40:43], v[144:147], a[180:183], v[40:43]\n\t"
    "v_mfma_f32_16x16x32_bf16 v[72:75], v[144:147], a[184:187], v[72:75]\n\t"
    "ds_read_b64_tr_b16 v[132:133], v196 offset:24576\n\t"
    "v_mfma_f32_16x16x32_bf16 v[72:75], v[148:151], a[200:203], v[72:75]\n\t"
    "ds_read_b64_tr_b16 v[134:135], v196 offset:28672\n\t"
    "v_mfma_f32_16x16x32_bf16 v[104:107], v[148:151], a[204:207], v[104:107]\n\t"
    "buffer_load_dwordx4 a[64:67], v241, s[88:91], s46 offen sc1\n\t"
    "v_mfma_f32_16x16x32_bf16 v[104:107], v[144:147], a[188:191], v[104:107]\n\t"
    "s_waitcnt lgkmcnt(4)\n\t"
    "v_mfma_f32_16x16x32_bf16 v[108:111], v[152:155], a[188:191], v[108:111]\n\t"
    "ds_read_b64_tr_b16 v[136:137], v197 offset:16384\n\t"
    "v_mfma_f32_16x16x32_bf16 v[108:111], v[156:159], a[204:207], v[108:111]\n\t"
    "ds_read_b64_tr_b16 v[138:139], v197 offset:20480\n\t"
    "v_mfma_f32_16x16x32_bf16 v[76:79], v[156:159], a[200:203], v[76:79]\n\t"
    "buffer_load_dwordx4 a[96:99], v241, s[88:91], s47 offen sc1\n\t"
    "v_mfma_f32_16x16x32_bf16 v[76:79], v[152:155], a[184:187], v[76:79]\n\t"
    "v_mfma_f32_16x16x32_bf16 v[44:47], v[152:155], a[180:183], v[44:47]\n\t"
    "ds_read_b64_tr_b16 v[140:141], v197 offset:24576\n\t"
    "v_mfma_f32_16x16x32_bf16 v[44:47], v[156:159], a[196:199], v[44:47]\n\t"
    "ds_read_b64_tr_b16 v[142:143], v197 offset:28672\n\t"
    "v_mfma_f32_16x16x32_bf16 v[12:15], v[156:159], a[192:195], v[12:15]\n\t"
    "v_mfma_f32_16x16x32_bf16 v[12:15], v[152:155], a[176:179], v[12:15]\n\t"
    "s_waitcnt lgkmcnt(4)\n\t"
    "v_mfma_f32_16x16x32_bf16 v[16:19], v[128:131], a[176:179], v[16:19]\n\t"
    "ds_read_b64_tr_b16 v[144:145], v198 offset:16384\n\t"
    "v_mfma_f32_16x16x32_bf16 v[16:19], v[132:135], a[192:195], v[16:19]\n\t"
    "ds_read_b64_tr_b16 v[146:147], v198 offset:20480\n\t"
    "v_mfma_f32_16x16x32_bf16 v[48:51], v[132:135], a[196:199], v[48:51]\n\t"
    "buffer_load_dwordx4 a[68:71], v246, s[88:91], s46 offen sc1\n\t"
    "v_mfma_f32_16x16x32_bf16 v[48:51], v[128:131], a[180:183], v[48:51]\n\t"
    "v_mfma_f32_16x16x32_bf16 v[80:83], v[128:131], a[184:187], v[80:83]\n\t"
    "ds_read_b64_tr_b16 v[148:149], v198 offset:24576\n\t"
    "v_mfma_f32_16x16x32_bf16 v[80:83], v[132:135], a[200:203], v[80:83]\n\t"
    "ds_read_b64_tr_b16 v[150:151], v198 offset:28672\n\t"
    "v_mfma_f32_16x16x32_bf16 v[112:115], v[132:135], a[204:207], v[112:115]\n\t"
    "buffer_load_dwordx4 a[100:103], v246, s[88:91], s47 offen sc1\n\t"
    "v_mfma_f32_16x16x32_bf16 v[112:115], v[128:131], a[188:191], v[112:115]\n\t"
    "s_waitcnt lgkmcnt(4)\n\t"
    "v_mfma_f32_16x16x32_bf16 v[116:119], v[136:139], a[188:191], v[116:119]\n\t"
    "ds_read_b64_tr_b16 v[152:153], v199 offset:16384\n\t"
    "v_mfma_f32_16x16x32_bf16 v[116:119], v[140:143], a[204:207], v[116:119]\n\t"
    "ds_read_b64_tr_b16 v[154:155], v199 offset:20480\n\t"
    "v_mfma_f32_16x16x32_bf16 v[84:87], v[140:143], a[200:203], v[84:87]\n\t"
    "buffer_load_dwordx4 a[72:75], v247, s[88:91], s46 offen sc1\n\t"
    "v_mfma_f32_16x16x32_bf16 v[84:87], v[136:139], a[184:187], v[84:87]\n\t"
    "v_mfma_f32_16x16x32_bf16 v[52:55], v[136:139], a[180:183], v[52:55]\n\t"
    "ds_read_b64_tr_b16 v[156:157], v199 offset:24576\n\t"
    "v_mfma_f32_16x16x32_bf16 v[52:55], v[140:143], a[196:199], v[52:55]\n\t"
    "ds_read_b64_tr_b16 v[158:159], v199 offset:28672\n\t"
    "v_mfma_f32_16x16x32_bf16 v[20:23], v[140:143], a[192:195], v[20:23]\n\t"
    "v_mfma_f32_16x16x32_bf16 v[20:23], v[136:139], a[176:179], v[20:23]\n\t"
    "s_waitcnt vmcnt(19) lgkmcnt(4)\n\t"
    "s_barrier\n\t"
    "v_mfma_f32_16x16x32_bf16 v[24:27], v[144:147], a[176:179], v[24:27]\n\t"
    "ds_read_b64_tr_b16 v[128:129], v192 offset:32768\n\t"
    "v_mfma_f32_16x16x32_bf16 v[24:27], v[148:151], a[192:195], v[24:27]\n\t"
    "ds_read_b64_tr_b16 v[130:131], v192 offset:36864\n\t"
    "v_mfma_f32_16x16x32_bf16 v[56:59], v[148:151], a[196:199], v[56:59]\n\t"
    "buffer_load_dwordx4 a[104:107], v247, s[88:91], s47 offen sc1\n\t"
    "v_mfma_f32_16x16x32_bf16 v[56:59], v[144:147], a[180:183], v[56:59]\n\t"
    "buffer_store_dwordx4 v[216:219], v248, s[88:91], s58 offen offset:3072 sc1\n\t"
    "v_mfma_f32_16x16x32_bf16 v[88:91], v[144:147], a[184:187], v[88:91]\n\t"
    "ds_read_b64_tr_b16 v[132:133], v192 offset:40960\n\t"
    "s_mov_b32 s68, 0xbfb8aa3b\n\t"
    "v_mfma_f32_16x16x32_bf16 v[88:91], v[148:151], a[200:203], v[88:91]\n\t"
    "ds_read_b64_tr_b16 v[134:135], v192 offset:45056\n\t"
    "v_mfma_f32_16x16x32_bf16 v[120:123], v[148:151], a[204:207], v[120:123]\n\t"
    "buffer_load_dwordx4 a[76:79], v249, s[88:91], s46 offen sc1\n\t"
    "v_mfma_f32_16x16x32_bf16 v[120:123], v[144:147], a[188:191], v[120:123]\n\t"
    "s_waitcnt lgkmcnt(4)\n\t"
    "v_mfma_f32_16x16x32_bf16 v[124:127], v[152:155], a[188:191], v[124:127]\n\t"
    "ds_read_b64_tr_b16 v[136:137], v193 offset:32768\n\t"
    "v_mfma_f32_16x16x32_bf16 v[124:127], v[156:159], a[204:207], v[124:127]\n\t"
    "ds_read_b64_tr_b16 v[138:139], v193 offset:36864\n\t"
    "v_mfma_f32_16x16x32_bf16 v[92:95], v[156:159], a[200:203], v[92:95]\n\t"
    "buffer_load_dwordx4 a[108:111], v249, s[88:91], s47 offen sc1\n\t"
    "v_mfma_f32_16x16x32_bf16 v[92:95], v[152:155], a[184:187], v[92:95]\n\t"
    "s_bfe_u32 s84, s17, 262152\n\t"
    "s_lshl_b32 s84, s84, 17\n\t"
    "s_bfe_u32 s46, s17, 262152\n\t"
    "v_mfma_f32_16x16x32_bf16 v[60:63], v[152:155], a[180:183], v[60:63]\n\t"
    "ds_read_b64_tr_b16 v[140:141], v193 offset:40960\n\t"
    "s_lshl_b32 s46, s46, 13\n\t"
    "v_mfma_f32_16x16x32_bf16 v[60:63], v[156:159], a[196:199], v[60:63]\n\t"
    "ds_read_b64_tr_b16 v[142:143], v193 offset:45056\n\t"
    "s_add_u32 s46, s46, s45\n\t"
    "v_mfma_f32_16x16x32_bf16 v[28:31], v[156:159], a[192:195], v[28:31]\n\t"
    "s_add_u32 s47, s46, 4096\n\t"
    "v_mfma_f32_16x16x32_bf16 v[28:31], v[152:155], a[176:179], v[28:31]\n\t"
    "s_waitcnt vmcnt(14) lgkmcnt(4)\n\t"
    "v_mfma_f32_16x16x32_bf16 v[0:3], v[128:131], a[0:3], v[0:3]\n\t"
    "ds_read_b64_tr_b16 v[144:145], v194 offset:32768\n\t"
    "v_mfma_f32_16x16x32_bf16 v[0:3], v[132:135], a[32:35], v[0:3]\n\t"
    "ds_read_b64_tr_b16 v[146:147], v194 offset:36864\n\t"
    "s_add_u32 m0, s48, 49152\n\t"
    "v_mfma_f32_16x16x32_bf16 v[32:35], v[132:135], a[36:39], v[32:35]\n\t"
    "buffer_load_dwordx4 v242, s[32:35], s84 offen sc1 lds\n\t"
    "v_mfma_f32_16x16x32_bf16 v[32:35], v[128:131], a[4:7], v[32:35]\n\t"
    "v_mfma_f32_16x16x32_bf16 v[64:67], v[128:131], a[8:11], v[64:67]\n\t"
    "ds_read_b64_tr_b16 v[148:149], v194 offset:40960\n\t"
    "v_mfma_f32_16x16x32_bf16 v[64:67], v[132:135], a[40:43], v[64:67]\n\t"
    "ds_read_b64_tr_b16 v[150:151], v194 offset:45056\n\t"
    "v_mfma_f32_16x16x32_bf16 v[96:99], v[132:135], a[44:47], v[96:99]\n\t"
    "buffer_load_dwordx4 v243, s[32:35], s84 offen offset:1024 sc1 lds\n\t"
    "v_mfma_f32_16x16x32_bf16 v[96:99], v[128:131], a[12:15], v[96:99]\n\t"
    "s_waitcnt lgkmcnt(4)\n\t"
    "v_mfma_f32_16x16x32_bf16 v[100:103], v[136:139], a[12:15], v[100:103]\n\t"
    "ds_read_b64_tr_b16 v[152:153], v195 offset:32768\n\t"
    "v_mfma_f32_16x16x32_bf16 v[100:103], v[140:143], a[44:47], v[100:103]\n\t"
    "ds_read_b64_tr_b16 v[154:155], v195 offset:36864\n\t"
    "v_mfma_f32_16x16x32_bf16 v[68:71], v[140:143], a[40:43], v[68:71]\n\t"
    "buffer_load_dwordx4 v244, s[32:35], s84 offen offset:2048 sc1 lds\n\t"
    "v_mfma_f32_16x16x32_bf16 v[68:71], v[136:139], a[8:11], v[68:71]\n\t"
    "v_mfma_f32_16x16x32_bf16 v[36:39], v[136:139], a[4:7], v[36:39]\n\t"
    "ds_read_b64_tr_b16 v[156:157], v195 offset:40960\n\t"
    "v_mfma_f32_16x16x32_bf16 v[36:39], v[140:143], a[36:39], v[36:39]\n\t"
    "ds_read_b64_tr_b16 v[158:159], v195 offset:45056\n\t"
    "v_mfma_f32_16x16x32_bf16 v[4:7], v[140:143], a[32:35], v[4:7]\n\t"
    "v_mfma_f32_16x16x32_bf16 v[4:7], v[136:139], a[0:3], v[4:7]\n\t"
    "s_waitcnt lgkmcnt(4)\n\t"
    "v_mfma_f32_16x16x32_bf16 v[8:11], v[144:147], a[0:3], v[8:11]\n\t"
    "ds_read_b64_tr_b16 v[128:129], v196 offset:32768\n\t"
    "v_mfma_f32_16x16x32_bf16 v[8:11], v[148:151], a[32:35], v[8:11]\n\t"
    "ds_read_b64_tr_b16 v[130:131], v196 offset:36864\n\t"
    "v_mfma_f32_16x16x32_bf16 v[40:43], v[148:151], a[36:39], v[40:43]\n\t"
    "buffer_load_dwordx4 v245, s[32:35], s84 offen offset:3072 sc1 lds\n\t"
    "v_mfma_f32_16x16x32_bf16 v[40:43], v[144:147], a[4:7], v[40:43]\n\t"
    "v_mfma_f32_16x16x32_bf16 v[72:75], v[144:147], a[8:11], v[72:75]\n\t"
    "ds_read_b64_tr_b16 v[132:133], v196 offset:40960\n\t"
    "v_mfma_f32_16x16x32_bf16 v[72:75], v[148:151], a[40:43], v[72:75]\n\t"
    "ds_read_b64_tr_b16 v[134:135], v196 offset:45056\n\t"
    "v_mfma_f32_16x16x32_bf16 v[104:107], v[148:151], a[44:47], v[104:107]\n\t"
    "buffer_load_dwordx4 a[144:147], v241, s[88:91], s46 offen sc1\n\t"
    "v_mfma_f32_16x16x32_bf16 v[104:107], v[144:147], a[12:15], v[104:107]\n\t"
    "s_waitcnt lgkmcnt(4)\n\t"
    "v_mfma_f32_16x16x32_bf16 v[108:111], v[152:155], a[12:15], v[108:111]\n\t"
    "ds_read_b64_tr_b16 v[136:137], v197 offset:32768\n\t"
    "v_mfma_f32_16x16x32_bf16 v[108:111], v[156:159], a[44:47], v[108:111]\n\t"
    "ds_read_b64_tr_b16 v[138:139], v197 offset:36864\n\t"
    "v_mfma_f32_16x16x32_bf16 v[76:79], v[156:159], a[40:43], v[76:79]\n\t"
    "buffer_load_dwordx4 a[160:163], v241, s[88:91], s47 offen sc1\n\t"
    "v_mfma_f32_16x16x32_bf16 v[76:79], v[152:155], a[8:11], v[76:79]\n\t"
    "v_mfma_f32_16x16x32_bf16 v[44:47], v[152:155], a[4:7], v[44:47]\n\t"
    "ds_read_b64_tr_b16 v[140:141], v197 offset:40960\n\t"
    "v_mfma_f32_16x16x32_bf16 v[44:47], v[156:159], a[36:39], v[44:47]\n\t"
    "ds_read_b64_tr_b16 v[142:143], v197 offset:45056\n\t"
    "v_mfma_f32_16x16x32_bf16 v[12:15], v[156:159], a[32:35], v[12:15]\n\t"
    "v_mfma_f32_16x16x32_bf16 v[12:15], v[152:155], a[0:3], v[12:15]\n\t"
    "s_waitcnt lgkmcnt(4)\n\t"
    "v_mfma_f32_16x16x32_bf16 v[16:19], v[128:131], a[0:3], v[16:19]\n\t"
    "ds_read_b64_tr_b16 v[144:145], v198 offset:32768\n\t"
    "v_mfma_f32_16x16x32_bf16 v[16:19], v[132:135], a[32:35], v[16:19]\n\t"
    "ds_read_b64_tr_b16 v[146:147], v198 offset:36864\n\t"
    "v_mfma_f32_16x16x32_bf16 v[48:51], v[132:135], a[36:39], v[48:51]\n\t"
    "buffer_load_dwordx4 a[148:151], v246, s[88:91], s46 offen sc1\n\t"
    "v_mfma_f32_16x16x32_bf16 v[48:51], v[128:131], a[4:7], v[48:51]\n\t"
    "v_mfma_f32_16x16x32_bf16 v[80:83], v[128:131], a[8:11], v[80:83]\n\t"
    "ds_read_b64_tr_b16 v[148:149], v198 offset:40960\n\t"
    "v_mfma_f32_16x16x32_bf16 v[80:83], v[132:135], a[40:43], v[80:83]\n\t"
    "ds_read_b64_tr_b16 v[150:151], v198 offset:45056\n\t"
    "v_mfma_f32_16x16x32_bf16 v[112:115], v[132:135], a[44:47], v[112:115]\n\t"
    "buffer_load_dwordx4 a[164:167], v246, s[88:91], s47 offen sc1\n\t"
    "v_mfma_f32_16x16x32_bf16 v[112:115], v[128:131], a[12:15], v[112:115]\n\t"
    "s_waitcnt lgkmcnt(4)\n\t"
    "v_mfma_f32_16x16x32_bf16 v[116:119], v[136:139], a[12:15], v[116:119]\n\t"
    "ds_read_b64_tr_b16 v[152:153], v199 offset:32768\n\t"
    "v_mfma_f32_16x16x32_bf16 v[116:119], v[140:143], a[44:47], v[116:119]\n\t"
    "ds_read_b64_tr_b16 v[154:155], v199 offset:36864\n\t"
    "v_mfma_f32_16x16x32_bf16 v[84:87], v[140:143], a[40:43], v[84:87]\n\t"
    "buffer_load_dwordx4 a[152:155], v247, s[88:91], s46 offen sc1\n\t"
    "v_mfma_f32_16x16x32_bf16 v[84:87], v[136:139], a[8:11], v[84:87]\n\t"
    "v_mfma_f32_16x16x32_bf16 v[52:55], v[136:139], a[4:7], v[52:55]\n\t"
    "ds_read_b64_tr_b16 v[156:157], v199 offset:40960\n\t"
    "v_mfma_f32_16x16x32_bf16 v[52:55], v[140:143], a[36:39], v[52:55]\n\t"
    "ds_read_b64_tr_b16 v[158:159], v199 offset:45056\n\t"
    "v_mfma_f32_16x16x32_bf16 v[20:23], v[140:143], a[32:35], v[20:23]\n\t"
    "v_mfma_f32_16x16x32_bf16 v[20:23], v[136:139], a[0:3], v[20:23]\n\t"
    "s_waitcnt vmcnt(18) lgkmcnt(4)\n\t"
    "s_barrier\n\t"
    "v_mfma_f32_16x16x32_bf16 v[24:27], v[144:147], a[0:3], v[24:27]\n\t"
    "ds_read_b64_tr_b16 v[128:129], v192 offset:49152\n\t"
    "v_mfma_f32_16x16x32_bf16 v[24:27], v[148:151], a[32:35], v[24:27]\n\t"
    "ds_read_b64_tr_b16 v[130:131], v192 offset:53248\n\t"
    "v_mfma_f32_16x16x32_bf16 v[56:59], v[148:151], a[36:39], v[56:59]\n\t"
    "buffer_load_dwordx4 a[168:171], v247, s[88:91], s47 offen sc1\n\t"
    "v_mfma_f32_16x16x32_bf16 v[56:59], v[144:147], a[4:7], v[56:59]\n\t"
    "v_mfma_f32_16x16x32_bf16 v[88:91], v[144:147], a[8:11], v[88:91]\n\t"
    "ds_read_b64_tr_b16 v[132:133], v192 offset:57344\n\t"
    "v_mfma_f32_16x16x32_bf16 v[88:91], v[148:151], a[40:43], v[88:91]\n\t"
    "ds_read_b64_tr_b16 v[134:135], v192 offset:61440\n\t"
    "v_mfma_f32_16x16x32_bf16 v[120:123], v[148:151], a[44:47], v[120:123]\n\t"
    "buffer_load_dwordx4 a[156:159], v249, s[88:91], s46 offen sc1\n\t"
    "v_mfma_f32_16x16x32_bf16 v[120:123], v[144:147], a[12:15], v[120:123]\n\t"
    "s_waitcnt lgkmcnt(4)\n\t"
    "v_mfma_f32_16x16x32_bf16 v[124:127], v[152:155], a[12:15], v[124:127]\n\t"
    "ds_read_b64_tr_b16 v[136:137], v193 offset:49152\n\t"
    "v_mfma_f32_16x16x32_bf16 v[124:127], v[156:159], a[44:47], v[124:127]\n\t"
    "ds_read_b64_tr_b16 v[138:139], v193 offset:53248\n\t"
    "v_mfma_f32_16x16x32_bf16 v[92:95], v[156:159], a[40:43], v[92:95]\n\t"
    "buffer_load_dwordx4 a[172:175], v249, s[88:91], s47 offen sc1\n\t"
    "v_mfma_f32_16x16x32_bf16 v[92:95], v[152:155], a[8:11], v[92:95]\n\t"
    "s_bfe_u32 s84, s17, 262156\n\t"
    "s_lshl_b32 s84, s84, 17\n\t"
    "s_bfe_u32 s46, s17, 262156\n\t"
    "v_mfma_f32_16x16x32_bf16 v[60:63], v[152:155], a[4:7], v[60:63]\n\t"
    "ds_read_b64_tr_b16 v[140:141], v193 offset:57344\n\t"
    "s_lshl_b32 s46, s46, 13\n\t"
    "v_mfma_f32_16x16x32_bf16 v[60:63], v[156:159], a[36:39], v[60:63]\n\t"
    "ds_read_b64_tr_b16 v[142:143], v193 offset:61440\n\t"
    "s_add_u32 s46, s46, s45\n\t"
    "v_mfma_f32_16x16x32_bf16 v[28:31], v[156:159], a[32:35], v[28:31]\n\t"
    "s_add_u32 s47, s46, 4096\n\t"
    "v_mfma_f32_16x16x32_bf16 v[28:31], v[152:155], a[0:3], v[28:31]\n\t"
    "s_waitcnt vmcnt(12) lgkmcnt(4)\n\t"
    "v_mfma_f32_16x16x32_bf16 v[0:3], v[128:131], a[64:67], v[0:3]\n\t"
    "ds_read_b64_tr_b16 v[144:145], v194 offset:49152\n\t"
    "v_mfma_f32_16x16x32_bf16 v[0:3], v[132:135], a[96:99], v[0:3]\n\t"
    "ds_read_b64_tr_b16 v[146:147], v194 offset:53248\n\t"
    "s_add_u32 m0, s48, 65536\n\t"
    "v_mfma_f32_16x16x32_bf16 v[32:35], v[132:135], a[100:103], v[32:35]\n\t"
    "buffer_load_dwordx4 v242, s[32:35], s84 offen sc1 lds\n\t"
    "v_mfma_f32_16x16x32_bf16 v[32:35], v[128:131], a[68:71], v[32:35]\n\t"
    "s_lshl_b32 s77, s49, 2\n\t"
    "v_mov_b32 v254, s77\n\t"
    "v_mfma_f32_16x16x32_bf16 v[64:67], v[128:131], a[72:75], v[64:67]\n\t"
    "ds_read_b64_tr_b16 v[148:149], v194 offset:57344\n\t"
    "v_mov_b32 v255, s20\n\t"
    "v_mfma_f32_16x16x32_bf16 v[64:67], v[132:135], a[104:107], v[64:67]\n\t"
    "ds_read_b64_tr_b16 v[150:151], v194 offset:61440\n\t"
    "v_mfma_f32_16x16x32_bf16 v[96:99], v[132:135], a[108:111], v[96:99]\n\t"
    "buffer_load_dwordx4 v243, s[32:35], s84 offen offset:1024 sc1 lds\n\t"
    "v_mfma_f32_16x16x32_bf16 v[96:99], v[128:131], a[76:79], v[96:99]\n\t"
    "s_waitcnt lgkmcnt(4)\n\t"
    "buffer_store_dword v255, v254, s[88:91], s54 offen sc1\n\t"
    "v_mfma_f32_16x16x32_bf16 v[100:103], v[136:139], a[76:79], v[100:103]\n\t"
    "ds_read_b64_tr_b16 v[152:153], v195 offset:49152\n\t"
    "s_add_u32 s56, s55, 0\n\t"
    "v_mfma_f32_16x16x32_bf16 v[100:103], v[140:143], a[108:111], v[100:103]\n\t"
    "ds_read_b64_tr_b16 v[154:155], v195 offset:53248\n\t"
    "s_add_u32 s57, s55, 4096\n\t"
    "v_mfma_f32_16x16x32_bf16 v[68:71], v[140:143], a[104:107], v[68:71]\n\t"
    "s_add_u32 s58, s55, 8192\n\t"
    "buffer_load_dwordx4 v244, s[32:35], s84 offen offset:2048 sc1 lds\n\t"
    "v_mfma_f32_16x16x32_bf16 v[68:71], v[136:139], a[72:75], v[68:71]\n\t"
    "v_mfma_f32_16x16x32_bf16 v[36:39], v[136:139], a[68:71], v[36:39]\n\t"
    "ds_read_b64_tr_b16 v[156:157], v195 offset:57344\n\t"
    "v_mfma_f32_16x16x32_bf16 v[36:39], v[140:143], a[100:103], v[36:39]\n\t"
    "ds_read_b64_tr_b16 v[158:159], v195 offset:61440\n\t"
    "v_mfma_f32_16x16x32_bf16 v[4:7], v[140:143], a[96:99], v[4:7]\n\t"
    "v_mfma_f32_16x16x32_bf16 v[4:7], v[136:139], a[64:67], v[4:7]\n\t"
    "s_waitcnt lgkmcnt(4)\n\t"
    "v_mfma_f32_16x16x32_bf16 v[8:11], v[144:147], a[64:67], v[8:11]\n\t"
    "ds_read_b64_tr_b16 v[128:129], v196 offset:49152\n\t"
    "v_mfma_f32_16x16x32_bf16 v[8:11], v[148:151], a[96:99], v[8:11]\n\t"
    "ds_read_b64_tr_b16 v[130:131], v196 offset:53248\n\t"
    "v_mfma_f32_16x16x32_bf16 v[40:43], v[148:151], a[100:103], v[40:43]\n\t"
    "buffer_load_dwordx4 v245, s[32:35], s84 offen offset:3072 sc1 lds\n\t"
    "v_mfma_f32_16x16x32_bf16 v[40:43], v[144:147], a[68:71], v[40:43]\n\t"
    "v_mfma_f32_16x16x32_bf16 v[72:75], v[144:147], a[72:75], v[72:75]\n\t"
    "ds_read_b64_tr_b16 v[132:133], v196 offset:57344\n\t"
    "v_mfma_f32_16x16x32_bf16 v[72:75], v[148:151], a[104:107], v[72:75]\n\t"
    "ds_read_b64_tr_b16 v[134:135], v196 offset:61440\n\t"
    "v_mfma_f32_16x16x32_bf16 v[104:107], v[148:151], a[108:111], v[104:107]\n\t"
    "buffer_load_dwordx4 a[176:179], v241, s[88:91], s46 offen sc1\n\t"
    "v_mfma_f32_16x16x32_bf16 v[104:107], v[144:147], a[76:79], v[104:107]\n\t"
    "s_waitcnt lgkmcnt(4)\n\t"
    "v_mfma_f32_16x16x32_bf16 v[108:111], v[152:155], a[76:79], v[108:111]\n\t"
    "ds_read_b64_tr_b16 v[136:137], v197 offset:49152\n\t"
    "v_mfma_f32_16x16x32_bf16 v[108:111], v[156:159], a[108:111], v[108:111]\n\t"
    "ds_read_b64_tr_b16 v[138:139], v197 offset:53248\n\t"
    "v_mfma_f32_16x16x32_bf16 v[76:79], v[156:159], a[104:107], v[76:79]\n\t"
    "buffer_load_dwordx4 a[192:195], v241, s[88:91], s47 offen sc1\n\t"
    "v_mfma_f32_16x16x32_bf16 v[76:79], v[152:155], a[72:75], v[76:79]\n\t"
    "v_mfma_f32_16x16x32_bf16 v[44:47], v[152:155], a[68:71], v[44:47]\n\t"
    "ds_read_b64_tr_b16 v[140:141], v197 offset:57344\n\t"
    "v_mfma_f32_16x16x32_bf16 v[44:47], v[156:159], a[100:103], v[44:47]\n\t"
    "ds_read_b64_tr_b16 v[142:143], v197 offset:61440\n\t"
    "v_mfma_f32_16x16x32_bf16 v[12:15], v[156:159], a[96:99], v[12:15]\n\t"
    "v_mfma_f32_16x16x32_bf16 v[12:15], v[152:155], a[64:67], v[12:15]\n\t"
    "s_waitcnt lgkmcnt(4)\n\t"
    "v_mfma_f32_16x16x32_bf16 v[16:19], v[128:131], a[64:67], v[16:19]\n\t"
    "ds_read_b64_tr_b16 v[144:145], v198 offset:49152\n\t"
    "v_mfma_f32_16x16x32_bf16 v[16:19], v[132:135], a[96:99], v[16:19]\n\t"
    "ds_read_b64_tr_b16 v[146:147], v198 offset:53248\n\t"
    "v_mfma_f32_16x16x32_bf16 v[48:51], v[132:135], a[100:103], v[48:51]\n\t"
    "buffer_load_dwordx4 a[180:183], v246, s[88:91], s46 offen sc1\n\t"
    "v_mfma_f32_16x16x32_bf16 v[48:51], v[128:131], a[68:71], v[48:51]\n\t"
    "v_mfma_f32_16x16x32_bf16 v[80:83], v[128:131], a[72:75], v[80:83]\n\t"
    "ds_read_b64_tr_b16 v[148:149], v198 offset:57344\n\t"
    "v_mfma_f32_16x16x32_bf16 v[80:83], v[132:135], a[104:107], v[80:83]\n\t"
    "ds_read_b64_tr_b16 v[150:151], v198 offset:61440\n\t"
    "v_mfma_f32_16x16x32_bf16 v[112:115], v[132:135], a[108:111], v[112:115]\n\t"
    "buffer_load_dwordx4 a[196:199], v246, s[88:91], s47 offen sc1\n\t"
    "v_mfma_f32_16x16x32_bf16 v[112:115], v[128:131], a[76:79], v[112:115]\n\t"
    "s_waitcnt lgkmcnt(4)\n\t"
    "v_mfma_f32_16x16x32_bf16 v[116:119], v[136:139], a[76:79], v[116:119]\n\t"
    "ds_read_b64_tr_b16 v[152:153], v199 offset:49152\n\t"
    "v_mfma_f32_16x16x32_bf16 v[116:119], v[140:143], a[108:111], v[116:119]\n\t"
    "ds_read_b64_tr_b16 v[154:155], v199 offset:53248\n\t"
    "v_mfma_f32_16x16x32_bf16 v[84:87], v[140:143], a[104:107], v[84:87]\n\t"
    "buffer_load_dwordx4 a[184:187], v247, s[88:91], s46 offen sc1\n\t"
    "v_mfma_f32_16x16x32_bf16 v[84:87], v[136:139], a[72:75], v[84:87]\n\t"
    "v_mfma_f32_16x16x32_bf16 v[52:55], v[136:139], a[68:71], v[52:55]\n\t"
    "ds_read_b64_tr_b16 v[156:157], v199 offset:57344\n\t"
    "v_mfma_f32_16x16x32_bf16 v[52:55], v[140:143], a[100:103], v[52:55]\n\t"
    "ds_read_b64_tr_b16 v[158:159], v199 offset:61440\n\t"
    "v_mfma_f32_16x16x32_bf16 v[20:23], v[140:143], a[96:99], v[20:23]\n\t"
    "v_mfma_f32_16x16x32_bf16 v[20:23], v[136:139], a[64:67], v[20:23]\n\t"
    "s_waitcnt vmcnt(18) lgkmcnt(4)\n\t"
    "s_barrier\n\t"
    "v_mfma_f32_16x16x32_bf16 v[24:27], v[144:147], a[64:67], v[24:27]\n\t"
    "ds_read_b64_tr_b16 v[128:129], v192 offset:0\n\t"
    "v_mfma_f32_16x16x32_bf16 v[24:27], v[148:151], a[96:99], v[24:27]\n\t"
    "ds_read_b64_tr_b16 v[130:131], v192 offset:4096\n\t"
    "v_mfma_f32_16x16x32_bf16 v[56:59], v[148:151], a[100:103], v[56:59]\n\t"
    "buffer_load_dwordx4 a[200:203], v247, s[88:91], s47 offen sc1\n\t"
    "v_mfma_f32_16x16x32_bf16 v[56:59], v[144:147], a[68:71], v[56:59]\n\t"
    "v_mfma_f32_16x16x32_bf16 v[88:91], v[144:147], a[72:75], v[88:91]\n\t"
    "ds_read_b64_tr_b16 v[132:133], v192 offset:8192\n\t"
    "v_mfma_f32_16x16x32_bf16 v[88:91], v[148:151], a[104:107], v[88:91]\n\t"
    "ds_read_b64_tr_b16 v[134:135], v192 offset:12288\n\t"
    "v_mfma_f32_16x16x32_bf16 v[120:123], v[148:151], a[108:111], v[120:123]\n\t"
    "buffer_load_dwordx4 a[188:191], v249, s[88:91], s46 offen sc1\n\t"
    "v_mfma_f32_16x16x32_bf16 v[120:123], v[144:147], a[76:79], v[120:123]\n\t"
    "s_waitcnt lgkmcnt(4)\n\t"
    "v_mfma_f32_16x16x32_bf16 v[124:127], v[152:155], a[76:79], v[124:127]\n\t"
    "ds_read_b64_tr_b16 v[136:137], v193 offset:0\n\t"
    "v_mfma_f32_16x16x32_bf16 v[124:127], v[156:159], a[108:111], v[124:127]\n\t"
    "ds_read_b64_tr_b16 v[138:139], v193 offset:4096\n\t"
    "v_mfma_f32_16x16x32_bf16 v[92:95], v[156:159], a[104:107], v[92:95]\n\t"
    "buffer_load_dwordx4 a[204:207], v249, s[88:91], s47 offen sc1\n\t"
    "v_mfma_f32_16x16x32_bf16 v[92:95], v[152:155], a[72:75], v[92:95]\n\t"
    "s_bfe_u32 s84, s17, 262160\n\t"
    "s_lshl_b32 s84, s84, 17\n\t"
    "s_bfe_u32 s46, s17, 262160\n\t"
    "v_mfma_f32_16x16x32_bf16 v[60:63], v[152:155], a[68:71], v[60:63]\n\t"
    "ds_read_b64_tr_b16 v[140:141], v193 offset:8192\n\t"
    "s_lshl_b32 s46, s46, 13\n\t"
    "v_mfma_f32_16x16x32_bf16 v[60:63], v[156:159], a[100:103], v[60:63]\n\t"
    "ds_read_b64_tr_b16 v[142:143], v193 offset:12288\n\t"
    "s_add_u32 s46, s46, s45\n\t"
    "v_mfma_f32_16x16x32_bf16 v[28:31], v[156:159], a[96:99], v[28:31]\n\t"
    "s_add_u32 s47, s46, 4096\n\t"
    "v_mfma_f32_16x16x32_bf16 v[28:31], v[152:155], a[64:67], v[28:31]\n\t"
    "s_waitcnt vmcnt(13) lgkmcnt(4)\n\t"
    "v_mfma_f32_16x16x32_bf16 v[0:3], v[128:131], a[144:147], v[0:3]\n\t"
    "ds_read_b64_tr_b16 v[144:145], v194 offset:0\n\t"
    "v_mfma_f32_16x16x32_bf16 v[0:3], v[132:135], a[160:163], v[0:3]\n\t"
    "ds_read_b64_tr_b16 v[146:147], v194 offset:4096\n\t"
    "s_add_u32 m0, s48, 81920\n\t"
    "v_mfma_f32_16x16x32_bf16 v[32:35], v[132:135], a[164:167], v[32:35]\n\t"
    "buffer_load_dwordx4 v242, s[32:35], s84 offen sc1 lds\n\t"
    "v_mfma_f32_16x16x32_bf16 v[32:35], v[128:131], a[148:151], v[32:35]\n\t"
    "v_mfma_f32_16x16x32_bf16 v[64:67], v[128:131], a[152:155], v[64:67]\n\t"
    "ds_read_b64_tr_b16 v[148:149], v194 offset:8192\n\t"
    "v_mfma_f32_16x16x32_bf16 v[64:67], v[132:135], a[168:171], v[64:67]\n\t"
    "ds_read_b64_tr_b16 v[150:151], v194 offset:12288\n\t"
    "v_mfma_f32_16x16x32_bf16 v[96:99], v[132:135], a[172:175], v[96:99]\n\t"
    "buffer_load_dwordx4 v243, s[32:35], s84 offen offset:1024 sc1 lds\n\t"
    "v_mfma_f32_16x16x32_bf16 v[96:99], v[128:131], a[156:159], v[96:99]\n\t"
    "s_waitcnt lgkmcnt(4)\n\t"
    "v_mfma_f32_16x16x32_bf16 v[100:103], v[136:139], a[156:159], v[100:103]\n\t"
    "ds_read_b64_tr_b16 v[152:153], v195 offset:0\n\t"
    "v_mfma_f32_16x16x32_bf16 v[100:103], v[140:143], a[172:175], v[100:103]\n\t"
    "ds_read_b64_tr_b16 v[154:155], v195 offset:4096\n\t"
    "v_mfma_f32_16x16x32_bf16 v[68:71], v[140:143], a[168:171], v[68:71]\n\t"
    "buffer_load_dwordx4 v244, s[32:35], s84 offen offset:2048 sc1 lds\n\t"
    "v_mfma_f32_16x16x32_bf16 v[68:71], v[136:139], a[152:155], v[68:71]\n\t"
    "v_mfma_f32_16x16x32_bf16 v[36:39], v[136:139], a[148:151], v[36:39]\n\t"
    "ds_read_b64_tr_b16 v[156:157], v195 offset:8192\n\t"
    "v_mfma_f32_16x16x32_bf16 v[36:39], v[140:143], a[164:167], v[36:39]\n\t"
    "ds_read_b64_tr_b16 v[158:159], v195 offset:12288\n\t"
    "v_mfma_f32_16x16x32_bf16 v[4:7], v[140:143], a[160:163], v[4:7]\n\t"
    "v_mfma_f32_16x16x32_bf16 v[4:7], v[136:139], a[144:147], v[4:7]\n\t"
    "s_waitcnt lgkmcnt(4)\n\t"
    "v_mfma_f32_16x16x32_bf16 v[8:11], v[144:147], a[144:147], v[8:11]\n\t"
    "ds_read_b64_tr_b16 v[128:129], v196 offset:0\n\t"
    "v_mfma_f32_16x16x32_bf16 v[8:11], v[148:151], a[160:163], v[8:11]\n\t"
    "ds_read_b64_tr_b16 v[130:131], v196 offset:4096\n\t"
    "v_mfma_f32_16x16x32_bf16 v[40:43], v[148:151], a[164:167], v[40:43]\n\t"
    "buffer_load_dwordx4 v245, s[32:35], s84 offen offset:3072 sc1 lds\n\t"
    "v_mfma_f32_16x16x32_bf16 v[40:43], v[144:147], a[148:151], v[40:43]\n\t"
    "v_mfma_f32_16x16x32_bf16 v[72:75], v[144:147], a[152:155], v[72:75]\n\t"
    "ds_read_b64_tr_b16 v[132:133], v196 offset:8192\n\t"
    "v_mfma_f32_16x16x32_bf16 v[72:75], v[148:151], a[168:171], v[72:75]\n\t"
    "ds_read_b64_tr_b16 v[134:135], v196 offset:12288\n\t"
    "v_mfma_f32_16x16x32_bf16 v[104:107], v[148:151], a[172:175], v[104:107]\n\t"
    "buffer_load_dwordx4 a[0:3], v241, s[88:91], s46 offen sc1\n\t"
    "v_mfma_f32_16x16x32_bf16 v[104:107], v[144:147], a[156:159], v[104:107]\n\t"
    "s_waitcnt lgkmcnt(4)\n\t"
    "v_mfma_f32_16x16x32_bf16 v[108:111], v[152:155], a[156:159], v[108:111]\n\t"
    "ds_read_b64_tr_b16 v[136:137], v197 offset:0\n\t"
    "v_mfma_f32_16x16x32_bf16 v[108:111], v[156:159], a[172:175], v[108:111]\n\t"
    "ds_read_b64_tr_b16 v[138:139], v197 offset:4096\n\t"
    "v_mfma_f32_16x16x32_bf16 v[76:79], v[156:159], a[168:171], v[76:79]\n\t"
    "buffer_load_dwordx4 a[32:35], v241, s[88:91], s47 offen sc1\n\t"
    "v_mfma_f32_16x16x32_bf16 v[76:79], v[152:155], a[152:155], v[76:79]\n\t"
    "v_mfma_f32_16x16x32_bf16 v[44:47], v[152:155], a[148:151], v[44:47]\n\t"
    "ds_read_b64_tr_b16 v[140:141], v197 offset:8192\n\t"
    "v_mfma_f32_16x16x32_bf16 v[44:47], v[156:159], a[164:167], v[44:47]\n\t"
    "ds_read_b64_tr_b16 v[142:143], v197 offset:12288\n\t"
    "v_mfma_f32_16x16x32_bf16 v[12:15], v[156:159], a[160:163], v[12:15]\n\t"
    "v_mfma_f32_16x16x32_bf16 v[12:15], v[152:155], a[144:147], v[12:15]\n\t"
    "s_waitcnt lgkmcnt(4)\n\t"
    "v_mfma_f32_16x16x32_bf16 v[16:19], v[128:131], a[144:147], v[16:19]\n\t"
    "ds_read_b64_tr_b16 v[144:145], v198 offset:0\n\t"
    "v_mfma_f32_16x16x32_bf16 v[16:19], v[132:135], a[160:163], v[16:19]\n\t"
    "ds_read_b64_tr_b16 v[146:147], v198 offset:4096\n\t"
    "v_mfma_f32_16x16x32_bf16 v[48:51], v[132:135], a[164:167], v[48:51]\n\t"
    "buffer_load_dwordx4 a[4:7], v246, s[88:91], s46 offen sc1\n\t"
    "v_mfma_f32_16x16x32_bf16 v[48:51], v[128:131], a[148:151], v[48:51]\n\t"
    "v_mfma_f32_16x16x32_bf16 v[80:83], v[128:131], a[152:155], v[80:83]\n\t"
    "ds_read_b64_tr_b16 v[148:149], v198 offset:8192\n\t"
    "v_mfma_f32_16x16x32_bf16 v[80:83], v[132:135], a[168:171], v[80:83]\n\t"
    "ds_read_b64_tr_b16 v[150:151], v198 offset:12288\n\t"
    "v_mfma_f32_16x16x32_bf16 v[112:115], v[132:135], a[172:175], v[112:115]\n\t"
    "buffer_load_dwordx4 a[36:39], v246, s[88:91], s47 offen sc1\n\t"
    "v_mfma_f32_16x16x32_bf16 v[112:115], v[128:131], a[156:159], v[112:115]\n\t"
    "s_waitcnt lgkmcnt(4)\n\t"
    "v_mfma_f32_16x16x32_bf16 v[116:119], v[136:139], a[156:159], v[116:119]\n\t"
    "ds_read_b64_tr_b16 v[152:153], v199 offset:0\n\t"
    "v_mfma_f32_16x16x32_bf16 v[116:119], v[140:143], a[172:175], v[116:119]\n\t"
    "ds_read_b64_tr_b16 v[154:155], v199 offset:4096\n\t"
    "v_mfma_f32_16x16x32_bf16 v[84:87], v[140:143], a[168:171], v[84:87]\n\t"
    "buffer_load_dwordx4 a[8:11], v247, s[88:91], s46 offen sc1\n\t"
    "v_mfma_f32_16x16x32_bf16 v[84:87], v[136:139], a[152:155], v[84:87]\n\t"
    "v_mfma_f32_16x16x32_bf16 v[52:55], v[136:139], a[148:151], v[52:55]\n\t"
    "ds_read_b64_tr_b16 v[156:157], v199 offset:8192\n\t"
    "v_mfma_f32_16x16x32_bf16 v[52:55], v[140:143], a[164:167], v[52:55]\n\t"
    "ds_read_b64_tr_b16 v[158:159], v199 offset:12288\n\t"
    "v_mfma_f32_16x16x32_bf16 v[20:23], v[140:143], a[160:163], v[20:23]\n\t"
    "v_mfma_f32_16x16x32_bf16 v[20:23], v[136:139], a[144:147], v[20:23]\n\t"
    "s_waitcnt vmcnt(17) lgkmcnt(4)\n\t"
    "s_barrier\n\t"
    "v_mfma_f32_16x16x32_bf16 v[24:27], v[144:147], a[144:147], v[24:27]\n\t"
    "ds_read_b64_tr_b16 v[128:129], v192 offset:16384\n\t"
    "v_mfma_f32_16x16x32_bf16 v[24:27], v[148:151], a[160:163], v[24:27]\n\t"
    "ds_read_b64_tr_b16 v[130:131], v192 offset:20480\n\t"
    "v_mfma_f32_16x16x32_bf16 v[56:59], v[148:151], a[164:167], v[56:59]\n\t"
    "buffer_load_dwordx4 a[40:43], v247, s[88:91], s47 offen sc1\n\t"
    "v_mfma_f32_16x16x32_bf16 v[56:59], v[144:147], a[148:151], v[56:59]\n\t"
    "v_mfma_f32_16x16x32_bf16 v[88:91], v[144:147], a[152:155], v[88:91]\n\t"
    "ds_read_b64_tr_b16 v[132:133], v192 offset:24576\n\t"
    "v_mfma_f32_16x16x32_bf16 v[88:91], v[148:151], a[168:171], v[88:91]\n\t"
    "ds_read_b64_tr_b16 v[134:135], v192 offset:28672\n\t"
    "v_mfma_f32_16x16x32_bf16 v[120:123], v[148:151], a[172:175], v[120:123]\n\t"
    "buffer_load_dwordx4 a[12:15], v249, s[88:91], s46 offen sc1\n\t"
    "v_mfma_f32_16x16x32_bf16 v[120:123], v[144:147], a[156:159], v[120:123]\n\t"
    "s_waitcnt lgkmcnt(4)\n\t"
    "v_mfma_f32_16x16x32_bf16 v[124:127], v[152:155], a[156:159], v[124:127]\n\t"
    "ds_read_b64_tr_b16 v[136:137], v193 offset:16384\n\t"
    "v_mfma_f32_16x16x32_bf16 v[124:127], v[156:159], a[172:175], v[124:127]\n\t"
    "ds_read_b64_tr_b16 v[138:139], v193 offset:20480\n\t"
    "v_mfma_f32_16x16x32_bf16 v[92:95], v[156:159], a[168:171], v[92:95]\n\t"
    "buffer_load_dwordx4 a[44:47], v249, s[88:91], s47 offen sc1\n\t"
    "v_mfma_f32_16x16x32_bf16 v[92:95], v[152:155], a[152:155], v[92:95]\n\t"
    "s_bfe_u32 s84, s17, 262164\n\t"
    "s_lshl_b32 s84, s84, 17\n\t"
    "s_bfe_u32 s46, s17, 262164\n\t"
    "v_mfma_f32_16x16x32_bf16 v[60:63], v[152:155], a[148:151], v[60:63]\n\t"
    "ds_read_b64_tr_b16 v[140:141], v193 offset:24576\n\t"
    "s_lshl_b32 s46, s46, 13\n\t"
    "v_mfma_f32_16x16x32_bf16 v[60:63], v[156:159], a[164:167], v[60:63]\n\t"
    "ds_read_b64_tr_b16 v[142:143], v193 offset:28672\n\t"
    "s_add_u32 s46, s46, s45\n\t"
    "v_mfma_f32_16x16x32_bf16 v[28:31], v[156:159], a[160:163], v[28:31]\n\t"
    "s_add_u32 s47, s46, 4096\n\t"
    "v_mfma_f32_16x16x32_bf16 v[28:31], v[152:155], a[144:147], v[28:31]\n\t"
    "s_waitcnt vmcnt(12) lgkmcnt(4)\n\t"
    "v_mfma_f32_16x16x32_bf16 v[0:3], v[128:131], a[176:179], v[0:3]\n\t"
    "ds_read_b64_tr_b16 v[144:145], v194 offset:16384\n\t"
    "v_mfma_f32_16x16x32_bf16 v[0:3], v[132:135], a[192:195], v[0:3]\n\t"
    "ds_read_b64_tr_b16 v[146:147], v194 offset:20480\n\t"
    "s_add_u32 m0, s48, 98304\n\t"
    "v_mfma_f32_16x16x32_bf16 v[32:35], v[132:135], a[196:199], v[32:35]\n\t"
    "buffer_load_dwordx4 v242, s[32:35], s84 offen sc1 lds\n\t"
    "v_mfma_f32_16x16x32_bf16 v[32:35], v[128:131], a[180:183], v[32:35]\n\t"
    "v_mfma_f32_16x16x32_bf16 v[64:67], v[128:131], a[184:187], v[64:67]\n\t"
    "ds_read_b64_tr_b16 v[148:149], v194 offset:24576\n\t"
    "v_mfma_f32_16x16x32_bf16 v[64:67], v[132:135], a[200:203], v[64:67]\n\t"
    "ds_read_b64_tr_b16 v[150:151], v194 offset:28672\n\t"
    "v_mfma_f32_16x16x32_bf16 v[96:99], v[132:135], a[204:207], v[96:99]\n\t"
    "buffer_load_dwordx4 v243, s[32:35], s84 offen offset:1024 sc1 lds\n\t"
    "v_mfma_f32_16x16x32_bf16 v[96:99], v[128:131], a[188:191], v[96:99]\n\t"
    "s_waitcnt lgkmcnt(4)\n\t"
    "v_mfma_f32_16x16x32_bf16 v[100:103], v[136:139], a[188:191], v[100:103]\n\t"
    "ds_read_b64_tr_b16 v[152:153], v195 offset:16384\n\t"
    "v_mfma_f32_16x16x32_bf16 v[100:103], v[140:143], a[204:207], v[100:103]\n\t"
    "ds_read_b64_tr_b16 v[154:155], v195 offset:20480\n\t"
    "v_mfma_f32_16x16x32_bf16 v[68:71], v[140:143], a[200:203], v[68:71]\n\t"
    "buffer_load_dwordx4 v244, s[32:35], s84 offen offset:2048 sc1 lds\n\t"
    "v_mfma_f32_16x16x32_bf16 v[68:71], v[136:139], a[184:187], v[68:71]\n\t"
    "v_mfma_f32_16x16x32_bf16 v[36:39], v[136:139], a[180:183], v[36:39]\n\t"
    "ds_read_b64_tr_b16 v[156:157], v195 offset:24576\n\t"
    "v_mfma_f32_16x16x32_bf16 v[36:39], v[140:143], a[196:199], v[36:39]\n\t"
    "ds_read_b64_tr_b16 v[158:159], v195 offset:28672\n\t"
    "v_mfma_f32_16x16x32_bf16 v[4:7], v[140:143], a[192:195], v[4:7]\n\t"
    "v_mfma_f32_16x16x32_bf16 v[4:7], v[136:139], a[176:179], v[4:7]\n\t"
    "s_waitcnt lgkmcnt(4)\n\t"
    "v_mfma_f32_16x16x32_bf16 v[8:11], v[144:147], a[176:179], v[8:11]\n\t"
    "ds_read_b64_tr_b16 v[128:129], v196 offset:16384\n\t"
    "v_mfma_f32_16x16x32_bf16 v[8:11], v[148:151], a[192:195], v[8:11]\n\t"
    "ds_read_b64_tr_b16 v[130:131], v196 offset:20480\n\t"
    "v_mfma_f32_16x16x32_bf16 v[40:43], v[148:151], a[196:199], v[40:43]\n\t"
    "buffer_load_dwordx4 v245, s[32:35], s84 offen offset:3072 sc1 lds\n\t"
    "v_mfma_f32_16x16x32_bf16 v[40:43], v[144:147], a[180:183], v[40:43]\n\t"
    "v_mfma_f32_16x16x32_bf16 v[72:75], v[144:147], a[184:187], v[72:75]\n\t"
    "ds_read_b64_tr_b16 v[132:133], v196 offset:24576\n\t"
    "v_mfma_f32_16x16x32_bf16 v[72:75], v[148:151], a[200:203], v[72:75]\n\t"
    "ds_read_b64_tr_b16 v[134:135], v196 offset:28672\n\t"
    "v_mfma_f32_16x16x32_bf16 v[104:107], v[148:151], a[204:207], v[104:107]\n\t"
    "buffer_load_dwordx4 a[64:67], v241, s[88:91], s46 offen sc1\n\t"
    "v_mfma_f32_16x16x32_bf16 v[104:107], v[144:147], a[188:191], v[104:107]\n\t"
    "s_waitcnt lgkmcnt(4)\n\t"
    "v_mfma_f32_16x16x32_bf16 v[108:111], v[152:155], a[188:191], v[108:111]\n\t"
    "ds_read_b64_tr_b16 v[136:137], v197 offset:16384\n\t"
    "v_mfma_f32_16x16x32_bf16 v[108:111], v[156:159], a[204:207], v[108:111]\n\t"
    "ds_read_b64_tr_b16 v[138:139], v197 offset:20480\n\t"
    "v_mfma_f32_16x16x32_bf16 v[76:79], v[156:159], a[200:203], v[76:79]\n\t"
    "buffer_load_dwordx4 a[96:99], v241, s[88:91], s47 offen sc1\n\t"
    "v_mfma_f32_16x16x32_bf16 v[76:79], v[152:155], a[184:187], v[76:79]\n\t"
    "v_mfma_f32_16x16x32_bf16 v[44:47], v[152:155], a[180:183], v[44:47]\n\t"
    "ds_read_b64_tr_b16 v[140:141], v197 offset:24576\n\t"
    "v_mfma_f32_16x16x32_bf16 v[44:47], v[156:159], a[196:199], v[44:47]\n\t"
    "ds_read_b64_tr_b16 v[142:143], v197 offset:28672\n\t"
    "v_mfma_f32_16x16x32_bf16 v[12:15], v[156:159], a[192:195], v[12:15]\n\t"
    "v_mfma_f32_16x16x32_bf16 v[12:15], v[152:155], a[176:179], v[12:15]\n\t"
    "s_waitcnt lgkmcnt(4)\n\t"
    "v_mfma_f32_16x16x32_bf16 v[16:19], v[128:131], a[176:179], v[16:19]\n\t"
    "ds_read_b64_tr_b16 v[144:145], v198 offset:16384\n\t"
    "v_mfma_f32_16x16x32_bf16 v[16:19], v[132:135], a[192:195], v[16:19]\n\t"
    "ds_read_b64_tr_b16 v[146:147], v198 offset:20480\n\t"
    "v_mfma_f32_16x16x32_bf16 v[48:51], v[132:135], a[196:199], v[48:51]\n\t"
    "buffer_load_dwordx4 a[68:71], v246, s[88:91], s46 offen sc1\n\t"
    "v_mfma_f32_16x16x32_bf16 v[48:51], v[128:131], a[180:183], v[48:51]\n\t"
    "buffer_load_dword v253, v252, s[88:91], s54 offen sc1\n\t"
    "v_mfma_f32_16x16x32_bf16 v[80:83], v[128:131], a[184:187], v[80:83]\n\t"
    "ds_read_b64_tr_b16 v[148:149], v198 offset:24576\n\t"
    "v_mfma_f32_16x16x32_bf16 v[80:83], v[132:135], a[200:203], v[80:83]\n\t"
    "ds_read_b64_tr_b16 v[150:151], v198 offset:28672\n\t"
    "v_mfma_f32_16x16x32_bf16 v[112:115], v[132:135], a[204:207], v[112:115]\n\t"
    "buffer_load_dwordx4 a[100:103], v246, s[88:91], s47 offen sc1\n\t"
    "v_mfma_f32_16x16x32_bf16 v[112:115], v[128:131], a[188:191], v[112:115]\n\t"
    "s_waitcnt lgkmcnt(4)\n\t"
    "v_mfma_f32_16x16x32_bf16 v[116:119], v[136:139], a[188:191], v[116:119]\n\t"
    "ds_read_b64_tr_b16 v[152:153], v199 offset:16384\n\t"
    "v_mfma_f32_16x16x32_bf16 v[116:119], v[140:143], a[204:207], v[116:119]\n\t"
    "ds_read_b64_tr_b16 v[154:155], v199 offset:20480\n\t"
    "v_mfma_f32_16x16x32_bf16 v[84:87], v[140:143], a[200:203], v[84:87]\n\t"
    "buffer_load_dwordx4 a[72:75], v247, s[88:91], s46 offen sc1\n\t"
    "v_mfma_f32_16x16x32_bf16 v[84:87], v[136:139], a[184:187], v[84:87]\n\t"
    "v_mfma_f32_16x16x32_bf16 v[52:55], v[136:139], a[180:183], v[52:55]\n\t"
    "ds_read_b64_tr_b16 v[156:157], v199 offset:24576\n\t"
    "v_mfma_f32_16x16x32_bf16 v[52:55], v[140:143], a[196:199], v[52:55]\n\t"
    "ds_read_b64_tr_b16 v[158:159], v199 offset:28672\n\t"
    "v_mfma_f32_16x16x32_bf16 v[20:23], v[140:143], a[192:195], v[20:23]\n\t"
    "v_mfma_f32_16x16x32_bf16 v[20:23], v[136:139], a[176:179], v[20:23]\n\t"
    "s_waitcnt vmcnt(18) lgkmcnt(4)\n\t"
    "s_barrier\n\t"
    "v_mfma_f32_16x16x32_bf16 v[24:27], v[144:147], a[176:179], v[24:27]\n\t"
    "ds_read_b64_tr_b16 v[128:129], v192 offset:32768\n\t"
    "v_mfma_f32_16x16x32_bf16 v[24:27], v[148:151], a[192:195], v[24:27]\n\t"
    "ds_read_b64_tr_b16 v[130:131], v192 offset:36864\n\t"
    "v_mfma_f32_16x16x32_bf16 v[56:59], v[148:151], a[196:199], v[56:59]\n\t"
    "buffer_load_dwordx4 a[104:107], v247, s[88:91], s47 offen sc1\n\t"
    "v_mfma_f32_16x16x32_bf16 v[56:59], v[144:147], a[180:183], v[56:59]\n\t"
    "v_mfma_f32_16x16x32_bf16 v[88:91], v[144:147], a[184:187], v[88:91]\n\t"
    "ds_read_b64_tr_b16 v[132:133], v192 offset:40960\n\t"
    "v_mfma_f32_16x16x32_bf16 v[88:91], v[148:151], a[200:203], v[88:91]\n\t"
    "ds_read_b64_tr_b16 v[134:135], v192 offset:45056\n\t"
    "v_mfma_f32_16x16x32_bf16 v[120:123], v[148:151], a[204:207], v[120:123]\n\t"
    "buffer_load_dwordx4 a[76:79], v249, s[88:91], s46 offen sc1\n\t"
    "v_mfma_f32_16x16x32_bf16 v[120:123], v[144:147], a[188:191], v[120:123]\n\t"
    "s_waitcnt lgkmcnt(4)\n\t"
    "v_mfma_f32_16x16x32_bf16 v[124:127], v[152:155], a[188:191], v[124:127]\n\t"
    "ds_read_b64_tr_b16 v[136:137], v193 offset:32768\n\t"
    "v_mfma_f32_16x16x32_bf16 v[124:127], v[156:159], a[204:207], v[124:127]\n\t"
    "ds_read_b64_tr_b16 v[138:139], v193 offset:36864\n\t"
    "v_mfma_f32_16x16x32_bf16 v[92:95], v[156:159], a[200:203], v[92:95]\n\t"
    "buffer_load_dwordx4 a[108:111], v249, s[88:91], s47 offen sc1\n\t"
    "v_mfma_f32_16x16x32_bf16 v[92:95], v[152:155], a[184:187], v[92:95]\n\t"
    "s_bfe_u32 s84, s17, 262168\n\t"
    "s_lshl_b32 s84, s84, 17\n\t"
    "s_bfe_u32 s46, s17, 262168\n\t"
    "v_mfma_f32_16x16x32_bf16 v[60:63], v[152:155], a[180:183], v[60:63]\n\t"
    "ds_read_b64_tr_b16 v[140:141], v193 offset:40960\n\t"
    "s_lshl_b32 s46, s46, 13\n\t"
    "v_mfma_f32_16x16x32_bf16 v[60:63], v[156:159], a[196:199], v[60:63]\n\t"
    "ds_read_b64_tr_b16 v[142:143], v193 offset:45056\n\t"
    "s_add_u32 s46, s46, s45\n\t"
    "v_mfma_f32_16x16x32_bf16 v[28:31], v[156:159], a[192:195], v[28:31]\n\t"
    "s_add_u32 s47, s46, 4096\n\t"
    "v_mfma_f32_16x16x32_bf16 v[28:31], v[152:155], a[176:179], v[28:31]\n\t"
    "s_waitcnt vmcnt(13) lgkmcnt(4)\n\t"
    "v_mfma_f32_16x16x32_bf16 v[0:3], v[128:131], a[0:3], v[0:3]\n\t"
    "ds_read_b64_tr_b16 v[144:145], v194 offset:32768\n\t"
    "v_mfma_f32_16x16x32_bf16 v[0:3], v[132:135], a[32:35], v[0:3]\n\t"
    "ds_read_b64_tr_b16 v[146:147], v194 offset:36864\n\t"
    "s_add_u32 m0, s48, 49152\n\t"
    "v_mfma_f32_16x16x32_bf16 v[32:35], v[132:135], a[36:39], v[32:35]\n\t"
    "buffer_load_dwordx4 v242, s[32:35], s84 offen sc1 lds\n\t"
    "v_mfma_f32_16x16x32_bf16 v[32:35], v[128:131], a[4:7], v[32:35]\n\t"
    "v_mfma_f32_16x16x32_bf16 v[64:67], v[128:131], a[8:11], v[64:67]\n\t"
    "ds_read_b64_tr_b16 v[148:149], v194 offset:40960\n\t"
    "v_mfma_f32_16x16x32_bf16 v[64:67], v[132:135], a[40:43], v[64:67]\n\t"
    "ds_read_b64_tr_b16 v[150:151], v194 offset:45056\n\t"
    "v_mfma_f32_16x16x32_bf16 v[96:99], v[132:135], a[44:47], v[96:99]\n\t"
    "buffer_load_dwordx4 v243, s[32:35], s84 offen offset:1024 sc1 lds\n\t"
    "v_mfma_f32_16x16x32_bf16 v[96:99], v[128:131], a[12:15], v[96:99]\n\t"
    "s_waitcnt lgkmcnt(4)\n\t"
    "v_mfma_f32_16x16x32_bf16 v[100:103], v[136:139], a[12:15], v[100:103]\n\t"
    "ds_read_b64_tr_b16 v[152:153], v195 offset:32768\n\t"
    "v_mfma_f32_16x16x32_bf16 v[100:103], v[140:143], a[44:47], v[100:103]\n\t"
    "ds_read_b64_tr_b16 v[154:155], v195 offset:36864\n\t"
    "v_mfma_f32_16x16x32_bf16 v[68:71], v[140:143], a[40:43], v[68:71]\n\t"
    "buffer_load_dwordx4 v244, s[32:35], s84 offen offset:2048 sc1 lds\n\t"
    "v_mfma_f32_16x16x32_bf16 v[68:71], v[136:139], a[8:11], v[68:71]\n\t"
    "v_mfma_f32_16x16x32_bf16 v[36:39], v[136:139], a[4:7], v[36:39]\n\t"
    "ds_read_b64_tr_b16 v[156:157], v195 offset:40960\n\t"
    "v_mfma_f32_16x16x32_bf16 v[36:39], v[140:143], a[36:39], v[36:39]\n\t"
    "ds_read_b64_tr_b16 v[158:159], v195 offset:45056\n\t"
    "v_mfma_f32_16x16x32_bf16 v[4:7], v[140:143], a[32:35], v[4:7]\n\t"
    "v_mfma_f32_16x16x32_bf16 v[4:7], v[136:139], a[0:3], v[4:7]\n\t"
    "s_waitcnt lgkmcnt(4)\n\t"
    "v_mfma_f32_16x16x32_bf16 v[8:11], v[144:147], a[0:3], v[8:11]\n\t"
    "ds_read_b64_tr_b16 v[128:129], v196 offset:32768\n\t"
    "v_mfma_f32_16x16x32_bf16 v[8:11], v[148:151], a[32:35], v[8:11]\n\t"
    "ds_read_b64_tr_b16 v[130:131], v196 offset:36864\n\t"
    "v_mfma_f32_16x16x32_bf16 v[40:43], v[148:151], a[36:39], v[40:43]\n\t"
    "buffer_load_dwordx4 v245, s[32:35], s84 offen offset:3072 sc1 lds\n\t"
    "v_mfma_f32_16x16x32_bf16 v[40:43], v[144:147], a[4:7], v[40:43]\n\t"
    "v_mfma_f32_16x16x32_bf16 v[72:75], v[144:147], a[8:11], v[72:75]\n\t"
    "ds_read_b64_tr_b16 v[132:133], v196 offset:40960\n\t"
    "v_mfma_f32_16x16x32_bf16 v[72:75], v[148:151], a[40:43], v[72:75]\n\t"
    "ds_read_b64_tr_b16 v[134:135], v196 offset:45056\n\t"
    "v_mfma_f32_16x16x32_bf16 v[104:107], v[148:151], a[44:47], v[104:107]\n\t"
    "buffer_load_dwordx4 a[144:147], v241, s[88:91], s46 offen sc1\n\t"
    "v_mfma_f32_16x16x32_bf16 v[104:107], v[144:147], a[12:15], v[104:107]\n\t"
    "s_waitcnt lgkmcnt(4)\n\t"
    "v_mfma_f32_16x16x32_bf16 v[108:111], v[152:155], a[12:15], v[108:111]\n\t"
    "ds_read_b64_tr_b16 v[136:137], v197 offset:32768\n\t"
    "v_mfma_f32_16x16x32_bf16 v[108:111], v[156:159], a[44:47], v[108:111]\n\t"
    "ds_read_b64_tr_b16 v[138:139], v197 offset:36864\n\t"
    "v_mfma_f32_16x16x32_bf16 v[76:79], v[156:159], a[40:43], v[76:79]\n\t"
    "buffer_load_dwordx4 a[160:163], v241, s[88:91], s47 offen sc1\n\t"
    "v_mfma_f32_16x16x32_bf16 v[76:79], v[152:155], a[8:11], v[76:79]\n\t"
    "v_mfma_f32_16x16x32_bf16 v[44:47], v[152:155], a[4:7], v[44:47]\n\t"
    "ds_read_b64_tr_b16 v[140:141], v197 offset:40960\n\t"
    "v_mfma_f32_16x16x32_bf16 v[44:47], v[156:159], a[36:39], v[44:47]\n\t"
    "ds_read_b64_tr_b16 v[142:143], v197 offset:45056\n\t"
    "v_mfma_f32_16x16x32_bf16 v[12:15], v[156:159], a[32:35], v[12:15]\n\t"
    "v_mfma_f32_16x16x32_bf16 v[12:15], v[152:155], a[0:3], v[12:15]\n\t"
    "s_waitcnt lgkmcnt(4)\n\t"
    "v_mfma_f32_16x16x32_bf16 v[16:19], v[128:131], a[0:3], v[16:19]\n\t"
    "ds_read_b64_tr_b16 v[144:145], v198 offset:32768\n\t"
    "v_mfma_f32_16x16x32_bf16 v[16:19], v[132:135], a[32:35], v[16:19]\n\t"
    "ds_read_b64_tr_b16 v[146:147], v198 offset:36864\n\t"
    "v_mfma_f32_16x16x32_bf16 v[48:51], v[132:135], a[36:39], v[48:51]\n\t"
    "buffer_load_dwordx4 a[148:151], v246, s[88:91], s46 offen sc1\n\t"
    "v_mfma_f32_16x16x32_bf16 v[48:51], v[128:131], a[4:7], v[48:51]\n\t"
    "v_mfma_f32_16x16x32_bf16 v[80:83], v[128:131], a[8:11], v[80:83]\n\t"
    "ds_read_b64_tr_b16 v[148:149], v198 offset:40960\n\t"
    "v_mfma_f32_16x16x32_bf16 v[80:83], v[132:135], a[40:43], v[80:83]\n\t"
    "ds_read_b64_tr_b16 v[150:151], v198 offset:45056\n\t"
    "v_mfma_f32_16x16x32_bf16 v[112:115], v[132:135], a[44:47], v[112:115]\n\t"
    "buffer_load_dwordx4 a[164:167], v246, s[88:91], s47 offen sc1\n\t"
    "v_mfma_f32_16x16x32_bf16 v[112:115], v[128:131], a[12:15], v[112:115]\n\t"
    "s_waitcnt lgkmcnt(4)\n\t"
    "v_mfma_f32_16x16x32_bf16 v[116:119], v[136:139], a[12:15], v[116:119]\n\t"
    "ds_read_b64_tr_b16 v[152:153], v199 offset:32768\n\t"
    "v_mfma_f32_16x16x32_bf16 v[116:119], v[140:143], a[44:47], v[116:119]\n\t"
    "ds_read_b64_tr_b16 v[154:155], v199 offset:36864\n\t"
    "v_mfma_f32_16x16x32_bf16 v[84:87], v[140:143], a[40:43], v[84:87]\n\t"
    "buffer_load_dwordx4 a[152:155], v247, s[88:91], s46 offen sc1\n\t"
    "v_mfma_f32_16x16x32_bf16 v[84:87], v[136:139], a[8:11], v[84:87]\n\t"
    "v_mfma_f32_16x16x32_bf16 v[52:55], v[136:139], a[4:7], v[52:55]\n\t"
    "ds_read_b64_tr_b16 v[156:157], v199 offset:40960\n\t"
    "v_mfma_f32_16x16x32_bf16 v[52:55], v[140:143], a[36:39], v[52:55]\n\t"
    "ds_read_b64_tr_b16 v[158:159], v199 offset:45056\n\t"
    "v_mfma_f32_16x16x32_bf16 v[20:23], v[140:143], a[32:35], v[20:23]\n\t"
    "v_mfma_f32_16x16x32_bf16 v[20:23], v[136:139], a[0:3], v[20:23]\n\t"
    "s_waitcnt vmcnt(18) lgkmcnt(4)\n\t"
    "s_barrier\n\t"
    "v_mfma_f32_16x16x32_bf16 v[24:27], v[144:147], a[0:3], v[24:27]\n\t"
    "ds_read_b64_tr_b16 v[128:129], v192 offset:49152\n\t"
    "v_mfma_f32_16x16x32_bf16 v[24:27], v[148:151], a[32:35], v[24:27]\n\t"
    "ds_read_b64_tr_b16 v[130:131], v192 offset:53248\n\t"
    "v_mfma_f32_16x16x32_bf16 v[56:59], v[148:151], a[36:39], v[56:59]\n\t"
    "buffer_load_dwordx4 a[168:171], v247, s[88:91], s47 offen sc1\n\t"
    "v_mfma_f32_16x16x32_bf16 v[56:59], v[144:147], a[4:7], v[56:59]\n\t"
    "v_mfma_f32_16x16x32_bf16 v[88:91], v[144:147], a[8:11], v[88:91]\n\t"
    "ds_read_b64_tr_b16 v[132:133], v192 offset:57344\n\t"
    "v_mfma_f32_16x16x32_bf16 v[88:91], v[148:151], a[40:43], v[88:91]\n\t"
    "ds_read_b64_tr_b16 v[134:135], v192 offset:61440\n\t"
    "v_mfma_f32_16x16x32_bf16 v[120:123], v[148:151], a[44:47], v[120:123]\n\t"
    "buffer_load_dwordx4 a[156:159], v249, s[88:91], s46 offen sc1\n\t"
    "v_mfma_f32_16x16x32_bf16 v[120:123], v[144:147], a[12:15], v[120:123]\n\t"
    "s_waitcnt lgkmcnt(4)\n\t"
    "v_mfma_f32_16x16x32_bf16 v[124:127], v[152:155], a[12:15], v[124:127]\n\t"
    "ds_read_b64_tr_b16 v[136:137], v193 offset:49152\n\t"
    "v_mfma_f32_16x16x32_bf16 v[124:127], v[156:159], a[44:47], v[124:127]\n\t"
    "ds_read_b64_tr_b16 v[138:139], v193 offset:53248\n\t"
    "v_mfma_f32_16x16x32_bf16 v[92:95], v[156:159], a[40:43], v[92:95]\n\t"
    "buffer_load_dwordx4 a[172:175], v249, s[88:91], s47 offen sc1\n\t"
    "v_mfma_f32_16x16x32_bf16 v[92:95], v[152:155], a[8:11], v[92:95]\n\t"
    "s_bfe_u32 s84, s17, 262172\n\t"
    "s_lshl_b32 s84, s84, 17\n\t"
    "s_bfe_u32 s46, s17, 262172\n\t"
    "v_mfma_f32_16x16x32_bf16 v[60:63], v[152:155], a[4:7], v[60:63]\n\t"
    "ds_read_b64_tr_b16 v[140:141], v193 offset:57344\n\t"
    "s_lshl_b32 s46, s46, 13\n\t"
    "v_mfma_f32_16x16x32_bf16 v[60:63], v[156:159], a[36:39], v[60:63]\n\t"
    "ds_read_b64_tr_b16 v[142:143], v193 offset:61440\n\t"
    "s_add_u32 s46, s46, s45\n\t"
    "v_mfma_f32_16x16x32_bf16 v[28:31], v[156:159], a[32:35], v[28:31]\n\t"
    "s_add_u32 s47, s46, 4096\n\t"
    "v_mfma_f32_16x16x32_bf16 v[28:31], v[152:155], a[0:3], v[28:31]\n\t"
    "s_waitcnt vmcnt(12) lgkmcnt(4)\n\t"
    "v_mfma_f32_16x16x32_bf16 v[0:3], v[128:131], a[64:67], v[0:3]\n\t"
    "ds_read_b64_tr_b16 v[144:145], v194 offset:49152\n\t"
    "v_mfma_f32_16x16x32_bf16 v[0:3], v[132:135], a[96:99], v[0:3]\n\t"
    "ds_read_b64_tr_b16 v[146:147], v194 offset:53248\n\t"
    "s_add_u32 m0, s48, 65536\n\t"
    "v_mfma_f32_16x16x32_bf16 v[32:35], v[132:135], a[100:103], v[32:35]\n\t"
    "buffer_load_dwordx4 v242, s[32:35], s84 offen sc1 lds\n\t"
    "v_mfma_f32_16x16x32_bf16 v[32:35], v[128:131], a[68:71], v[32:35]\n\t"
    "v_mfma_f32_16x16x32_bf16 v[64:67], v[128:131], a[72:75], v[64:67]\n\t"
    "ds_read_b64_tr_b16 v[148:149], v194 offset:57344\n\t"
    "v_mfma_f32_16x16x32_bf16 v[64:67], v[132:135], a[104:107], v[64:67]\n\t"
    "ds_read_b64_tr_b16 v[150:151], v194 offset:61440\n\t"
    "v_mfma_f32_16x16x32_bf16 v[96:99], v[132:135], a[108:111], v[96:99]\n\t"
    "buffer_load_dwordx4 v243, s[32:35], s84 offen offset:1024 sc1 lds\n\t"
    "v_mfma_f32_16x16x32_bf16 v[96:99], v[128:131], a[76:79], v[96:99]\n\t"
    "s_waitcnt lgkmcnt(4)\n\t"
    "v_mfma_f32_16x16x32_bf16 v[100:103], v[136:139], a[76:79], v[100:103]\n\t"
    "ds_read_b64_tr_b16 v[152:153], v195 offset:49152\n\t"
    "v_mfma_f32_16x16x32_bf16 v[100:103], v[140:143], a[108:111], v[100:103]\n\t"
    "ds_read_b64_tr_b16 v[154:155], v195 offset:53248\n\t"
    "v_mfma_f32_16x16x32_bf16 v[68:71], v[140:143], a[104:107], v[68:71]\n\t"
    "buffer_load_dwordx4 v244, s[32:35], s84 offen offset:2048 sc1 lds\n\t"
    "v_mfma_f32_16x16x32_bf16 v[68:71], v[136:139], a[72:75], v[68:71]\n\t"
    "v_mfma_f32_16x16x32_bf16 v[36:39], v[136:139], a[68:71], v[36:39]\n\t"
    "ds_read_b64_tr_b16 v[156:157], v195 offset:57344\n\t"
    "v_mfma_f32_16x16x32_bf16 v[36:39], v[140:143], a[100:103], v[36:39]\n\t"
    "ds_read_b64_tr_b16 v[158:159], v195 offset:61440\n\t"
    "v_mfma_f32_16x16x32_bf16 v[4:7], v[140:143], a[96:99], v[4:7]\n\t"
    "v_mfma_f32_16x16x32_bf16 v[4:7], v[136:139], a[64:67], v[4:7]\n\t"
    "s_waitcnt lgkmcnt(4)\n\t"
    "v_mfma_f32_16x16x32_bf16 v[8:11], v[144:147], a[64:67], v[8:11]\n\t"
    "ds_read_b64_tr_b16 v[128:129], v196 offset:49152\n\t"
    "v_mfma_f32_16x16x32_bf16 v[8:11], v[148:151], a[96:99], v[8:11]\n\t"
    "ds_read_b64_tr_b16 v[130:131], v196 offset:53248\n\t"
    "v_mfma_f32_16x16x32_bf16 v[40:43], v[148:151], a[100:103], v[40:43]\n\t"
    "buffer_load_dwordx4 v245, s[32:35], s84 offen offset:3072 sc1 lds\n\t"
    "v_mfma_f32_16x16x32_bf16 v[40:43], v[144:147], a[68:71], v[40:43]\n\t"
    "v_mfma_f32_16x16x32_bf16 v[72:75], v[144:147], a[72:75], v[72:75]\n\t"
    "ds_read_b64_tr_b16 v[132:133], v196 offset:57344\n\t"
    "v_mfma_f32_16x16x32_bf16 v[72:75], v[148:151], a[104:107], v[72:75]\n\t"
    "ds_read_b64_tr_b16 v[134:135], v196 offset:61440\n\t"
    "v_mfma_f32_16x16x32_bf16 v[104:107], v[148:151], a[108:111], v[104:107]\n\t"
    "buffer_load_dwordx4 a[176:179], v241, s[88:91], s46 offen sc1\n\t"
    "v_mfma_f32_16x16x32_bf16 v[104:107], v[144:147], a[76:79], v[104:107]\n\t"
    "s_waitcnt lgkmcnt(4)\n\t"
    "v_mfma_f32_16x16x32_bf16 v[108:111], v[152:155], a[76:79], v[108:111]\n\t"
    "ds_read_b64_tr_b16 v[136:137], v197 offset:49152\n\t"
    "v_mfma_f32_16x16x32_bf16 v[108:111], v[156:159], a[108:111], v[108:111]\n\t"
    "ds_read_b64_tr_b16 v[138:139], v197 offset:53248\n\t"
    "v_mfma_f32_16x16x32_bf16 v[76:79], v[156:159], a[104:107], v[76:79]\n\t"
    "buffer_load_dwordx4 a[192:195], v241, s[88:91], s47 offen sc1\n\t"
    "v_mfma_f32_16x16x32_bf16 v[76:79], v[152:155], a[72:75], v[76:79]\n\t"
    "v_mfma_f32_16x16x32_bf16 v[44:47], v[152:155], a[68:71], v[44:47]\n\t"
    "ds_read_b64_tr_b16 v[140:141], v197 offset:57344\n\t"
    "v_mfma_f32_16x16x32_bf16 v[44:47], v[156:159], a[100:103], v[44:47]\n\t"
    "ds_read_b64_tr_b16 v[142:143], v197 offset:61440\n\t"
    "v_mfma_f32_16x16x32_bf16 v[12:15], v[156:159], a[96:99], v[12:15]\n\t"
    "v_mfma_f32_16x16x32_bf16 v[12:15], v[152:155], a[64:67], v[12:15]\n\t"
    "s_waitcnt lgkmcnt(4)\n\t"
    "v_mfma_f32_16x16x32_bf16 v[16:19], v[128:131], a[64:67], v[16:19]\n\t"
    "ds_read_b64_tr_b16 v[144:145], v198 offset:49152\n\t"
    "v_mfma_f32_16x16x32_bf16 v[16:19], v[132:135], a[96:99], v[16:19]\n\t"
    "ds_read_b64_tr_b16 v[146:147], v198 offset:53248\n\t"
    "v_mfma_f32_16x16x32_bf16 v[48:51], v[132:135], a[100:103], v[48:51]\n\t"
    "buffer_load_dwordx4 a[180:183], v246, s[88:91], s46 offen sc1\n\t"
    "v_mfma_f32_16x16x32_bf16 v[48:51], v[128:131], a[68:71], v[48:51]\n\t"
    "v_mfma_f32_16x16x32_bf16 v[80:83], v[128:131], a[72:75], v[80:83]\n\t"
    "ds_read_b64_tr_b16 v[148:149], v198 offset:57344\n\t"
    "v_mfma_f32_16x16x32_bf16 v[80:83], v[132:135], a[104:107], v[80:83]\n\t"
    "ds_read_b64_tr_b16 v[150:151], v198 offset:61440\n\t"
    "v_mfma_f32_16x16x32_bf16 v[112:115], v[132:135], a[108:111], v[112:115]\n\t"
    "buffer_load_dwordx4 a[196:199], v246, s[88:91], s47 offen sc1\n\t"
    "v_mfma_f32_16x16x32_bf16 v[112:115], v[128:131], a[76:79], v[112:115]\n\t"
    "s_waitcnt lgkmcnt(4)\n\t"
    "v_mfma_f32_16x16x32_bf16 v[116:119], v[136:139], a[76:79], v[116:119]\n\t"
    "ds_read_b64_tr_b16 v[152:153], v199 offset:49152\n\t"
    "v_mfma_f32_16x16x32_bf16 v[116:119], v[140:143], a[108:111], v[116:119]\n\t"
    "ds_read_b64_tr_b16 v[154:155], v199 offset:53248\n\t"
    "v_mfma_f32_16x16x32_bf16 v[84:87], v[140:143], a[104:107], v[84:87]\n\t"
    "buffer_load_dwordx4 a[184:187], v247, s[88:91], s46 offen sc1\n\t"
    "v_mfma_f32_16x16x32_bf16 v[84:87], v[136:139], a[72:75], v[84:87]\n\t"
    "v_mfma_f32_16x16x32_bf16 v[52:55], v[136:139], a[68:71], v[52:55]\n\t"
    "ds_read_b64_tr_b16 v[156:157], v199 offset:57344\n\t"
    "v_mfma_f32_16x16x32_bf16 v[52:55], v[140:143], a[100:103], v[52:55]\n\t"
    "ds_read_b64_tr_b16 v[158:159], v199 offset:61440\n\t"
    "v_mfma_f32_16x16x32_bf16 v[20:23], v[140:143], a[96:99], v[20:23]\n\t"
    "v_mfma_f32_16x16x32_bf16 v[20:23], v[136:139], a[64:67], v[20:23]\n\t"
    "s_waitcnt vmcnt(17) lgkmcnt(4)\n\t"
    "s_barrier\n\t"
    "v_mfma_f32_16x16x32_bf16 v[24:27], v[144:147], a[64:67], v[24:27]\n\t"
    "ds_read_b64_tr_b16 v[128:129], v192 offset:0\n\t"
    "v_mfma_f32_16x16x32_bf16 v[24:27], v[148:151], a[96:99], v[24:27]\n\t"
    "ds_read_b64_tr_b16 v[130:131], v192 offset:4096\n\t"
    "v_mfma_f32_16x16x32_bf16 v[56:59], v[148:151], a[100:103], v[56:59]\n\t"
    "buffer_load_dwordx4 a[200:203], v247, s[88:91], s47 offen sc1\n\t"
    "v_mfma_f32_16x16x32_bf16 v[56:59], v[144:147], a[68:71], v[56:59]\n\t"
    "v_mfma_f32_16x16x32_bf16 v[88:91], v[144:147], a[72:75], v[88:91]\n\t"
    "ds_read_b64_tr_b16 v[132:133], v192 offset:8192\n\t"
    "v_mfma_f32_16x16x32_bf16 v[88:91], v[148:151], a[104:107], v[88:91]\n\t"
    "ds_read_b64_tr_b16 v[134:135], v192 offset:12288\n\t"
    "v_mfma_f32_16x16x32_bf16 v[120:123], v[148:151], a[108:111], v[120:123]\n\t"
    "buffer_load_dwordx4 a[188:191], v249, s[88:91], s46 offen sc1\n\t"
    "v_mfma_f32_16x16x32_bf16 v[120:123], v[144:147], a[76:79], v[120:123]\n\t"
    "s_waitcnt lgkmcnt(4)\n\t"
    "v_mfma_f32_16x16x32_bf16 v[124:127], v[152:155], a[76:79], v[124:127]\n\t"
    "ds_read_b64_tr_b16 v[136:137], v193 offset:0\n\t"
    "v_mfma_f32_16x16x32_bf16 v[124:127], v[156:159], a[108:111], v[124:127]\n\t"
    "ds_read_b64_tr_b16 v[138:139], v193 offset:4096\n\t"
    "v_cmp_ne_u32 vcc, s20, v253\n\t"
    "v_mfma_f32_16x16x32_bf16 v[92:95], v[156:159], a[104:107], v[92:95]\n\t"
    "buffer_load_dwordx4 a[204:207], v249, s[88:91], s47 offen sc1\n\t"
    "v_mfma_f32_16x16x32_bf16 v[92:95], v[152:155], a[72:75], v[92:95]\n\t"
    "s_cbranch_vccnz .Lfb_wd\n\t"
    ".Lfb_wd_r:\n\t"
    "buffer_load_dwordx4 a[212:215], v248, s[88:91], s56 offen sc1\n\t"
    "v_mfma_f32_16x16x32_bf16 v[60:63], v[152:155], a[68:71], v[60:63]\n\t"
    "ds_read_b64_tr_b16 v[140:141], v193 offset:8192\n\t"
    "v_mfma_f32_16x16x32_bf16 v[60:63], v[156:159], a[100:103], v[60:63]\n\t"
    "ds_read_b64_tr_b16 v[142:143], v193 offset:12288\n\t"
    "v_mfma_f32_16x16x32_bf16 v[28:31], v[156:159], a[96:99], v[28:31]\n\t"
    "buffer_load_dwordx4 a[216:219], v248, s[88:91], s57 offen sc1\n\t"
    "v_mfma_f32_16x16x32_bf16 v[28:31], v[152:155], a[64:67], v[28:31]\n\t"
    "s_waitcnt vmcnt(14) lgkmcnt(4)\n\t"
    "v_mfma_f32_16x16x32_bf16 v[0:3], v[128:131], a[144:147], v[0:3]\n\t"
    "ds_read_b64_tr_b16 v[144:145], v194 offset:0\n\t"
    "v_mfma_f32_16x16x32_bf16 v[0:3], v[132:135], a[160:163], v[0:3]\n\t"
    "ds_read_b64_tr_b16 v[146:147], v194 offset:4096\n\t"
    "v_mfma_f32_16x16x32_bf16 v[32:35], v[132:135], a[164:167], v[32:35]\n\t"
    "buffer_load_dwordx4 a[220:223], v248, s[88:91], s58 offen sc1\n\t"
    "v_mfma_f32_16x16x32_bf16 v[32:35], v[128:131], a[148:151], v[32:35]\n\t"
    "v_mfma_f32_16x16x32_bf16 v[64:67], v[128:131], a[152:155], v[64:67]\n\t"
    "ds_read_b64_tr_b16 v[148:149], v194 offset:8192\n\t"
    "v_mfma_f32_16x16x32_bf16 v[64:67], v[132:135], a[168:171], v[64:67]\n\t"
    "ds_read_b64_tr_b16 v[150:151], v194 offset:12288\n\t"
    "v_mfma_f32_16x16x32_bf16 v[96:99], v[132:135], a[172:175], v[96:99]\n\t"
    "buffer_load_dwordx4 a[244:247], v248, s[88:91], s56 offen offset:1024 sc1\n\t"
    "v_mfma_f32_16x16x32_bf16 v[96:99], v[128:131], a[156:159], v[96:99]\n\t"
    "s_waitcnt lgkmcnt(4)\n\t"
    "v_mfma_f32_16x16x32_bf16 v[100:103], v[136:139], a[156:159], v[100:103]\n\t"
    "ds_read_b64_tr_b16 v[152:153], v195 offset:0\n\t"
    "v_mfma_f32_16x16x32_bf16 v[100:103], v[140:143], a[172:175], v[100:103]\n\t"
    "ds_read_b64_tr_b16 v[154:155], v195 offset:4096\n\t"
    "v_mfma_f32_16x16x32_bf16 v[68:71], v[140:143], a[168:171], v[68:71]\n\t"
    "buffer_load_dwordx4 a[248:251], v248, s[88:91], s57 offen offset:1024 sc1\n\t"
    "v_mfma_f32_16x16x32_bf16 v[68:71], v[136:139], a[152:155], v[68:71]\n\t"
    "v_mfma_f32_16x16x32_bf16 v[36:39], v[136:139], a[148:151], v[36:39]\n\t"
    "ds_read_b64_tr_b16 v[156:157], v195 offset:8192\n\t"
    "v_mfma_f32_16x16x32_bf16 v[36:39], v[140:143], a[164:167], v[36:39]\n\t"
    "ds_read_b64_tr_b16 v[158:159], v195 offset:12288\n\t"
    "v_mfma_f32_16x16x32_bf16 v[4:7], v[140:143], a[160:163], v[4:7]\n\t"
    "buffer_load_dwordx4 a[252:255], v248, s[88:91], s58 offen offset:1024 sc1\n\t"
    "v_mfma_f32_16x16x32_bf16 v[4:7], v[136:139], a[144:147], v[4:7]\n\t"
    "s_waitcnt lgkmcnt(4)\n\t"
    "v_mfma_f32_16x16x32_bf16 v[8:11], v[144:147], a[144:147], v[8:11]\n\t"
    "ds_read_b64_tr_b16 v[128:129], v196 offset:0\n\t"
    "v_mfma_f32_16x16x32_bf16 v[8:11], v[148:151], a[160:163], v[8:11]\n\t"
    "ds_read_b64_tr_b16 v[130:131], v196 offset:4096\n\t"
    "v_mfma_f32_16x16x32_bf16 v[40:43], v[148:151], a[164:167], v[40:43]\n\t"
    "buffer_load_dwordx4 a[20:23], v248, s[88:91], s56 offen offset:2048 sc1\n\t"
    "v_mfma_f32_16x16x32_bf16 v[40:43], v[144:147], a[148:151], v[40:43]\n\t"
    "v_mfma_f32_16x16x32_bf16 v[72:75], v[144:147], a[152:155], v[72:75]\n\t"
    "ds_read_b64_tr_b16 v[132:133], v196 offset:8192\n\t"
    "v_mfma_f32_16x16x32_bf16 v[72:75], v[148:151], a[168:171], v[72:75]\n\t"
    "ds_read_b64_tr_b16 v[134:135], v196 offset:12288\n\t"
    "v_mfma_f32_16x16x32_bf16 v[104:107], v[148:151], a[172:175], v[104:107]\n\t"
    "buffer_load_dwordx4 a[24:27], v248, s[88:91], s57 offen offset:2048 sc1\n\t"
    "v_mfma_f32_16x16x32_bf16 v[104:107], v[144:147], a[156:159], v[104:107]\n\t"
    "s_waitcnt lgkmcnt(4)\n\t"
    "v_mfma_f32_16x16x32_bf16 v[108:111], v[152:155], a[156:159], v[108:111]\n\t"
    "ds_read_b64_tr_b16 v[136:137], v197 offset:0\n\t"
    "v_mfma_f32_16x16x32_bf16 v[108:111], v[156:159], a[172:175], v[108:111]\n\t"
    "ds_read_b64_tr_b16 v[138:139], v197 offset:4096\n\t"
    "v_mfma_f32_16x16x32_bf16 v[76:79], v[156:159], a[168:171], v[76:79]\n\t"
    "buffer_load_dwordx4 a[28:31], v248, s[88:91], s58 offen offset:2048 sc1\n\t"
    "v_mfma_f32_16x16x32_bf16 v[76:79], v[152:155], a[152:155], v[76:79]\n\t"
    "v_mfma_f32_16x16x32_bf16 v[44:47], v[152:155], a[148:151], v[44:47]\n\t"
    "ds_read_b64_tr_b16 v[140:141], v197 offset:8192\n\t"
    "v_mfma_f32_16x16x32_bf16 v[44:47], v[156:159], a[164:167], v[44:47]\n\t"
    "ds_read_b64_tr_b16 v[142:143], v197 offset:12288\n\t"
    "v_mfma_f32_16x16x32_bf16 v[12:15], v[156:159], a[160:163], v[12:15]\n\t"
    "buffer_load_dwordx4 a[84:87], v248, s[88:91], s56 offen offset:3072 sc1\n\t"
    "v_mfma_f32_16x16x32_bf16 v[12:15], v[152:155], a[144:147], v[12:15]\n\t"
    "s_waitcnt lgkmcnt(4)\n\t"
    "v_mfma_f32_16x16x32_bf16 v[16:19], v[128:131], a[144:147], v[16:19]\n\t"
    "ds_read_b64_tr_b16 v[144:145], v198 offset:0\n\t"
    "v_mfma_f32_16x16x32_bf16 v[16:19], v[132:135], a[160:163], v[16:19]\n\t"
    "ds_read_b64_tr_b16 v[146:147], v198 offset:4096\n\t"
    "v_mfma_f32_16x16x32_bf16 v[48:51], v[132:135], a[164:167], v[48:51]\n\t"
    "buffer_load_dwordx4 a[88:91], v248, s[88:91], s57 offen offset:3072 sc1\n\t"
    "v_mfma_f32_16x16x32_bf16 v[48:51], v[128:131], a[148:151], v[48:51]\n\t"
    "v_mfma_f32_16x16x32_bf16 v[80:83], v[128:131], a[152:155], v[80:83]\n\t"
    "ds_read_b64_tr_b16 v[148:149], v198 offset:8192\n\t"
    "v_mfma_f32_16x16x32_bf16 v[80:83], v[132:135], a[168:171], v[80:83]\n\t"
    "ds_read_b64_tr_b16 v[150:151], v198 offset:12288\n\t"
    "v_mfma_f32_16x16x32_bf16 v[112:115], v[132:135], a[172:175], v[112:115]\n\t"
    "buffer_load_dwordx4 a[92:95], v248, s[88:91], s58 offen offset:3072 sc1\n\t"
    "v_mfma_f32_16x16x32_bf16 v[112:115], v[128:131], a[156:159], v[112:115]\n\t"
    "s_waitcnt lgkmcnt(4)\n\t"
    "s_add_u32 s54, s54, 128\n\t"
    "v_lshrrev_b32 v254, 8, v248\n\t"
    "v_mfma_f32_16x16x32_bf16 v[116:119], v[136:139], a[156:159], v[116:119]\n\t"
    "ds_read_b64_tr_b16 v[152:153], v199 offset:0\n\t"
    "v_and_b32 v242, 240, v248\n\t"
    "v_mfma_f32_16x16x32_bf16 v[116:119], v[140:143], a[172:175], v[116:119]\n\t"
    "ds_read_b64_tr_b16 v[154:155], v199 offset:4096\n\t"
    "v_lshl_add_u32 v242, v254, 11, v242\n\t"
    "v_mfma_f32_16x16x32_bf16 v[84:87], v[140:143], a[168:171], v[84:87]\n\t"
    "v_add_u32 v243, 7168, v242\n\t"
    "v_add_u32 v244, 14336, v242\n\t"
    "v_mfma_f32_16x16x32_bf16 v[84:87], v[136:139], a[152:155], v[84:87]\n\t"
    "v_add_u32 v245, 21504, v242\n\t"
    "v_mfma_f32_16x16x32_bf16 v[52:55], v[136:139], a[148:151], v[52:55]\n\t"
    "ds_read_b64_tr_b16 v[156:157], v199 offset:8192\n\t"
    "v_mfma_f32_16x16x32_bf16 v[52:55], v[140:143], a[164:167], v[52:55]\n\t"
    "ds_read_b64_tr_b16 v[158:159], v199 offset:12288\n\t"
    "v_mfma_f32_16x16x32_bf16 v[20:23], v[140:143], a[160:163], v[20:23]\n\t"
    "v_mfma_f32_16x16x32_bf16 v[20:23], v[136:139], a[144:147], v[20:23]\n\t"
    "s_waitcnt vmcnt(20) lgkmcnt(4)\n\t"
    "s_barrier\n\t"
    "v_mfma_f32_16x16x32_bf16 v[24:27], v[144:147], a[144:147], v[24:27]\n\t"
    "ds_read_b64_tr_b16 v[128:129], v192 offset:16384\n\t"
    "v_mfma_f32_16x16x32_bf16 v[24:27], v[148:151], a[160:163], v[24:27]\n\t"
    "ds_read_b64_tr_b16 v[130:131], v192 offset:20480\n\t"
    "v_mfma_f32_16x16x32_bf16 v[56:59], v[148:151], a[164:167], v[56:59]\n\t"
    "v_mfma_f32_16x16x32_bf16 v[56:59], v[144:147], a[148:151], v[56:59]\n\t"
    "v_mfma_f32_16x16x32_bf16 v[88:91], v[144:147], a[152:155], v[88:91]\n\t"
    "ds_read_b64_tr_b16 v[132:133], v192 offset:24576\n\t"
    "v_mfma_f32_16x16x32_bf16 v[88:91], v[148:151], a[168:171], v[88:91]\n\t"
    "ds_read_b64_tr_b16 v[134:135], v192 offset:28672\n\t"
    "v_mfma_f32_16x16x32_bf16 v[120:123], v[148:151], a[172:175], v[120:123]\n\t"
    "v_mfma_f32_16x16x32_bf16 v[120:123], v[144:147], a[156:159], v[120:123]\n\t"
    "s_waitcnt lgkmcnt(4)\n\t"
    "v_mfma_f32_16x16x32_bf16 v[124:127], v[152:155], a[156:159], v[124:127]\n\t"
    "ds_read_b64_tr_b16 v[136:137], v193 offset:16384\n\t"
    "v_mfma_f32_16x16x32_bf16 v[124:127], v[156:159], a[172:175], v[124:127]\n\t"
    "ds_read_b64_tr_b16 v[138:139], v193 offset:20480\n\t"
    "v_mfma_f32_16x16x32_bf16 v[92:95], v[156:159], a[168:171], v[92:95]\n\t"
    "v_mfma_f32_16x16x32_bf16 v[92:95], v[152:155], a[152:155], v[92:95]\n\t"
    "v_mfma_f32_16x16x32_bf16 v[60:63], v[152:155], a[148:151], v[60:63]\n\t"
    "ds_read_b64_tr_b16 v[140:141], v193 offset:24576\n\t"
    "v_mfma_f32_16x16x32_bf16 v[60:63], v[156:159], a[164:167], v[60:63]\n\t"
    "ds_read_b64_tr_b16 v[142:143], v193 offset:28672\n\t"
    "v_mfma_f32_16x16x32_bf16 v[28:31], v[156:159], a[160:163], v[28:31]\n\t"
    "v_mfma_f32_16x16x32_bf16 v[28:31], v[152:155], a[144:147], v[28:31]\n\t"
    "s_waitcnt vmcnt(12) lgkmcnt(4)\n\t"
    "v_mfma_f32_16x16x32_bf16 v[0:3], v[128:131], a[176:179], v[0:3]\n\t"
    "ds_read_b64_tr_b16 v[144:145], v194 offset:16384\n\t"
    "v_mfma_f32_16x16x32_bf16 v[0:3], v[132:135], a[192:195], v[0:3]\n\t"
    "ds_read_b64_tr_b16 v[146:147], v194 offset:20480\n\t"
    "v_mfma_f32_16x16x32_bf16 v[32:35], v[132:135], a[196:199], v[32:35]\n\t"
    "v_mfma_f32_16x16x32_bf16 v[32:35], v[128:131], a[180:183], v[32:35]\n\t"
    "v_mfma_f32_16x16x32_bf16 v[64:67], v[128:131], a[184:187], v[64:67]\n\t"
    "ds_read_b64_tr_b16 v[148:149], v194 offset:24576\n\t"
    "v_mfma_f32_16x16x32_bf16 v[64:67], v[132:135], a[200:203], v[64:67]\n\t"
    "ds_read_b64_tr_b16 v[150:151], v194 offset:28672\n\t"
    "v_mfma_f32_16x16x32_bf16 v[96:99], v[132:135], a[204:207], v[96:99]\n\t"
    "v_mfma_f32_16x16x32_bf16 v[96:99], v[128:131], a[188:191], v[96:99]\n\t"
    "s_waitcnt lgkmcnt(4)\n\t"
    "v_mfma_f32_16x16x32_bf16 v[100:103], v[136:139], a[188:191], v[100:103]\n\t"
    "ds_read_b64_tr_b16 v[152:153], v195 offset:16384\n\t"
    "v_mfma_f32_16x16x32_bf16 v[100:103], v[140:143], a[204:207], v[100:103]\n\t"
    "ds_read_b64_tr_b16 v[154:155], v195 offset:20480\n\t"
    "v_mfma_f32_16x16x32_bf16 v[68:71], v[140:143], a[200:203], v[68:71]\n\t"
    "v_mfma_f32_16x16x32_bf16 v[68:71], v[136:139], a[184:187], v[68:71]\n\t"
    "v_mfma_f32_16x16x32_bf16 v[36:39], v[136:139], a[180:183], v[36:39]\n\t"
    "ds_read_b64_tr_b16 v[156:157], v195 offset:24576\n\t"
    "v_mfma_f32_16x16x32_bf16 v[36:39], v[140:143], a[196:199], v[36:39]\n\t"
    "ds_read_b64_tr_b16 v[158:159], v195 offset:28672\n\t"
    "v_mfma_f32_16x16x32_bf16 v[4:7], v[140:143], a[192:195], v[4:7]\n\t"
    "v_mfma_f32_16x16x32_bf16 v[4:7], v[136:139], a[176:179], v[4:7]\n\t"
    "s_waitcnt lgkmcnt(4)\n\t"
    "v_mfma_f32_16x16x32_bf16 v[8:11], v[144:147], a[176:179], v[8:11]\n\t"
    "ds_read_b64_tr_b16 v[128:129], v196 offset:16384\n\t"
    "v_mfma_f32_16x16x32_bf16 v[8:11], v[148:151], a[192:195], v[8:11]\n\t"
    "ds_read_b64_tr_b16 v[130:131], v196 offset:20480\n\t"
    "v_mfma_f32_16x16x32_bf16 v[40:43], v[148:151], a[196:199], v[40:43]\n\t"
    "v_mfma_f32_16x16x32_bf16 v[40:43], v[144:147], a[180:183], v[40:43]\n\t"
    "v_mfma_f32_16x16x32_bf16 v[72:75], v[144:147], a[184:187], v[72:75]\n\t"
    "ds_read_b64_tr_b16 v[132:133], v196 offset:24576\n\t"
    "v_mfma_f32_16x16x32_bf16 v[72:75], v[148:151], a[200:203], v[72:75]\n\t"
    "ds_read_b64_tr_b16 v[134:135], v196 offset:28672\n\t"
    "v_cvt_pk_bf16_f32 v32, v32, v33\n\t"
    "v_mfma_f32_16x16x32_bf16 v[104:107], v[148:151], a[204:207], v[104:107]\n\t"
    "v_cvt_pk_bf16_f32 v33, v34, v35\n\t"
    "v_cvt_pk_bf16_f32 v34, v36, v37\n\t"
    "v_mfma_f32_16x16x32_bf16 v[104:107], v[144:147], a[188:191], v[104:107]\n\t"
    "s_waitcnt lgkmcnt(4)\n\t"
    "v_cvt_pk_bf16_f32 v35, v38, v39\n\t"
    "v_mfma_f32_16x16x32_bf16 v[108:111], v[152:155], a[188:191], v[108:111]\n\t"
    "ds_read_b64_tr_b16 v[136:137], v197 offset:16384\n\t"
    "v_mfma_f32_16x16x32_bf16 v[108:111], v[156:159], a[204:207], v[108:111]\n\t"
    "ds_read_b64_tr_b16 v[138:139], v197 offset:20480\n\t"
    "v_mfma_f32_16x16x32_bf16 v[76:79], v[156:159], a[200:203], v[76:79]\n\t"
    "buffer_store_dwordx4 v[32:35], v248, s[88:91], s63 offen sc1\n\t"
    "v_mfma_f32_16x16x32_bf16 v[76:79], v[152:155], a[184:187], v[76:79]\n\t"
    "v_cvt_pk_bf16_f32 v64, v64, v65\n\t"
    "v_cvt_pk_bf16_f32 v65, v66, v67\n\t"
    "v_mfma_f32_16x16x32_bf16 v[44:47], v[152:155], a[180:183], v[44:47]\n\t"
    "ds_read_b64_tr_b16 v[140:141], v197 offset:24576\n\t"
    "v_cvt_pk_bf16_f32 v66, v68, v69\n\t"
    "v_mfma_f32_16x16x32_bf16 v[44:47], v[156:159], a[196:199], v[44:47]\n\t"
    "ds_read_b64_tr_b16 v[142:143], v197 offset:28672\n\t"
    "v_cvt_pk_bf16_f32 v67, v70, v71\n\t"
    "v_mfma_f32_16x16x32_bf16 v[12:15], v[156:159], a[192:195], v[12:15]\n\t"
    "buffer_store_dwordx4 v[64:67], v248, s[88:91], s64 offen sc1\n\t"
    "v_mfma_f32_16x16x32_bf16 v[12:15], v[152:155], a[176:179], v[12:15]\n\t"
    "s_waitcnt lgkmcnt(4)\n\t"
    "v_cvt_pk_bf16_f32 v96, v96, v97\n\t"
    "v_cvt_pk_bf16_f32 v97, v98, v99\n\t"
    "v_mfma_f32_16x16x32_bf16 v[48:51], v[128:131], a[180:183], v[48:51]\n\t"
    "ds_read_b64_tr_b16 v[144:145], v198 offset:16384\n\t"
    "v_cvt_pk_bf16_f32 v98, v100, v101\n\t"
    "v_mfma_f32_16x16x32_bf16 v[48:51], v[132:135], a[196:199], v[48:51]\n\t"
    "ds_read_b64_tr_b16 v[146:147], v198 offset:20480\n\t"
    "v_cvt_pk_bf16_f32 v99, v102, v103\n\t"
    "v_mfma_f32_16x16x32_bf16 v[80:83], v[132:135], a[200:203], v[80:83]\n\t"
    "ds_read_b64_tr_b16 v[148:149], v198 offset:24576\n\t"
    "v_mfma_f32_16x16x32_bf16 v[80:83], v[128:131], a[184:187], v[80:83]\n\t"
    "ds_read_b64_tr_b16 v[150:151], v198 offset:28672\n\t"
    "v_mfma_f32_16x16x32_bf16 v[112:115], v[128:131], a[188:191], v[112:115]\n\t"
    "buffer_store_dwordx4 v[96:99], v248, s[88:91], s65 offen sc1\n\t"
    "v_mfma_f32_16x16x32_bf16 v[112:115], v[132:135], a[204:207], v[112:115]\n\t"
    "s_waitcnt lgkmcnt(4)\n\t"
    "v_cvt_pk_bf16_f32 v40, v40, v41\n\t"
    "v_cvt_pk_bf16_f32 v41, v42, v43\n\t"
    "v_mfma_f32_16x16x32_bf16 v[116:119], v[136:139], a[188:191], v[116:119]\n\t"
    "ds_read_b64_tr_b16 v[152:153], v199 offset:16384\n\t"
    "v_cvt_pk_bf16_f32 v42, v44, v45\n\t"
    "v_mfma_f32_16x16x32_bf16 v[116:119], v[140:143], a[204:207], v[116:119]\n\t"
    "ds_read_b64_tr_b16 v[154:155], v199 offset:20480\n\t"
    "v_cvt_pk_bf16_f32 v43, v46, v47\n\t"
    "v_mfma_f32_16x16x32_bf16 v[84:87], v[140:143], a[200:203], v[84:87]\n\t"
    "ds_read_b64_tr_b16 v[156:157], v199 offset:24576\n\t"
    "v_mfma_f32_16x16x32_bf16 v[84:87], v[136:139], a[184:187], v[84:87]\n\t"
    "ds_read_b64_tr_b16 v[158:159], v199 offset:28672\n\t"
    "v_mfma_f32_16x16x32_bf16 v[52:55], v[136:139], a[180:183], v[52:55]\n\t"
    "buffer_store_dwordx4 v[40:43], v248, s[88:91], s63 offen offset:1024 sc1\n\t"
    "v_mfma_f32_16x16x32_bf16 v[52:55], v[140:143], a[196:199], v[52:55]\n\t"
    "s_waitcnt lgkmcnt(4)\n\t"
    "v_cvt_pk_bf16_f32 v72, v72, v73\n\t"
    "v_cvt_pk_bf16_f32 v73, v74, v75\n\t"
    "v_mfma_f32_16x16x32_bf16 v[56:59], v[144:147], a[180:183], v[56:59]\n\t"
    "v_cvt_pk_bf16_f32 v74, v76, v77\n\t"
    "v_cvt_pk_bf16_f32 v75, v78, v79\n\t"
    "v_mfma_f32_16x16x32_bf16 v[56:59], v[148:151], a[196:199], v[56:59]\n\t"
    "buffer_store_dwordx4 v[72:75], v248, s[88:91], s64 offen offset:1024 sc1\n\t"
    "v_mfma_f32_16x16x32_bf16 v[88:91], v[148:151], a[200:203], v[88:91]\n\t"
    "v_cvt_pk_bf16_f32 v104, v104, v105\n\t"
    "v_cvt_pk_bf16_f32 v105, v106, v107\n\t"
    "v_mfma_f32_16x16x32_bf16 v[88:91], v[144:147], a[184:187], v[88:91]\n\t"
    "v_cvt_pk_bf16_f32 v106, v108, v109\n\t"
    "v_cvt_pk_bf16_f32 v107, v110, v111\n\t"
    "v_mfma_f32_16x16x32_bf16 v[120:123], v[144:147], a[188:191], v[120:123]\n\t"
    "buffer_store_dwordx4 v[104:107], v248, s[88:91], s65 offen offset:1024 sc1\n\t"
    "v_mfma_f32_16x16x32_bf16 v[120:123], v[148:151], a[204:207], v[120:123]\n\t"
    "s_waitcnt lgkmcnt(0)\n\t"
    "v_cvt_pk_bf16_f32 v48, v48, v49\n\t"
    "v_cvt_pk_bf16_f32 v49, v50, v51\n\t"
    "v_mfma_f32_16x16x32_bf16 v[124:127], v[152:155], a[188:191], v[124:127]\n\t"
    "v_cvt_pk_bf16_f32 v50, v52, v53\n\t"
    "v_cvt_pk_bf16_f32 v51, v54, v55\n\t"
    "v_mfma_f32_16x16x32_bf16 v[124:127], v[156:159], a[204:207], v[124:127]\n\t"
    "buffer_store_dwordx4 v[48:51], v248, s[88:91], s63 offen offset:2048 sc1\n\t"
    "v_mfma_f32_16x16x32_bf16 v[92:95], v[156:159], a[200:203], v[92:95]\n\t"
    "v_cvt_pk_bf16_f32 v80, v80, v81\n\t"
    "v_cvt_pk_bf16_f32 v81, v82, v83\n\t"
    "v_mfma_f32_16x16x32_bf16 v[92:95], v[152:155], a[184:187], v[92:95]\n\t"
    "v_cvt_pk_bf16_f32 v82, v84, v85\n\t"
    "v_cvt_pk_bf16_f32 v83, v86, v87\n\t"
    "v_mfma_f32_16x16x32_bf16 v[60:63], v[152:155], a[180:183], v[60:63]\n\t"
    "buffer_store_dwordx4 v[80:83], v248, s[88:91], s64 offen offset:2048 sc1\n\t"
    "v_mfma_f32_16x16x32_bf16 v[60:63], v[156:159], a[196:199], v[60:63]\n\t"
    "v_cvt_pk_bf16_f32 v112, v112, v113\n\t"
    "v_cvt_pk_bf16_f32 v113, v114, v115\n\t"
    "v_mfma_f32_16x16x32_bf16 v[16:19], v[128:131], a[176:179], v[16:19]\n\t"
    "v_cvt_pk_bf16_f32 v114, v116, v117\n\t"
    "v_cvt_pk_bf16_f32 v115, v118, v119\n\t"
    "v_mfma_f32_16x16x32_bf16 v[16:19], v[132:135], a[192:195], v[16:19]\n\t"
    "buffer_store_dwordx4 v[112:115], v248, s[88:91], s65 offen offset:2048 sc1\n\t"
    "v_mfma_f32_16x16x32_bf16 v[20:23], v[136:139], a[176:179], v[20:23]\n\t"
    "v_mfma_f32_16x16x32_bf16 v[20:23], v[140:143], a[192:195], v[20:23]\n\t"
    "v_mfma_f32_16x16x32_bf16 v[24:27], v[144:147], a[176:179], v[24:27]\n\t"
    "v_mfma_f32_16x16x32_bf16 v[24:27], v[148:151], a[192:195], v[24:27]\n\t"
    "v_cvt_pk_bf16_f32 v56, v56, v57\n\t"
    "v_cvt_pk_bf16_f32 v57, v58, v59\n\t"
    "v_mfma_f32_16x16x32_bf16 v[28:31], v[152:155], a[176:179], v[28:31]\n\t"
    "v_cvt_pk_bf16_f32 v58, v60, v61\n\t"
    "v_cvt_pk_bf16_f32 v59, v62, v63\n\t"
    "v_mfma_f32_16x16x32_bf16 v[28:31], v[156:159], a[192:195], v[28:31]\n\t"
    "s_waitcnt vmcnt(9)\n\t"
    "buffer_store_dwordx4 v[56:59], v248, s[88:91], s63 offen offset:3072 sc1\n\t"
    "v_mfma_f32_16x16x32_bf16 a[208:211], v[234:237], a[212:215], a[208:211]\n\t"
    "v_cvt_pk_bf16_f32 v88, v88, v89\n\t"
    "v_cvt_pk_bf16_f32 v89, v90, v91\n\t"
    "v_mfma_f32_16x16x32_bf16 a[224:227], v[232:235], a[212:215], a[224:227]\n\t"
    "v_cvt_pk_bf16_f32 v90, v92, v93\n\t"
    "v_cvt_pk_bf16_f32 v91, v94, v95\n\t"
    "v_mfma_f32_16x16x32_bf16 a[208:211], v[234:237], a[216:219], a[208:211]\n\t"
    "buffer_store_dwordx4 v[88:91], v248, s[88:91], s64 offen offset:3072 sc1\n\t"
    "v_mfma_f32_16x16x32_bf16 a[224:227], v[232:235], a[216:219], a[224:227]\n\t"
    "v_cvt_pk_bf16_f32 v120, v120, v121\n\t"
    "v_cvt_pk_bf16_f32 v121, v122, v123\n\t"
    "v_mfma_f32_16x16x32_bf16 a[208:211], v[234:237], a[220:223], a[208:211]\n\t"
    "v_cvt_pk_bf16_f32 v122, v124, v125\n\t"
    "v_cvt_pk_bf16_f32 v123, v126, v127\n\t"
    "v_mfma_f32_16x16x32_bf16 a[224:227], v[232:235], a[220:223], a[224:227]\n\t"
    "buffer_store_dwordx4 v[120:123], v248, s[88:91], s65 offen offset:3072 sc1\n\t"
    "v_mfma_f32_16x16x32_bf16 a[240:243], v[234:237], a[244:247], a[240:243]\n\t"
    "v_mfma_f32_16x16x32_bf16 a[128:131], v[232:235], a[244:247], a[128:131]\n\t"
    "v_mfma_f32_16x16x32_bf16 a[240:243], v[234:237], a[248:251], a[240:243]\n\t"
    "v_mfma_f32_16x16x32_bf16 a[128:131], v[232:235], a[248:251], a[128:131]\n\t"
    "v_mfma_f32_16x16x32_bf16 a[240:243], v[234:237], a[252:255], a[240:243]\n\t"
    "v_mfma_f32_16x16x32_bf16 a[128:131], v[232:235], a[252:255], a[128:131]\n\t"
    "v_mfma_f32_16x16x32_bf16 a[16:19], v[234:237], a[20:23], a[16:19]\n\t"
    "v_mfma_f32_16x16x32_bf16 a[48:51], v[232:235], a[20:23], a[48:51]\n\t"
    "v_mfma_f32_16x16x32_bf16 a[16:19], v[234:237], a[24:27], a[16:19]\n\t"
    "v_mfma_f32_16x16x32_bf16 a[48:51], v[232:235], a[24:27], a[48:51]\n\t"
    "v_mfma_f32_16x16x32_bf16 a[16:19], v[234:237], a[28:31], a[16:19]\n\t"
    "v_mfma_f32_16x16x32_bf16 a[48:51], v[232:235], a[28:31], a[48:51]\n\t"
    "v_mfma_f32_16x16x32_bf16 a[80:83], v[234:237], a[84:87], a[80:83]\n\t"
    "v_mfma_f32_16x16x32_bf16 a[112:115], v[232:235], a[84:87], a[112:115]\n\t"
    "v_mfma_f32_16x16x32_bf16 a[80:83], v[234:237], a[88:91], a[80:83]\n\t"
    "v_mfma_f32_16x16x32_bf16 a[112:115], v[232:235], a[88:91], a[112:115]\n\t"
    "v_mfma_f32_16x16x32_bf16 a[80:83], v[234:237], a[92:95], a[80:83]\n\t"
    "v_mfma_f32_16x16x32_bf16 a[112:115], v[232:235], a[92:95], a[112:115]\n\t"
    "buffer_load_dword v253, v252, s[88:91], s54 offen sc1\n\t"
    "s_add_u32 s63, s62, 0\n\t"
    "s_add_u32 s64, s62, 4096\n\t"
    "s_add_u32 s65, s62, 8192\n\t"
    "s_mov_b32 s69, s68\n\t"
    "s_mov_b32 s70, 1.0\n\t"
    "s_mov_b32 s71, 1.0\n\t"
    ".p2align 3\n\t"
    "v_accvgpr_read_b32 v200, a208\n\t"
    "v_accvgpr_read_b32 v201, a209\n\t"
    "v_accvgpr_read_b32 v202, a210\n\t"
    "v_accvgpr_read_b32 v203, a211\n\t"
    "v_accvgpr_read_b32 v204, a224\n\t"
    "v_accvgpr_read_b32 v205, a225\n\t"
    "v_accvgpr_read_b32 v206, a226\n\t"
    "v_accvgpr_read_b32 v207, a227\n\t"
    "v_pk_mul_f32 v[128:129], v[200:201], s[68:69]\n\t"
    "v_pk_mul_f32 v[130:131], v[202:203], s[68:69]\n\t"
    "v_pk_mul_f32 v[132:133], v[204:205], s[68:69]\n\t"
    "v_pk_mul_f32 v[134:135], v[206:207], s[68:69]\n\t"
    "v_exp_f32 v128, v128\n\t"
    "v_exp_f32 v129, v129\n\t"
    "v_exp_f32 v130, v130\n\t"
    "v_exp_f32 v131, v131\n\t"
    "v_exp_f32 v132, v132\n\t"
    "v_exp_f32 v133, v133\n\t"
    "v_exp_f32 v134, v134\n\t"
    "v_exp_f32 v135, v135\n\t"
    "v_pk_add_f32 v[128:129], v[128:129], s[70:71]\n\t"
    "v_pk_add_f32 v[130:131], v[130:131], s[70:71]\n\t"
    "v_pk_add_f32 v[132:133], v[132:133], s[70:71]\n\t"
    "v_pk_add_f32 v[134:135], v[134:135], s[70:71]\n\t"
    "v_rcp_f32 v128, v128\n\t"
    "v_rcp_f32 v129, v129\n\t"
    "v_rcp_f32 v130, v130\n\t"
    "v_rcp_f32 v131, v131\n\t"
    "v_rcp_f32 v132, v132\n\t"
    "v_rcp_f32 v133, v133\n\t"
    "v_rcp_f32 v134, v134\n\t"
    "v_rcp_f32 v135, v135\n\t"
    "v_pk_mul_f32 v[200:201], v[128:129], v[200:201]\n\t"
    "v_pk_mul_f32 v[202:203], v[130:131], v[202:203]\n\t"
    "v_pk_mul_f32 v[204:205], v[132:133], v[204:205]\n\t"
    "v_pk_mul_f32 v[206:207], v[134:135], v[206:207]\n\t"
    "s_waitcnt vmcnt(1)\n\t"
    "s_lshl_b32 s77, s49, 2\n\t"
    "v_mov_b32 v254, s77\n\t"
    "v_mov_b32 v255, s20\n\t"
    "buffer_store_dword v255, v254, s[88:91], s54 offen sc1\n\t"
    "s_waitcnt vmcnt(1)\n\t"
    "v_cmp_ne_u32 vcc, s20, v253\n\t"
    "s_nop 3\n\t"
    "s_cbranch_vccnz .Lfb_late\n\t"
    "buffer_load_dwordx4 a[228:231], v248, s[88:91], s63 offen sc1\n\t"
    "buffer_load_dwordx4 a[232:235], v248, s[88:91], s64 offen sc1\n\t"
    "buffer_load_dwordx4 a[236:239], v248, s[88:91], s65 offen sc1\n\t"
    "buffer_load_dwordx4 a[132:135], v248, s[88:91], s63 offen offset:1024 sc1\n\t"
    "buffer_load_dwordx4 a[136:139], v248, s[88:91], s64 offen offset:1024 sc1\n\t"
    "buffer_load_dwordx4 a[140:143], v248, s[88:91], s65 offen offset:1024 sc1\n\t"
    "buffer_load_dwordx4 a[52:55], v248, s[88:91], s63 offen offset:2048 sc1\n\t"
    "buffer_load_dwordx4 a[56:59], v248, s[88:91], s64 offen offset:2048 sc1\n\t"
    "buffer_load_dwordx4 a[60:63], v248, s[88:91], s65 offen offset:2048 sc1\n\t"
    "buffer_load_dwordx4 a[116:119], v248, s[88:91], s63 offen offset:3072 sc1\n\t"
    "buffer_load_dwordx4 a[120:123], v248, s[88:91], s64 offen offset:3072 sc1\n\t"
    "buffer_load_dwordx4 a[124:127], v248, s[88:91], s65 offen offset:3072 sc1\n\t"
    ".p2align 3\n\t"
    "v_accvgpr_read_b32 v208, a240\n\t"
    "v_accvgpr_read_b32 v209, a241\n\t"
    "v_accvgpr_read_b32 v210, a242\n\t"
    "v_accvgpr_read_b32 v211, a243\n\t"
    "v_accvgpr_read_b32 v212, a128\n\t"
    "v_accvgpr_read_b32 v213, a129\n\t"
    "v_accvgpr_read_b32 v214, a130\n\t"
    "v_accvgpr_read_b32 v215, a131\n\t"
    "v_pk_mul_f32 v[128:129], v[208:209], s[68:69]\n\t"
    "v_pk_mul_f32 v[130:131], v[210:211], s[68:69]\n\t"
    "v_pk_mul_f32 v[132:133], v[212:213], s[68:69]\n\t"
    "v_pk_mul_f32 v[134:135], v[214:215], s[68:69]\n\t"
    "v_exp_f32 v128, v128\n\t"
    "v_exp_f32 v129, v129\n\t"
    "v_exp_f32 v130, v130\n\t"
    "v_exp_f32 v131, v131\n\t"
    "v_exp_f32 v132, v132\n\t"
    "v_exp_f32 v133, v133\n\t"
    "v_exp_f32 v134, v134\n\t"
    "v_exp_f32 v135, v135\n\t"
    "v_pk_add_f32 v[128:129], v[128:129], s[70:71]\n\t"
    "v_pk_add_f32 v[130:131], v[130:131], s[70:71]\n\t"
    "v_pk_add_f32 v[132:133], v[132:133], s[70:71]\n\t"
    "v_pk_add_f32 v[134:135], v[134:135], s[70:71]\n\t"
    "v_rcp_f32 v128, v128\n\t"
    "v_rcp_f32 v129, v129\n\t"
    "v_rcp_f32 v130, v130\n\t"
    "v_rcp_f32 v131, v131\n\t"
    "v_rcp_f32 v132, v132\n\t"
    "v_rcp_f32 v133, v133\n\t"
    "v_rcp_f32 v134, v134\n\t"
    "v_rcp_f32 v135, v135\n\t"
    "v_pk_mul_f32 v[208:209], v[128:129], v[208:209]\n\t"
    "v_pk_mul_f32 v[210:211], v[130:131], v[210:211]\n\t"
    "v_pk_mul_f32 v[212:213], v[132:133], v[212:213]\n\t"
    "v_pk_mul_f32 v[214:215], v[134:135], v[214:215]\n\t"
    "v_accvgpr_read_b32 v216, a16\n\t"
    "v_accvgpr_read_b32 v217, a17\n\t"
    "v_accvgpr_read_b32 v218, a18\n\t"
    "v_accvgpr_read_b32 v219, a19\n\t"
    "v_accvgpr_read_b32 v220, a48\n\t"
    "v_accvgpr_read_b32 v221, a49\n\t"
    "v_accvgpr_read_b32 v222, a50\n\t"
    "v_accvgpr_read_b32 v223, a51\n\t"
    "v_pk_mul_f32 v[128:129], v[216:217], s[68:69]\n\t"
    "v_pk_mul_f32 v[130:131], v[218:219], s[68:69]\n\t"
    "v_pk_mul_f32 v[132:133], v[220:221], s[68:69]\n\t"
    "v_pk_mul_f32 v[134:135], v[222:223], s[68:69]\n\t"
    "v_exp_f32 v128, v128\n\t"
    "v_exp_f32 v129, v129\n\t"
    "v_exp_f32 v130, v130\n\t"
    "v_exp_f32 v131, v131\n\t"
    "v_exp_f32 v132, v132\n\t"
    "v_exp_f32 v133, v133\n\t"
    "v_exp_f32 v134, v134\n\t"
    "v_exp_f32 v135, v135\n\t"
    "v_pk_add_f32 v[128:129], v[128:129], s[70:71]\n\t"
    "v_pk_add_f32 v[130:131], v[130:131], s[70:71]\n\t"
    "v_pk_add_f32 v[132:133], v[132:133], s[70:71]\n\t"
    "v_pk_add_f32 v[134:135], v[134:135], s[70:71]\n\t"
    "v_rcp_f32 v128, v128\n\t"
    "v_rcp_f32 v129, v129\n\t"
    "v_rcp_f32 v130, v130\n\t"
    "v_rcp_f32 v131, v131\n\t"
    "v_rcp_f32 v132, v132\n\t"
    "v_rcp_f32 v133, v133\n\t"
    "v_rcp_f32 v134, v134\n\t"
    "v_rcp_f32 v135, v135\n\t"
    "v_pk_mul_f32 v[216:217], v[128:129], v[216:217]\n\t"
    "v_pk_mul_f32 v[218:219], v[130:131], v[218:219]\n\t"
    "v_pk_mul_f32 v[220:221], v[132:133], v[220:221]\n\t"
    "v_pk_mul_f32 v[222:223], v[134:135], v[222:223]\n\t"
    "v_accvgpr_read_b32 v224, a80\n\t"
    "v_accvgpr_read_b32 v225, a81\n\t"
    "v_accvgpr_read_b32 v226, a82\n\t"
    "v_accvgpr_read_b32 v227, a83\n\t"
    "v_accvgpr_read_b32 v228, a112\n\t"
    "v_accvgpr_read_b32 v229, a113\n\t"
    "v_accvgpr_read_b32 v230, a114\n\t"
    "v_accvgpr_read_b32 v231, a115\n\t"
    "v_pk_mul_f32 v[128:129], v[224:225], s[68:69]\n\t"
    "v_pk_mul_f32 v[130:131], v[226:227], s[68:69]\n\t"
    "v_pk_mul_f32 v[132:133], v[228:229], s[68:69]\n\t"
    "v_pk_mul_f32 v[134:135], v[230:231], s[68:69]\n\t"
    "v_exp_f32 v128, v128\n\t"
    "v_exp_f32 v129, v129\n\t"
    "v_exp_f32 v130, v130\n\t"
    "v_exp_f32 v131, v131\n\t"
    "v_exp_f32 v132, v132\n\t"
    "v_exp_f32 v133, v133\n\t"
    "v_exp_f32 v134, v134\n\t"
    "v_exp_f32 v135, v135\n\t"
    "v_pk_add_f32 v[128:129], v[128:129], s[70:71]\n\t"
    "v_pk_add_f32 v[130:131], v[130:131], s[70:71]\n\t"
    "v_pk_add_f32 v[132:133], v[132:133], s[70:71]\n\t"
    "v_pk_add_f32 v[134:135], v[134:135], s[70:71]\n\t"
    "v_rcp_f32 v128, v128\n\t"
    "v_rcp_f32 v129, v129\n\t"
    "v_rcp_f32 v130, v130\n\t"
    "v_rcp_f32 v131, v131\n\t"
    "v_rcp_f32 v132, v132\n\t"
    "v_rcp_f32 v133, v133\n\t"
    "v_rcp_f32 v134, v134\n\t"
    "v_rcp_f32 v135, v135\n\t"
    "v_pk_mul_f32 v[224:225], v[128:129], v[224:225]\n\t"
    "v_pk_mul_f32 v[226:227], v[130:131], v[226:227]\n\t"
    "v_pk_mul_f32 v[228:229], v[132:133], v[228:229]\n\t"
    "v_pk_mul_f32 v[230:231], v[134:135], v[230:231]\n\t"
    ".Lfb_fin:\n\t"
    "s_waitcnt vmcnt(11)\n\t"
    "v_mfma_f32_16x16x32_bf16 v[0:3], v[234:237], a[228:231], v[0:3]\n\t"
    "v_mfma_f32_16x16x32_bf16 v[4:7], v[232:235], a[228:231], v[4:7]\n\t"
    "s_waitcnt vmcnt(10)\n\t"
    "v_mfma_f32_16x16x32_bf16 v[0:3], v[234:237], a[232:235], v[0:3]\n\t"
    "v_mfma_f32_16x16x32_bf16 v[4:7], v[232:235], a[232:235], v[4:7]\n\t"
    "s_waitcnt vmcnt(9)\n\t"
    "v_mfma_f32_16x16x32_bf16 v[0:3], v[234:237], a[236:239], v[0:3]\n\t"
    "v_mfma_f32_16x16x32_bf16 v[4:7], v[232:235], a[236:239], v[4:7]\n\t"
    "s_waitcnt vmcnt(8)\n\t"
    "v_mfma_f32_16x16x32_bf16 v[8:11], v[234:237], a[132:135], v[8:11]\n\t"
    "v_mfma_f32_16x16x32_bf16 v[12:15], v[232:235], a[132:135], v[12:15]\n\t"
    "s_waitcnt vmcnt(7)\n\t"
    "v_mfma_f32_16x16x32_bf16 v[8:11], v[234:237], a[136:139], v[8:11]\n\t"
    "v_mfma_f32_16x16x32_bf16 v[12:15], v[232:235], a[136:139], v[12:15]\n\t"
    "s_waitcnt vmcnt(6)\n\t"
    "v_mfma_f32_16x16x32_bf16 v[8:11], v[234:237], a[140:143], v[8:11]\n\t"
    "v_mfma_f32_16x16x32_bf16 v[12:15], v[232:235], a[140:143], v[12:15]\n\t"
    "v_pk_fma_f32 v[160:161], v[0:1], v[200:201], v[160:161]\n\t"
    "v_pk_fma_f32 v[162:163], v[2:3], v[202:203], v[162:163]\n\t"
    "v_cvt_pk_bf16_f32 v128, v160, v161\n\t"
    "v_cvt_pk_bf16_f32 v129, v162, v163\n\t"
    "ds_write_b64 v238, v[128:129] offset:0\n\t"
    "v_pk_fma_f32 v[164:165], v[4:5], v[204:205], v[164:165]\n\t"
    "v_pk_fma_f32 v[166:167], v[6:7], v[206:207], v[166:167]\n\t"
    "v_cvt_pk_bf16_f32 v130, v164, v165\n\t"
    "v_cvt_pk_bf16_f32 v131, v166, v167\n\t"
    "ds_write_b64 v238, v[130:131] offset:32\n\t"
    "s_waitcnt vmcnt(5)\n\t"
    "v_mfma_f32_16x16x32_bf16 v[16:19], v[234:237], a[52:55], v[16:19]\n\t"
    "v_mfma_f32_16x16x32_bf16 v[20:23], v[232:235], a[52:55], v[20:23]\n\t"
    "s_waitcnt vmcnt(4)\n\t"
    "v_mfma_f32_16x16x32_bf16 v[16:19], v[234:237], a[56:59], v[16:19]\n\t"
    "v_mfma_f32_16x16x32_bf16 v[20:23], v[232:235], a[56:59], v[20:23]\n\t"
    "s_waitcnt vmcnt(3)\n\t"
    "v_mfma_f32_16x16x32_bf16 v[16:19], v[234:237], a[60:63], v[16:19]\n\t"
    "v_mfma_f32_16x16x32_bf16 v[20:23], v[232:235], a[60:63], v[20:23]\n\t"
    "v_pk_fma_f32 v[168:169], v[8:9], v[208:209], v[168:169]\n\t"
    "v_pk_fma_f32 v[170:171], v[10:11], v[210:211], v[170:171]\n\t"
    "v_cvt_pk_bf16_f32 v132, v168, v169\n\t"
    "v_cvt_pk_bf16_f32 v133, v170, v171\n\t"
    "ds_write_b64 v238, v[132:133] offset:64\n\t"
    "v_pk_fma_f32 v[172:173], v[12:13], v[212:213], v[172:173]\n\t"
    "v_pk_fma_f32 v[174:175], v[14:15], v[214:215], v[174:175]\n\t"
    "v_cvt_pk_bf16_f32 v134, v172, v173\n\t"
    "v_cvt_pk_bf16_f32 v135, v174, v175\n\t"
    "ds_write_b64 v238, v[134:135] offset:96\n\t"
    "s_waitcnt vmcnt(2)\n\t"
    "v_mfma_f32_16x16x32_bf16 v[24:27], v[234:237], a[116:119], v[24:27]\n\t"
    "v_mfma_f32_16x16x32_bf16 v[28:31], v[232:235], a[116:119], v[28:31]\n\t"
    "s_waitcnt vmcnt(1)\n\t"
    "v_mfma_f32_16x16x32_bf16 v[24:27], v[234:237], a[120:123], v[24:27]\n\t"
    "v_mfma_f32_16x16x32_bf16 v[28:31], v[232:235], a[120:123], v[28:31]\n\t"
    "s_waitcnt vmcnt(0)\n\t"
    "v_mfma_f32_16x16x32_bf16 v[24:27], v[234:237], a[124:127], v[24:27]\n\t"
    "v_mfma_f32_16x16x32_bf16 v[28:31], v[232:235], a[124:127], v[28:31]\n\t"
    "v_pk_fma_f32 v[176:177], v[16:17], v[216:217], v[176:177]\n\t"
    "v_pk_fma_f32 v[178:179], v[18:19], v[218:219], v[178:179]\n\t"
    "v_cvt_pk_bf16_f32 v136, v176, v177\n\t"
    "v_cvt_pk_bf16_f32 v137, v178, v179\n\t"
    "ds_write_b64 v238, v[136:137] offset:128\n\t"
    "v_pk_fma_f32 v[180:181], v[20:21], v[220:221], v[180:181]\n\t"
    "v_pk_fma_f32 v[182:183], v[22:23], v[222:223], v[182:183]\n\t"
    "v_cvt_pk_bf16_f32 v138, v180, v181\n\t"
    "v_cvt_pk_bf16_f32 v139, v182, v183\n\t"
    "ds_write_b64 v238, v[138:139] offset:160\n\t"
    "v_pk_fma_f32 v[184:185], v[24:25], v[224:225], v[184:185]\n\t"
    "v_pk_fma_f32 v[186:187], v[26:27], v[226:227], v[186:187]\n\t"
    "v_cvt_pk_bf16_f32 v140, v184, v185\n\t"
    "v_cvt_pk_bf16_f32 v141, v186, v187\n\t"
    "ds_write_b64 v238, v[140:141] offset:192\n\t"
    "v_pk_fma_f32 v[188:189], v[28:29], v[228:229], v[188:189]\n\t"
    "v_pk_fma_f32 v[190:191], v[30:31], v[230:231], v[190:191]\n\t"
    "v_cvt_pk_bf16_f32 v142, v188, v189\n\t"
    "v_cvt_pk_bf16_f32 v143, v190, v191\n\t"
    "ds_write_b64 v238, v[142:143] offset:224\n\t"
    "ds_read_b128 v[144:147], v239 offset:0\n\t"
    "ds_read_b128 v[148:151], v239 offset:1088\n\t"
    "ds_read_b128 v[152:155], v239 offset:2176\n\t"
    "ds_read_b128 v[156:159], v239 offset:3264\n\t"
    "s_waitcnt lgkmcnt(3)\n\t"
    "buffer_store_dwordx4 v[144:147], v242, s[36:39], s53 offen nt\n\t"
    "s_waitcnt lgkmcnt(2)\n\t"
    "buffer_store_dwordx4 v[148:151], v243, s[36:39], s53 offen offset:1024 nt\n\t"
    "s_waitcnt lgkmcnt(1)\n\t"
    "buffer_store_dwordx4 v[152:155], v244, s[36:39], s53 offen offset:2048 nt\n\t"
    "s_waitcnt lgkmcnt(0)\n\t"
    "buffer_store_dwordx4 v[156:159], v245, s[36:39], s53 offen offset:3072 nt\n\t"
    "s_endpgm\n\t"
    ".Lfb_wk:\n\t"
    "s_mov_b32 s77, 0\n\t"
    "13:\n\t"
    "buffer_load_dword v230, v231, s[88:91], 0 offen sc1\n\t"
    "s_waitcnt vmcnt(0)\n\t"
    "v_cmp_ne_u32 vcc, s20, v230\n\t"
    "s_add_u32 s77, s77, 1\n\t"
    "s_nop 2\n\t"
    "s_cbranch_vccz 14f\n\t"
    "s_sleep 1\n\t"
    "s_cmp_lt_u32 s77, 131072\n\t"
    "s_cbranch_scc1 13b\n\t"
    "14:\n\t"
    "s_branch .Lfb_wk_r\n\t"
    ".Lfb_fh:\n\t"
    "v_lshrrev_b32 v254, 4, v248\n\t"
    "v_min_u32 v254, 15, v254\n\t"
    "v_lshlrev_b32 v254, 11, v254\n\t"
    "v_mov_b32 v255, s20\n\t"
    "buffer_store_dword v255, v254, s[88:91], s22 offen sc1\n\t"
    "s_branch .Lfb_fh_r\n\t"
    ".Lfb_wh:\n\t"
    "s_mov_b32 s77, 0\n\t"
    "16:\n\t"
    "buffer_load_dword v253, v252, s[88:91], 0 offen sc1\n\t"
    "s_waitcnt vmcnt(0)\n\t"
    "v_cmp_ne_u32 vcc, s20, v253\n\t"
    "s_add_u32 s77, s77, 1\n\t"
    "s_nop 2\n\t"
    "s_cbranch_vccz 17f\n\t"
    "s_sleep 1\n\t"
    "s_cmp_lt_u32 s77, 131072\n\t"
    "s_cbranch_scc1 16b\n\t"
    "17:\n\t"
    "s_branch .Lfb_wh_r\n\t"
    ".Lfb_fo:\n\t"
    "v_lshrrev_b32 v254, 4, v248\n\t"
    "v_min_u32 v254, 15, v254\n\t"
    "v_lshlrev_b32 v254, 11, v254\n\t"
    "v_mov_b32 v255, s20\n\t"
    "buffer_store_dword v255, v254, s[88:91], s22 offen offset:1024 sc1\n\t"
    "s_branch .Lfb_fo_r\n\t"
    ".Lfb_wo:\n\t"
    "s_mov_b32 s77, 0\n\t"
    "18:\n\t"
    "buffer_load_dword v253, v252, s[88:91], 0 offen sc1\n\t"
    "s_waitcnt vmcnt(0)\n\t"
    "v_cmp_ne_u32 vcc, s20, v253\n\t"
    "s_add_u32 s77, s77, 1\n\t"
    "s_nop 2\n\t"
    "s_cbranch_vccz 19f\n\t"
    "s_sleep 1\n\t"
    "s_cmp_lt_u32 s77, 131072\n\t"
    "s_cbranch_scc1 18b\n\t"
    "19:\n\t"
    "s_branch .Lfb_wo_r\n\t"
    ".Lfb_e1:\n\t"
    "s_mov_b32 s66, 50593792\n\t"
    "buffer_load_dwordx4 v[224:227], v248, s[88:91], s66 offen sc1\n\t"
    "s_branch .Lfb_e1_r\n\t"
    ".Lfb_e2:\n\t"
    "s_mov_b32 s77, 0\n\t"
    "s_waitcnt vmcnt(20)\n\t"
    "50:\n\t"
    "v_xor_b32 v224, s20, v224\n\t"
    "v_xor_b32 v225, s20, v225\n\t"
    "v_xor_b32 v226, s20, v226\n\t"
    "v_xor_b32 v227, s20, v227\n\t"
    "v_or3_b32 v224, v224, v225, v226\n\t"
    "v_or_b32 v224, v224, v227\n\t"
    "v_cmp_ne_u32 vcc, 0, v224\n\t"
    "s_add_u32 s77, s77, 1\n\t"
    "s_nop 2\n\t"
    "s_cbranch_vccz 51f\n\t"
    "buffer_load_dwordx4 v[224:227], v248, s[88:91], s66 offen sc1\n\t"
    "s_waitcnt vmcnt(0)\n\t"
    "s_cmp_lt_u32 s77, 131072\n\t"
    "s_cbranch_scc1 50b\n\t"
    "51:\n\t"
    "s_mov_b32 s66, 50331648\n\t"
    "v_mov_b32 v254, 0\n\t"
    "v_mov_b32 v255, s20\n\t"
    "buffer_store_dword v255, v254, s[88:91], s66 offen sc1\n\t"
    "s_branch .Lfb_e2_r\n\t"
    ".Lfb_wd:\n\t"
    "s_mov_b32 s77, 0\n\t"
    "24:\n\t"
    "buffer_load_dword v253, v252, s[88:91], s54 offen sc1\n\t"
    "s_waitcnt vmcnt(0)\n\t"
    "v_cmp_ne_u32 vcc, s20, v253\n\t"
    "s_add_u32 s77, s77, 1\n\t"
    "s_nop 2\n\t"
    "s_cbranch_vccz 25f\n\t"
    "s_sleep 1\n\t"
    "s_cmp_lt_u32 s77, 131072\n\t"
    "s_cbranch_scc1 24b\n\t"
    "25:\n\t"
    "s_branch .Lfb_wd_r\n\t"
    ".Lfb_late:\n\t"
    ".p2align 3\n\t"
    "v_accvgpr_read_b32 v208, a240\n\t"
    "v_accvgpr_read_b32 v209, a241\n\t"
    "v_accvgpr_read_b32 v210, a242\n\t"
    "v_accvgpr_read_b32 v211, a243\n\t"
    "v_accvgpr_read_b32 v212, a128\n\t"
    "v_accvgpr_read_b32 v213, a129\n\t"
    "v_accvgpr_read_b32 v214, a130\n\t"
    "v_accvgpr_read_b32 v215, a131\n\t"
    "v_pk_mul_f32 v[128:129], v[208:209], s[68:69]\n\t"
    "v_pk_mul_f32 v[130:131], v[210:211], s[68:69]\n\t"
    "v_pk_mul_f32 v[132:133], v[212:213], s[68:69]\n\t"
    "v_pk_mul_f32 v[134:135], v[214:215], s[68:69]\n\t"
    "v_exp_f32 v128, v128\n\t"
    "v_exp_f32 v129, v129\n\t"
    "v_exp_f32 v130, v130\n\t"
    "v_exp_f32 v131, v131\n\t"
    "v_exp_f32 v132, v132\n\t"
    "v_exp_f32 v133, v133\n\t"
    "v_exp_f32 v134, v134\n\t"
    "v_exp_f32 v135, v135\n\t"
    "v_pk_add_f32 v[128:129], v[128:129], s[70:71]\n\t"
    "v_pk_add_f32 v[130:131], v[130:131], s[70:71]\n\t"
    "v_pk_add_f32 v[132:133], v[132:133], s[70:71]\n\t"
    "v_pk_add_f32 v[134:135], v[134:135], s[70:71]\n\t"
    "v_rcp_f32 v128, v128\n\t"
    "v_rcp_f32 v129, v129\n\t"
    "v_rcp_f32 v130, v130\n\t"
    "v_rcp_f32 v131, v131\n\t"
    "v_rcp_f32 v132, v132\n\t"
    "v_rcp_f32 v133, v133\n\t"
    "v_rcp_f32 v134, v134\n\t"
    "v_rcp_f32 v135, v135\n\t"
    "v_pk_mul_f32 v[208:209], v[128:129], v[208:209]\n\t"
    "v_pk_mul_f32 v[210:211], v[130:131], v[210:211]\n\t"
    "v_pk_mul_f32 v[212:213], v[132:133], v[212:213]\n\t"
    "v_pk_mul_f32 v[214:215], v[134:135], v[214:215]\n\t"
    "v_accvgpr_read_b32 v216, a16\n\t"
    "v_accvgpr_read_b32 v217, a17\n\t"
    "v_accvgpr_read_b32 v218, a18\n\t"
    "v_accvgpr_read_b32 v219, a19\n\t"
    "v_accvgpr_read_b32 v220, a48\n\t"
    "v_accvgpr_read_b32 v221, a49\n\t"
    "v_accvgpr_read_b32 v222, a50\n\t"
    "v_accvgpr_read_b32 v223, a51\n\t"
    "v_pk_mul_f32 v[128:129], v[216:217], s[68:69]\n\t"
    "v_pk_mul_f32 v[130:131], v[218:219], s[68:69]\n\t"
    "v_pk_mul_f32 v[132:133], v[220:221], s[68:69]\n\t"
    "v_pk_mul_f32 v[134:135], v[222:223], s[68:69]\n\t"
    "v_exp_f32 v128, v128\n\t"
    "v_exp_f32 v129, v129\n\t"
    "v_exp_f32 v130, v130\n\t"
    "v_exp_f32 v131, v131\n\t"
    "v_exp_f32 v132, v132\n\t"
    "v_exp_f32 v133, v133\n\t"
    "v_exp_f32 v134, v134\n\t"
    "v_exp_f32 v135, v135\n\t"
    "v_pk_add_f32 v[128:129], v[128:129], s[70:71]\n\t"
    "v_pk_add_f32 v[130:131], v[130:131], s[70:71]\n\t"
    "v_pk_add_f32 v[132:133], v[132:133], s[70:71]\n\t"
    "v_pk_add_f32 v[134:135], v[134:135], s[70:71]\n\t"
    "v_rcp_f32 v128, v128\n\t"
    "v_rcp_f32 v129, v129\n\t"
    "v_rcp_f32 v130, v130\n\t"
    "v_rcp_f32 v131, v131\n\t"
    "v_rcp_f32 v132, v132\n\t"
    "v_rcp_f32 v133, v133\n\t"
    "v_rcp_f32 v134, v134\n\t"
    "v_rcp_f32 v135, v135\n\t"
    "v_pk_mul_f32 v[216:217], v[128:129], v[216:217]\n\t"
    "v_pk_mul_f32 v[218:219], v[130:131], v[218:219]\n\t"
    "v_pk_mul_f32 v[220:221], v[132:133], v[220:221]\n\t"
    "v_pk_mul_f32 v[222:223], v[134:135], v[222:223]\n\t"
    "v_accvgpr_read_b32 v224, a80\n\t"
    "v_accvgpr_read_b32 v225, a81\n\t"
    "v_accvgpr_read_b32 v226, a82\n\t"
    "v_accvgpr_read_b32 v227, a83\n\t"
    "v_accvgpr_read_b32 v228, a112\n\t"
    "v_accvgpr_read_b32 v229, a113\n\t"
    "v_accvgpr_read_b32 v230, a114\n\t"
    "v_accvgpr_read_b32 v231, a115\n\t"
    "v_pk_mul_f32 v[128:129], v[224:225], s[68:69]\n\t"
    "v_pk_mul_f32 v[130:131], v[226:227], s[68:69]\n\t"
    "v_pk_mul_f32 v[132:133], v[228:229], s[68:69]\n\t"
    "v_pk_mul_f32 v[134:135], v[230:231], s[68:69]\n\t"
    "v_exp_f32 v128, v128\n\t"
    "v_exp_f32 v129, v129\n\t"
    "v_exp_f32 v130, v130\n\t"
    "v_exp_f32 v131, v131\n\t"
    "v_exp_f32 v132, v132\n\t"
    "v_exp_f32 v133, v133\n\t"
    "v_exp_f32 v134, v134\n\t"
    "v_exp_f32 v135, v135\n\t"
    "v_pk_add_f32 v[128:129], v[128:129], s[70:71]\n\t"
    "v_pk_add_f32 v[130:131], v[130:131], s[70:71]\n\t"
    "v_pk_add_f32 v[132:133], v[132:133], s[70:71]\n\t"
    "v_pk_add_f32 v[134:135], v[134:135], s[70:71]\n\t"
    "v_rcp_f32 v128, v128\n\t"
    "v_rcp_f32 v129, v129\n\t"
    "v_rcp_f32 v130, v130\n\t"
    "v_rcp_f32 v131, v131\n\t"
    "v_rcp_f32 v132, v132\n\t"
    "v_rcp_f32 v133, v133\n\t"
    "v_rcp_f32 v134, v134\n\t"
    "v_rcp_f32 v135, v135\n\t"
    "v_pk_mul_f32 v[224:225], v[128:129], v[224:225]\n\t"
    "v_pk_mul_f32 v[226:227], v[130:131], v[226:227]\n\t"
    "v_pk_mul_f32 v[228:229], v[132:133], v[228:229]\n\t"
    "v_pk_mul_f32 v[230:231], v[134:135], v[230:231]\n\t"
    "s_mov_b32 s77, 0\n\t"
    "43:\n\t"
    "buffer_load_dword v253, v252, s[88:91], s54 offen sc1\n\t"
    "s_waitcnt vmcnt(0)\n\t"
    "v_cmp_ne_u32 vcc, s20, v253\n\t"
    "s_add_u32 s77, s77, 1\n\t"
    "s_nop 2\n\t"
    "s_cbranch_vccz 44f\n\t"
    "s_sleep 1\n\t"
    "s_cmp_lt_u32 s77, 131072\n\t"
    "s_cbranch_scc1 43b\n\t"
    "44:\n\t"
    "buffer_load_dwordx4 a[228:231], v248, s[88:91], s63 offen sc1\n\t"
    "buffer_load_dwordx4 a[232:235], v248, s[88:91], s64 offen sc1\n\t"
    "buffer_load_dwordx4 a[236:239], v248, s[88:91], s65 offen sc1\n\t"
    "buffer_load_dwordx4 a[132:135], v248, s[88:91], s63 offen offset:1024 sc1\n\t"
    "buffer_load_dwordx4 a[136:139], v248, s[88:91], s64 offen offset:1024 sc1\n\t"
    "buffer_load_dwordx4 a[140:143], v248, s[88:91], s65 offen offset:1024 sc1\n\t"
    "buffer_load_dwordx4 a[52:55], v248, s[88:91], s63 offen offset:2048 sc1\n\t"
    "buffer_load_dwordx4 a[56:59], v248, s[88:91], s64 offen offset:2048 sc1\n\t"
    "buffer_load_dwordx4 a[60:63], v248, s[88:91], s65 offen offset:2048 sc1\n\t"
    "buffer_load_dwordx4 a[116:119], v248, s[88:91], s63 offen offset:3072 sc1\n\t"
    "buffer_load_dwordx4 a[120:123], v248, s[88:91], s64 offen offset:3072 sc1\n\t"
    "buffer_load_dwordx4 a[124:127], v248, s[88:91], s65 offen offset:3072 sc1\n\t"
    "s_branch .Lfb_fin\n\t"
    ".Lfb_slow:\n\t"
    "s_waitcnt vmcnt(0) lgkmcnt(0)\n\t"
    "s_barrier\n\t"
    "s_mov_b32 s87, -1\n\t"
    "s_mov_b32 s77, 0\n\t"
    "v_mov_b32 v208, 0xff800000\n\t"
    "v_mov_b32 v209, 0xff800000\n\t"
    "v_mov_b32 v210, 0xff800000\n\t"
    "v_mov_b32 v211, 0xff800000\n\t"
    "6:\n\t"
    "buffer_load_dwordx4 a[16:19], v248, s[80:83], s77 offen sc1\n\t"
    "buffer_load_dwordx4 a[48:51], v248, s[80:83], s77 offen offset:1024 sc1\n\t"
    "buffer_load_dwordx4 a[80:83], v248, s[80:83], s77 offen offset:2048 sc1\n\t"
    "buffer_load_dwordx4 a[112:115], v248, s[80:83], s77 offen offset:3072 sc1\n\t"
    "s_waitcnt vmcnt(0)\n\t"
    "v_mfma_f32_16x16x32_bf16 v[160:163], a[16:19], a[0:3], 0\n\t"
    "v_mfma_f32_16x16x32_bf16 v[160:163], a[48:51], a[32:35], v[160:163]\n\t"
    "v_mfma_f32_16x16x32_bf16 v[160:163], a[80:83], a[64:67], v[160:163]\n\t"
    "v_mfma_f32_16x16x32_bf16 v[160:163], a[112:115], a[96:99], v[160:163]\n\t"
    "v_mfma_f32_16x16x32_bf16 v[164:167], a[112:115], a[100:103], 0\n\t"
    "v_mfma_f32_16x16x32_bf16 v[164:167], a[80:83], a[68:71], v[164:167]\n\t"
    "v_mfma_f32_16x16x32_bf16 v[164:167], a[48:51], a[36:39], v[164:167]\n\t"
    "v_mfma_f32_16x16x32_bf16 v[164:167], a[16:19], a[4:7], v[164:167]\n\t"
    "v_mfma_f32_16x16x32_bf16 v[168:171], a[16:19], a[8:11], 0\n\t"
    "v_mfma_f32_16x16x32_bf16 v[168:171], a[48:51], a[40:43], v[168:171]\n\t"
    "v_mfma_f32_16x16x32_bf16 v[168:171], a[80:83], a[72:75], v[168:171]\n\t"
    "v_mfma_f32_16x16x32_bf16 v[168:171], a[112:115], a[104:107], v[168:171]\n\t"
    "v_mfma_f32_16x16x32_bf16 v[172:175], a[112:115], a[108:111], 0\n\t"
    "v_mfma_f32_16x16x32_bf16 v[172:175], a[80:83], a[76:79], v[172:175]\n\t"
    "v_mfma_f32_16x16x32_bf16 v[172:175], a[48:51], a[44:47], v[172:175]\n\t"
    "v_mfma_f32_16x16x32_bf16 v[172:175], a[16:19], a[12:15], v[172:175]\n\t"
    "s_nop 7\n\t"
    "v_max3_f32 v208, v208, v160, v161\n\t"
    "v_max3_f32 v208, v208, v162, v163\n\t"
    "v_max3_f32 v209, v209, v164, v165\n\t"
    "v_max3_f32 v209, v209, v166, v167\n\t"
    "v_max3_f32 v210, v210, v168, v169\n\t"
    "v_max3_f32 v210, v210, v170, v171\n\t"
    "v_max3_f32 v211, v211, v172, v173\n\t"
    "v_max3_f32 v211, v211, v174, v175\n\t"
    "s_add_u32 s77, s77, 4096\n\t"
    "s_and_b32 s78, s77, 8192\n\t"
    "s_add_u32 s77, s77, s78\n\t"
    "s_cmp_lt_u32 s77, s82\n\t"
    "s_cbranch_scc1 6b\n\t"
    "s_sub_u32 s84, s85, 16384\n\t"
    "s_mov_b32 s96, 134217728\n\t"
    "s_mov_b32 s97, 134217728\n\t"
    "s_mov_b32 s98, 134217728\n\t"
    "s_mov_b32 s99, 134217728\n\t"
    "s_add_u32 m0, s85, 32768\n\t"
    "s_nop 0\n\t"
    "buffer_load_dwordx4 v248, s[80:83], s84 offen sc1 lds\n\t"
    "buffer_load_dwordx4 v248, s[80:83], s84 offen offset:1024 sc1 lds\n\t"
    "buffer_load_dwordx4 v248, s[80:83], s84 offen offset:2048 sc1 lds\n\t"
    "buffer_load_dwordx4 v248, s[80:83], s84 offen offset:3072 sc1 lds\n\t"
    "s_add_u32 s84, s84, 16384\n\t"
    "s_add_u32 m0, s85, 49152\n\t"
    "s_nop 0\n\t"
    "buffer_load_dwordx4 v248, s[80:83], s84 offen sc1 lds\n\t"
    "buffer_load_dwordx4 v248, s[80:83], s84 offen offset:1024 sc1 lds\n\t"
    "buffer_load_dwordx4 v248, s[80:83], s84 offen offset:2048 sc1 lds\n\t"
    "buffer_load_dwordx4 v248, s[80:83], s84 offen offset:3072 sc1 lds\n\t"
    "s_add_u32 s84, s84, 16384\n\t"
    "s_add_u32 m0, s85, 65536\n\t"
    "s_nop 0\n\t"
    "buffer_load_dwordx4 v248, s[80:83], s84 offen sc1 lds\n\t"
    "buffer_load_dwordx4 v248, s[80:83], s84 offen offset:1024 sc1 lds\n\t"
    "buffer_load_dwordx4 v248, s[80:83], s84 offen offset:2048 sc1 lds\n\t"
    "buffer_load_dwordx4 v248, s[80:83], s84 offen offset:3072 sc1 lds\n\t"
    "s_add_u32 s84, s84, 16384\n\t"
    "s_add_u32 m0, s85, 81920\n\t"
    "s_nop 0\n\t"
    "buffer_load_dwordx4 v248, s[80:83], s84 offen sc1 lds\n\t"
    "buffer_load_dwordx4 v248, s[80:83], s84 offen offset:1024 sc1 lds\n\t"
    "buffer_load_dwordx4 v248, s[80:83], s84 offen offset:2048 sc1 lds\n\t"
    "buffer_load_dwordx4 v248, s[80:83], s84 offen offset:3072 sc1 lds\n\t"
    "s_add_u32 s84, s84, 16384\n\t"
    "s_add_u32 m0, s85, 98304\n\t"
    "s_nop 0\n\t"
    "buffer_load_dwordx4 v248, s[80:83], s84 offen sc1 lds\n\t"
    "buffer_load_dwordx4 v248, s[80:83], s84 offen offset:1024 sc1 lds\n\t"
    "buffer_load_dwordx4 v248, s[80:83], s84 offen offset:2048 sc1 lds\n\t"
    "buffer_load_dwordx4 v248, s[80:83], s84 offen offset:3072 sc1 lds\n\t"
    "s_add_u32 s84, s84, 16384\n\t"
    "s_add_u32 m0, s85, 114688\n\t"
    "s_nop 0\n\t"
    "buffer_load_dwordx4 v248, s[80:83], s84 offen sc1 lds\n\t"
    "buffer_load_dwordx4 v248, s[80:83], s84 offen offset:1024 sc1 lds\n\t"
    "buffer_load_dwordx4 v248, s[80:83], s84 offen offset:2048 sc1 lds\n\t"
    "buffer_load_dwordx4 v248, s[80:83], s84 offen offset:3072 sc1 lds\n\t"
    "s_add_u32 s84, s84, 16384\n\t"
    "v_mov_b32 v254, 0x3f803f80\n\t"
    "v_accvgpr_write_b32 a240, v254\n\t"
    "v_accvgpr_write_b32 a241, v254\n\t"
    "v_accvgpr_write_b32 a242, v254\n\t"
    "v_accvgpr_write_b32 a243, v254\n\t"
    "v_add_u32 v250, 65536, v248\n\t"
    "v_add_u32 v251, 131072, v248\n\t"
    "s_waitcnt vmcnt(0)\n\t"
    "s_barrier\n\t"
    "ds_read_b128 a[16:19], v248 offset:49152\n\t"
    "ds_read_b128 a[48:51], v248 offset:50176\n\t"
    "ds_read_b128 a[80:83], v248 offset:51200\n\t"
    "ds_read_b128 a[112:115], v248 offset:52224\n\t"
    "ds_read_b128 a[20:23], v248 offset:53248\n\t"
    "ds_read_b128 a[52:55], v248 offset:54272\n\t"
    "ds_read_b128 a[84:87], v248 offset:55296\n\t"
    "ds_read_b128 a[116:119], v248 offset:56320\n\t"
    "s_waitcnt lgkmcnt(0)\n\t"
    "s_branch .Lfb_att\n\t"
    ".Lfb_end:\n\t"
    :
    : "{s[0:1]}"(__builtin_amdgcn_kernarg_segment_ptr()), "{s2}"(blockIdx.x), "{v0}"(threadIdx.x)
    : "memory", "scc", "v1", "v2", "v3", "v4", "v5", "v6", "v7", "v8", "v9", "v10", "v11", "v12", "v13", "v14", "v15", "v16", "v17", "v18", "v19", "v20", "v21", "v22", "v23", "v24", "v25", "v26", "v27", "v28", "v29", "v30", "v31", "v32", "v33", "v34", "v35", "v36", "v37", "v38", "v39", "v40", "v41", "v42", "v43", "v44", "v45", "v46", "v47", "v48", "v49", "v50", "v51", "v52", "v53", "v54", "v55", "v56", "v57", "v58", "v59", "v60", "v61", "v62", "v63", "v64", "v65", "v66", "v67", "v68", "v69", "v70", "v71", "v72", "v73", "v74", "v75", "v76", "v77", "v78", "v79", "v80", "v81", "v82", "v83", "v84", "v85", "v86", "v87", "v88", "v89", "v90", "v91", "v92", "v93", "v94", "v95", "v96", "v97", "v98", "v99", "v100", "v101", "v102", "v103", "v104", "v105", "v106", "v107", "v108", "v109", "v110", "v111", "v112", "v113", "v114", "v115", "v116", "v117", "v118", "v119", "v120", "v121", "v122", "v123", "v124", "v125", "v126", "v127", "v128", "v129", "v130", "v131", "v132", "v133", "v134", "v135", "v136", "v137", "v138", "v139", "v140", "v141", "v142", "v143", "v144", "v145", "v146", "v147", "v148", "v149", "v150", "v151", "v152", "v153", "v154", "v155", "v156", "v157", "v158", "v159", "v160", "v161", "v162", "v163", "v164", "v165", "v166", "v167", "v168", "v169", "v170", "v171", "v172", "v173", "v174", "v175", "v176", "v177", "v178", "v179", "v180", "v181", "v182", "v183", "v184", "v185", "v186", "v187", "v188", "v189", "v190", "v191", "v192", "v193", "v194", "v195", "v196", "v197", "v198", "v199", "v200", "v201", "v202", "v203", "v204", "v205", "v206", "v207", "v208", "v209", "v210", "v211", "v212", "v213", "v214", "v215", "v216", "v217", "v218", "v219", "v220", "v221", "v222", "v223", "v224", "v225", "v226", "v227", "v228", "v229", "v230", "v231", "v232", "v233", "v234", "v235", "v236", "v237", "v238", "v239", "v240", "v241", "v242", "v243", "v244", "v245", "v246", "v247", "v248", "v249", "v250", "v251", "v252", "v253", "v254", "v255", "a0", "a1", "a2", "a3", "a4", "a5", "a6", "a7", "a8", "a9", "a10", "a11", "a12", "a13", "a14", "a15", "a16", "a17", "a18", "a19", "a20", "a21", "a22", "a23", "a24", "a25", "a26", "a27", "a28", "a29", "a30", "a31", "a32", "a33", "a34", "a35", "a36", "a37", "a38", "a39", "a40", "a41", "a42", "a43", "a44", "a45", "a46", "a47", "a48", "a49", "a50", "a51", "a52", "a53", "a54", "a55", "a56", "a57", "a58", "a59", "a60", "a61", "a62", "a63", "a64", "a65", "a66", "a67", "a68", "a69", "a70", "a71", "a72", "a73", "a74", "a75", "a76", "a77", "a78", "a79", "a80", "a81", "a82", "a83", "a84", "a85", "a86", "a87", "a88", "a89", "a90", "a91", "a92", "a93", "a94", "a95", "a96", "a97", "a98", "a99", "a100", "a101", "a102", "a103", "a104", "a105", "a106", "a107", "a108", "a109", "a110", "a111", "a112", "a113", "a114", "a115", "a116", "a117", "a118", "a119", "a120", "a121", "a122", "a123", "a124", "a125", "a126", "a127", "a128", "a129", "a130", "a131", "a132", "a133", "a134", "a135", "a136", "a137", "a138", "a139", "a140", "a141", "a142", "a143", "a144", "a145", "a146", "a147", "a148", "a149", "a150", "a151", "a152", "a153", "a154", "a155", "a156", "a157", "a158", "a159", "a160", "a161", "a162", "a163", "a164", "a165", "a166", "a167", "a168", "a169", "a170", "a171", "a172", "a173", "a174", "a175", "a176", "a177", "a178", "a179", "a180", "a181", "a182", "a183", "a184", "a185", "a186", "a187", "a188", "a189", "a190", "a191", "a192", "a193", "a194", "a195", "a196", "a197", "a198", "a199", "a200", "a201", "a202", "a203", "a204", "a205", "a206", "a207", "a208", "a209", "a210", "a211", "a212", "a213", "a214", "a215", "a216", "a217", "a218", "a219", "a220", "a221", "a222", "a223", "a224", "a225", "a226", "a227", "a228", "a229", "a230", "a231", "a232", "a233", "a234", "a235", "a236", "a237", "a238", "a239", "a240", "a241", "a242", "a243", "a244", "a245", "a246", "a247", "a248", "a249", "a250", "a251", "a252", "a253", "a254", "a255", "vcc", "s3", "s4", "s5", "s6", "s7", "s8", "s9", "s10", "s11", "s12", "s13", "s14", "s15", "s16", "s17", "s18", "s19", "s20", "s21", "s22", "s23", "s24", "s25", "s26", "s27", "s28", "s29", "s30", "s31", "s33", "s34", "s35", "s36", "s37", "s38", "s39", "s40", "s41", "s42", "s43", "s44", "s45", "s46", "s47", "s48", "s49", "s50", "s51", "s52", "s53", "s54", "s55", "s56", "s57", "s58", "s59", "s60", "s61", "s62", "s63", "s64", "s65", "s66", "s67", "s68", "s69", "s70", "s71", "s72", "s73", "s74", "s75", "s76", "s77", "s78", "s79", "s80", "s81", "s82", "s83", "s84", "s85", "s86", "s87", "s88", "s89", "s90", "s91", "s92", "s93", "s94", "s95", "s96", "s97", "s98", "s99");
}

extern "C" void kernel_launch(void* const* d_in, const int* in_sizes, int n_in, void* d_out, int out_size, void* d_ws, size_t ws_size, hipStream_t stream) {
  Args a;
  a.x = (const u16*)d_in[0]; a.kc = (const u16*)d_in[1]; a.vc = (const u16*)d_in[2]; a.wq = (const u16*)d_in[3]; a.wo = (const u16*)d_in[4];
  a.wg = (const u16*)d_in[6]; a.wu = (const u16*)d_in[7]; a.wd = (const u16*)d_in[8]; a.ws = (char*)d_ws; a.out = (u16*)d_out;
  static const uint64_t kOrd[16] = {0xfedbac7654329810ull, 0xfecbda7654239810ull, 0xfdceba7653249810ull, 0xedfcba7643529810ull, 0xfedcba7546329810ull, 0xfedcba6574329810ull, 0xfedcb76a54329810ull, 0xfedca7b654328910ull, 0x00dcba9876543210ull, 0xfedcba9876543210ull, 0x00dcba9876543210ull, 0xfedcba9876543210ull, 0x00dcba9876543210ull, 0xfedcba9876543210ull, 0x00dcba9876543210ull, 0xfedcba9876543210ull};
  for (int i = 0; i < 16; ++i) a.ord[i] = kOrd[i];
  (void)hipFuncSetAttribute((const void*)fused_block, hipFuncAttributeMaxDynamicSharedMemorySize, LDS_BYTES);
  hipLaunchKernelGGL(fused_block, dim3(256), dim3(256), LDS_BYTES, stream, a);
}
```
